```python
import torch

def _softmax(x, axis=-1):
    return torch.softmax(x, dim=axis)

def _gelu(x):
    return torch.nn.functional.gelu(x, approximate='tanh')

def _silu(x):
    return torch.nn.functional.silu(x)

def _sigmoid(x):
    return torch.sigmoid(x)

def _layernorm(x, axis=-1, eps=1e-05):
    m = x.mean(dim=axis, keepdim=True)
    v = x.var(dim=axis, keepdim=True, unbiased=False)
    return (x - m) / torch.sqrt(v + eps)

def _scatter_add(tgt, idx, src, axis=0):
    return tgt.clone().index_add_(axis, idx, src)

def _segment_sum(src, seg, n):
    out = torch.zeros((n,) + src.shape[1:], dtype=src.dtype)
    return out.index_add_(0, seg, src)

def _pad(x, pw):
    flat = tuple((v for pair in reversed(pw) for v in pair))
    return torch.nn.functional.pad(x, flat)
kernel_name = 'adaLN_cond__sliding_block_cached__glu_gated_geglu__prompt'

def setup_inputs():

    _w = lambda a: (lambda t: t.to(torch.bfloat16) if t.is_floating_point() else t)(torch.as_tensor(a))

    def _A_setup():
        B, Sq, D = (1, 2048, 576)
        Hq, Hkv, Sk, Dh = (32, 16, 2048, 128)
        assert Hq % Hkv == 0, (Hq, Hkv)
        return {'x': _w((lambda *s: torch.randn(*s, dtype=torch.bfloat16) * 0.05)(B, Sq, D)), 'k_cache': _w((lambda *s: torch.randn(*s, dtype=torch.bfloat16) * 1.6)(B, Hkv, Sk, Dh)), 'v_cache': _w((lambda *s: torch.randn(*s, dtype=torch.bfloat16) * 0.8)(B, Hkv, Sk, Dh)), 'wq': _w((lambda *s: torch.randn(*s, dtype=torch.bfloat16) * 1.6)(D, Hq * Dh) * D ** (-0.5)), 'wo': _w((lambda *s: torch.randn(*s, dtype=torch.bfloat16) * 0.8)(Hq * Dh, D) * (Hq * Dh) ** (-0.5))}
    _A_in = _A_setup()

    def _B_setup():
        B, S, Dm, Df = (1, 2048, 576, 4608)
        return {'x': _w((lambda *s: torch.randn(*s, dtype=torch.bfloat16) * 0.05)(B, S, Dm)), 'w_gate': _w((lambda *s: torch.randn(*s, dtype=torch.bfloat16) * 0.8)(Dm, Df) * Dm ** (-0.5)), 'w_up': _w((lambda *s: torch.randn(*s, dtype=torch.bfloat16) * 0.8)(Dm, Df) * Dm ** (-0.5)), 'w_down': _w((lambda *s: torch.randn(*s, dtype=torch.bfloat16) * 0.8)(Df, Dm) * Df ** (-0.5))}
    _B_in = _B_setup()
    return {'A_x': _A_in['x'], 'A_k_cache': _A_in['k_cache'], 'A_v_cache': _A_in['v_cache'], 'A_wq': _A_in['wq'], 'A_wo': _A_in['wo'], 'B_x': _B_in['x'], 'B_w_gate': _B_in['w_gate'], 'B_w_up': _B_in['w_up'], 'B_w_down': _B_in['w_down'], 'c': _w((lambda *s: torch.randn(*s, dtype=torch.bfloat16) * 0.1)(1, 576) * 8.0), 'w_mod': _w((lambda *s: torch.randn(*s, dtype=torch.bfloat16) * 0.1)(576, 6 * 576) * 576 ** (-0.5) * 8.0)}

def reference(A_x, A_k_cache, A_v_cache, A_wq, A_wo, B_x, B_w_gate, B_w_up, B_w_down, c, w_mod):

    def _A_ref(x, k_cache, v_cache, wq, wo):
        B, Sq, D = x.shape
        Hq, Dh = (32, 128)
        Hkv, Sk = (k_cache.shape[1], k_cache.shape[2])
        W = min(4096, max(256, Sk // 4))
        rep = Hq // Hkv
        q = (lambda x, *d: x.permute(*d))((x @ wq).reshape(B, Sq, Hq, Dh), 0, 2, 1, 3)
        k = (lambda x, n, axis: torch.repeat_interleave(x, n, dim=axis))(k_cache[:, :, Sk - W:, :], rep, axis=1)
        v = (lambda x, n, axis: torch.repeat_interleave(x, n, dim=axis))(v_cache[:, :, Sk - W:, :], rep, axis=1)
        scores = torch.einsum('bhqd,bhkd->bhqk', q, k) * Dh ** (-0.5)
        p = _softmax(scores, axis=-1)
        o = torch.einsum('bhqk,bhkd->bhqd', p, v)
        return (lambda x, *d: x.permute(*d))(o, 0, 2, 1, 3).reshape(B, Sq, Hq * Dh) @ wo

    def _B_ref(x, w_gate, w_up, w_down):
        g = torch.einsum('bsd,df->bsf', x, w_gate)
        u = torch.einsum('bsd,df->bsf', x, w_up)
        h = _gelu(g) * u
        return torch.einsum('bsf,fd->bsd', h, w_down)
    x = A_x
    mod = (_silu(c) @ w_mod).reshape(c.shape[0], 6, 1, -1)
    sa, ca, ga, sb, cb, gb = (mod[:, 0].clone(), mod[:, 1].clone(), mod[:, 2].clone(), mod[:, 3].clone(), mod[:, 4].clone(), mod[:, 5].clone())
    h = _layernorm(x, axis=-1) * (1 + ca) + sa
    x = x + ga * _A_ref(h, A_k_cache, A_v_cache, A_wq, A_wo)
    h = _layernorm(x, axis=-1) * (1 + cb) + sb
    x = x + gb * _B_ref(h, B_w_gate, B_w_up, B_w_down)
    return x
```

```cpp
#include <hip/hip_runtime.h>
#include <hip/hip_bf16.h>
#include <stdint.h>

typedef unsigned short bf16_t;
__device__ __forceinline__ float bf2f(bf16_t v) { return __uint_as_float(((unsigned)v) << 16); }
__device__ __forceinline__ bf16_t f2bf(float f) { unsigned u = __float_as_uint(f); u += 0x7fffu + ((u >> 16) & 1u); return (bf16_t)(u >> 16); }
__device__ __forceinline__ float rbf(float f) { return bf2f(f2bf(f)); }

constexpr int S = 2048, D = 576, HQ = 32, HKV = 16, DH = 128, SK = 2048, W = 512, DF = 4608;

__global__ void k_mod(const bf16_t* c, const bf16_t* wmod, float* mod) {
  int n = blockIdx.x * blockDim.x + threadIdx.x;
  if (n >= 6 * D) return;
  float acc = 0.f;
  for (int k = 0; k < D; ++k) {
    float cv = bf2f(c[k]);
    float s = rbf(cv / (1.f + __expf(-cv)));
    acc += s * bf2f(wmod[(size_t)k * 6 * D + n]);
  }
  mod[n] = rbf(acc);
}

template <typename T> __device__ float ld(const T* p, size_t i);
template <> __device__ float ld<float>(const float* p, size_t i) { return p[i]; }
template <> __device__ float ld<bf16_t>(const bf16_t* p, size_t i) { return bf2f(p[i]); }

template <typename T>
__global__ void k_ln(const T* x, const float* shift, const float* scale, bf16_t* h) {
  int row = blockIdx.x; int t = threadIdx.x;
  float v[9]; float s = 0.f;
  for (int i = 0; i < 9; ++i) { v[i] = ld<T>(x, (size_t)row * D + t + 64 * i); s += v[i]; }
  for (int o = 32; o; o >>= 1) s += __shfl_xor(s, o);
  float m = s / D; float q = 0.f;
  for (int i = 0; i < 9; ++i) { float d = v[i] - m; q += d * d; }
  for (int o = 32; o; o >>= 1) q += __shfl_xor(q, o);
  float rstd = rsqrtf(q / D + 1e-5f);
  for (int i = 0; i < 9; ++i) { int c = t + 64 * i; h[(size_t)row * D + c] = f2bf((v[i] - m) * rstd * (1.f + scale[c]) + shift[c]); }
}

__global__ void k_gemm(const bf16_t* A, const bf16_t* B, float* C, int M, int N, int K) {
  __shared__ float As[16][65], Bs[16][65];
  int tx = threadIdx.x & 15, ty = threadIdx.x >> 4;
  int m0 = blockIdx.y * 64, n0 = blockIdx.x * 64;
  float acc[4][4] = {};
  for (int k0 = 0; k0 < K; k0 += 16) {
    for (int i = threadIdx.x; i < 16 * 64; i += 256) {
      int r = i >> 4, c = i & 15; As[c][r] = bf2f(A[(size_t)(m0 + r) * K + k0 + c]);
      int kr = i >> 6, nc = i & 63; Bs[kr][nc] = bf2f(B[(size_t)(k0 + kr) * N + n0 + nc]);
    }
    __syncthreads();
    for (int k = 0; k < 16; ++k) {
      float a[4], b[4];
      for (int i = 0; i < 4; ++i) { a[i] = As[k][ty * 4 + i]; b[i] = Bs[k][tx * 4 + i]; }
      for (int i = 0; i < 4; ++i) for (int j = 0; j < 4; ++j) acc[i][j] += a[i] * b[j];
    }
    __syncthreads();
  }
  for (int i = 0; i < 4; ++i) for (int j = 0; j < 4; ++j) C[(size_t)(m0 + ty * 4 + i) * N + n0 + tx * 4 + j] = acc[i][j];
}

__global__ void k_tobf(const float* in, bf16_t* out, size_t n) {
  size_t i = blockIdx.x * (size_t)blockDim.x + threadIdx.x; if (i < n) out[i] = f2bf(in[i]);
}

__global__ void k_attn(const bf16_t* q, const bf16_t* kc, const bf16_t* vc, bf16_t* o) {
  __shared__ float qs[8][DH]; __shared__ float p[8][W]; __shared__ float red[8];
  int h = blockIdx.x, r0 = blockIdx.y * 8, t = threadIdx.x; int kvh = h / 2;
  for (int i = t; i < 8 * DH; i += 256) qs[i / DH][i % DH] = bf2f(q[(size_t)(r0 + i / DH) * (HQ * DH) + h * DH + i % DH]);
  __syncthreads();
  const bf16_t* K = kc + ((size_t)kvh * SK + (SK - W)) * DH; const bf16_t* V = vc + ((size_t)kvh * SK + (SK - W)) * DH;
  for (int key = t; key < W; key += 256) {
    float a[8] = {};
    for (int d = 0; d < DH; ++d) { float kv = bf2f(K[(size_t)key * DH + d]); for (int r = 0; r < 8; ++r) a[r] += qs[r][d] * kv; }
    for (int r = 0; r < 8; ++r) p[r][key] = a[r] * 0.08838834764831845f;
  }
  __syncthreads();
  int wv = t >> 6, ln = t & 63;
  for (int r = wv; r < 8; r += 4) {
    float m = -1e30f; for (int k = ln; k < W; k += 64) m = fmaxf(m, p[r][k]);
    for (int o2 = 32; o2; o2 >>= 1) m = fmaxf(m, __shfl_xor(m, o2));
    float s = 0.f; for (int k = ln; k < W; k += 64) { float e = __expf(p[r][k] - m); p[r][k] = e; s += e; }
    for (int o2 = 32; o2; o2 >>= 1) s += __shfl_xor(s, o2);
    if (ln == 0) red[r] = s;
  }
  __syncthreads();
  for (int i = t; i < 8 * DH; i += 256) {
    int r = i / DH, d = i % DH; float a = 0.f;
    for (int k = 0; k < W; ++k) a += p[r][k] * bf2f(V[(size_t)k * DH + d]);
    o[(size_t)(r0 + r) * (HQ * DH) + h * DH + d] = f2bf(a / red[r]);
  }
}

__global__ void k_res1(const bf16_t* x, const float* attn, const float* g, float* x1) {
  size_t i = blockIdx.x * (size_t)blockDim.x + threadIdx.x; if (i >= (size_t)S * D) return;
  int c = i % D; x1[i] = bf2f(x[i]) + g[c] * attn[i];
}
__global__ void k_geglu(const float* g, const float* u, bf16_t* a) {
  size_t i = blockIdx.x * (size_t)blockDim.x + threadIdx.x; if (i >= (size_t)S * DF) return;
  float x = g[i]; float t = tanhf(0.7978845608028654f * (x + 0.044715f * x * x * x));
  a[i] = f2bf(0.5f * x * (1.f + t) * u[i]);
}
__global__ void k_res2(const float* x1, const float* dn, const float* g, bf16_t* out) {
  size_t i = blockIdx.x * (size_t)blockDim.x + threadIdx.x; if (i >= (size_t)S * D) return;
  int c = i % D; out[i] = f2bf(x1[i] + g[c] * dn[i]);
}

extern "C" void kernel_launch(void* const* d_in, const int* in_sizes, int n_in, void* d_out, int out_size, void* d_ws, size_t ws_size, hipStream_t stream) {
  const bf16_t* x = (const bf16_t*)d_in[0]; const bf16_t* kc = (const bf16_t*)d_in[1]; const bf16_t* vc = (const bf16_t*)d_in[2];
  const bf16_t* wq = (const bf16_t*)d_in[3]; const bf16_t* wo = (const bf16_t*)d_in[4];
  const bf16_t* wg = (const bf16_t*)d_in[6]; const bf16_t* wu = (const bf16_t*)d_in[7]; const bf16_t* wd = (const bf16_t*)d_in[8];
  const bf16_t* c = (const bf16_t*)d_in[9]; const bf16_t* wmod = (const bf16_t*)d_in[10];
  char* ws = (char*)d_ws; size_t off = 0;
  auto alloc = [&](size_t b) { void* p = ws + off; off += (b + 255) / 256 * 256; return p; };
  float* mod = (float*)alloc(6 * D * 4);
  bf16_t* h = (bf16_t*)alloc((size_t)S * D * 2);
  float* f1 = (float*)alloc((size_t)S * DF * 4);
  float* f2 = (float*)alloc((size_t)S * DF * 4);
  bf16_t* qb = (bf16_t*)alloc((size_t)S * 4096 * 2);
  bf16_t* ob = (bf16_t*)alloc((size_t)S * 4096 * 2);
  float* x1 = (float*)alloc((size_t)S * D * 4);
  bf16_t* act = (bf16_t*)alloc((size_t)S * DF * 2);
  k_mod<<<(6 * D + 63) / 64, 64, 0, stream>>>(c, wmod, mod);
  k_ln<bf16_t><<<S, 64, 0, stream>>>(x, mod + 0 * D, mod + 1 * D, h);
  k_gemm<<<dim3(4096 / 64, S / 64), 256, 0, stream>>>(h, wq, f1, S, 4096, D);
  k_tobf<<<(S * 4096 + 255) / 256, 256, 0, stream>>>(f1, qb, (size_t)S * 4096);
  k_attn<<<dim3(HQ, S / 8), 256, 0, stream>>>(qb, kc, vc, ob);
  k_gemm<<<dim3(D / 64, S / 64), 256, 0, stream>>>(ob, wo, f1, S, D, 4096);
  k_res1<<<(S * D + 255) / 256, 256, 0, stream>>>(x, f1, mod + 2 * D, x1);
  k_ln<float><<<S, 64, 0, stream>>>(x1, mod + 3 * D, mod + 4 * D, h);
  k_gemm<<<dim3(DF / 64, S / 64), 256, 0, stream>>>(h, wg, f1, S, DF, D);
  k_gemm<<<dim3(DF / 64, S / 64), 256, 0, stream>>>(h, wu, f2, S, DF, D);
  k_geglu<<<(S * DF + 255) / 256, 256, 0, stream>>>(f1, f2, act);
  k_gemm<<<dim3(D / 64, S / 64), 256, 0, stream>>>(act, wd, f1, S, D, DF);
  k_res2<<<(S * D + 255) / 256, 256, 0, stream>>>(x1, f1, mod + 5 * D, (bf16_t*)d_out);
}
```

```cpp
#include <hip/hip_runtime.h>
#include <stdint.h>
#include <utility>
#include <type_traits>

typedef unsigned short u16;
typedef unsigned long long u64;
typedef __bf16 b2 __attribute__((ext_vector_type(2)));
typedef __bf16 b4 __attribute__((ext_vector_type(4)));
typedef __bf16 b8 __attribute__((ext_vector_type(8)));
typedef float f2 __attribute__((ext_vector_type(2)));
typedef float f4 __attribute__((ext_vector_type(4)));
typedef float f16v __attribute__((ext_vector_type(16)));
typedef unsigned u2 __attribute__((ext_vector_type(2)));
typedef unsigned u4 __attribute__((ext_vector_type(4)));
#define LAS __attribute__((address_space(3)))
#define DI __device__ __forceinline__
#define GAP() __builtin_amdgcn_sched_barrier(0)
template <int V> using IC = std::integral_constant<int, V>;

constexpr int S = 2048, D = 576, HQ = 32, DH = 128, SK = 2048, WIN = 512, DF = 4608;
constexpr int ROWB = D * 2;
constexpr int ABYTES = 64 * ROWB;
constexpr int SLICE = 8 * ROWB;
constexpr int SLOT = 9216;
constexpr size_t WS_MOD = 0;
constexpr size_t WS_X = 65536;
constexpr size_t WS_H2 = WS_X + (size_t)32 * 64 * SLICE;
constexpr size_t WS_X1 = WS_H2 + (size_t)32 * ABYTES;
constexpr size_t WS_END = WS_X1 + (size_t)256 * SLICE;

DI float bf2f(u16 v) { return __uint_as_float(((unsigned)v) << 16); }
DI float bflo(unsigned w) { return __uint_as_float(w << 16); }
DI float bfhi(unsigned w) { return __uint_as_float(w & 0xffff0000u); }
DI unsigned pk(float lo, float hi) { f2 v = {lo, hi}; b2 b = __builtin_convertvector(v, b2); return __builtin_bit_cast(unsigned, b); }
DI float rbf(float f) { return bflo(pk(f, f) & 0xffffu); }
DI u16 f2bf(float f) { return (u16)(pk(f, f) & 0xffffu); }

__global__ __launch_bounds__(256) void k_mod(const u16* __restrict__ c, const u16* __restrict__ wmod, u16* __restrict__ mod) {
  __shared__ float red[4][32];
  const int tid = threadIdx.x, lane = tid & 63, w = tid >> 6;
  const int n0 = blockIdx.x * 32 + (tid & 3) * 8, r0 = tid >> 2;
  u4 v[9]; float s[9];
#pragma unroll
  for (int i = 0; i < 9; ++i) v[i] = *(const u4*)(wmod + (size_t)(r0 + 64 * i) * (6 * D) + n0);
#pragma unroll
  for (int i = 0; i < 9; ++i) { float x = bf2f(c[r0 + 64 * i]); s[i] = rbf(x / (1.f + __expf(-x))); }
  float acc[8] = {};
#pragma unroll
  for (int i = 0; i < 9; ++i)
#pragma unroll
    for (int e = 0; e < 4; ++e) { acc[2 * e] += s[i] * bflo(v[i][e]); acc[2 * e + 1] += s[i] * bfhi(v[i][e]); }
#pragma unroll
  for (int e = 0; e < 8; ++e) {
    float a = acc[e];
    for (int o = 4; o < 64; o <<= 1) a += __shfl_xor(a, o);
    if (lane < 4) red[w][lane * 8 + e] = a;
  }
  __syncthreads();
  if (tid < 32) mod[blockIdx.x * 32 + tid] = f2bf(red[0][tid] + red[1][tid] + red[2][tid] + red[3][tid]);
}

DI void zdecode(int z, int& lr, int& cc) {
  const int w = z / 144, y = z - w * 144;
  if (y < 128) { const int tp = y >> 5; lr = (y >> 2) & 7; cc = 18 * w + 4 * tp + (y & 3); }
  else { const int y2 = y - 128; lr = y2 >> 1; cc = 18 * w + 16 + (y2 & 1); }
}

DI float ln_apply(float x, float m, float rs, float c1, float sh) {
  const float xm = rbf(x - m);
  const float y = rbf(xm * rs);
  return rbf(y * c1) + sh;
}

__global__ __launch_bounds__(256) void k_red1(const char* __restrict__ X, const u16* __restrict__ x, const u16* __restrict__ mod, char* __restrict__ x1g, char* __restrict__ h2g) {
  __shared__ float xs[8][D];
  __shared__ float st[8][2];
  const int tid = threadIdx.x, d = blockIdx.x & 7, c = blockIdx.x >> 3;
  if (tid < 192) {
    const char* src = X + (size_t)(c * 8 + d) * 8 * SLICE;
    u4 v[3][8];
#pragma unroll
    for (int i = 0; i < 3; ++i)
#pragma unroll
      for (int j = 0; j < 8; ++j) v[i][j] = *(const u4*)(src + j * SLICE + (tid + 192 * i) * 16);
#pragma unroll
    for (int i = 0; i < 3; ++i) {
      const int z = tid + 192 * i; int lr, cc; zdecode(z, lr, cc);
      const u4 xv = *(const u4*)(x + (size_t)(c * 64 + d * 8 + lr) * D + cc * 8);
      const u4 gv = *(const u4*)(mod + 2 * D + cc * 8);
      u4 o;
#pragma unroll
      for (int e = 0; e < 4; ++e) {
        float lo = 0.f, hi = 0.f;
#pragma unroll
        for (int j = 0; j < 8; ++j) { lo += bflo(v[i][j][e]); hi += bfhi(v[i][j][e]); }
        const unsigned a = pk(lo, hi);
        const unsigned t = pk(bflo(a) * bflo(gv[e]), bfhi(a) * bfhi(gv[e]));
        o[e] = pk(bflo(xv[e]) + bflo(t), bfhi(xv[e]) + bfhi(t));
        xs[lr][cc * 8 + 2 * e] = bflo(o[e]); xs[lr][cc * 8 + 2 * e + 1] = bfhi(o[e]);
      }
      *(u4*)(x1g + (size_t)blockIdx.x * SLICE + z * 16) = o;
    }
  }
  __syncthreads();
  {
    const int w = tid >> 6, lane = tid & 63;
#pragma unroll
    for (int rr = 0; rr < 2; ++rr) {
      const int r = 2 * w + rr; float v[9]; float s = 0.f;
#pragma unroll
      for (int i = 0; i < 9; ++i) { v[i] = xs[r][lane + 64 * i]; s += v[i]; }
      for (int o = 1; o < 64; o <<= 1) s += __shfl_xor(s, o);
      const float m = s * (1.f / D); float q = 0.f;
#pragma unroll
      for (int i = 0; i < 9; ++i) { const float t = v[i] - m; q += t * t; }
      for (int o = 1; o < 64; o <<= 1) q += __shfl_xor(q, o);
      if (lane == 0) { st[r][0] = rbf(m); st[r][1] = 1.f / rbf(sqrtf(rbf(rbf(q * (1.f / D)) + 1e-5f))); }
    }
  }
  __syncthreads();
  if (tid < 192) {
#pragma unroll
    for (int i = 0; i < 3; ++i) {
      const int y = tid + 192 * i, lr = y / 72, pos = y - lr * 72, row = d * 8 + lr;
      const int q = pos ^ ((row >> 1) & 7), ks = q >> 2, g = q & 3;
      const float m = st[lr][0], rs = st[lr][1];
      u4 o;
#pragma unroll
      for (int h = 0; h < 2; ++h) {
        const int k0 = 32 * ks + 16 * h + 4 * g;
        const u2 cv = *(const u2*)(mod + 4 * D + k0), sv = *(const u2*)(mod + 3 * D + k0);
        float r4[4];
#pragma unroll
        for (int e = 0; e < 4; ++e) {
          const float cb = (e & 1) ? bfhi(cv[e >> 1]) : bflo(cv[e >> 1]), sb = (e & 1) ? bfhi(sv[e >> 1]) : bflo(sv[e >> 1]);
          r4[e] = ln_apply(xs[lr][k0 + e], m, rs, rbf(1.f + cb), sb);
        }
        o[2 * h] = pk(r4[0], r4[1]); o[2 * h + 1] = pk(r4[2], r4[3]);
      }
      *(u4*)(h2g + (size_t)c * ABYTES + (size_t)row * ROWB + pos * 16) = o;
    }
  }
}

__global__ __launch_bounds__(192) void k_red2(const char* __restrict__ X, const char* __restrict__ x1g, const u16* __restrict__ mod, u16* __restrict__ out) {
  const int tid = threadIdx.x, d = blockIdx.x & 7, c = blockIdx.x >> 3;
  const char* src = X + (size_t)c * 64 * SLICE + (size_t)d * SLICE;
  u4 v[3][8];
#pragma unroll
  for (int i = 0; i < 3; ++i)
#pragma unroll
    for (int j = 0; j < 8; ++j) v[i][j] = *(const u4*)(src + (size_t)j * 8 * SLICE + (tid + 192 * i) * 16);
#pragma unroll
  for (int i = 0; i < 3; ++i) {
    const int z = tid + 192 * i; int lr, cc; zdecode(z, lr, cc);
    const u4 xv = *(const u4*)(x1g + (size_t)blockIdx.x * SLICE + z * 16);
    const u4 gv = *(const u4*)(mod + 5 * D + cc * 8);
    u4 o;
#pragma unroll
    for (int e = 0; e < 4; ++e) {
      float lo = 0.f, hi = 0.f;
#pragma unroll
      for (int j = 0; j < 8; ++j) { lo += bflo(v[i][j][e]); hi += bfhi(v[i][j][e]); }
      const unsigned a = pk(lo, hi);
      const unsigned t = pk(bflo(a) * bflo(gv[e]), bfhi(a) * bfhi(gv[e]));
      o[e] = pk(bflo(xv[e]) + bflo(t), bfhi(xv[e]) + bfhi(t));
    }
    *(u4*)(out + (size_t)(c * 64 + d * 8 + lr) * D + cc * 8) = o;
  }
}

template <int IMM> DI void dma(unsigned voff, const char* g) { asm volatile("global_load_lds_dwordx4 %0, %1 offset:%2" :: "v"(voff), "s"(g), "i"(IMM) : "memory"); }
DI void setm0(unsigned a) { asm volatile("s_mov_b32 m0, %0" :: "s"(a) : "memory"); }
DI b4 ldT(unsigned base, int off) { return __builtin_amdgcn_ds_read_tr16_b64_v4bf16((LAS b4*)(size_t)(base + off)); }
DI b8 ldA(unsigned base, int off) { return *(LAS b8*)(size_t)(base + off); }
template <bool AG, bool ZERO> DI void mfma16(f4& c, const b8& a, const b8& b) {
  if constexpr (AG) {
    if constexpr (ZERO) asm volatile("v_mfma_f32_16x16x32_bf16 %0, %1, %2, 0" : "=a"(c) : "v"(a), "v"(b));
    else asm volatile("v_mfma_f32_16x16x32_bf16 %0, %1, %2, %0" : "+a"(c) : "v"(a), "v"(b));
  } else {
    if constexpr (ZERO) asm volatile("v_mfma_f32_16x16x32_bf16 %0, %1, %2, 0" : "=&v"(c) : "v"(a), "v"(b));
    else asm volatile("v_mfma_f32_16x16x32_bf16 %0, %1, %2, %0" : "+v"(c) : "v"(a), "v"(b));
  }
}

template <int NVG> DI void settle(f4 (&acc)[9][4]) {
  asm volatile("s_nop 7\n\ts_nop 7" ::: "memory");
#pragma unroll
  for (int t = 0; t < 9; ++t)
#pragma unroll
    for (int m = 0; m < 4; ++m) { if (t * 4 + m < 36 - NVG) asm volatile("" : "+a"(acc[t][m])); else asm volatile("" : "+v"(acc[t][m])); }
}
struct GS {
  b4 wlo[2][9], whi[2][9];
  b8 af[2][4];
  unsigned wbase, ring;
};
DI void issue_all(const GS& s, int slot, const unsigned (&voff)[9], const char* gp) {
  const unsigned sl = s.ring + slot * SLOT;
  setm0(sl + 4096); asm volatile("s_nop 0");
  dma<-4096>(voff[0], gp); dma<-3072>(voff[1], gp); dma<-2048>(voff[2], gp); dma<-1024>(voff[3], gp);
  dma<0>(voff[4], gp); dma<1024>(voff[5], gp); dma<2048>(voff[6], gp); dma<3072>(voff[7], gp);
  setm0(sl + 8192); asm volatile("s_nop 0"); dma<0>(voff[8], gp);
}
template <int PAR, int AP, bool ZERO, bool DMA, bool RDW, bool RDA, int NVG>
DI void step(GS& s, f4 (&acc)[9][4], const unsigned (&voff)[9], const char* gp, unsigned ab) {
  const unsigned sl = s.ring + PAR * SLOT;
  const unsigned wb = s.wbase + (PAR ^ 1) * SLOT;
  auto mm = [&](auto ic) {
    constexpr int I = decltype(ic)::value, t = I >> 2, m = I & 3;
    mfma16<(I < 36 - NVG), ZERO>(acc[t][m], __builtin_shufflevector(s.wlo[PAR][t], s.whi[PAR][t], 0, 1, 2, 3, 4, 5, 6, 7), s.af[AP][m]);
  };
  auto rd = [&](auto ic) {
    constexpr int I = decltype(ic)::value;
    if constexpr (I < 4) { if constexpr (RDA) s.af[AP ^ 1][I] = ldA(ab, I * 16 * ROWB); }
    else if constexpr (RDW) {
      if constexpr (((I - 4) & 1) == 0) s.wlo[PAR ^ 1][(I - 4) >> 1] = ldT(wb, 32 * ((I - 4) >> 1));
      else s.whi[PAR ^ 1][(I - 4) >> 1] = ldT(wb, 32 * ((I - 4) >> 1) + 4608);
    }
  };
  auto gap = [&](auto gc) {
    constexpr int G = decltype(gc)::value;
    mm(IC<2 * G>{});
    if constexpr (DMA) {
      if constexpr (G == 0) { asm volatile("s_waitcnt lgkmcnt(0)" ::: "memory"); setm0(sl + 4096); }
      if constexpr (G >= 1 && G <= 4) dma<(2 * (G - 1) - 4) * 1024>(voff[2 * (G - 1)], gp);
      if constexpr (G == 5) dma<0>(voff[8], gp);
    }
    if constexpr (G >= 6 && G <= 16) rd(IC<2 * (G - 6)>{});
    GAP();
    mm(IC<2 * G + 1>{});
    if constexpr (DMA) {
      if constexpr (G >= 1 && G <= 4) dma<(2 * (G - 1) + 1 - 4) * 1024>(voff[2 * (G - 1) + 1], gp);
      if constexpr (G == 4) setm0(sl + 8192);
    }
    if constexpr (G == 5 && RDW) { if constexpr (DMA) asm volatile("s_waitcnt vmcnt(9)" ::: "memory"); else asm volatile("s_waitcnt vmcnt(0)" ::: "memory"); }
    if constexpr (G >= 6 && G <= 16) rd(IC<2 * (G - 6) + 1>{});
    GAP();
  };
  [&]<int... I>(std::integer_sequence<int, I...>) { (gap(IC<I>{}), ...); }(std::make_integer_sequence<int, 18>{});
}
DI void read_w(GS& s, int par) {
#pragma unroll
  for (int t = 0; t < 9; ++t) { s.wlo[par][t] = ldT(s.wbase + par * SLOT, 32 * t); s.whi[par][t] = ldT(s.wbase + par * SLOT, 32 * t + 4608); }
}
DI void read_a(GS& s, int set, unsigned ab) {
#pragma unroll
  for (int m = 0; m < 4; ++m) s.af[set][m] = ldA(ab, m * 16 * ROWB);
}
DI void store_partials(const f4 (&acc)[9][4], char* base, size_t dstride, int w, int lane) {
  const int G = lane >> 4, c = lane & 15, lr = c & 7, dh = c >> 3;
#pragma unroll
  for (int m = 0; m < 4; ++m) {
    char* dst = base + (size_t)(2 * m + dh) * dstride + w * 2304 + lr * 64 + (2 * (G & 1) + (G >> 1)) * 16;
#pragma unroll
    for (int tp = 0; tp < 4; ++tp) {
      const f4 a = acc[2 * tp][m], b = acc[2 * tp + 1][m];
      auto r0 = __builtin_amdgcn_permlane16_swap(pk(a[0], a[1]), pk(b[0], b[1]), false, false);
      auto r1 = __builtin_amdgcn_permlane16_swap(pk(a[2], a[3]), pk(b[2], b[3]), false, false);
      *(u4*)(dst + tp * 512) = (u4){r0[0], r1[0], r0[1], r1[1]};
    }
  }
#pragma unroll
  for (int mp = 0; mp < 2; ++mp) {
    const f4 a = acc[8][2 * mp], b = acc[8][2 * mp + 1];
    auto r0 = __builtin_amdgcn_permlane16_swap(pk(a[0], a[1]), pk(b[0], b[1]), false, false);
    auto r1 = __builtin_amdgcn_permlane16_swap(pk(a[2], a[3]), pk(b[2], b[3]), false, false);
    char* dst = base + (size_t)(2 * (2 * mp + (G & 1)) + dh) * dstride + w * 2304 + 2048 + lr * 32 + (G >> 1) * 16;
    *(u4*)dst = (u4){r0[0], r1[0], r0[1], r1[1]};
  }
}

__global__ __launch_bounds__(256, 1) void k_ffn(const char* __restrict__ h2g, const u16* __restrict__ wg, const u16* __restrict__ wu, const u16* __restrict__ wd, char* __restrict__ X) {
  extern __shared__ __attribute__((aligned(16))) char lds[];
  const int tid = threadIdx.x, w = __builtin_amdgcn_readfirstlane(tid >> 6), lane = tid & 63;
  const int j = blockIdx.x & 7, c = blockIdx.x >> 3;
  const unsigned ldsb = (unsigned)(size_t)lds;
  GS s;
  unsigned voffA[9], voffB[9];
#pragma unroll
  for (int i = 0; i < 9; ++i) {
    const int idx = i * 64 + lane, imm = i < 8 ? (i - 4) * 1024 : 0, r = idx / 18, ch = idx % 18;
    voffA[i] = r * (DF * 2) + ch * 16 - imm + 4096;
    voffB[i] = r * ROWB + ch * 16 - imm + 4096;
  }
  const char* gG = (const char*)wg + (size_t)(576 * j + 144 * w) * 2 - 4096;
  const char* gU = (const char*)wu + (size_t)(576 * j + 144 * w) * 2 - 4096;
  const char* gD = (const char*)wd + (size_t)(576 * j) * ROWB + 288 * w - 4096;
  constexpr size_t KS_GU = (size_t)32 * DF * 2, KS_D = (size_t)32 * ROWB;
  s.ring = __builtin_amdgcn_readfirstlane(ldsb + ABYTES + w * 2 * SLOT);
  const int g = lane >> 4, q = (lane & 15) >> 2, p = lane & 3, cl = lane & 15;
  s.wbase = s.ring + (4 * g + q) * 288 + 8 * p;
  const unsigned sw = (cl >> 1) & 7;
  const unsigned ab_e = ldsb + cl * ROWB + ((g ^ sw) * 16), ab_o = ldsb + cl * ROWB + (((4 + g) ^ sw) * 16);
  {
    const char* hs = h2g + (size_t)c * ABYTES;
    const unsigned vo = lane * 16;
#pragma unroll
    for (int i = 0; i < 18; ++i) { setm0(__builtin_amdgcn_readfirstlane(ldsb + (w * 18 + i) * 1024)); asm volatile("s_nop 0"); dma<0>(vo, hs + (size_t)(w * 18 + i) * 1024); }
  }
  issue_all(s, 0, voffA, gG); issue_all(s, 1, voffA, gU);
  asm volatile("s_waitcnt vmcnt(9)" ::: "memory");
  __builtin_amdgcn_s_barrier();
  read_w(s, 0); read_a(s, 0, ab_e);
  f4 aG[9][4], aU[9][4];
  auto kpair = [&](auto zc, auto lastc, int k) {
    constexpr bool Z = decltype(zc)::value, LAST = decltype(lastc)::value;
    const char* g0 = gG + (size_t)(k + 1) * KS_GU; const char* u0 = gU + (size_t)(k + 1) * KS_GU;
    step<0, 0, Z, true, true, false, 0>(s, aG, voffA, g0, 0);
    step<1, 0, Z, true, true, true, 32 / 4>(s, aU, voffA, u0, ab_o + 128 * (k >> 1));
    if constexpr (!LAST) {
      step<0, 1, false, true, true, false, 0>(s, aG, voffA, g0 + KS_GU, 0);
      step<1, 1, false, true, true, true, 32 / 4>(s, aU, voffA, u0 + KS_GU, ab_e + 128 * ((k + 2) >> 1));
    } else {
      step<0, 1, false, true, true, false, 0>(s, aG, voffB, gD, 0);
      step<1, 1, false, true, true, false, 32 / 4>(s, aU, voffB, gD + KS_D, 0);
    }
  };
  kpair(std::true_type{}, std::false_type{}, 0);
  for (int k = 2; k < 16; k += 2) kpair(std::false_type{}, std::false_type{}, k);
  kpair(std::false_type{}, std::true_type{}, 16);
  settle<0>(aG); settle<8>(aU);
  u2 act[9][4];
#pragma unroll
  for (int t = 0; t < 9; ++t)
#pragma unroll
    for (int m = 0; m < 4; ++m) {
      float r[4];
#pragma unroll
      for (int e = 0; e < 4; ++e) {
        const float x = aG[t][m][e];
        const float z = x * __builtin_fmaf(x * x, -0.10294324f, -2.3022082f);
        r[e] = x * aU[t][m][e] * __builtin_amdgcn_rcpf(1.f + __builtin_amdgcn_exp2f(z));
      }
      act[t][m] = (u2){pk(r[0], r[1]), pk(r[2], r[3])};
    }
  asm volatile("s_waitcnt lgkmcnt(0)" ::: "memory");
  __builtin_amdgcn_s_barrier();
#pragma unroll
  for (int t = 0; t < 9; ++t) {
    const int tt = 9 * w + t, ks = tt >> 1, a = tt & 1;
    const unsigned ad = ldsb + cl * ROWB + (((4 * ks + g) ^ sw) * 16) + 8 * a;
#pragma unroll
    for (int m = 0; m < 4; ++m) *(LAS u2*)(size_t)(ad + m * 16 * ROWB) = act[t][m];
  }
  asm volatile("s_waitcnt lgkmcnt(0)" ::: "memory");
  __builtin_amdgcn_s_barrier();
  read_a(s, 0, ab_e);
  step<0, 0, true, true, true, true, 0>(s, aG, voffB, gD + 2 * KS_D, ab_o);
  step<1, 1, false, true, true, true, 0>(s, aG, voffB, gD + 3 * KS_D, ab_e + 128);
  for (int k = 2; k < 16; k += 2) {
    step<0, 0, false, true, true, true, 0>(s, aG, voffB, gD + (size_t)(k + 2) * KS_D, ab_o + 128 * (k >> 1));
    step<1, 1, false, true, true, true, 0>(s, aG, voffB, gD + (size_t)(k + 3) * KS_D, ab_e + 128 * ((k + 2) >> 1));
  }
  step<0, 0, false, false, true, true, 0>(s, aG, voffB, gD, ab_o + 128 * 8);
  step<1, 1, false, false, false, false, 0>(s, aG, voffB, gD, 0);
  settle<0>(aG);
  store_partials(aG, X + (size_t)c * 64 * SLICE + (size_t)j * 8 * SLICE, SLICE, w, lane);
}


constexpr int QSLOT = 8192;
constexpr float QSCALE = 0.08838834764831845f * 1.4426950408889634f;
constexpr float THR = 8.0f;
template <bool ZERO> DI void mfma32a(f16v& c, const b8& a, const b8& b) {
  if constexpr (ZERO) asm volatile("v_mfma_f32_32x32x16_bf16 %0, %1, %2, 0" : "=a"(c) : "v"(a), "v"(b));
  else asm volatile("v_mfma_f32_32x32x16_bf16 %0, %1, %2, %0" : "+a"(c) : "v"(a), "v"(b));
}
DI float max3f(float a, float b, float c) { float r; asm("v_max3_f32 %0, %1, %2, %3" : "=v"(r) : "v"(a), "v"(b), "v"(c)); return r; }
DI float max2f(float a, float b) { float r; asm("v_max_f32_e32 %0, %1, %2" : "=v"(r) : "v"(a), "v"(b)); return r; }
DI float fadd_s(float a, float b) { float r; asm("v_add_f32_e32 %0, %1, %2" : "=v"(r) : "v"(a), "v"(b)); return r; }
DI float fsub_s(float a, float b) { float r; asm("v_sub_f32_e32 %0, %1, %2" : "=v"(r) : "v"(a), "v"(b)); return r; }
DI float swap32_max(float x) { auto r = __builtin_amdgcn_permlane32_swap(__float_as_uint(x), __float_as_uint(x), false, false); return max2f(__uint_as_float(r[0]), __uint_as_float(r[1])); }
DI float swap32_add(float x) { auto r = __builtin_amdgcn_permlane32_swap(__float_as_uint(x), __float_as_uint(x), false, false); return __uint_as_float(r[0]) + __uint_as_float(r[1]); }

struct QS {
  b4 wlo[2][8], whi[2][8];
  b8 hf[2][4];
  unsigned wb[4];
  unsigned hb[4];
  unsigned ring;
};
template <int PAR, bool ZERO, bool DMA, bool RD>
DI void qstep(QS& s, f16v (&acc)[4][2], const unsigned (&voff)[8], const char* gp, unsigned hoff) {
  const unsigned sl = s.ring + PAR * QSLOT;
  auto rd = [&](auto ic) {
    constexpr int I = decltype(ic)::value;
    if constexpr (RD) {
      if constexpr (I < 4) {
        s.hf[PAR ^ 1][I] = ldA(s.hb[2 * (PAR ^ 1) + (I >> 1)] + hoff, (I & 1) * 32 * ROWB);
      } else {
        constexpr int J = I - 4, f = J >> 1, t = J & 1, ks = f >> 2, dt = f & 3;
        const b4 v = ldT(s.wb[dt], (PAR ^ 1) * QSLOT + 4096 * ks + 1024 * t);
        if constexpr (t == 0) s.wlo[PAR ^ 1][f] = v; else s.whi[PAR ^ 1][f] = v;
      }
    }
  };
  auto gap = [&](auto gc) {
    constexpr int G = decltype(gc)::value, ks = G >> 3, dt = (G >> 1) & 3, qb = G & 1, f = ks * 4 + dt;
    mfma32a<ZERO && ks == 0>(acc[dt][qb], __builtin_shufflevector(s.wlo[PAR][f], s.whi[PAR][f], 0, 1, 2, 3, 4, 5, 6, 7), s.hf[PAR][ks * 2 + qb]);
    if constexpr (DMA) {
      if constexpr (G == 0) { asm volatile("s_waitcnt lgkmcnt(0)" ::: "memory"); setm0(sl + 4096); }
      if constexpr (G >= 1 && G <= 4) { dma<(2 * (G - 1) - 4) * 1024>(voff[2 * (G - 1)], gp); dma<(2 * (G - 1) + 1 - 4) * 1024>(voff[2 * (G - 1) + 1], gp); }
    }
    if constexpr (G == 5 && RD) { if constexpr (DMA) asm volatile("s_waitcnt vmcnt(8)" ::: "memory"); else asm volatile("s_waitcnt vmcnt(0)" ::: "memory"); }
    if constexpr (G >= 6 && G <= 12) { rd(IC<3 * (G - 6)>{}); rd(IC<3 * (G - 6) + 1>{}); if constexpr (3 * (G - 6) + 2 < 20) rd(IC<3 * (G - 6) + 2>{}); }
    GAP();
  };
  [&]<int... I>(std::integer_sequence<int, I...>) { (gap(IC<I>{}), ...); }(std::make_integer_sequence<int, 16>{});
}

struct AT {
  f16v S[2][2][2];
  f16v negm[2];
  f16v O[4][2];
  b8 q[2][8];
  b8 P[2][4];
  float m[2], l[2], rs0[2], rs1[2], fct[2], pm0[2], pm1[2];
  unsigned kb_[2], vb_[2];
  unsigned voff[8];
  unsigned m0k, m0v;
  const char *gk, *gv;
  bool pend;
};
template <bool FIRST> DI void qk_mfma(f16v& s, const b8& k, const b8& q, const f16v& negm, bool) {}
template <int MODE> DI void qk(f16v& s, const b8& k, const b8& q, const f16v& negm) {
  if constexpr (MODE == 0) asm volatile("v_mfma_f32_32x32x16_bf16 %0, %1, %2, 0" : "=&v"(s) : "v"(k), "a"(q));
  else if constexpr (MODE == 1) asm volatile("v_mfma_f32_32x32x16_bf16 %0, %1, %2, %3" : "=&v"(s) : "v"(k), "a"(q), "v"(negm));
  else asm volatile("v_mfma_f32_32x32x16_bf16 %0, %1, %2, %0" : "+v"(s) : "v"(k), "a"(q));
}
template <int X, int E> DI void exp1(AT& a, int kb) {}
template <int XP, int KB, int E> DI void sm_exp(AT& a) { constexpr int qb = E >> 4, r = E & 15; a.S[XP][KB][qb][r] = __builtin_amdgcn_exp2f(a.S[XP][KB][qb][r]); }
template <int XP, int KB, int E> DI void sm_add(AT& a) { constexpr int qb = E >> 4, r = E & 15; if constexpr (r & 1) a.rs1[qb] = fadd_s(a.rs1[qb], a.S[XP][KB][qb][r]); else a.rs0[qb] = fadd_s(a.rs0[qb], a.S[XP][KB][qb][r]); }
template <int XP, int C> DI void sm_pack(AT& a) {
  constexpr int kb = C >> 4, qb = (C >> 3) & 1, s = (C >> 2) & 1, p = C & 3;
  u4 w = __builtin_bit_cast(u4, a.P[qb][kb * 2 + s]);
  w[p] = pk(a.S[XP][kb][qb][8 * s + 2 * p], a.S[XP][kb][qb][8 * s + 2 * p + 1]);
  a.P[qb][kb * 2 + s] = __builtin_bit_cast(b8, w);
}
template <int X, bool FIRST, bool DK, bool DV> DI void phaseA(AT& a, int t) {
  constexpr int XP = X ^ 1;
  const unsigned kslot = X * 16384;
  b8 kf[16];
  auto kread = [&](auto ic) { constexpr int I = decltype(ic)::value, ks = I >> 1, kb = I & 1; kf[I] = ldA(a.kb_[ks & 1], kslot + 8192 * kb + 512 * (ks >> 1)); };
  kread(IC<0>{}); kread(IC<1>{}); kread(IC<2>{}); kread(IC<3>{});
  const char* gk = a.gk + (size_t)(t + 1) * 16384; const char* gv = a.gv + (size_t)t * 16384;
  auto gap = [&](auto gc) {
    constexpr int G = decltype(gc)::value, ks = G >> 2, kb = (G >> 1) & 1, qb = G & 1, I = G >> 1;
    qk<(ks == 0) ? (FIRST ? 0 : 1) : 2>(a.S[X][kb][qb], kf[I], a.q[qb][ks], a.negm[qb]);
    if constexpr ((G & 1) == 0 && I + 4 < 16) kread(IC<I + 4>{});
    if constexpr (DK) { if constexpr (G == 0) setm0(a.m0k + (X ^ 1) * 16384); if constexpr ((G & 1) && G < 16) dma<((G >> 1) - 4) * 1024>(a.voff[G >> 1], gk); }
    if constexpr (DV) { if constexpr (G == 16) setm0(a.m0v + X * 16384); if constexpr ((G & 1) && G > 16) dma<(((G - 16) >> 1) - 4) * 1024>(a.voff[(G - 16) >> 1], gv); }
    if constexpr (!FIRST) {
      if constexpr (G < 16) { sm_exp<XP, 1, 2 * G>(a); sm_exp<XP, 1, 2 * G + 1>(a); sm_pack<XP, G>(a); }
      else { sm_pack<XP, G>(a); sm_add<XP, 1, 2 * (G - 16)>(a); sm_add<XP, 1, 2 * (G - 16) + 1>(a); }
    }
    GAP();
  };
  [&]<int... I>(std::integer_sequence<int, I...>) { (gap(IC<I>{}), ...); }(std::make_integer_sequence<int, 32>{});
  if constexpr (!FIRST) {
#pragma unroll
    for (int qb = 0; qb < 2; ++qb) a.l[qb] = fadd_s(a.l[qb], fadd_s(a.rs0[qb], a.rs1[qb]));
  }
}
template <int X, bool NOPV, bool ZERO> DI void phaseB(AT& a) {
  const unsigned vslot = (X ^ 1) * 16384;
  b4 vlo[16], vhi[16];
  auto vread = [&](auto ic) {
    constexpr int I = decltype(ic)::value, k4 = I >> 2, db = I & 3;
    vlo[I] = ldT(a.vb_[0], vslot + 2048 * (2 * k4) + 512 * db);
    vhi[I] = ldT(a.vb_[1], vslot + 2048 * (2 * k4 + 1) + 512 * db);
  };
  if constexpr (!NOPV) { vread(IC<0>{}); vread(IC<1>{}); vread(IC<2>{}); }
  float rmax[2];
  auto gap = [&](auto gc) {
    constexpr int G = decltype(gc)::value, I = G >> 1, k4 = I >> 2, db = I & 3, qb = G & 1;
    if constexpr (!NOPV) {
      mfma32a<ZERO && k4 == 0>(a.O[db][qb], __builtin_shufflevector(vlo[I], vhi[I], 0, 1, 2, 3, 4, 5, 6, 7), a.P[qb][k4]);
      if constexpr ((G & 1) == 0 && I + 3 < 16) vread(IC<I + 3>{});
    }
    if constexpr (G >= 4 && G < 12) {
      constexpr int i = G - 4, q = i >> 2, r = 4 * (i & 3);
      if constexpr ((i & 3) == 0) { a.pm0[q] = max3f(a.S[X][0][q][0], a.S[X][0][q][1], a.S[X][1][q][0]); a.pm1[q] = max3f(a.S[X][0][q][2], a.S[X][0][q][3], a.S[X][1][q][1]);
                                    a.pm0[q] = max3f(a.pm0[q], a.S[X][1][q][2], a.S[X][1][q][3]); }
      else { a.pm0[q] = max3f(a.pm0[q], a.S[X][0][q][r], a.S[X][0][q][r + 1]); a.pm1[q] = max3f(a.pm1[q], a.S[X][0][q][r + 2], a.S[X][0][q][r + 3]);
             a.pm0[q] = max3f(a.pm0[q], a.S[X][1][q][r], a.S[X][1][q][r + 1]); a.pm1[q] = max3f(a.pm1[q], a.S[X][1][q][r + 2], a.S[X][1][q][r + 3]); }
    }
    if constexpr (G == 12 || G == 13) { constexpr int q = G - 12; rmax[q] = swap32_max(max2f(a.pm0[q], a.pm1[q])); }
    if constexpr (G == 14) {
      if constexpr (NOPV) {
#pragma unroll
        for (int q = 0; q < 2; ++q) {
          a.m[q] = rmax[q]; a.l[q] = 0.f; a.fct[q] = 1.f;
#pragma unroll
          for (int kb = 0; kb < 2; ++kb)
#pragma unroll
            for (int e = 0; e < 16; ++e) a.S[X][kb][q][e] = fsub_s(a.S[X][kb][q][e], rmax[q]);
#pragma unroll
          for (int e = 0; e < 16; ++e) a.negm[q][e] = -rmax[q];
        }
      } else {
        const bool grew = (rmax[0] > THR) | (rmax[1] > THR);
        if (__builtin_expect(__any((int)grew), 0)) {
#pragma unroll
          for (int q = 0; q < 2; ++q) {
            const float dl = max2f(rmax[q], 0.f); a.m[q] += dl; a.fct[q] = __builtin_amdgcn_exp2f(-dl);
#pragma unroll
            for (int kb = 0; kb < 2; ++kb)
#pragma unroll
              for (int e = 0; e < 16; ++e) a.S[X][kb][q][e] = fsub_s(a.S[X][kb][q][e], dl);
#pragma unroll
            for (int e = 0; e < 16; ++e) a.negm[q][e] = -a.m[q];
          }
          a.pend = true;
        }
      }
      a.rs0[0] = a.rs0[1] = a.rs1[0] = a.rs1[1] = 0.f;
    }
    if constexpr (G >= 15) {
      constexpr int e0 = 2 * (G - 15);
      if constexpr (e0 < 32) { sm_exp<X, 0, e0>(a); sm_exp<X, 0, e0 + 1>(a); }
      if constexpr (G >= 16) { sm_add<X, 0, e0 - 2>(a); sm_add<X, 0, e0 - 1>(a); }
    }
    GAP();
  };
  [&]<int... I>(std::integer_sequence<int, I...>) { (gap(IC<I>{}), ...); }(std::make_integer_sequence<int, 32>{});
  if constexpr (!NOPV) {
    if (__builtin_expect(a.pend, 0)) {
      asm volatile("s_nop 7\n\ts_nop 7" ::: "memory");
#pragma unroll
      for (int db = 0; db < 4; ++db)
#pragma unroll
        for (int q = 0; q < 2; ++q) {
          asm volatile("" : "+a"(a.O[db][q]));
          f16v o = a.O[db][q];
#pragma unroll
          for (int e = 0; e < 16; ++e) o[e] *= a.fct[q];
          a.O[db][q] = o;
          asm volatile("" : "+a"(a.O[db][q]));
        }
      a.l[0] *= a.fct[0]; a.l[1] *= a.fct[1]; a.pend = false;
      asm volatile("s_nop 1" ::: "memory");
    }
  }
}

__global__ __launch_bounds__(256, 1) void k_att(const u16* __restrict__ x, const u16* __restrict__ mod, const u16* __restrict__ wq, const u16* __restrict__ kc, const u16* __restrict__ vc,
                                                const u16* __restrict__ wo, char* __restrict__ X) {
  extern __shared__ __attribute__((aligned(16))) char lds[];
  const int tid = threadIdx.x, w = __builtin_amdgcn_readfirstlane(tid >> 6), lane = tid & 63;
  const int j = blockIdx.x & 7, c = blockIdx.x >> 3;
  const unsigned ldsb = (unsigned)(size_t)lds;
  const int hh = lane >> 5, r32 = lane & 31;
  f16v Qt[4][2];
  {
    QS s;
    unsigned voffQ[8];
#pragma unroll
    for (int i = 0; i < 8; ++i) voffQ[i] = (4 * i + (lane >> 4)) * 8192 + 16 * ((lane & 15) ^ ((lane >> 4) << 2)) - (i - 4) * 1024 + 4096;
    const char* gq = (const char*)wq + (size_t)(4 * j + w) * 256 - 4096;
    s.ring = __builtin_amdgcn_readfirstlane(ldsb + ABYTES + w * 2 * QSLOT);
    constexpr size_t KS_Q = (size_t)32 * 8192;
    auto issueq = [&](int slot, const char* gp) {
      setm0(s.ring + slot * QSLOT + 4096); asm volatile("s_nop 0");
      dma<-4096>(voffQ[0], gp); dma<-3072>(voffQ[1], gp); dma<-2048>(voffQ[2], gp); dma<-1024>(voffQ[3], gp);
      dma<0>(voffQ[4], gp); dma<1024>(voffQ[5], gp); dma<2048>(voffQ[6], gp); dma<3072>(voffQ[7], gp);
    };
    issueq(0, gq); issueq(1, gq + KS_Q);
    {
      const int rr = lane >> 3, sub = lane & 7;
      u4 xv[2][9], cv[9], sv[9];
#pragma unroll
      for (int ps = 0; ps < 2; ++ps)
#pragma unroll
        for (int i = 0; i < 9; ++i) xv[ps][i] = *(const u4*)(x + (size_t)(c * 64 + 16 * w + 8 * ps + rr) * D + (sub + 8 * i) * 8);
#pragma unroll
      for (int i = 0; i < 9; ++i) { cv[i] = *(const u4*)(mod + D + (sub + 8 * i) * 8); sv[i] = *(const u4*)(mod + (sub + 8 * i) * 8); }
#pragma unroll
      for (int ps = 0; ps < 2; ++ps) {
        f2 sum = {0.f, 0.f};
#pragma unroll
        for (int i = 0; i < 9; ++i)
#pragma unroll
          for (int e = 0; e < 4; ++e) sum += (f2){bflo(xv[ps][i][e]), bfhi(xv[ps][i][e])};
        float sm = sum[0] + sum[1];
        sm += __shfl_xor(sm, 1); sm += __shfl_xor(sm, 2); sm += __shfl_xor(sm, 4);
        const float mean = sm * (1.f / D);
        f2 sq = {0.f, 0.f};
#pragma unroll
        for (int i = 0; i < 9; ++i)
#pragma unroll
          for (int e = 0; e < 4; ++e) { const f2 dlt = (f2){bflo(xv[ps][i][e]), bfhi(xv[ps][i][e])} - mean; sq += dlt * dlt; }
        float q = sq[0] + sq[1];
        q += __shfl_xor(q, 1); q += __shfl_xor(q, 2); q += __shfl_xor(q, 4);
        const float mr = rbf(mean), rs = 1.f / rbf(sqrtf(rbf(rbf(q * (1.f / D)) + 1e-5f)));
        const int row = 16 * w + 8 * ps + rr;
        const unsigned ad = ldsb + row * ROWB + ((sub ^ ((row >> 1) & 7)) * 16);
#pragma unroll
        for (int i = 0; i < 9; ++i) {
          u4 o;
#pragma unroll
          for (int e = 0; e < 4; ++e) {
            f2 v = (f2){bflo(xv[ps][i][e]), bfhi(xv[ps][i][e])} - mr;
            unsigned t = pk(v[0], v[1]);
            v = (f2){bflo(t), bfhi(t)} * rs; t = pk(v[0], v[1]);
            f2 c1 = (f2){bflo(cv[i][e]), bfhi(cv[i][e])} + 1.f; const unsigned c1p = pk(c1[0], c1[1]);
            v = (f2){bflo(t), bfhi(t)} * (f2){bflo(c1p), bfhi(c1p)}; t = pk(v[0], v[1]);
            v = (f2){bflo(t), bfhi(t)} + (f2){bflo(sv[i][e]), bfhi(sv[i][e])};
            o[e] = pk(v[0], v[1]);
          }
          *(LAS u4*)(size_t)(ad + i * 128) = o;
        }
      }
    }
    {
      const int blk = (lane >> 4) & 1, q = (lane & 15) >> 2, p = lane & 3;
#pragma unroll
      for (int dt = 0; dt < 4; ++dt) s.wb[dt] = s.ring + (8 * hh + q) * 256 + 64 * (dt ^ q) + 32 * blk + 16 * (p & 1) + 8 * (p >> 1);
#pragma unroll
      for (int i = 0; i < 4; ++i) s.hb[i] = ldsb + r32 * ROWB + (((2 * i + hh) ^ ((r32 >> 1) & 7)) * 16);
    }
    asm volatile("s_waitcnt vmcnt(8) lgkmcnt(0)" ::: "memory");
    __builtin_amdgcn_s_barrier();
#pragma unroll
    for (int f = 0; f < 8; ++f) { s.wlo[0][f] = ldT(s.wb[f & 3], 4096 * (f >> 2)); s.whi[0][f] = ldT(s.wb[f & 3], 4096 * (f >> 2) + 1024); }
#pragma unroll
    for (int i = 0; i < 4; ++i) s.hf[0][i] = ldA(s.hb[i >> 1], (i & 1) * 32 * ROWB);
    qstep<0, true, true, true>(s, Qt, voffQ, gq + 2 * KS_Q, 0);
    qstep<1, false, true, true>(s, Qt, voffQ, gq + 3 * KS_Q, 128);
    for (int n = 2; n < 16; n += 2) {
      qstep<0, false, true, true>(s, Qt, voffQ, gq + (size_t)(n + 2) * KS_Q, 128 * ((n + 1) >> 1));
      qstep<1, false, true, true>(s, Qt, voffQ, gq + (size_t)(n + 3) * KS_Q, 128 * ((n + 2) >> 1));
    }
    qstep<0, false, false, true>(s, Qt, voffQ, gq, 128 * 8);
    qstep<1, false, false, false>(s, Qt, voffQ, gq, 0);
  }
  AT a;
  asm volatile("s_nop 7\n\ts_nop 7" ::: "memory");
#pragma unroll
  for (int dt = 0; dt < 4; ++dt)
#pragma unroll
    for (int qb = 0; qb < 2; ++qb) {
      asm volatile("" : "+a"(Qt[dt][qb]));
#pragma unroll
      for (int s2 = 0; s2 < 2; ++s2) {
        u4 w4;
#pragma unroll
        for (int e = 0; e < 4; ++e) w4[e] = pk(Qt[dt][qb][8 * s2 + 2 * e] * QSCALE, Qt[dt][qb][8 * s2 + 2 * e + 1] * QSCALE);
        a.q[qb][2 * dt + s2] = __builtin_bit_cast(b8, w4);
        asm volatile("" : "+a"(a.q[qb][2 * dt + s2]));
      }
    }
  {
    const int kvl = w >> 1, half = w & 1;
    const unsigned kr = ldsb + kvl * 65536, vr = kr + 32768;
#pragma unroll
    for (int i = 0; i < 2; ++i) a.kb_[i] = kr + 2048 * (r32 >> 3) + 64 * (r32 & 7) + 16 * ((2 * i + hh) ^ ((r32 >> 2) & 3));
    const int blk = (lane >> 4) & 1, q = (lane & 15) >> 2, p = lane & 3;
#pragma unroll
    for (int t = 0; t < 2; ++t) a.vb_[t] = vr + 64 * (4 * hh + q) + 16 * ((2 * blk + (p >> 1)) ^ ((2 * t + hh) & 3)) + 8 * (p & 1);
    const int st = lane >> 5, r7 = (lane >> 2) & 7, pos = lane & 3;
#pragma unroll
    for (int i = 0; i < 8; ++i) {
      const int rg = 4 * half + (i >> 1);
      a.voff[i] = rg * 2048 + r7 * 256 + (i & 1) * 128 + st * 64 + 16 * (pos ^ ((2 * (rg & 1) + (r7 >> 2)) & 3)) - (i - 4) * 1024 + 4096;
    }
    a.m0k = __builtin_amdgcn_readfirstlane(kr + 8192 * half + 4096); a.m0v = __builtin_amdgcn_readfirstlane(vr + 8192 * half + 4096);
    const size_t kvoff = ((size_t)(2 * j + kvl) * SK + (SK - WIN)) * DH * 2;
    a.gk = (const char*)kc + kvoff - 4096; a.gv = (const char*)vc + kvoff - 4096;
    a.pend = false;
  }
  asm volatile("s_waitcnt lgkmcnt(0)" ::: "memory");
  __builtin_amdgcn_s_barrier();
  setm0(a.m0k); asm volatile("s_nop 0");
  dma<-4096>(a.voff[0], a.gk); dma<-3072>(a.voff[1], a.gk); dma<-2048>(a.voff[2], a.gk); dma<-1024>(a.voff[3], a.gk);
  dma<0>(a.voff[4], a.gk); dma<1024>(a.voff[5], a.gk); dma<2048>(a.voff[6], a.gk); dma<3072>(a.voff[7], a.gk);
  auto tile_bar = [&]() { asm volatile("s_waitcnt vmcnt(0) lgkmcnt(0)" ::: "memory"); __builtin_amdgcn_s_barrier(); };
  tile_bar(); phaseA<0, true, true, true>(a, 0); asm volatile("s_nop 7\n\ts_nop 3" ::: "memory"); GAP(); phaseB<0, true, false>(a);
  tile_bar(); phaseA<1, false, true, true>(a, 1); phaseB<1, false, true>(a);
  for (int t = 2; t < 6; t += 2) {
    tile_bar(); phaseA<0, false, true, true>(a, t); phaseB<0, false, false>(a);
    tile_bar(); phaseA<1, false, true, true>(a, t + 1); phaseB<1, false, false>(a);
  }
  tile_bar(); phaseA<0, false, true, true>(a, 6); phaseB<0, false, false>(a);
  tile_bar(); phaseA<1, false, false, true>(a, 7); phaseB<1, false, false>(a);
  {
    [&]<int... I>(std::integer_sequence<int, I...>) { (sm_exp<1, 1, I>(a), ...); }(std::make_integer_sequence<int, 32>{});
    [&]<int... I>(std::integer_sequence<int, I...>) { (sm_pack<1, I>(a), ...); }(std::make_integer_sequence<int, 32>{});
    [&]<int... I>(std::integer_sequence<int, I...>) { (sm_add<1, 1, I>(a), ...); }(std::make_integer_sequence<int, 32>{});
#pragma unroll
    for (int qb = 0; qb < 2; ++qb) a.l[qb] = fadd_s(a.l[qb], fadd_s(a.rs0[qb], a.rs1[qb]));
    tile_bar();
    const unsigned vslot = 16384;
    b4 vlo[16], vhi[16];
#pragma unroll
    for (int I = 0; I < 16; ++I) { vlo[I] = ldT(a.vb_[0], vslot + 2048 * (2 * (I >> 2)) + 512 * (I & 3)); vhi[I] = ldT(a.vb_[1], vslot + 2048 * (2 * (I >> 2) + 1) + 512 * (I & 3)); }
    asm volatile("s_nop 1" ::: "memory"); GAP();
#pragma unroll
    for (int G = 0; G < 32; ++G) { const int I = G >> 1; mfma32a<false>(a.O[I & 3][G & 1], __builtin_shufflevector(vlo[I], vhi[I], 0, 1, 2, 3, 4, 5, 6, 7), a.P[G & 1][I >> 2]); }
  }
  GS s;
  unsigned voffB[9];
#pragma unroll
  for (int i = 0; i < 9; ++i) { const int idx = i * 64 + lane, imm = i < 8 ? (i - 4) * 1024 : 0; voffB[i] = (idx / 18) * ROWB + (idx % 18) * 16 - imm + 4096; }
  const char* gO = (const char*)wo + (size_t)(512 * j) * ROWB + 288 * w - 4096;
  constexpr size_t KS_D = (size_t)32 * ROWB;
  s.ring = __builtin_amdgcn_readfirstlane(ldsb + ABYTES + w * 2 * SLOT);
  const int g = lane >> 4, q = (lane & 15) >> 2, p = lane & 3, cl = lane & 15;
  s.wbase = s.ring + (4 * g + q) * 288 + 8 * p;
  const unsigned sw = (cl >> 1) & 7;
  const unsigned ab_e = ldsb + cl * ROWB + ((g ^ sw) * 16), ab_o = ldsb + cl * ROWB + (((4 + g) ^ sw) * 16);
  asm volatile("s_waitcnt lgkmcnt(0)" ::: "memory");
  __builtin_amdgcn_s_barrier();
  issue_all(s, 0, voffB, gO); issue_all(s, 1, voffB, gO + KS_D);
  asm volatile("s_nop 7\n\ts_nop 7" ::: "memory");
#pragma unroll
  for (int qb = 0; qb < 2; ++qb) {
    const float il = __builtin_amdgcn_rcpf(swap32_add(a.l[qb]));
    const int row = 32 * qb + r32; const unsigned swr = (r32 >> 1) & 7;
#pragma unroll
    for (int db = 0; db < 4; ++db) {
      asm volatile("" : "+a"(a.O[db][qb]));
      const f16v o = a.O[db][qb];
#pragma unroll
      for (int u = 0; u < 2; ++u) {
        const u4 wv = {pk(o[4 * u] * il, o[4 * u + 1] * il), pk(o[4 * u + 2] * il, o[4 * u + 3] * il), pk(o[4 * u + 8] * il, o[4 * u + 9] * il), pk(o[4 * u + 10] * il, o[4 * u + 11] * il)};
        *(LAS u4*)(size_t)(ldsb + row * ROWB + (((4 * (4 * w + db) + 2 * u + hh) ^ swr) * 16)) = wv;
      }
    }
  }
  asm volatile("s_waitcnt vmcnt(9) lgkmcnt(0)" ::: "memory");
  __builtin_amdgcn_s_barrier();
  read_w(s, 0); read_a(s, 0, ab_e);
  f4 acc[9][4];
  step<0, 0, true, true, true, true, 0>(s, acc, voffB, gO + 2 * KS_D, ab_o);
  step<1, 1, false, true, true, true, 0>(s, acc, voffB, gO + 3 * KS_D, ab_e + 128);
  for (int k = 2; k < 14; k += 2) {
    step<0, 0, false, true, true, true, 0>(s, acc, voffB, gO + (size_t)(k + 2) * KS_D, ab_o + 128 * (k >> 1));
    step<1, 1, false, true, true, true, 0>(s, acc, voffB, gO + (size_t)(k + 3) * KS_D, ab_e + 128 * ((k + 2) >> 1));
  }
  step<0, 0, false, false, true, true, 0>(s, acc, voffB, gO, ab_o + 128 * 7);
  step<1, 1, false, false, false, false, 0>(s, acc, voffB, gO, 0);
  settle<0>(acc);
  store_partials(acc, X + (size_t)c * 64 * SLICE + (size_t)j * SLICE, (size_t)8 * SLICE, w, lane);
}

extern "C" void kernel_launch(void* const* d_in, const int* in_sizes, int n_in, void* d_out, int out_size, void* d_ws, size_t ws_size, hipStream_t stream) {
  const u16* x = (const u16*)d_in[0]; const u16* kc = (const u16*)d_in[1]; const u16* vc = (const u16*)d_in[2];
  const u16* wq = (const u16*)d_in[3]; const u16* wo = (const u16*)d_in[4];
  const u16* wg = (const u16*)d_in[6]; const u16* wu = (const u16*)d_in[7]; const u16* wd = (const u16*)d_in[8];
  const u16* cc = (const u16*)d_in[9]; const u16* wmod = (const u16*)d_in[10];
  char* ws = (char*)d_ws;
  u16* mod = (u16*)(ws + WS_MOD);
  constexpr int LDS_FFN = ABYTES + 8 * SLOT;
  (void)hipFuncSetAttribute((const void*)k_ffn, hipFuncAttributeMaxDynamicSharedMemorySize, LDS_FFN);
  (void)hipFuncSetAttribute((const void*)k_att, hipFuncAttributeMaxDynamicSharedMemorySize, LDS_FFN);
  k_mod<<<108, 256, 0, stream>>>(cc, wmod, mod);
  k_att<<<256, 256, LDS_FFN, stream>>>(x, mod, wq, kc, vc, wo, ws + WS_X);
  k_red1<<<256, 256, 0, stream>>>(ws + WS_X, x, mod, ws + WS_X1, ws + WS_H2);
  k_ffn<<<256, 256, LDS_FFN, stream>>>(ws + WS_H2, wg, wu, wd, ws + WS_X);
  k_red2<<<256, 192, 0, stream>>>(ws + WS_X, ws + WS_X1, mod, (u16*)d_out);
}
```

```cpp
#include <hip/hip_runtime.h>
#include <stdint.h>
#include <utility>
#include <type_traits>

typedef unsigned short u16;
typedef unsigned long long u64;
typedef __bf16 b2 __attribute__((ext_vector_type(2)));
typedef __bf16 b4 __attribute__((ext_vector_type(4)));
typedef __bf16 b8 __attribute__((ext_vector_type(8)));
typedef float f2 __attribute__((ext_vector_type(2)));
typedef float f4 __attribute__((ext_vector_type(4)));
typedef float f16v __attribute__((ext_vector_type(16)));
typedef unsigned u2 __attribute__((ext_vector_type(2)));
typedef unsigned u4 __attribute__((ext_vector_type(4)));
typedef __attribute__((address_space(1))) unsigned gu32;
#define LAS __attribute__((address_space(3)))
#define DI __device__ __forceinline__
#define GAP() __builtin_amdgcn_sched_barrier(0)
#define BAR() do { asm volatile("" ::: "memory"); __builtin_amdgcn_s_barrier(); asm volatile("" ::: "memory"); } while (0)
template <int V> using IC = std::integral_constant<int, V>;
#define AUX_X 16
#define AUX_H 18
#define PFD_K 1
#define UNROLL(N, F) [&]<int... I_>(std::integer_sequence<int, I_...>) { (F(IC<I_>{}), ...); }(std::make_integer_sequence<int, N>{})

constexpr int S = 2048, D = 576, DH = 128, SK = 2048, WIN = 512, DF = 4608;
constexpr int ROWB = D * 2;
constexpr int ABYTES = 64 * ROWB;
constexpr int SLICE = 8 * ROWB;
constexpr int SLOT = 9216;
constexpr int QSLOT = 8192;
constexpr int LDS_DUMMY = ABYTES + 8 * SLOT;
constexpr int LDS_X1 = LDS_DUMMY + 1024;
constexpr int LDS_BYTES = LDS_X1 + SLICE;
constexpr size_t WS_MOD = 0;
constexpr size_t WS_FLAG = 8192;
constexpr int FLAG_STRIDE = 16;
constexpr size_t WS_SINK = 8192 + 65536;
constexpr size_t WS_X = 131072;
constexpr size_t WS_H2 = WS_X + (size_t)32 * 64 * SLICE;
constexpr size_t WS_DBG = 200u << 20;
#define STAMP(i)

DI float bf2f(u16 v) { return __uint_as_float(((unsigned)v) << 16); }
DI float bflo(unsigned w) { return __uint_as_float(w << 16); }
DI float bfhi(unsigned w) { return __uint_as_float(w & 0xffff0000u); }
DI f2 bf2(unsigned w) { return (f2){bflo(w), bfhi(w)}; }
DI unsigned pk(float lo, float hi) { f2 v = {lo, hi}; b2 b = __builtin_convertvector(v, b2); return __builtin_bit_cast(unsigned, b); }
DI unsigned pk(f2 v) { b2 b = __builtin_convertvector(v, b2); return __builtin_bit_cast(unsigned, b); }
DI float rbf(float f) { return bflo(pk(f, f)); }
DI u16 f2bf(float f) { return (u16)(pk(f, f) & 0xffffu); }
DI float fmaxx(float a, float b) { return __builtin_elementwise_maximum(a, b); }
DI float fmax3(float a, float b, float c) { return __builtin_elementwise_maximum(__builtin_elementwise_maximum(a, b), c); }

__global__ __launch_bounds__(256) void k_pre(const u16* __restrict__ c, const u16* __restrict__ wmod, u16* __restrict__ mod, unsigned* __restrict__ flags) {
  __shared__ float red[4][32];
  const int tid = threadIdx.x, lane = tid & 63, w = tid >> 6;
  for (int i = blockIdx.x * 256 + tid; i < 1024; i += 108 * 256) __hip_atomic_store((gu32*)(flags + i * FLAG_STRIDE), 0u, __ATOMIC_RELAXED, __HIP_MEMORY_SCOPE_AGENT);
  {
    const int n0 = blockIdx.x * 32 + (tid & 3) * 8, r0 = tid >> 2;
    u4 v[9]; float s[9];
#pragma unroll
    for (int i = 0; i < 9; ++i) v[i] = *(const u4*)(wmod + (size_t)(r0 + 64 * i) * (6 * D) + n0);
#pragma unroll
    for (int i = 0; i < 9; ++i) { float x = bf2f(c[r0 + 64 * i]); s[i] = rbf(x / (1.f + __expf(-x))); }
    float acc[8] = {};
#pragma unroll
    for (int i = 0; i < 9; ++i)
#pragma unroll
      for (int e = 0; e < 4; ++e) { acc[2 * e] += s[i] * bflo(v[i][e]); acc[2 * e + 1] += s[i] * bfhi(v[i][e]); }
#pragma unroll
    for (int e = 0; e < 8; ++e) {
      float a = acc[e];
      for (int o = 4; o < 64; o <<= 1) a += __shfl_xor(a, o);
      if (lane < 4) red[w][lane * 8 + e] = a;
    }
    __syncthreads();
    if (tid < 32) mod[blockIdx.x * 32 + tid] = f2bf(red[0][tid] + red[1][tid] + red[2][tid] + red[3][tid]);
  }
}

DI void zdecode(int z, int& lr, int& cc) {
  const int w = z / 144, y = z - w * 144;
  if (y < 128) { const int tp = y >> 5; lr = (y >> 2) & 7; cc = 18 * w + 4 * tp + (y & 3); }
  else { const int y2 = y - 128; lr = y2 >> 1; cc = 18 * w + 16 + (y2 & 1); }
}

template <int IMM> DI void dma(unsigned voff, const char* g) { asm volatile("global_load_lds_dwordx4 %0, %1 offset:%2" :: "v"(voff), "s"(g), "i"(IMM) : "memory"); }
DI void setm0(unsigned a) { asm volatile("s_mov_b32 m0, %0" :: "s"(a) : "memory"); }
DI void touch(const char* p, unsigned voff, unsigned ldsdummy) {
  asm volatile("s_mov_b32 m0, %0\n\ts_nop 0\n\tglobal_load_lds_dword %1, %2" :: "s"(ldsdummy), "v"(voff), "s"(p) : "memory");
}
DI b4 ldT(unsigned base, int off) { return __builtin_amdgcn_ds_read_tr16_b64_v4bf16((LAS b4*)(size_t)(base + off)); }
DI b8 ldA(unsigned base, int off) { return *(LAS b8*)(size_t)(base + off); }
DI b8 cat(const b4& lo, const b4& hi) { return __builtin_shufflevector(lo, hi, 0, 1, 2, 3, 4, 5, 6, 7); }
template <bool AG, bool ZERO> DI void mfma16(f4& c, const b8& a, const b8& b) {
  if constexpr (AG) {
    if constexpr (ZERO) asm volatile("v_mfma_f32_16x16x32_bf16 %0, %1, %2, 0" : "=a"(c) : "v"(a), "v"(b));
    else asm volatile("v_mfma_f32_16x16x32_bf16 %0, %1, %2, %0" : "+a"(c) : "v"(a), "v"(b));
  } else {
    if constexpr (ZERO) asm volatile("v_mfma_f32_16x16x32_bf16 %0, %1, %2, 0" : "=&v"(c) : "v"(a), "v"(b));
    else asm volatile("v_mfma_f32_16x16x32_bf16 %0, %1, %2, %0" : "+v"(c) : "v"(a), "v"(b));
  }
}
template <bool AG, bool ZERO> DI void mfma32(f16v& c, const b8& a, const b8& b) {
  if constexpr (AG) {
    if constexpr (ZERO) asm volatile("v_mfma_f32_32x32x16_bf16 %0, %1, %2, 0" : "=a"(c) : "v"(a), "v"(b));
    else asm volatile("v_mfma_f32_32x32x16_bf16 %0, %1, %2, %0" : "+a"(c) : "v"(a), "v"(b));
  } else {
    if constexpr (ZERO) asm volatile("v_mfma_f32_32x32x16_bf16 %0, %1, %2, 0" : "=&v"(c) : "v"(a), "v"(b));
    else asm volatile("v_mfma_f32_32x32x16_bf16 %0, %1, %2, %0" : "+v"(c) : "v"(a), "v"(b));
  }
}
template <int NVG> DI void settle(f4 (&acc)[9][4]) {
  asm volatile("s_nop 7\n\ts_nop 7" ::: "memory");
#pragma unroll
  for (int t = 0; t < 9; ++t)
#pragma unroll
    for (int m = 0; m < 4; ++m) { if (t * 4 + m < 36 - NVG) asm volatile("" : "+a"(acc[t][m])); else asm volatile("" : "+v"(acc[t][m])); }
}

struct GS {
  b4 wlo[2][9], whi[2][9];
  b8 af[2][4];
  unsigned wbase, ring;
};
DI void issue_all(const GS& s, int slot, const unsigned (&voff)[9], const char* gp) {
  const unsigned sl = s.ring + slot * SLOT;
  setm0(sl + 4096); asm volatile("s_nop 0");
  dma<-4096>(voff[0], gp); dma<-3072>(voff[1], gp); dma<-2048>(voff[2], gp); dma<-1024>(voff[3], gp);
  dma<0>(voff[4], gp); dma<1024>(voff[5], gp); dma<2048>(voff[6], gp); dma<3072>(voff[7], gp);
  setm0(sl + 8192); asm volatile("s_nop 0"); dma<0>(voff[8], gp);
}
template <int PAR, int AP, bool ZERO, bool RDW, bool RDA, bool DMA, int VM, int NVG, typename PF>
DI void step(GS& s, f4 (&acc)[9][4], const unsigned (&voff)[9], const char* gp, unsigned ab, PF&& pf) {
  const unsigned sl = s.ring + (PAR ^ 1) * SLOT;
  const unsigned wb = s.wbase + (PAR ^ 1) * SLOT;
  auto mm = [&](auto ic) {
    constexpr int I = decltype(ic)::value, t = I >> 2, m = I & 3;
    mfma16<(I < 36 - NVG), ZERO>(acc[t][m], cat(s.wlo[PAR][t], s.whi[PAR][t]), s.af[AP][m]);
  };
  auto rd = [&](auto ic) {
    constexpr int I = decltype(ic)::value;
    if constexpr (I < 4) { if constexpr (RDA) s.af[AP ^ 1][I] = ldA(ab, I * 16 * ROWB); }
    else if constexpr (RDW) {
      if constexpr (((I - 4) & 1) == 0) s.wlo[PAR ^ 1][(I - 4) >> 1] = ldT(wb, 32 * ((I - 4) >> 1));
      else s.whi[PAR ^ 1][(I - 4) >> 1] = ldT(wb, 32 * ((I - 4) >> 1) + 4608);
    }
  };
  auto gap = [&](auto gc) {
    constexpr int G = decltype(gc)::value;
    mm(IC<2 * G>{});
    if constexpr (G == 0 && RDW) asm volatile("s_waitcnt vmcnt(%0)" :: "i"(VM) : "memory");
    if constexpr (G <= 10) rd(IC<2 * G>{});
    if constexpr (DMA) {
      if constexpr (G == 12) { asm volatile("s_waitcnt lgkmcnt(0)" ::: "memory"); setm0(sl + 4096); }
      if constexpr (G >= 13 && G <= 16) dma<(2 * (G - 13) - 4) * 1024>(voff[2 * (G - 13)], gp);
      if constexpr (G == 17) dma<0>(voff[8], gp);
    }
    GAP();
    mm(IC<2 * G + 1>{});
    if constexpr (G <= 10) rd(IC<2 * G + 1>{});
    if constexpr (DMA) {
      if constexpr (G >= 13 && G <= 16) dma<(2 * (G - 13) + 1 - 4) * 1024>(voff[2 * (G - 13) + 1], gp);
      if constexpr (G == 16) setm0(sl + 8192);
    }
    if constexpr (G == 17) pf();
    GAP();
  };
  UNROLL(18, gap);
}
DI void read_w(GS& s, int par) {
#pragma unroll
  for (int t = 0; t < 9; ++t) { s.wlo[par][t] = ldT(s.wbase + par * SLOT, 32 * t); s.whi[par][t] = ldT(s.wbase + par * SLOT, 32 * t + 4608); }
}
DI void read_a(GS& s, int set, unsigned ab) {
#pragma unroll
  for (int m = 0; m < 4; ++m) s.af[set][m] = ldA(ab, m * 16 * ROWB);
}
template <int NS, int NEXT> DI void gemm_rows(GS& s, f4 (&acc)[9][4], const unsigned (&voff)[9], const char* gW, unsigned ab_e, unsigned ab_o,
                                              const unsigned (&nvoff)[9], const char* nx0, const char* nx1, const char* nx2) {
  constexpr size_t KS = (size_t)32 * ROWB;
  auto no = [&]() {};
  step<0, 0, true, true, true, true, 9, 0>(s, acc, voff, gW + 3 * KS, ab_o, no);
  step<1, 1, false, true, true, true, 9, 0>(s, acc, voff, gW + 4 * KS, ab_e + 128, no);
  for (int k = 2; k < NS - 4; k += 2) {
    step<0, 0, false, true, true, true, 9, 0>(s, acc, voff, gW + (size_t)(k + 3) * KS, ab_o + 128 * (k >> 1), no);
    step<1, 1, false, true, true, true, 9, 0>(s, acc, voff, gW + (size_t)(k + 4) * KS, ab_e + 128 * ((k + 2) >> 1), no);
  }
  step<0, 0, false, true, true, true, 9, 0>(s, acc, voff, gW + (size_t)(NS - 1) * KS, ab_o + 128 * ((NS - 4) >> 1), no);
  step<1, 1, false, true, true, NEXT != 0, 9, 0>(s, acc, nvoff, nx0, ab_e + 128 * ((NS - 2) >> 1), no);
  step<0, 0, false, true, true, NEXT != 0, NEXT != 0 ? 9 : 0, 0>(s, acc, nvoff, nx1, ab_o + 128 * ((NS - 2) >> 1), no);
  step<1, 1, false, NEXT == 1, false, NEXT == 1, 9, 0>(s, acc, nvoff, nx2, 0, no);
}
DI void store_partials(const f4 (&acc)[9][4], __amdgpu_buffer_rsrc_t rs, unsigned base, unsigned dstride, int w, int lane) {
  const int G = lane >> 4, c = lane & 15, lr = c & 7, dh = c >> 3;
#pragma unroll
  for (int m = 0; m < 4; ++m) {
    const unsigned dst = base + (2 * m + dh) * dstride + w * 2304 + lr * 64 + (2 * (G & 1) + (G >> 1)) * 16;
#pragma unroll
    for (int tp = 0; tp < 4; ++tp) {
      const f4 a = acc[2 * tp][m], b = acc[2 * tp + 1][m];
      auto r0 = __builtin_amdgcn_permlane16_swap(pk(a[0], a[1]), pk(b[0], b[1]), false, false);
      auto r1 = __builtin_amdgcn_permlane16_swap(pk(a[2], a[3]), pk(b[2], b[3]), false, false);
      __builtin_amdgcn_raw_buffer_store_b128((u4){r0[0], r1[0], r0[1], r1[1]}, rs, dst + tp * 512, 0, 16);
    }
  }
#pragma unroll
  for (int mp = 0; mp < 2; ++mp) {
    const f4 a = acc[8][2 * mp], b = acc[8][2 * mp + 1];
    auto r0 = __builtin_amdgcn_permlane16_swap(pk(a[0], a[1]), pk(b[0], b[1]), false, false);
    auto r1 = __builtin_amdgcn_permlane16_swap(pk(a[2], a[3]), pk(b[2], b[3]), false, false);
    const unsigned dst = base + (2 * (2 * mp + (G & 1)) + dh) * dstride + w * 2304 + 2048 + lr * 32 + (G >> 1) * 16;
    __builtin_amdgcn_raw_buffer_store_b128((u4){r0[0], r1[0], r0[1], r1[1]}, rs, dst, 0, 16);
  }
}

DI void publish(unsigned* flag) {
  asm volatile("s_waitcnt vmcnt(0)" ::: "memory");
  BAR();
  if (threadIdx.x == 0) __hip_atomic_store((gu32*)flag, 1u, __ATOMIC_RELAXED, __HIP_MEMORY_SCOPE_AGENT);
}
DI void await8(unsigned* flags8, unsigned* tmo, int w, int lane) {
  if (w == 0) {
    for (unsigned spins = 0;; ++spins) {
      const unsigned v = __hip_atomic_load((gu32*)(flags8 + (lane & 7) * FLAG_STRIDE), __ATOMIC_RELAXED, __HIP_MEMORY_SCOPE_AGENT);
      if (__all(v != 0u)) break;
      if (spins > (1u << 17)) { if (lane == 0) __hip_atomic_store((gu32*)tmo, 1u, __ATOMIC_RELAXED, __HIP_MEMORY_SCOPE_AGENT); break; }
      __builtin_amdgcn_s_sleep(1);
    }
  }
  BAR();
}
DI u4 gated_residual(const u4 (&v)[8], const u4& res, const u4& g) {
  u4 o;
#pragma unroll
  for (int e = 0; e < 4; ++e) {
    f2 s = bf2(v[0][e]);
#pragma unroll
    for (int j = 1; j < 8; ++j) s += bf2(v[j][e]);
    const unsigned t = pk(bf2(pk(s)) * bf2(g[e]));
    o[e] = pk(bf2(res[e]) + bf2(t));
  }
  return o;
}

constexpr float QSCALE = 0.08838834764831845f * 1.4426950408889634f;
constexpr float THR = 40.0f;
struct QS {
  b4 wlo[2][8], whi[2][8];
  b8 hf[2][4];
  unsigned wb[4];
  unsigned hb[4];
  unsigned ring;
};
template <int PAR, bool ZERO, bool RD, bool DMA, int VM>
DI void qstep(QS& s, f16v (&acc)[4][2], const unsigned (&voff)[8], const char* gp, unsigned hoff) {
  const unsigned sl = s.ring + (PAR ^ 1) * QSLOT;
  auto rd = [&](auto ic) {
    constexpr int I = decltype(ic)::value;
    if constexpr (RD) {
      if constexpr (I < 4) {
        s.hf[PAR ^ 1][I] = ldA(s.hb[2 * (PAR ^ 1) + (I >> 1)] + hoff, (I & 1) * 32 * ROWB);
      } else {
        constexpr int J = I - 4, f = J >> 1, t = J & 1, ks = f >> 2, dt = f & 3;
        const b4 v = ldT(s.wb[dt], (PAR ^ 1) * QSLOT + 4096 * ks + 1024 * t);
        if constexpr (t == 0) s.wlo[PAR ^ 1][f] = v; else s.whi[PAR ^ 1][f] = v;
      }
    }
  };
  auto gap = [&](auto gc) {
    constexpr int G = decltype(gc)::value, ks = G >> 3, dt = (G >> 1) & 3, qb = G & 1, f = ks * 4 + dt;
    mfma32<false, ZERO && ks == 0>(acc[dt][qb], cat(s.wlo[PAR][f], s.whi[PAR][f]), s.hf[PAR][ks * 2 + qb]);
    if constexpr (G == 0 && RD) asm volatile("s_waitcnt vmcnt(%0)" :: "i"(VM) : "memory");
    if constexpr (G <= 6) { rd(IC<3 * G>{}); rd(IC<3 * G + 1>{}); if constexpr (3 * G + 2 < 20) rd(IC<3 * G + 2>{}); }
    if constexpr (DMA) {
      if constexpr (G == 9) { asm volatile("s_waitcnt lgkmcnt(0)" ::: "memory"); setm0(sl + 4096); }
      if constexpr (G >= 10 && G <= 13) { dma<(2 * (G - 10) - 4) * 1024>(voff[2 * (G - 10)], gp); dma<(2 * (G - 10) + 1 - 4) * 1024>(voff[2 * (G - 10) + 1], gp); }
    }
    GAP();
  };
  UNROLL(16, gap);
}

struct AT {
  f16v S[2][2][2];
  f16v negm[2];
  f16v O[4][2];
  b8 q[2][8];
  b8 P[2][4];
  float m[2], l[2], rs0[2], rs1[2], fct[2], pm0[2], pm1[2];
  float one;
  unsigned kb_[2], vb_[2];
  unsigned voff[8];
  unsigned m0k, m0v;
  const char *gk, *gv;
  bool pend;
};
template <int MODE> DI void qk(f16v& s, const b8& k, const b8& q, const f16v& negm) {
  if constexpr (MODE == 0) asm volatile("v_mfma_f32_32x32x16_bf16 %0, %1, %2, 0" : "=&v"(s) : "v"(k), "a"(q));
  else if constexpr (MODE == 1) asm volatile("v_mfma_f32_32x32x16_bf16 %0, %1, %2, %3" : "=&v"(s) : "v"(k), "a"(q), "v"(negm));
  else asm volatile("v_mfma_f32_32x32x16_bf16 %0, %1, %2, %0" : "+v"(s) : "v"(k), "a"(q));
}
template <int XP, int KB, int E> DI void sm_exp(AT& a) { constexpr int qb = E >> 4, r = E & 15; a.S[XP][KB][qb][r] = __builtin_amdgcn_exp2f(a.S[XP][KB][qb][r]); }
template <int XP, int KB, int E> DI void sm_add(AT& a) {
  constexpr int qb = E >> 4, r = E & 15;
  if constexpr (r & 1) a.rs1[qb] = __builtin_fmaf(a.S[XP][KB][qb][r], a.one, a.rs1[qb]); else a.rs0[qb] += a.S[XP][KB][qb][r];
}
template <int XP, int C> DI void sm_pack(AT& a) {
  constexpr int kb = C >> 4, qb = (C >> 3) & 1, s = (C >> 2) & 1, p = C & 3;
  u4 w = __builtin_bit_cast(u4, a.P[qb][kb * 2 + s]);
  w[p] = pk(a.S[XP][kb][qb][8 * s + 2 * p], a.S[XP][kb][qb][8 * s + 2 * p + 1]);
  a.P[qb][kb * 2 + s] = __builtin_bit_cast(b8, w);
  if constexpr (p == 3) asm volatile("" : "+v"(a.P[qb][kb * 2 + s]));
}
template <int X, bool FIRST, bool DK, bool DV, typename PF> DI void phaseA(AT& a, int t, PF&& pf) {
  constexpr int XP = X ^ 1;
  const unsigned kslot = X * 16384;
  b8 kf[16];
  auto kread = [&](auto ic) { constexpr int I = decltype(ic)::value, ks = I >> 1, kb = I & 1; kf[I] = ldA(a.kb_[ks & 1], kslot + 8192 * kb + 512 * (ks >> 1)); };
  kread(IC<0>{}); kread(IC<1>{}); kread(IC<2>{}); kread(IC<3>{});
  const char* gk = a.gk + (size_t)(t + 1) * 16384; const char* gv = a.gv + (size_t)t * 16384;
  pf();
  auto gap = [&](auto gc) {
    constexpr int G = decltype(gc)::value, ks = G >> 2, kb = (G >> 1) & 1, qb = G & 1, I = G >> 1;
    qk<(ks == 0) ? (FIRST ? 0 : 1) : 2>(a.S[X][kb][qb], kf[I], a.q[qb][ks], a.negm[qb]);
    if constexpr ((G & 1) == 0 && I + 4 < 16) kread(IC<I + 4>{});
    if constexpr (DK) { if constexpr (G == 0) setm0(a.m0k + (X ^ 1) * 16384); if constexpr ((G & 1) && G < 16) dma<((G >> 1) - 4) * 1024>(a.voff[G >> 1], gk); }
    if constexpr (DV) { if constexpr (G == 16) setm0(a.m0v + X * 16384); if constexpr ((G & 1) && G > 16) dma<(((G - 16) >> 1) - 4) * 1024>(a.voff[(G - 16) >> 1], gv); }
    if constexpr (!FIRST) {
      if constexpr (G < 16) { sm_exp<XP, 1, 2 * G>(a); sm_exp<XP, 1, 2 * G + 1>(a); sm_pack<XP, G>(a); }
      else { sm_pack<XP, G>(a); sm_add<XP, 1, 2 * (G - 16)>(a); sm_add<XP, 1, 2 * (G - 16) + 1>(a); }
    }
    GAP();
  };
  UNROLL(32, gap);
  if constexpr (!FIRST) {
#pragma unroll
    for (int qb = 0; qb < 2; ++qb) { a.l[qb] += a.rs0[qb] + a.rs1[qb]; asm volatile("" : "+v"(a.l[qb])); }
  }
}
DI float swap32_max(float x) { auto r = __builtin_amdgcn_permlane32_swap(__float_as_uint(x), __float_as_uint(x), false, false); return fmaxx(__uint_as_float(r[0]), __uint_as_float(r[1])); }
DI float swap32_add(float x) { auto r = __builtin_amdgcn_permlane32_swap(__float_as_uint(x), __float_as_uint(x), false, false); return __uint_as_float(r[0]) + __uint_as_float(r[1]); }
template <int X, bool NOPV, bool ZERO> DI void phaseB(AT& a) {
  const unsigned vslot = (X ^ 1) * 16384;
  b4 vlo[16], vhi[16];
  auto vread = [&](auto ic) {
    constexpr int I = decltype(ic)::value, k4 = I >> 2, db = I & 3;
    vlo[I] = ldT(a.vb_[0], vslot + 2048 * (2 * k4) + 512 * db);
    vhi[I] = ldT(a.vb_[1], vslot + 2048 * (2 * k4 + 1) + 512 * db);
  };
  if constexpr (!NOPV) { vread(IC<0>{}); vread(IC<1>{}); vread(IC<2>{}); }
  float rmax[2];
  auto pv = [&](auto gc) {
    constexpr int G = decltype(gc)::value, I = G >> 1, k4 = I >> 2, db = I & 3, qb = G & 1;
    if constexpr (!NOPV) {
      mfma32<true, ZERO && k4 == 0>(a.O[db][qb], cat(vlo[I], vhi[I]), a.P[qb][k4]);
      if constexpr ((G & 1) == 0 && I + 3 < 16) vread(IC<I + 3>{});
    }
  };
  auto gap1 = [&](auto gc) {
    constexpr int G = decltype(gc)::value;
    pv(gc);
    if constexpr (G >= 4 && G < 12) {
      constexpr int i = G - 4, q = i >> 2, r = 4 * (i & 3);
      if constexpr ((i & 3) == 0) { a.pm0[q] = fmax3(a.S[X][0][q][0], a.S[X][0][q][1], a.S[X][1][q][0]); a.pm1[q] = fmax3(a.S[X][0][q][2], a.S[X][0][q][3], a.S[X][1][q][1]);
                                    a.pm0[q] = fmax3(a.pm0[q], a.S[X][1][q][2], a.S[X][1][q][3]); }
      else { a.pm0[q] = fmax3(a.pm0[q], a.S[X][0][q][r], a.S[X][0][q][r + 1]); a.pm1[q] = fmax3(a.pm1[q], a.S[X][0][q][r + 2], a.S[X][0][q][r + 3]);
             a.pm0[q] = fmax3(a.pm0[q], a.S[X][1][q][r], a.S[X][1][q][r + 1]); a.pm1[q] = fmax3(a.pm1[q], a.S[X][1][q][r + 2], a.S[X][1][q][r + 3]); }
    }
    if constexpr (G == 12 || G == 13) { constexpr int q = G - 12; rmax[q] = swap32_max(fmaxx(a.pm0[q], a.pm1[q])); }
    GAP();
  };
  UNROLL(14, gap1);
  if constexpr (NOPV) {
#pragma unroll
    for (int q = 0; q < 2; ++q) {
      a.m[q] = rmax[q]; a.l[q] = 0.f; a.fct[q] = 1.f;
#pragma unroll
      for (int kb = 0; kb < 2; ++kb)
#pragma unroll
        for (int e = 0; e < 16; ++e) a.S[X][kb][q][e] -= rmax[q];
#pragma unroll
      for (int e = 0; e < 16; ++e) a.negm[q][e] = -rmax[q];
    }
  } else {
    const bool grew = (rmax[0] > THR) | (rmax[1] > THR);
    if (__builtin_expect(__any((int)grew), 0)) {
#pragma unroll
      for (int q = 0; q < 2; ++q) {
        const float dl = fmaxx(rmax[q], 0.f); a.m[q] += dl; a.fct[q] = __builtin_amdgcn_exp2f(-dl);
#pragma unroll
        for (int kb = 0; kb < 2; ++kb)
#pragma unroll
          for (int e = 0; e < 16; ++e) a.S[X][kb][q][e] -= dl;
#pragma unroll
        for (int e = 0; e < 16; ++e) a.negm[q][e] = -a.m[q];
      }
      a.pend = true;
    }
  }
  a.rs0[0] = a.rs0[1] = a.rs1[0] = a.rs1[1] = 0.f;
  GAP();
  auto gap2 = [&](auto gc) {
    constexpr int G = decltype(gc)::value + 14;
    pv(IC<G>{});
    if constexpr (G >= 15) {
      constexpr int e0 = 2 * (G - 15);
      if constexpr (e0 < 32) { sm_exp<X, 0, e0>(a); sm_exp<X, 0, e0 + 1>(a); }
      if constexpr (G >= 16) { sm_add<X, 0, e0 - 2>(a); sm_add<X, 0, e0 - 1>(a); }
    }
    GAP();
  };
  UNROLL(18, gap2);
  asm volatile("" : "+v"(a.S[X][0][0]), "+v"(a.S[X][0][1]), "+v"(a.rs0[0]), "+v"(a.rs0[1]), "+v"(a.rs1[0]), "+v"(a.rs1[1]));
  if constexpr (!NOPV) {
    if (__builtin_expect(a.pend, 0)) {
      asm volatile("s_nop 7\n\ts_nop 7" ::: "memory");
#pragma unroll
      for (int db = 0; db < 4; ++db)
#pragma unroll
        for (int q = 0; q < 2; ++q) {
          asm volatile("" : "+a"(a.O[db][q]));
          f16v o = a.O[db][q];
#pragma unroll
          for (int e = 0; e < 16; ++e) o[e] *= a.fct[q];
          a.O[db][q] = o;
          asm volatile("" : "+a"(a.O[db][q]));
        }
      a.l[0] *= a.fct[0]; a.l[1] *= a.fct[1]; a.pend = false;
      asm volatile("s_nop 1" ::: "memory");
    }
  }
}

struct Params { const u16 *x, *mod, *wq, *kc, *vc, *wo, *wg, *wu, *wd; u16* out; char* ws; float one; unsigned gap; };

__global__ __launch_bounds__(256, 1) void k_main(Params p) {
  extern __shared__ __attribute__((aligned(16))) char lds[];
  const int tid = threadIdx.x, w = __builtin_amdgcn_readfirstlane(tid >> 6), lane = tid & 63;
  const int j = blockIdx.x & 7, c = blockIdx.x >> 3;
  const unsigned ldsb = (unsigned)(size_t)lds;
  const int hh = lane >> 5, r32 = lane & 31;
  unsigned* const flags = (unsigned*)(p.ws + WS_FLAG);
  unsigned* const tmo = flags + 3 * 256 * FLAG_STRIDE;
  STAMP(0);
  AT a;
  {
    f16v Qt[4][2];
    QS s;
    unsigned voffQ[8];
#pragma unroll
    for (int i = 0; i < 8; ++i) voffQ[i] = (4 * i + (lane >> 4)) * 8192 + 16 * ((lane & 15) ^ ((lane >> 4) << 2)) - (i - 4) * 1024 + 4096;
    const char* gq = (const char*)p.wq + (size_t)(4 * j + w) * 256 - 4096;
    s.ring = __builtin_amdgcn_readfirstlane(ldsb + ABYTES + w * 2 * QSLOT);
    constexpr size_t KS_Q = (size_t)32 * 8192;
    auto issueq = [&](int slot, const char* gp) {
      setm0(s.ring + slot * QSLOT + 4096); asm volatile("s_nop 0");
      dma<-4096>(voffQ[0], gp); dma<-3072>(voffQ[1], gp); dma<-2048>(voffQ[2], gp); dma<-1024>(voffQ[3], gp);
      dma<0>(voffQ[4], gp); dma<1024>(voffQ[5], gp); dma<2048>(voffQ[6], gp); dma<3072>(voffQ[7], gp);
    };
    issueq(0, gq); issueq(1, gq + KS_Q);
    {
      const int rr = lane >> 3, sub = lane & 7;
      u4 xv[2][9], cv[9], sv[9];
#pragma unroll
      for (int i = 0; i < 9; ++i) { cv[i] = *(const u4*)(p.mod + D + (sub + 8 * i) * 8); sv[i] = *(const u4*)(p.mod + (sub + 8 * i) * 8); }
      asm volatile("" ::: "memory");
#pragma unroll
      for (int ps = 0; ps < 2; ++ps)
#pragma unroll
        for (int i = 0; i < 9; ++i) {
          xv[ps][i] = *(const u4*)(p.x + (size_t)(c * 64 + 16 * w + 8 * ps + rr) * D + (sub + 8 * i) * 8);
        }
      GAP();
      float mr[2], rs[2];
#pragma unroll
      for (int ps = 0; ps < 2; ++ps) {
        f2 sum = {0.f, 0.f};
#pragma unroll
        for (int i = 0; i < 9; ++i)
#pragma unroll
          for (int e = 0; e < 4; ++e) sum += bf2(xv[ps][i][e]);
        float sm = sum[0] + sum[1];
        sm += __shfl_xor(sm, 1); sm += __shfl_xor(sm, 2); sm += __shfl_xor(sm, 4);
        const float mean = sm * (1.f / D);
        f2 sq = {0.f, 0.f};
#pragma unroll
        for (int i = 0; i < 9; ++i)
#pragma unroll
          for (int e = 0; e < 4; ++e) { const f2 dlt = bf2(xv[ps][i][e]) - mean; sq += dlt * dlt; }
        float q = sq[0] + sq[1];
        q += __shfl_xor(q, 1); q += __shfl_xor(q, 2); q += __shfl_xor(q, 4);
        mr[ps] = rbf(mean); rs[ps] = __builtin_amdgcn_rcpf(rbf(__builtin_amdgcn_sqrtf(rbf(rbf(q * (1.f / D)) + 1e-5f))));
      }
      asm volatile("" : "+v"(mr[0]), "+v"(mr[1]), "+v"(rs[0]), "+v"(rs[1]), "+v"(cv[8]), "+v"(sv[8]));
      {
        const unsigned dummy = __builtin_amdgcn_readfirstlane(ldsb + LDS_DUMMY + w * 256);
        const int li = tid < 144 ? tid : tid - 112;
        touch((const char*)p.wq + (size_t)512 * j * 2, (18 * c + (li >> 3)) * 8192 + (li & 7) * 128, dummy);
        touch((const char*)(w < 2 ? p.kc : p.vc) + ((size_t)(2 * j + (w & 1)) * SK + (SK - WIN)) * DH * 2, (32 * c + (tid & 31)) * 128, dummy);
        touch((const char*)p.wo + (size_t)512 * j * ROWB, (16 * c) * ROWB + li * 128, dummy);
      }
#pragma unroll
      for (int i = 0; i < 9; ++i) {
        f2 c1[4], sh[4];
#pragma unroll
        for (int e = 0; e < 4; ++e) { c1[e] = bf2(pk(bf2(cv[i][e]) + 1.f)); sh[e] = bf2(sv[i][e]); }
#pragma unroll
        for (int ps = 0; ps < 2; ++ps) {
          const int row = 16 * w + 8 * ps + rr;
          u4 o;
#pragma unroll
          for (int e = 0; e < 4; ++e) {
            unsigned t = pk(bf2(xv[ps][i][e]) - mr[ps]);
            t = pk(bf2(t) * rs[ps]);
            t = pk(bf2(t) * c1[e]);
            o[e] = pk(bf2(t) + sh[e]);
          }
          *(LAS u4*)(size_t)(ldsb + row * ROWB + ((sub ^ ((row >> 1) & 7)) * 16) + i * 128) = o;
        }
      }
    }
    {
      const int blk = (lane >> 4) & 1, q = (lane & 15) >> 2, pp = lane & 3;
#pragma unroll
      for (int dt = 0; dt < 4; ++dt) s.wb[dt] = s.ring + (8 * hh + q) * 256 + 64 * (dt ^ q) + 32 * blk + 16 * (pp & 1) + 8 * (pp >> 1);
#pragma unroll
      for (int i = 0; i < 4; ++i) s.hb[i] = ldsb + r32 * ROWB + (((2 * i + hh) ^ ((r32 >> 1) & 7)) * 16);
    }
    asm volatile("s_waitcnt vmcnt(11) lgkmcnt(0)" ::: "memory");
    BAR();
    STAMP(1);
#pragma unroll
    for (int f = 0; f < 8; ++f) { s.wlo[0][f] = ldT(s.wb[f & 3], 4096 * (f >> 2)); s.whi[0][f] = ldT(s.wb[f & 3], 4096 * (f >> 2) + 1024); }
#pragma unroll
    for (int i = 0; i < 4; ++i) s.hf[0][i] = ldA(s.hb[i >> 1], (i & 1) * 32 * ROWB);
    asm volatile("s_waitcnt lgkmcnt(0)" ::: "memory");
    issueq(0, gq + 2 * KS_Q);
    qstep<0, true, true, true, 11>(s, Qt, voffQ, gq + 3 * KS_Q, 0);
    qstep<1, false, true, true, 8>(s, Qt, voffQ, gq + 4 * KS_Q, 128);
    for (int n = 2; n < 14; n += 2) {
      qstep<0, false, true, true, 8>(s, Qt, voffQ, gq + (size_t)(n + 3) * KS_Q, 128 * ((n + 1) >> 1));
      qstep<1, false, true, true, 8>(s, Qt, voffQ, gq + (size_t)(n + 4) * KS_Q, 128 * ((n + 2) >> 1));
    }
    qstep<0, false, true, true, 8>(s, Qt, voffQ, gq + (size_t)17 * KS_Q, 128 * 7);
    qstep<1, false, true, false, 8>(s, Qt, voffQ, gq, 128 * 8);
    qstep<0, false, true, false, 0>(s, Qt, voffQ, gq, 128 * 8);
    qstep<1, false, false, false, 0>(s, Qt, voffQ, gq, 0);
    STAMP(2);
    {
      const int kvl = w >> 1, half = w & 1;
      const unsigned kr = ldsb + kvl * 65536, vr = kr + 32768;
#pragma unroll
      for (int i = 0; i < 2; ++i) a.kb_[i] = kr + 2048 * (r32 >> 3) + 64 * (r32 & 7) + 16 * ((2 * i + hh) ^ ((r32 >> 2) & 3));
      const int blk = (lane >> 4) & 1, q = (lane & 15) >> 2, pp = lane & 3;
#pragma unroll
      for (int t = 0; t < 2; ++t) a.vb_[t] = vr + 64 * (4 * hh + q) + 16 * ((2 * blk + (pp >> 1)) ^ ((2 * t + hh) & 3)) + 8 * (pp & 1);
      const int st = lane >> 5, r7 = (lane >> 2) & 7, pos = lane & 3;
#pragma unroll
      for (int i = 0; i < 8; ++i) {
        const int rg = 4 * half + (i >> 1);
        a.voff[i] = rg * 2048 + r7 * 256 + (i & 1) * 128 + st * 64 + 16 * (pos ^ ((2 * (rg & 1) + (r7 >> 2)) & 3)) - (i - 4) * 1024 + 4096;
      }
      a.m0k = __builtin_amdgcn_readfirstlane(kr + 8192 * half + 4096); a.m0v = __builtin_amdgcn_readfirstlane(vr + 8192 * half + 4096);
      const size_t kvoff = ((size_t)(2 * j + kvl) * SK + (SK - WIN)) * DH * 2;
      a.gk = (const char*)p.kc + kvoff - 4096; a.gv = (const char*)p.vc + kvoff - 4096;
      a.pend = false; a.one = p.one;
    }
    asm volatile("s_waitcnt lgkmcnt(0)" ::: "memory");
    BAR();
    setm0(a.m0k); asm volatile("s_nop 0");
    dma<-4096>(a.voff[0], a.gk); dma<-3072>(a.voff[1], a.gk); dma<-2048>(a.voff[2], a.gk); dma<-1024>(a.voff[3], a.gk);
    dma<0>(a.voff[4], a.gk); dma<1024>(a.voff[5], a.gk); dma<2048>(a.voff[6], a.gk); dma<3072>(a.voff[7], a.gk);
    asm volatile("s_nop 7" ::: "memory");
#pragma unroll
    for (int dt = 0; dt < 4; ++dt)
#pragma unroll
      for (int qb = 0; qb < 2; ++qb) {
        asm volatile("" : "+v"(Qt[dt][qb]));
#pragma unroll
        for (int s2 = 0; s2 < 2; ++s2) {
          u4 w4;
#pragma unroll
          for (int e = 0; e < 4; ++e) w4[e] = pk((f2){Qt[dt][qb][8 * s2 + 2 * e], Qt[dt][qb][8 * s2 + 2 * e + 1]} * QSCALE);
          a.q[qb][2 * dt + s2] = __builtin_bit_cast(b8, w4);
          asm volatile("" : "+a"(a.q[qb][2 * dt + s2]));
        }
      }
  }
  STAMP(3);
  {
    auto tile_bar = [&]() { asm volatile("s_waitcnt vmcnt(0) lgkmcnt(0)" ::: "memory"); BAR(); };
    auto nopf = [&]() {};
    tile_bar(); phaseA<0, true, true, true>(a, 0, nopf); asm volatile("s_nop 7\n\ts_nop 3" ::: "memory"); GAP(); phaseB<0, true, false>(a);
    tile_bar(); phaseA<1, false, true, true>(a, 1, nopf); phaseB<1, false, true>(a);
    for (int t = 2; t < 6; t += 2) {
      tile_bar(); phaseA<0, false, true, true>(a, t, nopf); phaseB<0, false, false>(a);
      tile_bar(); phaseA<1, false, true, true>(a, t + 1, nopf); phaseB<1, false, false>(a);
    }
    tile_bar(); phaseA<0, false, true, true>(a, 6, nopf); phaseB<0, false, false>(a);
    tile_bar(); phaseA<1, false, false, true>(a, 7, nopf); phaseB<1, false, false>(a);
    { auto f1 = [&](auto ic) { sm_exp<1, 1, decltype(ic)::value>(a); }; UNROLL(32, f1);
      auto f2_ = [&](auto ic) { sm_pack<1, decltype(ic)::value>(a); }; UNROLL(32, f2_);
      auto f3 = [&](auto ic) { sm_add<1, 1, decltype(ic)::value>(a); }; UNROLL(32, f3); }
#pragma unroll
    for (int qb = 0; qb < 2; ++qb) a.l[qb] += a.rs0[qb] + a.rs1[qb];
    tile_bar();
    const unsigned vslot = 16384;
    b4 vlo[16], vhi[16];
#pragma unroll
    for (int I = 0; I < 16; ++I) { vlo[I] = ldT(a.vb_[0], vslot + 2048 * (2 * (I >> 2)) + 512 * (I & 3)); vhi[I] = ldT(a.vb_[1], vslot + 2048 * (2 * (I >> 2) + 1) + 512 * (I & 3)); }
    asm volatile("s_nop 1" ::: "memory"); GAP();
#pragma unroll
    for (int G = 0; G < 32; ++G) { const int I = G >> 1; mfma32<true, false>(a.O[I & 3][G & 1], cat(vlo[I], vhi[I]), a.P[G & 1][I >> 2]); }
  }
  STAMP(4);
  int tid2 = threadIdx.x; asm volatile("" : "+v"(tid2));
  {
  const int tid = tid2, lane = tid2 & 63, hh = lane >> 5, r32 = lane & 31;
  GS s;
  unsigned voffB[9];
#pragma unroll
  for (int i = 0; i < 9; ++i) { const int idx = i * 64 + lane, imm = i < 8 ? (i - 4) * 1024 : 0; voffB[i] = (idx / 18) * ROWB + (idx % 18) * 16 - imm + 4096; }
  s.ring = __builtin_amdgcn_readfirstlane(ldsb + ABYTES + w * 2 * SLOT);
  const int g4 = lane >> 4, cl = lane & 15;
  s.wbase = s.ring + (4 * g4 + ((lane & 15) >> 2)) * 288 + 8 * (lane & 3);
  const unsigned sw = (cl >> 1) & 7;
  const unsigned ab_e = ldsb + cl * ROWB + ((g4 ^ sw) * 16), ab_o = ldsb + cl * ROWB + (((4 + g4) ^ sw) * 16);
  const __amdgpu_buffer_rsrc_t rX = __builtin_amdgcn_make_buffer_rsrc(p.ws + WS_X + (size_t)c * 64 * SLICE, 0, 64 * SLICE, 0x00020000);
  unsigned voffA[9];
#pragma unroll
  for (int i = 0; i < 9; ++i) { const int idx = i * 64 + lane, imm = i < 8 ? (i - 4) * 1024 : 0; voffA[i] = (idx / 18) * (DF * 2) + (idx % 18) * 16 - imm + 4096; }
  const char* gG = (const char*)p.wg + (size_t)(576 * j + 144 * w) * 2 - 4096;
  const char* gU = (const char*)p.wu + (size_t)(576 * j + 144 * w) * 2 - 4096;
  const char* gD = (const char*)p.wd + (size_t)(576 * j) * ROWB + 288 * w - 4096;
  f4 aG[9][4];
  {
    const char* gO = (const char*)p.wo + (size_t)(512 * j) * ROWB + 288 * w - 4096;
    asm volatile("s_waitcnt lgkmcnt(0)" ::: "memory");
    BAR();
    issue_all(s, 0, voffB, gO); issue_all(s, 1, voffB, gO + (size_t)32 * ROWB);
    asm volatile("s_nop 7" ::: "memory");
#pragma unroll
    for (int qb = 0; qb < 2; ++qb) {
      const float il = __builtin_amdgcn_rcpf(swap32_add(a.l[qb]));
      const int row = 32 * qb + r32; const unsigned swr = (r32 >> 1) & 7;
#pragma unroll
      for (int db = 0; db < 4; ++db) {
        asm volatile("" : "+a"(a.O[db][qb]));
        const f16v o = a.O[db][qb];
#pragma unroll
        for (int u = 0; u < 2; ++u) {
          const u4 wv = {pk((f2){o[4 * u], o[4 * u + 1]} * il), pk((f2){o[4 * u + 2], o[4 * u + 3]} * il), pk((f2){o[4 * u + 8], o[4 * u + 9]} * il), pk((f2){o[4 * u + 10], o[4 * u + 11]} * il)};
          *(LAS u4*)(size_t)(ldsb + row * ROWB + (((4 * (4 * w + db) + 2 * u + hh) ^ swr) * 16)) = wv;
        }
      }
    }
    asm volatile("s_waitcnt vmcnt(9) lgkmcnt(0)" ::: "memory");
    BAR();
    STAMP(5);
    read_w(s, 0); read_a(s, 0, ab_e);
    asm volatile("s_waitcnt lgkmcnt(0)" ::: "memory");
    issue_all(s, 0, voffB, gO + (size_t)2 * 32 * ROWB);
    gemm_rows<16, 2>(s, aG, voffB, gO, ab_e, ab_o, voffA, gG, gU, gG);
    STAMP(6);
    settle<0>(aG);
    store_partials(aG, rX, j * SLICE, 8 * SLICE, w, lane);
  }
  {
    int zlr[3], zcc[3];
    publish(flags + (0 * 256 + blockIdx.x) * FLAG_STRIDE);
    STAMP(7);
    const int t3 = tid < 192 ? tid : tid - 64;
    u4 xr[3], gv[3];
#pragma unroll
    for (int i = 0; i < 3; ++i) {
      zdecode(t3 + 192 * i, zlr[i], zcc[i]);
      xr[i] = *(const u4*)(p.x + (size_t)(c * 64 + j * 8 + zlr[i]) * D + zcc[i] * 8);
      gv[i] = *(const u4*)(p.mod + 2 * D + zcc[i] * 8);
    }
    const int lrow = 2 * w + hh, grow = j * 8 + lrow;
    u2 cbv[3][2], sbv[3][2];
#pragma unroll
    for (int i = 0; i < 3; ++i) {
      const int pos = (i < 2 || r32 < 8) ? r32 + 32 * i : r32;
      const int q = pos ^ ((grow >> 1) & 7), k0 = 32 * (q >> 2) + 4 * (q & 3);
#pragma unroll
      for (int h = 0; h < 2; ++h) { cbv[i][h] = *(const u2*)(p.mod + 4 * D + k0 + 16 * h); sbv[i][h] = *(const u2*)(p.mod + 3 * D + k0 + 16 * h); }
    }
    await8(flags + (0 * 256 + c * 8) * FLAG_STRIDE, tmo, w, lane);
    STAMP(8);
    {
      u4 v[3][8];
#pragma unroll
      for (int i = 0; i < 3; ++i)
#pragma unroll
        for (int jj = 0; jj < 8; ++jj) v[i][jj] = __builtin_amdgcn_raw_buffer_load_b128(rX, (j * 8 + jj) * SLICE + (t3 + 192 * i) * 16, 0, AUX_X);
#pragma unroll
      for (int i = 0; i < 3; ++i) {
        const u4 x1 = gated_residual(v[i], xr[i], gv[i]);
        if (tid < 192) {
          *(LAS u4*)(size_t)(ldsb + LDS_X1 + (tid + 192 * i) * 16) = x1;
          LAS f4* dst = (LAS f4*)(size_t)(ldsb + (zlr[i] * D + zcc[i] * 8) * 4);
          const f2 a0 = bf2(x1[0]), a1 = bf2(x1[1]), a2 = bf2(x1[2]), a3 = bf2(x1[3]);
          dst[0] = (f4){a0[0], a0[1], a1[0], a1[1]}; dst[1] = (f4){a2[0], a2[1], a3[0], a3[1]};
        }
      }
    }
    asm volatile("s_waitcnt lgkmcnt(0)" ::: "memory");
    BAR();
    STAMP(9);
    {
      f4 v[3][2];
      f2 sum = {0.f, 0.f};
#pragma unroll
      for (int i = 0; i < 3; ++i) {
        const bool on = (i < 2 || r32 < 8);
        const int pos = on ? r32 + 32 * i : r32;
        const int q = pos ^ ((grow >> 1) & 7), k0 = 32 * (q >> 2) + 4 * (q & 3);
#pragma unroll
        for (int h = 0; h < 2; ++h) {
          v[i][h] = *(LAS f4*)(size_t)(ldsb + (lrow * D + k0 + 16 * h) * 4);
          if (on) sum += (f2){v[i][h][0] + v[i][h][1], v[i][h][2] + v[i][h][3]};
        }
      }
      float sm = sum[0] + sum[1];
#pragma unroll
      for (int o = 1; o < 32; o <<= 1) sm += __shfl_xor(sm, o);
      const float mean = sm * (1.f / D);
      float q2 = 0.f;
#pragma unroll
      for (int i = 0; i < 3; ++i)
        if (i < 2 || r32 < 8) {
#pragma unroll
          for (int h = 0; h < 2; ++h)
#pragma unroll
            for (int e = 0; e < 4; ++e) { const float dl = v[i][h][e] - mean; q2 += dl * dl; }
        }
#pragma unroll
      for (int o = 1; o < 32; o <<= 1) q2 += __shfl_xor(q2, o);
      const float mr = rbf(mean), rs = __builtin_amdgcn_rcpf(rbf(__builtin_amdgcn_sqrtf(rbf(rbf(q2 * (1.f / D)) + 1e-5f))));
      const __amdgpu_buffer_rsrc_t rH = __builtin_amdgcn_make_buffer_rsrc(p.ws + WS_H2 + (size_t)c * ABYTES, 0, ABYTES, 0x00020000);
#pragma unroll
      for (int i = 0; i < 3; ++i) {
        u4 o;
#pragma unroll
        for (int h = 0; h < 2; ++h)
#pragma unroll
          for (int e = 0; e < 2; ++e) {
            unsigned t = pk((f2){v[i][h][2 * e], v[i][h][2 * e + 1]} - mr);
            t = pk(bf2(t) * rs);
            t = pk(bf2(t) * bf2(pk(bf2(cbv[i][h][e]) + 1.f)));
            o[2 * h + e] = pk(bf2(t) + bf2(sbv[i][h][e]));
          }
        if (i < 2 || r32 < 8) __builtin_amdgcn_raw_buffer_store_b128(o, rH, grow * ROWB + (r32 + 32 * i) * 16, 0, 16);
      }
      publish(flags + (1 * 256 + blockIdx.x) * FLAG_STRIDE);
      STAMP(10);
      if (w != 0) {
        const unsigned dummy = __builtin_amdgcn_readfirstlane(ldsb + LDS_DUMMY + w * 256);
        int li = tid - 64; li = li < 162 ? li : li - 94;
        const int r = 18 * c + li / 9, l = li % 9;
        touch((const char*)p.wg + (size_t)576 * j * 2, r * (DF * 2) + l * 128, dummy);
        touch((const char*)p.wu + (size_t)576 * j * 2, r * (DF * 2) + l * 128, dummy);
      }
      await8(flags + (1 * 256 + c * 8) * FLAG_STRIDE, tmo, w, lane);
      STAMP(11);
      u4 hv[18];
#pragma unroll
      for (int i = 0; i < 18; ++i) hv[i] = __builtin_amdgcn_raw_buffer_load_b128(rH, (i * 256 + tid) * 16, 0, AUX_H);
#pragma unroll
      for (int i = 0; i < 18; ++i) *(LAS u4*)(size_t)(ldsb + (i * 256 + tid) * 16) = hv[i];
    }
    asm volatile("s_waitcnt vmcnt(0) lgkmcnt(0)" ::: "memory");
    BAR();
  }
  STAMP(12);
  {
    constexpr size_t KS_GU = (size_t)32 * DF * 2, KS_D = (size_t)32 * ROWB;
    read_w(s, 0); read_a(s, 0, ab_e);
    asm volatile("s_waitcnt lgkmcnt(0)" ::: "memory");
    issue_all(s, 0, voffA, gG + KS_GU);
    f4 aU[9][4];
    auto no = [&]() {};
    auto pfd = [&]() {
      const unsigned dummy = __builtin_amdgcn_readfirstlane(ldsb + LDS_DUMMY + w * 256);
      int li = tid; li = li < 162 ? li : li - 94;
      touch((const char*)p.wd + (size_t)576 * j * ROWB, (18 * c) * ROWB + li * 128, dummy);
    };
    auto kpair = [&](auto zc, auto lastc, auto vmc, int k, auto&& pf) {
      constexpr bool Z = decltype(zc)::value, LAST = decltype(lastc)::value; constexpr int VM = decltype(vmc)::value;
      const char* u1 = gU + (size_t)(k + 1) * KS_GU; const char* g2 = gG + (size_t)(k + 2) * KS_GU;
      step<0, 0, Z, true, false, true, 9, 0>(s, aG, voffA, u1, 0, no);
      if constexpr (!LAST) {
        step<1, 0, Z, true, true, true, 9, 8>(s, aU, voffA, g2, ab_o + 128 * (k >> 1), pf);
        step<0, 1, false, true, false, true, VM, 0>(s, aG, voffA, u1 + KS_GU, 0, no);
        step<1, 1, false, true, true, true, VM, 8>(s, aU, voffA, g2 + KS_GU, ab_e + 128 * ((k + 2) >> 1), no);
      } else {
        step<1, 0, Z, true, true, true, 9, 8>(s, aU, voffB, gD, ab_o + 128 * (k >> 1), no);
        step<0, 1, false, true, false, true, 9, 0>(s, aG, voffB, gD + KS_D, 0, no);
        step<1, 1, false, true, false, true, 9, 8>(s, aU, voffB, gD + 2 * KS_D, 0, no);
      }
    };
    kpair(std::true_type{}, std::false_type{}, IC<9>{}, 0, no);
    for (int k = 2; k < 10; k += 2) kpair(std::false_type{}, std::false_type{}, IC<9>{}, k, no);
    kpair(std::false_type{}, std::false_type{}, IC<10>{}, 10, pfd);
    for (int k = 12; k < 16; k += 2) kpair(std::false_type{}, std::false_type{}, IC<9>{}, k, no);
    kpair(std::false_type{}, std::true_type{}, IC<9>{}, 16, no);
    STAMP(13);
    settle<0>(aG); settle<8>(aU);
    u2 act[9][4];
#pragma unroll
    for (int t = 0; t < 9; ++t)
#pragma unroll
      for (int m = 0; m < 4; ++m)
#pragma unroll
        for (int e = 0; e < 2; ++e) {
          const f2 x = {aG[t][m][2 * e], aG[t][m][2 * e + 1]}, u = {aU[t][m][2 * e], aU[t][m][2 * e + 1]};
          const f2 z = x * (x * x * -0.10294324f + -2.3022082f);
          const f2 dn = (f2){__builtin_amdgcn_exp2f(z[0]), __builtin_amdgcn_exp2f(z[1])} + 1.f;
          act[t][m][e] = pk((x * u) * (f2){__builtin_amdgcn_rcpf(dn[0]), __builtin_amdgcn_rcpf(dn[1])});
        }
    asm volatile("s_waitcnt lgkmcnt(0)" ::: "memory");
    BAR();
#pragma unroll
    for (int t = 0; t < 9; ++t) {
      const int tt = 9 * w + t, ks = tt >> 1, ah = tt & 1;
      const unsigned ad = ldsb + cl * ROWB + (((4 * ks + g4) ^ sw) * 16) + 8 * ah;
#pragma unroll
      for (int m = 0; m < 4; ++m) *(LAS u2*)(size_t)(ad + m * 16 * ROWB) = act[t][m];
    }
    asm volatile("s_waitcnt lgkmcnt(0)" ::: "memory");
    BAR();
    STAMP(14);
    read_a(s, 0, ab_e);
    gemm_rows<18, 0>(s, aG, voffB, gD, ab_e, ab_o, voffB, gD, gD, gD);
    STAMP(15);
  }
  {
    int tid3 = threadIdx.x; asm volatile("" : "+v"(tid3));
    const int tid = tid3, lane = tid3 & 63;
    settle<0>(aG);
    store_partials(aG, rX, j * 8 * SLICE, SLICE, w, lane);
    publish(flags + (2 * 256 + blockIdx.x) * FLAG_STRIDE);
    STAMP(16);
    const int t3 = tid < 192 ? tid : tid - 64;
    int zlr[3], zcc[3];
    u4 gv[3], x1[3];
#pragma unroll
    for (int i = 0; i < 3; ++i) {
      zdecode(t3 + 192 * i, zlr[i], zcc[i]);
      gv[i] = *(const u4*)(p.mod + 5 * D + zcc[i] * 8);
      x1[i] = *(LAS u4*)(size_t)(ldsb + LDS_X1 + (t3 + 192 * i) * 16);
    }
    await8(flags + (2 * 256 + c * 8) * FLAG_STRIDE, tmo, w, lane);
    STAMP(17);
    u4 v[3][8];
#pragma unroll
    for (int i = 0; i < 3; ++i)
#pragma unroll
      for (int jj = 0; jj < 8; ++jj) v[i][jj] = __builtin_amdgcn_raw_buffer_load_b128(rX, (jj * 8 + j) * SLICE + (t3 + 192 * i) * 16, 0, AUX_X);
#pragma unroll
    for (int i = 0; i < 3; ++i) {
      const u4 o = gated_residual(v[i], x1[i], gv[i]);
      if (tid < 192) *(u4*)(p.out + (size_t)(c * 64 + j * 8 + zlr[i]) * D + zcc[i] * 8) = o;
    }
  }
  STAMP(18);
  }
}

extern "C" void kernel_launch(void* const* d_in, const int* in_sizes, int n_in, void* d_out, int out_size, void* d_ws, size_t ws_size, hipStream_t stream) {
  char* ws = (char*)d_ws;
  Params p;
  p.x = (const u16*)d_in[0]; p.kc = (const u16*)d_in[1]; p.vc = (const u16*)d_in[2]; p.wq = (const u16*)d_in[3]; p.wo = (const u16*)d_in[4];
  p.wg = (const u16*)d_in[6]; p.wu = (const u16*)d_in[7]; p.wd = (const u16*)d_in[8];
  p.mod = (const u16*)(ws + WS_MOD); p.out = (u16*)d_out; p.ws = ws; p.one = 1.0f; p.gap = 0u;
  (void)hipFuncSetAttribute((const void*)k_main, hipFuncAttributeMaxDynamicSharedMemorySize, LDS_BYTES);
  k_pre<<<108, 256, 0, stream>>>((const u16*)d_in[9], (const u16*)d_in[10], (u16*)(ws + WS_MOD), (unsigned*)(ws + WS_FLAG));
  k_main<<<256, 256, LDS_BYTES, stream>>>(p);
}
```

```cpp
#include <hip/hip_runtime.h>
#include <stdint.h>
#include <utility>
#include <type_traits>

typedef unsigned short u16;
typedef unsigned long long u64;
typedef __bf16 b2 __attribute__((ext_vector_type(2)));
typedef __bf16 b4 __attribute__((ext_vector_type(4)));
typedef __bf16 b8 __attribute__((ext_vector_type(8)));
typedef float f2 __attribute__((ext_vector_type(2)));
typedef float f4 __attribute__((ext_vector_type(4)));
typedef float f16v __attribute__((ext_vector_type(16)));
typedef unsigned u2 __attribute__((ext_vector_type(2)));
typedef unsigned u4 __attribute__((ext_vector_type(4)));
typedef __attribute__((address_space(1))) unsigned gu32;
typedef __attribute__((address_space(1))) unsigned long long gu64;
#define LAS __attribute__((address_space(3)))
#define DI __device__ __forceinline__
#define GAP() __builtin_amdgcn_sched_barrier(0)
#define BAR() do { asm volatile("" ::: "memory"); __builtin_amdgcn_s_barrier(); asm volatile("" ::: "memory"); } while (0)
template <int V> using IC = std::integral_constant<int, V>;
#define AUX_X 16
#define AUX_H 18
#define PFD_K 1
#define UNROLL(N, F) [&]<int... I_>(std::integer_sequence<int, I_...>) { (F(IC<I_>{}), ...); }(std::make_integer_sequence<int, N>{})

constexpr int S = 2048, D = 576, DH = 128, SK = 2048, WIN = 512, DF = 4608;
constexpr int ROWB = D * 2;
constexpr int ABYTES = 64 * ROWB;
constexpr int SLICE = 8 * ROWB;
constexpr int SLOT = 9216;
constexpr int QSLOT = 8192;
constexpr int LDS_DUMMY = ABYTES + 8 * SLOT;
constexpr int LDS_X1 = LDS_DUMMY + 256;
constexpr int LDS_MOD = LDS_X1 + SLICE;
constexpr int LDS_BYTES = LDS_MOD + 6 * D * 2;
static_assert(LDS_BYTES <= 163840, "LDS");
constexpr size_t WS_EPOCH = 0;
constexpr size_t WS_TMO = 256;
constexpr size_t WS_MODG = 4096;
constexpr size_t WS_FLAG = 32768;
constexpr int FLAG_STRIDE = 8;
constexpr size_t WS_X = 131072;
constexpr size_t WS_H2 = WS_X + (size_t)32 * 64 * SLICE;
constexpr size_t WS_DBG = 200u << 20;
#define STAMP(i)

DI float bf2f(u16 v) { return __uint_as_float(((unsigned)v) << 16); }
DI float bflo(unsigned w) { return __uint_as_float(w << 16); }
DI float bfhi(unsigned w) { return __uint_as_float(w & 0xffff0000u); }
DI f2 bf2(unsigned w) { return (f2){bflo(w), bfhi(w)}; }
DI unsigned pk(float lo, float hi) { f2 v = {lo, hi}; b2 b = __builtin_convertvector(v, b2); return __builtin_bit_cast(unsigned, b); }
DI unsigned pk(f2 v) { b2 b = __builtin_convertvector(v, b2); return __builtin_bit_cast(unsigned, b); }
DI float rbf(float f) { return bflo(pk(f, f)); }
DI f2 rb2(f2 v) { return (f2){__uint_as_float(pk(0.f, v[0])), __uint_as_float(pk(0.f, v[1]))}; }
template <int CTRL> DI float dpp(float v) { return __uint_as_float(__builtin_amdgcn_update_dpp(0u, __float_as_uint(v), CTRL, 0xf, 0xf, false)); }
DI float sum8(float v) { v += dpp<0xB1>(v); v += dpp<0x4E>(v); v += dpp<0x141>(v); return v; }
DI float swap16_add(float x) { auto r = __builtin_amdgcn_permlane16_swap(__float_as_uint(x), __float_as_uint(x), false, false); return __uint_as_float(r[0]) + __uint_as_float(r[1]); }
DI float swap32_add(float x) { auto r = __builtin_amdgcn_permlane32_swap(__float_as_uint(x), __float_as_uint(x), false, false); return __uint_as_float(r[0]) + __uint_as_float(r[1]); }
DI float sum32(float v) { v = sum8(v); v += dpp<0x140>(v); return swap16_add(v); }
DI u16 f2bf(float f) { return (u16)(pk(f, f) & 0xffffu); }
DI float fmaxx(float a, float b) { return __builtin_elementwise_maximum(a, b); }
DI float fmax3(float a, float b, float c) { return __builtin_elementwise_maximum(__builtin_elementwise_maximum(a, b), c); }

DI void zdecode(int z, int& lr, int& cc) {
  const int w = z / 144, y = z - w * 144;
  if (y < 128) { const int tp = y >> 5; lr = (y >> 2) & 7; cc = 18 * w + 4 * tp + (y & 3); }
  else { const int y2 = y - 128; lr = y2 >> 1; cc = 18 * w + 16 + (y2 & 1); }
}

template <int IMM> DI void dma(unsigned voff, const char* g) { asm volatile("global_load_lds_dwordx4 %0, %1 offset:%2" :: "v"(voff), "s"(g), "i"(IMM) : "memory"); }
DI void setm0(unsigned a) { asm volatile("s_mov_b32 m0, %0" :: "s"(a) : "memory"); }
DI void touch(const char* p, unsigned voff, unsigned ldsdummy) {
  asm volatile("s_mov_b32 m0, %0\n\ts_nop 0\n\tglobal_load_lds_dword %1, %2" :: "s"(ldsdummy), "v"(voff), "s"(p) : "memory");
}
DI b4 ldT(unsigned base, int off) { return __builtin_amdgcn_ds_read_tr16_b64_v4bf16((LAS b4*)(size_t)(base + off)); }
DI b8 ldA(unsigned base, int off) { return *(LAS b8*)(size_t)(base + off); }
DI b8 cat(const b4& lo, const b4& hi) { return __builtin_shufflevector(lo, hi, 0, 1, 2, 3, 4, 5, 6, 7); }
template <bool AG, bool ZERO> DI void mfma16(f4& c, const b8& a, const b8& b) {
  if constexpr (AG) {
    if constexpr (ZERO) asm volatile("v_mfma_f32_16x16x32_bf16 %0, %1, %2, 0" : "=a"(c) : "v"(a), "v"(b));
    else asm volatile("v_mfma_f32_16x16x32_bf16 %0, %1, %2, %0" : "+a"(c) : "v"(a), "v"(b));
  } else {
    if constexpr (ZERO) asm volatile("v_mfma_f32_16x16x32_bf16 %0, %1, %2, 0" : "=&v"(c) : "v"(a), "v"(b));
    else asm volatile("v_mfma_f32_16x16x32_bf16 %0, %1, %2, %0" : "+v"(c) : "v"(a), "v"(b));
  }
}
template <bool AG, bool ZERO> DI void mfma32(f16v& c, const b8& a, const b8& b) {
  if constexpr (AG) {
    if constexpr (ZERO) asm volatile("v_mfma_f32_32x32x16_bf16 %0, %1, %2, 0" : "=a"(c) : "v"(a), "v"(b));
    else asm volatile("v_mfma_f32_32x32x16_bf16 %0, %1, %2, %0" : "+a"(c) : "v"(a), "v"(b));
  } else {
    if constexpr (ZERO) asm volatile("v_mfma_f32_32x32x16_bf16 %0, %1, %2, 0" : "=&v"(c) : "v"(a), "v"(b));
    else asm volatile("v_mfma_f32_32x32x16_bf16 %0, %1, %2, %0" : "+v"(c) : "v"(a), "v"(b));
  }
}
template <int NVG> DI void settle(f4 (&acc)[9][4]) {
  asm volatile("s_nop 7\n\ts_nop 7" ::: "memory");
#pragma unroll
  for (int t = 0; t < 9; ++t)
#pragma unroll
    for (int m = 0; m < 4; ++m) { if (t * 4 + m < 36 - NVG) asm volatile("" : "+a"(acc[t][m])); else asm volatile("" : "+v"(acc[t][m])); }
}

struct GS {
  b4 wlo[2][9], whi[2][9];
  b8 af[2][4];
  unsigned wbase, ring;
};
DI void issue_all(const GS& s, int slot, const unsigned (&voff)[9], const char* gp) {
  const unsigned sl = s.ring + slot * SLOT;
  setm0(sl + 4096); asm volatile("s_nop 0");
  dma<-4096>(voff[0], gp); dma<-3072>(voff[1], gp); dma<-2048>(voff[2], gp); dma<-1024>(voff[3], gp);
  dma<0>(voff[4], gp); dma<1024>(voff[5], gp); dma<2048>(voff[6], gp); dma<3072>(voff[7], gp);
  setm0(sl + 8192); asm volatile("s_nop 0"); dma<0>(voff[8], gp);
}
template <int PAR, int AP, bool ZERO, bool RDW, bool RDA, bool DMA, int VM, int NVG, typename PF>
DI void step(GS& s, f4 (&acc)[9][4], const unsigned (&voff)[9], const char* gp, unsigned ab, PF&& pf) {
  const unsigned sl = s.ring + (PAR ^ 1) * SLOT;
  const unsigned wb = s.wbase + (PAR ^ 1) * SLOT;
  auto mm = [&](auto ic) {
    constexpr int I = decltype(ic)::value, t = I >> 2, m = I & 3;
    mfma16<(I < 36 - NVG), ZERO>(acc[t][m], cat(s.wlo[PAR][t], s.whi[PAR][t]), s.af[AP][m]);
  };
  auto rd = [&](auto ic) {
    constexpr int I = decltype(ic)::value;
    if constexpr (I < 4) { if constexpr (RDA) s.af[AP ^ 1][I] = ldA(ab, I * 16 * ROWB); }
    else if constexpr (RDW) {
      if constexpr (((I - 4) & 1) == 0) s.wlo[PAR ^ 1][(I - 4) >> 1] = ldT(wb, 32 * ((I - 4) >> 1));
      else s.whi[PAR ^ 1][(I - 4) >> 1] = ldT(wb, 32 * ((I - 4) >> 1) + 4608);
    }
  };
  auto gap = [&](auto gc) {
    constexpr int G = decltype(gc)::value;
    mm(IC<2 * G>{});
    if constexpr (G == 0 && RDW) asm volatile("s_waitcnt vmcnt(%0)" :: "i"(VM) : "memory");
    if constexpr (G <= 10) rd(IC<2 * G>{});
    if constexpr (DMA) {
      if constexpr (G == 12) { asm volatile("s_waitcnt lgkmcnt(0)" ::: "memory"); setm0(sl + 4096); }
      if constexpr (G >= 13 && G <= 16) dma<(2 * (G - 13) - 4) * 1024>(voff[2 * (G - 13)], gp);
      if constexpr (G == 17) dma<0>(voff[8], gp);
    }
    GAP();
    mm(IC<2 * G + 1>{});
    if constexpr (G <= 10) rd(IC<2 * G + 1>{});
    if constexpr (DMA) {
      if constexpr (G >= 13 && G <= 16) dma<(2 * (G - 13) + 1 - 4) * 1024>(voff[2 * (G - 13) + 1], gp);
      if constexpr (G == 16) setm0(sl + 8192);
    }
    if constexpr (G == 17) pf();
    GAP();
  };
  UNROLL(18, gap);
}
DI void read_w(GS& s, int par) {
#pragma unroll
  for (int t = 0; t < 9; ++t) { s.wlo[par][t] = ldT(s.wbase + par * SLOT, 32 * t); s.whi[par][t] = ldT(s.wbase + par * SLOT, 32 * t + 4608); }
}
DI void read_a(GS& s, int set, unsigned ab) {
#pragma unroll
  for (int m = 0; m < 4; ++m) s.af[set][m] = ldA(ab, m * 16 * ROWB);
}
template <int NS, int NEXT> DI void gemm_rows(GS& s, f4 (&acc)[9][4], const unsigned (&voff)[9], const char* gW, unsigned ab_e, unsigned ab_o,
                                              const unsigned (&nvoff)[9], const char* nx0, const char* nx1, const char* nx2) {
  constexpr size_t KS = (size_t)32 * ROWB;
  auto no = [&]() {};
  step<0, 0, true, true, true, true, 9, 0>(s, acc, voff, gW + 3 * KS, ab_o, no);
  step<1, 1, false, true, true, true, 9, 0>(s, acc, voff, gW + 4 * KS, ab_e + 128, no);
  for (int k = 2; k < NS - 4; k += 2) {
    step<0, 0, false, true, true, true, 9, 0>(s, acc, voff, gW + (size_t)(k + 3) * KS, ab_o + 128 * (k >> 1), no);
    step<1, 1, false, true, true, true, 9, 0>(s, acc, voff, gW + (size_t)(k + 4) * KS, ab_e + 128 * ((k + 2) >> 1), no);
  }
  step<0, 0, false, true, true, true, 9, 0>(s, acc, voff, gW + (size_t)(NS - 1) * KS, ab_o + 128 * ((NS - 4) >> 1), no);
  step<1, 1, false, true, true, NEXT != 0, 9, 0>(s, acc, nvoff, nx0, ab_e + 128 * ((NS - 2) >> 1), no);
  step<0, 0, false, true, true, NEXT != 0, NEXT != 0 ? 9 : 0, 0>(s, acc, nvoff, nx1, ab_o + 128 * ((NS - 2) >> 1), no);
  step<1, 1, false, NEXT == 1, false, NEXT == 1, 9, 0>(s, acc, nvoff, nx2, 0, no);
}
DI void store_partials(const f4 (&acc)[9][4], __amdgpu_buffer_rsrc_t rs, unsigned base, unsigned dstride, int w, int lane) {
  const int G = lane >> 4, c = lane & 15, lr = c & 7, dh = c >> 3;
#pragma unroll
  for (int m = 0; m < 4; ++m) {
    const unsigned dst = base + (2 * m + dh) * dstride + w * 2304 + lr * 64 + (2 * (G & 1) + (G >> 1)) * 16;
#pragma unroll
    for (int tp = 0; tp < 4; ++tp) {
      const f4 a = acc[2 * tp][m], b = acc[2 * tp + 1][m];
      auto r0 = __builtin_amdgcn_permlane16_swap(pk(a[0], a[1]), pk(b[0], b[1]), false, false);
      auto r1 = __builtin_amdgcn_permlane16_swap(pk(a[2], a[3]), pk(b[2], b[3]), false, false);
      __builtin_amdgcn_raw_buffer_store_b128((u4){r0[0], r1[0], r0[1], r1[1]}, rs, dst + tp * 512, 0, 16);
    }
  }
#pragma unroll
  for (int mp = 0; mp < 2; ++mp) {
    const f4 a = acc[8][2 * mp], b = acc[8][2 * mp + 1];
    auto r0 = __builtin_amdgcn_permlane16_swap(pk(a[0], a[1]), pk(b[0], b[1]), false, false);
    auto r1 = __builtin_amdgcn_permlane16_swap(pk(a[2], a[3]), pk(b[2], b[3]), false, false);
    const unsigned dst = base + (2 * (2 * mp + (G & 1)) + dh) * dstride + w * 2304 + 2048 + lr * 32 + (G >> 1) * 16;
    __builtin_amdgcn_raw_buffer_store_b128((u4){r0[0], r1[0], r0[1], r1[1]}, rs, dst, 0, 16);
  }
}

constexpr unsigned SPIN_CAP = 1u << 17;
DI void publish(u64* flag, u64 tag) {
  asm volatile("s_waitcnt vmcnt(0)" ::: "memory");
  BAR();
  if (threadIdx.x == 0) __hip_atomic_store((gu64*)flag, tag, __ATOMIC_RELAXED, __HIP_MEMORY_SCOPE_AGENT);
}
DI void await8(u64* flags8, u64 tag, unsigned* tmo, int w, int lane) {
  if (w == 0) {
    for (unsigned spins = 0;; ++spins) {
      const u64 v = __hip_atomic_load((gu64*)(flags8 + (lane & 7) * FLAG_STRIDE), __ATOMIC_RELAXED, __HIP_MEMORY_SCOPE_AGENT);
      if (__all(v == tag)) break;
      if (spins > SPIN_CAP) { if (lane == 0) __hip_atomic_store((gu32*)tmo, 1u, __ATOMIC_RELAXED, __HIP_MEMORY_SCOPE_AGENT); break; }
      __builtin_amdgcn_s_sleep(1);
    }
  }
  BAR();
}
template <int P0, int NP> DI void gather_mod(char* ws, u64 tag, unsigned ldsb, unsigned* tmo, int tid) {
  constexpr int N = (NP + 255) / 256;
  const __amdgpu_buffer_rsrc_t rM = __builtin_amdgcn_make_buffer_rsrc(ws + WS_MODG, 0, 6 * D * 8, 0x00020000);
  const u64 gtag = tag << 16;
  const unsigned thi = (unsigned)(gtag >> 32), tlo = (unsigned)gtag & 0xffff0000u;
  u4 gr[N];
  for (unsigned spins = 0;; ++spins) {
    bool ok = true;
#pragma unroll
    for (int i = 0; i < N; ++i) gr[i] = __builtin_amdgcn_raw_buffer_load_b128(rM, (P0 + (tid + 256 * i < NP ? tid + 256 * i : (tid & 63))) * 16, 0, 16);
#pragma unroll
    for (int i = 0; i < N; ++i) ok &= gr[i][1] == thi && gr[i][3] == thi && (gr[i][0] & 0xffff0000u) == tlo && (gr[i][2] & 0xffff0000u) == tlo;
    if (__all(ok)) break;
    if (spins > SPIN_CAP / 4) { if ((tid & 63) == 0) __hip_atomic_store((gu32*)tmo, 2u, __ATOMIC_RELAXED, __HIP_MEMORY_SCOPE_AGENT); break; }
    __builtin_amdgcn_s_sleep(2);
  }
#pragma unroll
  for (int i = 0; i < N; ++i)
    if (tid + 256 * i < NP) *(LAS unsigned*)(size_t)(ldsb + LDS_MOD + (P0 + tid + 256 * i) * 4) = (gr[i][0] & 0xffffu) | (gr[i][2] << 16);
}
DI u4 gated_residual(const u4 (&v)[8], const u4& res, const u4& g) {
  u4 o;
#pragma unroll
  for (int e = 0; e < 4; ++e) {
    f2 s = bf2(v[0][e]);
#pragma unroll
    for (int j = 1; j < 8; ++j) s += bf2(v[j][e]);
    const unsigned t = pk(bf2(pk(s)) * bf2(g[e]));
    o[e] = pk(bf2(res[e]) + bf2(t));
  }
  return o;
}

constexpr float QSCALE = 0.08838834764831845f * 1.4426950408889634f;
constexpr float THR = 40.0f;
struct QS {
  b4 wlo[2][8], whi[2][8];
  b8 hf[2][4];
  unsigned wb[4];
  unsigned hb[4];
  unsigned ring;
};
template <int PAR, bool ZERO, bool RD, bool DMA, int VM, typename PF>
DI void qstep(QS& s, f16v (&acc)[4][2], const unsigned (&voff)[8], const char* gp, unsigned hoff, PF&& pf) {
  const unsigned sl = s.ring + (PAR ^ 1) * QSLOT;
  auto rd = [&](auto ic) {
    constexpr int I = decltype(ic)::value;
    if constexpr (RD) {
      if constexpr (I < 4) {
        s.hf[PAR ^ 1][I] = ldA(s.hb[2 * (PAR ^ 1) + (I >> 1)] + hoff, (I & 1) * 32 * ROWB);
      } else {
        constexpr int J = I - 4, f = J >> 1, t = J & 1, ks = f >> 2, dt = f & 3;
        const b4 v = ldT(s.wb[dt], (PAR ^ 1) * QSLOT + 4096 * ks + 1024 * t);
        if constexpr (t == 0) s.wlo[PAR ^ 1][f] = v; else s.whi[PAR ^ 1][f] = v;
      }
    }
  };
  auto gap = [&](auto gc) {
    constexpr int G = decltype(gc)::value, ks = G >> 3, dt = (G >> 1) & 3, qb = G & 1, f = ks * 4 + dt;
    mfma32<false, ZERO && ks == 0>(acc[dt][qb], cat(s.wlo[PAR][f], s.whi[PAR][f]), s.hf[PAR][ks * 2 + qb]);
    if constexpr (G == 0 && RD) asm volatile("s_waitcnt vmcnt(%0)" :: "i"(VM) : "memory");
    if constexpr (G <= 6) { rd(IC<3 * G>{}); rd(IC<3 * G + 1>{}); if constexpr (3 * G + 2 < 20) rd(IC<3 * G + 2>{}); }
    if constexpr (DMA) {
      if constexpr (G == 9) { asm volatile("s_waitcnt lgkmcnt(0)" ::: "memory"); setm0(sl + 4096); }
      if constexpr (G >= 10 && G <= 13) { dma<(2 * (G - 10) - 4) * 1024>(voff[2 * (G - 10)], gp); dma<(2 * (G - 10) + 1 - 4) * 1024>(voff[2 * (G - 10) + 1], gp); }
    }
    if constexpr (G == 15) pf();
    GAP();
  };
  UNROLL(16, gap);
}

struct AT {
  f16v S[2][2][2];
  f16v negm[2];
  f16v O[4][2];
  b8 q[2][8];
  b8 P[2][4];
  float m[2], l[2], rs0[2], rs1[2], fct[2], pm0[2], pm1[2];
  float one;
  unsigned kb_[2], vb_[2];
  unsigned voff[8];
  unsigned m0k, m0v;
  const char *gk, *gv;
  bool pend;
};
template <int MODE> DI void qk(f16v& s, const b8& k, const b8& q, const f16v& negm) {
  if constexpr (MODE == 0) asm volatile("v_mfma_f32_32x32x16_bf16 %0, %1, %2, 0" : "=&v"(s) : "v"(k), "a"(q));
  else if constexpr (MODE == 1) asm volatile("v_mfma_f32_32x32x16_bf16 %0, %1, %2, %3" : "=&v"(s) : "v"(k), "a"(q), "v"(negm));
  else asm volatile("v_mfma_f32_32x32x16_bf16 %0, %1, %2, %0" : "+v"(s) : "v"(k), "a"(q));
}
template <int XP, int KB, int E> DI void sm_exp(AT& a) { constexpr int qb = E >> 4, r = E & 15; a.S[XP][KB][qb][r] = __builtin_amdgcn_exp2f(a.S[XP][KB][qb][r]); }
template <int XP, int KB, int E> DI void sm_add(AT& a) {
  constexpr int qb = E >> 4, r = E & 15;
  if constexpr (r & 1) a.rs1[qb] = __builtin_fmaf(a.S[XP][KB][qb][r], a.one, a.rs1[qb]); else a.rs0[qb] += a.S[XP][KB][qb][r];
}
template <int XP, int C> DI void sm_pack(AT& a) {
  constexpr int kb = C >> 4, qb = (C >> 3) & 1, s = (C >> 2) & 1, p = C & 3;
  u4 w = __builtin_bit_cast(u4, a.P[qb][kb * 2 + s]);
  w[p] = pk(a.S[XP][kb][qb][8 * s + 2 * p], a.S[XP][kb][qb][8 * s + 2 * p + 1]);
  a.P[qb][kb * 2 + s] = __builtin_bit_cast(b8, w);
  if constexpr (p == 3) asm volatile("" : "+v"(a.P[qb][kb * 2 + s]));
}
template <int X, bool FIRST, bool DK, bool DV, typename PF> DI void phaseA(AT& a, int t, PF&& pf) {
  constexpr int XP = X ^ 1;
  const unsigned kslot = X * 16384;
  b8 kf[16];
  auto kread = [&](auto ic) { constexpr int I = decltype(ic)::value, ks = I >> 1, kb = I & 1; kf[I] = ldA(a.kb_[ks & 1], kslot + 8192 * kb + 512 * (ks >> 1)); };
  kread(IC<0>{}); kread(IC<1>{}); kread(IC<2>{}); kread(IC<3>{});
  const char* gk = a.gk + (size_t)(t + 1) * 16384; const char* gv = a.gv + (size_t)t * 16384;
  pf();
  auto gap = [&](auto gc) {
    constexpr int G = decltype(gc)::value, ks = G >> 2, kb = (G >> 1) & 1, qb = G & 1, I = G >> 1;
    qk<(ks == 0) ? (FIRST ? 0 : 1) : 2>(a.S[X][kb][qb], kf[I], a.q[qb][ks], a.negm[qb]);
    if constexpr ((G & 1) == 0 && I + 4 < 16) kread(IC<I + 4>{});
    if constexpr (DK) { if constexpr (G == 0) setm0(a.m0k + (X ^ 1) * 16384); if constexpr ((G & 1) && G < 16) dma<((G >> 1) - 4) * 1024>(a.voff[G >> 1], gk); }
    if constexpr (DV) { if constexpr (G == 16) setm0(a.m0v + X * 16384); if constexpr ((G & 1) && G > 16) dma<(((G - 16) >> 1) - 4) * 1024>(a.voff[(G - 16) >> 1], gv); }
    if constexpr (!FIRST) {
      if constexpr (G < 16) { sm_exp<XP, 1, 2 * G>(a); sm_exp<XP, 1, 2 * G + 1>(a); sm_pack<XP, G>(a); }
      else { sm_pack<XP, G>(a); sm_add<XP, 1, 2 * (G - 16)>(a); sm_add<XP, 1, 2 * (G - 16) + 1>(a); }
    }
    GAP();
  };
  UNROLL(32, gap);
  if constexpr (!FIRST) {
#pragma unroll
    for (int qb = 0; qb < 2; ++qb) { a.l[qb] += a.rs0[qb] + a.rs1[qb]; asm volatile("" : "+v"(a.l[qb])); }
  }
}
DI float swap32_max(float x) { auto r = __builtin_amdgcn_permlane32_swap(__float_as_uint(x), __float_as_uint(x), false, false); return fmaxx(__uint_as_float(r[0]), __uint_as_float(r[1])); }
template <int X, bool NOPV, bool ZERO> DI void phaseB(AT& a) {
  const unsigned vslot = (X ^ 1) * 16384;
  b4 vlo[16], vhi[16];
  auto vread = [&](auto ic) {
    constexpr int I = decltype(ic)::value, k4 = I >> 2, db = I & 3;
    vlo[I] = ldT(a.vb_[0], vslot + 2048 * (2 * k4) + 512 * db);
    vhi[I] = ldT(a.vb_[1], vslot + 2048 * (2 * k4 + 1) + 512 * db);
  };
  if constexpr (!NOPV) { vread(IC<0>{}); vread(IC<1>{}); vread(IC<2>{}); }
  float rmax[2];
  auto pv = [&](auto gc) {
    constexpr int G = decltype(gc)::value, I = G >> 1, k4 = I >> 2, db = I & 3, qb = G & 1;
    if constexpr (!NOPV) {
      mfma32<true, ZERO && k4 == 0>(a.O[db][qb], cat(vlo[I], vhi[I]), a.P[qb][k4]);
      if constexpr ((G & 1) == 0 && I + 3 < 16) vread(IC<I + 3>{});
    }
  };
  auto gap1 = [&](auto gc) {
    constexpr int G = decltype(gc)::value;
    pv(gc);
    if constexpr (G >= 4 && G < 12) {
      constexpr int i = G - 4, q = i >> 2, r = 4 * (i & 3);
      if constexpr ((i & 3) == 0) { a.pm0[q] = fmax3(a.S[X][0][q][0], a.S[X][0][q][1], a.S[X][1][q][0]); a.pm1[q] = fmax3(a.S[X][0][q][2], a.S[X][0][q][3], a.S[X][1][q][1]);
                                    a.pm0[q] = fmax3(a.pm0[q], a.S[X][1][q][2], a.S[X][1][q][3]); }
      else { a.pm0[q] = fmax3(a.pm0[q], a.S[X][0][q][r], a.S[X][0][q][r + 1]); a.pm1[q] = fmax3(a.pm1[q], a.S[X][0][q][r + 2], a.S[X][0][q][r + 3]);
             a.pm0[q] = fmax3(a.pm0[q], a.S[X][1][q][r], a.S[X][1][q][r + 1]); a.pm1[q] = fmax3(a.pm1[q], a.S[X][1][q][r + 2], a.S[X][1][q][r + 3]); }
    }
    if constexpr (G == 12 || G == 13) { constexpr int q = G - 12; rmax[q] = swap32_max(fmaxx(a.pm0[q], a.pm1[q])); }
    GAP();
  };
  UNROLL(14, gap1);
  if constexpr (NOPV) {
#pragma unroll
    for (int q = 0; q < 2; ++q) {
      a.m[q] = rmax[q]; a.l[q] = 0.f; a.fct[q] = 1.f;
#pragma unroll
      for (int kb = 0; kb < 2; ++kb)
#pragma unroll
        for (int e = 0; e < 16; ++e) a.S[X][kb][q][e] -= rmax[q];
#pragma unroll
      for (int e = 0; e < 16; ++e) a.negm[q][e] = -rmax[q];
    }
  } else {
    const bool grew = (rmax[0] > THR) | (rmax[1] > THR);
    if (__builtin_expect(__any((int)grew), 0)) {
#pragma unroll
      for (int q = 0; q < 2; ++q) {
        const float dl = fmaxx(rmax[q], 0.f); a.m[q] += dl; a.fct[q] = __builtin_amdgcn_exp2f(-dl);
#pragma unroll
        for (int kb = 0; kb < 2; ++kb)
#pragma unroll
          for (int e = 0; e < 16; ++e) a.S[X][kb][q][e] -= dl;
#pragma unroll
        for (int e = 0; e < 16; ++e) a.negm[q][e] = -a.m[q];
      }
      a.pend = true;
    }
  }
  a.rs0[0] = a.rs0[1] = a.rs1[0] = a.rs1[1] = 0.f;
  GAP();
  auto gap2 = [&](auto gc) {
    constexpr int G = decltype(gc)::value + 14;
    pv(IC<G>{});
    if constexpr (G >= 15) {
      constexpr int e0 = 2 * (G - 15);
      if constexpr (e0 < 32) { sm_exp<X, 0, e0>(a); sm_exp<X, 0, e0 + 1>(a); }
      if constexpr (G >= 16) { sm_add<X, 0, e0 - 2>(a); sm_add<X, 0, e0 - 1>(a); }
    }
    GAP();
  };
  UNROLL(18, gap2);
  asm volatile("" : "+v"(a.S[X][0][0]), "+v"(a.S[X][0][1]), "+v"(a.rs0[0]), "+v"(a.rs0[1]), "+v"(a.rs1[0]), "+v"(a.rs1[1]));
  if constexpr (!NOPV) {
    if (__builtin_expect(a.pend, 0)) {
      asm volatile("s_nop 7\n\ts_nop 7" ::: "memory");
#pragma unroll
      for (int db = 0; db < 4; ++db)
#pragma unroll
        for (int q = 0; q < 2; ++q) {
          asm volatile("" : "+a"(a.O[db][q]));
          f16v o = a.O[db][q];
#pragma unroll
          for (int e = 0; e < 16; ++e) o[e] *= a.fct[q];
          a.O[db][q] = o;
          asm volatile("" : "+a"(a.O[db][q]));
        }
      a.l[0] *= a.fct[0]; a.l[1] *= a.fct[1]; a.pend = false;
      asm volatile("s_nop 1" ::: "memory");
    }
  }
}

struct Params { const u16 *x, *c, *wmod, *wq, *kc, *vc, *wo, *wg, *wu, *wd; u16* out; char* ws; float one; unsigned gap; };

__global__ __launch_bounds__(256, 1) void k_main(Params p) {
  extern __shared__ __attribute__((aligned(16))) char lds[];
  const int tid = threadIdx.x, w = __builtin_amdgcn_readfirstlane(tid >> 6), lane = tid & 63;
  const int j = blockIdx.x & 7, c = blockIdx.x >> 3;
  const unsigned ldsb = (unsigned)(size_t)lds;
  const int hh = lane >> 5, r32 = lane & 31;
  u64* const flags = (u64*)(p.ws + WS_FLAG);
  unsigned* const tmo = (unsigned*)(p.ws + WS_TMO);
  STAMP(0);
  const int modrole = blockIdx.x < 72 ? 1 : (blockIdx.x < 144 ? 2 : 0);
  u4 wm[9]; u16 cval[9];
  if (modrole == 1) {
#pragma unroll
    for (int i = 0; i < 5; ++i) { const int r = i < 4 || tid < 128 ? (tid >> 1) + 128 * i : 0; wm[i] = *(const u4*)(p.wmod + (size_t)r * (6 * D) + blockIdx.x * 16 + (tid & 1) * 8); cval[i] = p.c[r]; }
  } else if (modrole == 2) {
#pragma unroll
    for (int i = 0; i < 9; ++i) { wm[i] = *(const u4*)(p.wmod + (size_t)((tid >> 2) + 64 * i) * (6 * D) + 2 * D + (blockIdx.x - 72) * 32 + (tid & 3) * 8); cval[i] = p.c[(tid >> 2) + 64 * i]; }
  }
  const u64 e0 = __hip_atomic_load((gu64*)(p.ws + WS_EPOCH), __ATOMIC_RELAXED, __HIP_MEMORY_SCOPE_AGENT);
  AT a;
  u64 tag;
  {
    f16v Qt[4][2];
    QS s;
    unsigned voffQ[8];
#pragma unroll
    for (int i = 0; i < 8; ++i) voffQ[i] = (4 * i + (lane >> 4)) * 8192 + 16 * ((lane & 15) ^ ((lane >> 4) << 2)) - (i - 4) * 1024 + 4096;
    const char* gq = (const char*)p.wq + (size_t)(4 * j + w) * 256 - 4096;
    s.ring = __builtin_amdgcn_readfirstlane(ldsb + ABYTES + w * 2 * QSLOT);
    constexpr size_t KS_Q = (size_t)32 * 8192;
    auto issueq = [&](int slot, const char* gp) {
      setm0(s.ring + slot * QSLOT + 4096); asm volatile("s_nop 0");
      dma<-4096>(voffQ[0], gp); dma<-3072>(voffQ[1], gp); dma<-2048>(voffQ[2], gp); dma<-1024>(voffQ[3], gp);
      dma<0>(voffQ[4], gp); dma<1024>(voffQ[5], gp); dma<2048>(voffQ[6], gp); dma<3072>(voffQ[7], gp);
    };
    {
      const int rr = lane >> 3, sub = lane & 7;
      u4 xv[2][9];
#pragma unroll
      for (int ps = 0; ps < 2; ++ps)
#pragma unroll
        for (int i = 0; i < 9; ++i) xv[ps][i] = *(const u4*)(p.x + (size_t)(c * 64 + 16 * w + 8 * ps + rr) * D + (sub + 8 * i) * 8);
      GAP();
      tag = ((u64)__builtin_amdgcn_readfirstlane((unsigned)(e0 >> 32)) << 32 | (u64)__builtin_amdgcn_readfirstlane((unsigned)e0)) + 1;
      const u64 gtag = tag << 16;
      auto modpart = [&](auto nc, auto lc, int col0) {
        constexpr int N = decltype(nc)::value, L = decltype(lc)::value;
        float acc[8] = {};
#pragma unroll
        for (int i = 0; i < N; ++i) {
          const float xc = bf2f(cval[i]);
          float sc = rbf(xc * __builtin_amdgcn_rcpf(1.f + __expf(-xc)));
          if (L == 2 && i == 4 && tid >= 128) sc = 0.f;
#pragma unroll
          for (int e = 0; e < 4; ++e) { acc[2 * e] += sc * bflo(wm[i][e]); acc[2 * e + 1] += sc * bfhi(wm[i][e]); }
        }
        LAS float* red = (LAS float*)(size_t)(ldsb + LDS_X1);
#pragma unroll
        for (int e = 0; e < 8; ++e) {
          float t = acc[e];
          if (L == 2) t += dpp<0x4E>(t);
          t += dpp<0x124>(t); t += dpp<0x128>(t);
          t = swap32_add(swap16_add(t));
          if (lane < L) red[w * 32 + lane * 8 + e] = t;
        }
        asm volatile("s_waitcnt lgkmcnt(0)" ::: "memory");
        BAR();
        if (tid < 8 * L) __hip_atomic_store((gu64*)(p.ws + WS_MODG) + col0 + tid, gtag | f2bf(red[tid] + red[32 + tid] + red[64 + tid] + red[96 + tid]), __ATOMIC_RELAXED, __HIP_MEMORY_SCOPE_AGENT);
      };
      if (modrole == 1) modpart(IC<5>{}, IC<2>{}, blockIdx.x * 16);
      STAMP(20);
      issueq(0, gq); issueq(1, gq + KS_Q);
      {
        const int li = tid < 144 ? tid : tid - 112;
        touch((const char*)p.wq + (size_t)512 * j * 2, (18 * c + (li >> 3)) * 8192 + (li & 7) * 128, __builtin_amdgcn_readfirstlane(ldsb + LDS_DUMMY));
      }
      f2 y[2][9][4];
#pragma unroll
      for (int ps = 0; ps < 2; ++ps) {
        f2 sum = {0.f, 0.f};
#pragma unroll
        for (int i = 0; i < 9; ++i)
#pragma unroll
          for (int e = 0; e < 4; ++e) { y[ps][i][e] = bf2(xv[ps][i][e]); sum += y[ps][i][e]; }
        float sm = sum[0] + sum[1];
        sm = sum8(sm);
        const float mean = sm * (1.f / D);
        f2 sq = {0.f, 0.f};
#pragma unroll
        for (int i = 0; i < 9; ++i)
#pragma unroll
          for (int e = 0; e < 4; ++e) { const f2 dlt = y[ps][i][e] - mean; sq += dlt * dlt; }
        float q = sq[0] + sq[1];
        q = sum8(q);
        const float mr = rbf(mean), rs = __builtin_amdgcn_rcpf(rbf(__builtin_amdgcn_sqrtf(rbf(rbf(q * (1.f / D)) + 1e-5f))));
#pragma unroll
        for (int i = 0; i < 9; ++i)
#pragma unroll
          for (int e = 0; e < 4; ++e) y[ps][i][e] = rb2(rb2(y[ps][i][e] - mr) * rs);
#pragma unroll
        for (int i = 0; i < 9; ++i) asm volatile("" : "+v"(y[ps][i][0]), "+v"(y[ps][i][1]), "+v"(y[ps][i][2]), "+v"(y[ps][i][3]));
      }
      STAMP(21);
      if (modrole == 2) modpart(IC<9>{}, IC<4>{}, 2 * D + (blockIdx.x - 72) * 32);
      gather_mod<0, 576>(p.ws, tag, ldsb, tmo, tid);
      STAMP(22);
      asm volatile("s_waitcnt lgkmcnt(0)" ::: "memory");
      BAR();
      STAMP(23);
#pragma unroll
      for (int i = 0; i < 9; ++i) {
        const u4 cv = *(LAS u4*)(size_t)(ldsb + LDS_MOD + (D + (sub + 8 * i) * 8) * 2), sv = *(LAS u4*)(size_t)(ldsb + LDS_MOD + ((sub + 8 * i) * 8) * 2);
        f2 c1[4];
#pragma unroll
        for (int e = 0; e < 4; ++e) c1[e] = rb2(bf2(cv[e]) + 1.f);
#pragma unroll
        for (int ps = 0; ps < 2; ++ps) {
          const int row = 16 * w + 8 * ps + rr;
          u4 o;
#pragma unroll
          for (int e = 0; e < 4; ++e) o[e] = pk(rb2(y[ps][i][e] * c1[e]) + bf2(sv[e]));
          *(LAS u4*)(size_t)(ldsb + row * ROWB + ((sub ^ ((row >> 1) & 7)) * 16) + i * 128) = o;
        }
      }
    }
    {
      const int blk = (lane >> 4) & 1, q = (lane & 15) >> 2, pp = lane & 3;
#pragma unroll
      for (int dt = 0; dt < 4; ++dt) s.wb[dt] = s.ring + (8 * hh + q) * 256 + 64 * (dt ^ q) + 32 * blk + 16 * (pp & 1) + 8 * (pp >> 1);
#pragma unroll
      for (int i = 0; i < 4; ++i) s.hb[i] = ldsb + r32 * ROWB + (((2 * i + hh) ^ ((r32 >> 1) & 7)) * 16);
    }
    asm volatile("s_waitcnt lgkmcnt(0)" ::: "memory");
    BAR();
    STAMP(1);
#pragma unroll
    for (int f = 0; f < 8; ++f) { s.wlo[0][f] = ldT(s.wb[f & 3], 4096 * (f >> 2)); s.whi[0][f] = ldT(s.wb[f & 3], 4096 * (f >> 2) + 1024); }
#pragma unroll
    for (int i = 0; i < 4; ++i) s.hf[0][i] = ldA(s.hb[i >> 1], (i & 1) * 32 * ROWB);
    asm volatile("s_waitcnt lgkmcnt(0)" ::: "memory");
    issueq(0, gq + 2 * KS_Q);
    auto no = [&]() {};
    auto pfkv = [&]() {
      touch((const char*)(w < 2 ? p.kc : p.vc) + ((size_t)(2 * j + (w & 1)) * SK + (SK - WIN)) * DH * 2, (32 * c + (tid & 31)) * 128, __builtin_amdgcn_readfirstlane(ldsb + LDS_DUMMY));
    };
    qstep<0, true, true, true, 8>(s, Qt, voffQ, gq + 3 * KS_Q, 0, no);
    qstep<1, false, true, true, 8>(s, Qt, voffQ, gq + 4 * KS_Q, 128, pfkv);
    qstep<0, false, true, true, 9>(s, Qt, voffQ, gq + 5 * KS_Q, 128, no);
    qstep<1, false, true, true, 9>(s, Qt, voffQ, gq + 6 * KS_Q, 256, no);
    for (int n = 4; n < 14; n += 2) {
      qstep<0, false, true, true, 8>(s, Qt, voffQ, gq + (size_t)(n + 3) * KS_Q, 128 * ((n + 1) >> 1), no);
      qstep<1, false, true, true, 8>(s, Qt, voffQ, gq + (size_t)(n + 4) * KS_Q, 128 * ((n + 2) >> 1), no);
    }
    qstep<0, false, true, true, 8>(s, Qt, voffQ, gq + (size_t)17 * KS_Q, 128 * 7, no);
    qstep<1, false, true, false, 8>(s, Qt, voffQ, gq, 128 * 8, no);
    qstep<0, false, true, false, 0>(s, Qt, voffQ, gq, 128 * 8, no);
    qstep<1, false, false, false, 0>(s, Qt, voffQ, gq, 0, no);
    STAMP(2);
    {
      const int kvl = w >> 1, half = w & 1;
      const unsigned kr = ldsb + kvl * 65536, vr = kr + 32768;
#pragma unroll
      for (int i = 0; i < 2; ++i) a.kb_[i] = kr + 2048 * (r32 >> 3) + 64 * (r32 & 7) + 16 * ((2 * i + hh) ^ ((r32 >> 2) & 3));
      const int blk = (lane >> 4) & 1, q = (lane & 15) >> 2, pp = lane & 3;
#pragma unroll
      for (int t = 0; t < 2; ++t) a.vb_[t] = vr + 64 * (4 * hh + q) + 16 * ((2 * blk + (pp >> 1)) ^ ((2 * t + hh) & 3)) + 8 * (pp & 1);
      const int st = lane >> 5, r7 = (lane >> 2) & 7, pos = lane & 3;
#pragma unroll
      for (int i = 0; i < 8; ++i) {
        const int rg = 4 * half + (i >> 1);
        a.voff[i] = rg * 2048 + r7 * 256 + (i & 1) * 128 + st * 64 + 16 * (pos ^ ((2 * (rg & 1) + (r7 >> 2)) & 3)) - (i - 4) * 1024 + 4096;
      }
      a.m0k = __builtin_amdgcn_readfirstlane(kr + 8192 * half + 4096); a.m0v = __builtin_amdgcn_readfirstlane(vr + 8192 * half + 4096);
      const size_t kvoff = ((size_t)(2 * j + kvl) * SK + (SK - WIN)) * DH * 2;
      a.gk = (const char*)p.kc + kvoff - 4096; a.gv = (const char*)p.vc + kvoff - 4096;
      a.pend = false; a.one = p.one;
    }
    asm volatile("s_waitcnt lgkmcnt(0)" ::: "memory");
    BAR();
    setm0(a.m0k); asm volatile("s_nop 0");
    dma<-4096>(a.voff[0], a.gk); dma<-3072>(a.voff[1], a.gk); dma<-2048>(a.voff[2], a.gk); dma<-1024>(a.voff[3], a.gk);
    dma<0>(a.voff[4], a.gk); dma<1024>(a.voff[5], a.gk); dma<2048>(a.voff[6], a.gk); dma<3072>(a.voff[7], a.gk);
    asm volatile("s_nop 7" ::: "memory");
#pragma unroll
    for (int dt = 0; dt < 4; ++dt)
#pragma unroll
      for (int qb = 0; qb < 2; ++qb) {
        asm volatile("" : "+v"(Qt[dt][qb]));
#pragma unroll
        for (int s2 = 0; s2 < 2; ++s2) {
          u4 w4;
#pragma unroll
          for (int e = 0; e < 4; ++e) w4[e] = pk((f2){Qt[dt][qb][8 * s2 + 2 * e], Qt[dt][qb][8 * s2 + 2 * e + 1]} * QSCALE);
          a.q[qb][2 * dt + s2] = __builtin_bit_cast(b8, w4);
          asm volatile("" : "+a"(a.q[qb][2 * dt + s2]));
        }
      }
  }
  STAMP(3);
  {
    auto tile_bar = [&]() { asm volatile("s_waitcnt vmcnt(0) lgkmcnt(0)" ::: "memory"); BAR(); };
    auto nopf = [&]() {};
    tile_bar(); phaseA<0, true, true, true>(a, 0, nopf); asm volatile("s_nop 7\n\ts_nop 3" ::: "memory"); GAP(); phaseB<0, true, false>(a);
    tile_bar(); phaseA<1, false, true, true>(a, 1, nopf); phaseB<1, false, true>(a);
    auto pfwo = [&]() {
      const int li = tid < 144 ? tid : tid - 112;
      touch((const char*)p.wo + (size_t)512 * j * ROWB, (16 * c) * ROWB + li * 128, __builtin_amdgcn_readfirstlane(ldsb + LDS_DUMMY));
    };
    for (int t = 2; t < 6; t += 2) {
      tile_bar(); phaseA<0, false, true, true>(a, t, [&]() { if (t == 2) pfwo(); }); phaseB<0, false, false>(a);
      tile_bar(); phaseA<1, false, true, true>(a, t + 1, nopf); phaseB<1, false, false>(a);
    }
    tile_bar(); phaseA<0, false, true, true>(a, 6, nopf); phaseB<0, false, false>(a);
    tile_bar(); phaseA<1, false, false, true>(a, 7, nopf); phaseB<1, false, false>(a);
    { auto f1 = [&](auto ic) { sm_exp<1, 1, decltype(ic)::value>(a); }; UNROLL(32, f1);
      auto f2_ = [&](auto ic) { sm_pack<1, decltype(ic)::value>(a); }; UNROLL(32, f2_);
      auto f3 = [&](auto ic) { sm_add<1, 1, decltype(ic)::value>(a); }; UNROLL(32, f3); }
#pragma unroll
    for (int qb = 0; qb < 2; ++qb) a.l[qb] += a.rs0[qb] + a.rs1[qb];
    tile_bar();
    const unsigned vslot = 16384;
    b4 vlo[16], vhi[16];
#pragma unroll
    for (int I = 0; I < 16; ++I) { vlo[I] = ldT(a.vb_[0], vslot + 2048 * (2 * (I >> 2)) + 512 * (I & 3)); vhi[I] = ldT(a.vb_[1], vslot + 2048 * (2 * (I >> 2) + 1) + 512 * (I & 3)); }
    asm volatile("s_nop 1" ::: "memory"); GAP();
#pragma unroll
    for (int G = 0; G < 32; ++G) { const int I = G >> 1; mfma32<true, false>(a.O[I & 3][G & 1], cat(vlo[I], vhi[I]), a.P[G & 1][I >> 2]); }
  }
  STAMP(4);
  int tid2 = threadIdx.x; asm volatile("" : "+v"(tid2));
  {
  const int tid = tid2, lane = tid2 & 63, hh = lane >> 5, r32 = lane & 31;
  GS s;
  unsigned voffB[9];
#pragma unroll
  for (int i = 0; i < 9; ++i) { const int idx = i * 64 + lane, imm = i < 8 ? (i - 4) * 1024 : 0; voffB[i] = (idx / 18) * ROWB + (idx % 18) * 16 - imm + 4096; }
  s.ring = __builtin_amdgcn_readfirstlane(ldsb + ABYTES + w * 2 * SLOT);
  const int g4 = lane >> 4, cl = lane & 15;
  s.wbase = s.ring + (4 * g4 + ((lane & 15) >> 2)) * 288 + 8 * (lane & 3);
  const unsigned sw = (cl >> 1) & 7;
  const unsigned ab_e = ldsb + cl * ROWB + ((g4 ^ sw) * 16), ab_o = ldsb + cl * ROWB + (((4 + g4) ^ sw) * 16);
  const __amdgpu_buffer_rsrc_t rX = __builtin_amdgcn_make_buffer_rsrc(p.ws + WS_X + (size_t)c * 64 * SLICE, 0, 64 * SLICE, 0x00020000);
  unsigned voffA[9];
#pragma unroll
  for (int i = 0; i < 9; ++i) { const int idx = i * 64 + lane, imm = i < 8 ? (i - 4) * 1024 : 0; voffA[i] = (idx / 18) * (DF * 2) + (idx % 18) * 16 - imm + 4096; }
  const char* gG = (const char*)p.wg + (size_t)(576 * j + 144 * w) * 2 - 4096;
  const char* gU = (const char*)p.wu + (size_t)(576 * j + 144 * w) * 2 - 4096;
  const char* gD = (const char*)p.wd + (size_t)(576 * j) * ROWB + 288 * w - 4096;
  f4 aG[9][4];
  {
    const char* gO = (const char*)p.wo + (size_t)(512 * j) * ROWB + 288 * w - 4096;
    asm volatile("s_waitcnt lgkmcnt(0)" ::: "memory");
    BAR();
    issue_all(s, 0, voffB, gO); issue_all(s, 1, voffB, gO + (size_t)32 * ROWB);
    asm volatile("s_nop 7" ::: "memory");
#pragma unroll
    for (int qb = 0; qb < 2; ++qb) {
      const float il = __builtin_amdgcn_rcpf(swap32_add(a.l[qb]));
      const int row = 32 * qb + r32; const unsigned swr = (r32 >> 1) & 7;
#pragma unroll
      for (int db = 0; db < 4; ++db) {
        asm volatile("" : "+a"(a.O[db][qb]));
        const f16v o = a.O[db][qb];
#pragma unroll
        for (int u = 0; u < 2; ++u) {
          const u4 wv = {pk((f2){o[4 * u], o[4 * u + 1]} * il), pk((f2){o[4 * u + 2], o[4 * u + 3]} * il), pk((f2){o[4 * u + 8], o[4 * u + 9]} * il), pk((f2){o[4 * u + 10], o[4 * u + 11]} * il)};
          *(LAS u4*)(size_t)(ldsb + row * ROWB + (((4 * (4 * w + db) + 2 * u + hh) ^ swr) * 16)) = wv;
        }
      }
    }
    asm volatile("s_waitcnt vmcnt(9) lgkmcnt(0)" ::: "memory");
    BAR();
    STAMP(5);
    read_w(s, 0); read_a(s, 0, ab_e);
    asm volatile("s_waitcnt lgkmcnt(0)" ::: "memory");
    issue_all(s, 0, voffB, gO + (size_t)2 * 32 * ROWB);
    gemm_rows<16, 2>(s, aG, voffB, gO, ab_e, ab_o, voffA, gG, gU, gG);
    STAMP(6);
    settle<0>(aG);
    store_partials(aG, rX, j * SLICE, 8 * SLICE, w, lane);
  }
  {
    int zlr[3], zcc[3];
    publish(flags + (0 * 256 + blockIdx.x) * FLAG_STRIDE, tag);
    STAMP(7);
    const int t3 = tid < 192 ? tid : tid - 64;
    u4 xr[3], gv[3];
#pragma unroll
    for (int i = 0; i < 3; ++i) {
      zdecode(t3 + 192 * i, zlr[i], zcc[i]);
      xr[i] = *(const u4*)(p.x + (size_t)(c * 64 + j * 8 + zlr[i]) * D + zcc[i] * 8);
    }
    gather_mod<576, 1152>(p.ws, tag, ldsb, tmo, tid);
    asm volatile("s_waitcnt lgkmcnt(0)" ::: "memory");
    await8(flags + (0 * 256 + c * 8) * FLAG_STRIDE, tag, tmo, w, lane);
    const int lrow = 2 * w + hh, grow = j * 8 + lrow;
    u2 cbv[3][2], sbv[3][2];
#pragma unroll
    for (int i = 0; i < 3; ++i) {
      gv[i] = *(LAS u4*)(size_t)(ldsb + LDS_MOD + (2 * D + zcc[i] * 8) * 2);
      const int pos = (i < 2 || r32 < 8) ? r32 + 32 * i : r32;
      const int q = pos ^ ((grow >> 1) & 7), k0 = 32 * (q >> 2) + 4 * (q & 3);
#pragma unroll
      for (int h = 0; h < 2; ++h) { cbv[i][h] = *(LAS u2*)(size_t)(ldsb + LDS_MOD + (4 * D + k0 + 16 * h) * 2); sbv[i][h] = *(LAS u2*)(size_t)(ldsb + LDS_MOD + (3 * D + k0 + 16 * h) * 2); }
    }
    STAMP(8);
    {
      u4 v[3][8];
#pragma unroll
      for (int i = 0; i < 3; ++i)
#pragma unroll
        for (int jj = 0; jj < 8; ++jj) v[i][jj] = __builtin_amdgcn_raw_buffer_load_b128(rX, (j * 8 + jj) * SLICE + (t3 + 192 * i) * 16, 0, AUX_X);
#pragma unroll
      for (int i = 0; i < 3; ++i) {
        const u4 x1 = gated_residual(v[i], xr[i], gv[i]);
        if (tid < 192) {
          *(LAS u4*)(size_t)(ldsb + LDS_X1 + (tid + 192 * i) * 16) = x1;
          LAS f4* dst = (LAS f4*)(size_t)(ldsb + (zlr[i] * D + zcc[i] * 8) * 4);
          const f2 a0 = bf2(x1[0]), a1 = bf2(x1[1]), a2 = bf2(x1[2]), a3 = bf2(x1[3]);
          dst[0] = (f4){a0[0], a0[1], a1[0], a1[1]}; dst[1] = (f4){a2[0], a2[1], a3[0], a3[1]};
        }
      }
    }
    asm volatile("s_waitcnt lgkmcnt(0)" ::: "memory");
    BAR();
    STAMP(9);
    {
      f4 v[3][2];
      f2 sum = {0.f, 0.f};
#pragma unroll
      for (int i = 0; i < 3; ++i) {
        const bool on = (i < 2 || r32 < 8);
        const int pos = on ? r32 + 32 * i : r32;
        const int q = pos ^ ((grow >> 1) & 7), k0 = 32 * (q >> 2) + 4 * (q & 3);
#pragma unroll
        for (int h = 0; h < 2; ++h) {
          v[i][h] = *(LAS f4*)(size_t)(ldsb + (lrow * D + k0 + 16 * h) * 4);
          if (on) sum += (f2){v[i][h][0] + v[i][h][1], v[i][h][2] + v[i][h][3]};
        }
      }
      float sm = sum[0] + sum[1];
      sm = sum32(sm);
      const float mean = sm * (1.f / D);
      float q2 = 0.f;
#pragma unroll
      for (int i = 0; i < 3; ++i)
        if (i < 2 || r32 < 8) {
#pragma unroll
          for (int h = 0; h < 2; ++h)
#pragma unroll
            for (int e = 0; e < 4; ++e) { const float dl = v[i][h][e] - mean; q2 += dl * dl; }
        }
      q2 = sum32(q2);
      const float mr = rbf(mean), rs = __builtin_amdgcn_rcpf(rbf(__builtin_amdgcn_sqrtf(rbf(rbf(q2 * (1.f / D)) + 1e-5f))));
      const __amdgpu_buffer_rsrc_t rH = __builtin_amdgcn_make_buffer_rsrc(p.ws + WS_H2 + (size_t)c * ABYTES, 0, ABYTES, 0x00020000);
#pragma unroll
      for (int i = 0; i < 3; ++i) {
        u4 o;
#pragma unroll
        for (int h = 0; h < 2; ++h)
#pragma unroll
          for (int e = 0; e < 2; ++e) {
            unsigned t = pk((f2){v[i][h][2 * e], v[i][h][2 * e + 1]} - mr);
            t = pk(bf2(t) * rs);
            t = pk(bf2(t) * bf2(pk(bf2(cbv[i][h][e]) + 1.f)));
            o[2 * h + e] = pk(bf2(t) + bf2(sbv[i][h][e]));
          }
        if (i < 2 || r32 < 8) __builtin_amdgcn_raw_buffer_store_b128(o, rH, grow * ROWB + (r32 + 32 * i) * 16, 0, 16);
      }
      publish(flags + (1 * 256 + blockIdx.x) * FLAG_STRIDE, tag);
      STAMP(10);
      if (w != 0) {
        const unsigned dummy = __builtin_amdgcn_readfirstlane(ldsb + LDS_DUMMY + 0 * w);
        int li = tid - 64; li = li < 162 ? li : li - 94;
        const int r = 18 * c + li / 9, l = li % 9;
        touch((const char*)p.wg + (size_t)576 * j * 2, r * (DF * 2) + l * 128, dummy);
        touch((const char*)p.wu + (size_t)576 * j * 2, r * (DF * 2) + l * 128, dummy);
      }
      await8(flags + (1 * 256 + c * 8) * FLAG_STRIDE, tag, tmo, w, lane);
      STAMP(11);
      u4 hv[18];
#pragma unroll
      for (int i = 0; i < 18; ++i) hv[i] = __builtin_amdgcn_raw_buffer_load_b128(rH, (i * 256 + tid) * 16, 0, AUX_H);
#pragma unroll
      for (int i = 0; i < 18; ++i) *(LAS u4*)(size_t)(ldsb + (i * 256 + tid) * 16) = hv[i];
    }
    asm volatile("s_waitcnt vmcnt(0) lgkmcnt(0)" ::: "memory");
    BAR();
  }
  STAMP(12);
  {
    constexpr size_t KS_GU = (size_t)32 * DF * 2, KS_D = (size_t)32 * ROWB;
    read_w(s, 0); read_a(s, 0, ab_e);
    asm volatile("s_waitcnt lgkmcnt(0)" ::: "memory");
    issue_all(s, 0, voffA, gG + KS_GU);
    f4 aU[9][4];
    auto no = [&]() {};
    auto pfd = [&]() {
      const unsigned dummy = __builtin_amdgcn_readfirstlane(ldsb + LDS_DUMMY + 0 * w);
      int li = tid; li = li < 162 ? li : li - 94;
      touch((const char*)p.wd + (size_t)576 * j * ROWB, (18 * c) * ROWB + li * 128, dummy);
    };
    auto kpair = [&](auto zc, auto lastc, auto vmc, int k, auto&& pf) {
      constexpr bool Z = decltype(zc)::value, LAST = decltype(lastc)::value; constexpr int VM = decltype(vmc)::value;
      const char* u1 = gU + (size_t)(k + 1) * KS_GU; const char* g2 = gG + (size_t)(k + 2) * KS_GU;
      step<0, 0, Z, true, false, true, 9, 0>(s, aG, voffA, u1, 0, no);
      if constexpr (!LAST) {
        step<1, 0, Z, true, true, true, 9, 8>(s, aU, voffA, g2, ab_o + 128 * (k >> 1), pf);
        step<0, 1, false, true, false, true, VM, 0>(s, aG, voffA, u1 + KS_GU, 0, no);
        step<1, 1, false, true, true, true, VM, 8>(s, aU, voffA, g2 + KS_GU, ab_e + 128 * ((k + 2) >> 1), no);
      } else {
        step<1, 0, Z, true, true, true, 9, 8>(s, aU, voffB, gD, ab_o + 128 * (k >> 1), no);
        step<0, 1, false, true, false, true, 9, 0>(s, aG, voffB, gD + KS_D, 0, no);
        step<1, 1, false, true, false, true, 9, 8>(s, aU, voffB, gD + 2 * KS_D, 0, no);
      }
    };
    kpair(std::true_type{}, std::false_type{}, IC<9>{}, 0, no);
    for (int k = 2; k < 10; k += 2) kpair(std::false_type{}, std::false_type{}, IC<9>{}, k, no);
    kpair(std::false_type{}, std::false_type{}, IC<10>{}, 10, pfd);
    for (int k = 12; k < 16; k += 2) kpair(std::false_type{}, std::false_type{}, IC<9>{}, k, no);
    kpair(std::false_type{}, std::true_type{}, IC<9>{}, 16, no);
    STAMP(13);
    settle<0>(aG); settle<8>(aU);
    u2 act[9][4];
#pragma unroll
    for (int t = 0; t < 9; ++t)
#pragma unroll
      for (int m = 0; m < 4; ++m)
#pragma unroll
        for (int e = 0; e < 2; ++e) {
          const f2 x = {aG[t][m][2 * e], aG[t][m][2 * e + 1]}, u = {aU[t][m][2 * e], aU[t][m][2 * e + 1]};
          const f2 z = x * (x * x * -0.10294324f + -2.3022082f);
          const f2 dn = (f2){__builtin_amdgcn_exp2f(z[0]), __builtin_amdgcn_exp2f(z[1])} + 1.f;
          act[t][m][e] = pk((x * u) * (f2){__builtin_amdgcn_rcpf(dn[0]), __builtin_amdgcn_rcpf(dn[1])});
        }
    asm volatile("s_waitcnt lgkmcnt(0)" ::: "memory");
    BAR();
#pragma unroll
    for (int t = 0; t < 9; ++t) {
      const int tt = 9 * w + t, ks = tt >> 1, ah = tt & 1;
      const unsigned ad = ldsb + cl * ROWB + (((4 * ks + g4) ^ sw) * 16) + 8 * ah;
#pragma unroll
      for (int m = 0; m < 4; ++m) *(LAS u2*)(size_t)(ad + m * 16 * ROWB) = act[t][m];
    }
    asm volatile("s_waitcnt lgkmcnt(0)" ::: "memory");
    BAR();
    STAMP(14);
    read_a(s, 0, ab_e);
    gemm_rows<18, 0>(s, aG, voffB, gD, ab_e, ab_o, voffB, gD, gD, gD);
    STAMP(15);
  }
  {
    int tid3 = threadIdx.x; asm volatile("" : "+v"(tid3));
    const int tid = tid3, lane = tid3 & 63;
    settle<0>(aG);
    store_partials(aG, rX, j * 8 * SLICE, SLICE, w, lane);
    publish(flags + (2 * 256 + blockIdx.x) * FLAG_STRIDE, tag);
    STAMP(16);
    const int t3 = tid < 192 ? tid : tid - 64;
    int zlr[3], zcc[3];
    u4 gv[3], x1[3];
#pragma unroll
    for (int i = 0; i < 3; ++i) {
      zdecode(t3 + 192 * i, zlr[i], zcc[i]);
      gv[i] = *(LAS u4*)(size_t)(ldsb + LDS_MOD + (5 * D + zcc[i] * 8) * 2);
      x1[i] = *(LAS u4*)(size_t)(ldsb + LDS_X1 + (t3 + 192 * i) * 16);
    }
    if (blockIdx.x == 0 && w == 1) {
      for (unsigned spins = 0; spins <= SPIN_CAP; ++spins) {
        bool ok = true;
#pragma unroll
        for (int i = 0; i < 4; ++i) ok &= __hip_atomic_load((gu64*)(flags + (lane + 64 * i) * FLAG_STRIDE), __ATOMIC_RELAXED, __HIP_MEMORY_SCOPE_AGENT) == tag;
        if (__all(ok)) break;
        __builtin_amdgcn_s_sleep(4);
      }
      if (lane == 0) __hip_atomic_store((gu64*)(p.ws + WS_EPOCH), tag, __ATOMIC_RELAXED, __HIP_MEMORY_SCOPE_AGENT);
    }
    await8(flags + (2 * 256 + c * 8) * FLAG_STRIDE, tag, tmo, w, lane);
    STAMP(17);
    u4 v[3][8];
#pragma unroll
    for (int i = 0; i < 3; ++i)
#pragma unroll
      for (int jj = 0; jj < 8; ++jj) v[i][jj] = __builtin_amdgcn_raw_buffer_load_b128(rX, (jj * 8 + j) * SLICE + (t3 + 192 * i) * 16, 0, AUX_X);
#pragma unroll
    for (int i = 0; i < 3; ++i) {
      const u4 o = gated_residual(v[i], x1[i], gv[i]);
      if (tid < 192) *(u4*)(p.out + (size_t)(c * 64 + j * 8 + zlr[i]) * D + zcc[i] * 8) = o;
    }
  }
  STAMP(18);
  }
}

extern "C" void kernel_launch(void* const* d_in, const int* in_sizes, int n_in, void* d_out, int out_size, void* d_ws, size_t ws_size, hipStream_t stream) {
  Params p;
  p.x = (const u16*)d_in[0]; p.kc = (const u16*)d_in[1]; p.vc = (const u16*)d_in[2]; p.wq = (const u16*)d_in[3]; p.wo = (const u16*)d_in[4];
  p.wg = (const u16*)d_in[6]; p.wu = (const u16*)d_in[7]; p.wd = (const u16*)d_in[8];
  p.c = (const u16*)d_in[9]; p.wmod = (const u16*)d_in[10];
  p.out = (u16*)d_out; p.ws = (char*)d_ws; p.one = 1.0f; p.gap = 0u;
  (void)hipFuncSetAttribute((const void*)k_main, hipFuncAttributeMaxDynamicSharedMemorySize, LDS_BYTES);
  k_main<<<256, 256, LDS_BYTES, stream>>>(p);
}
```

```cpp
#include <hip/hip_runtime.h>
#include <stdint.h>
#include <utility>
#include <type_traits>

typedef unsigned short u16;
typedef unsigned long long u64;
typedef __bf16 b2 __attribute__((ext_vector_type(2)));
typedef __bf16 b4 __attribute__((ext_vector_type(4)));
typedef __bf16 b8 __attribute__((ext_vector_type(8)));
typedef float f2 __attribute__((ext_vector_type(2)));
typedef float f4 __attribute__((ext_vector_type(4)));
typedef float f16v __attribute__((ext_vector_type(16)));
typedef unsigned u2 __attribute__((ext_vector_type(2)));
typedef unsigned u4 __attribute__((ext_vector_type(4)));
typedef __attribute__((address_space(1))) unsigned gu32;
typedef __attribute__((address_space(1))) unsigned long long gu64;
#define LAS __attribute__((address_space(3)))
#define DI __device__ __forceinline__
#define GAP() __builtin_amdgcn_sched_barrier(0)
#define BAR() do { asm volatile("" ::: "memory"); __builtin_amdgcn_s_barrier(); asm volatile("" ::: "memory"); } while (0)
template <int V> using IC = std::integral_constant<int, V>;
#define AUX_X 16
#define AUX_H 18
#define PFD_K 14
#define UNROLL(N, F) [&]<int... I_>(std::integer_sequence<int, I_...>) { (F(IC<I_>{}), ...); }(std::make_integer_sequence<int, N>{})

constexpr int S = 2048, D = 576, DH = 128, SK = 2048, WIN = 512, DF = 4608;
constexpr int ROWB = D * 2;
constexpr int ABYTES = 64 * ROWB;
constexpr int SLICE = 8 * ROWB;
constexpr int SLOT = 9216;
constexpr int QSLOT = 8192;
constexpr int LDS_DUMMY = ABYTES + 8 * SLOT;
constexpr int LDS_X1 = LDS_DUMMY + 256;
constexpr int LDS_MOD = LDS_X1 + SLICE;
constexpr int LDS_BYTES = LDS_MOD + 6 * D * 2;
static_assert(LDS_BYTES <= 163840, "LDS");
constexpr size_t WS_EPOCH = 0;
constexpr size_t WS_TMO = 256;
constexpr size_t WS_MODG = 4096;
constexpr size_t WS_MODP = 81920;
constexpr size_t WS_FLAG = 32768;
constexpr int FLAG_STRIDE = 8;
constexpr size_t WS_X = 262144;
constexpr size_t WS_H2 = WS_X + (size_t)32 * 64 * SLICE;
constexpr size_t WS_DBG = 200u << 20;
#define STAMP(i)
#define PST(i)

DI float bf2f(u16 v) { return __uint_as_float(((unsigned)v) << 16); }
DI float bflo(unsigned w) { return __uint_as_float(w << 16); }
DI float bfhi(unsigned w) { return __uint_as_float(w & 0xffff0000u); }
DI f2 bf2(unsigned w) { return (f2){bflo(w), bfhi(w)}; }
DI unsigned pk(float lo, float hi) { f2 v = {lo, hi}; b2 b = __builtin_convertvector(v, b2); return __builtin_bit_cast(unsigned, b); }
DI unsigned pk(f2 v) { b2 b = __builtin_convertvector(v, b2); return __builtin_bit_cast(unsigned, b); }
DI float rbf(float f) { return bflo(pk(f, f)); }
DI f2 rb2(f2 v) { return (f2){__uint_as_float(pk(0.f, v[0])), __uint_as_float(pk(0.f, v[1]))}; }
template <int CTRL> DI float dpp(float v) { return __uint_as_float(__builtin_amdgcn_update_dpp(0u, __float_as_uint(v), CTRL, 0xf, 0xf, false)); }
DI float sum8(float v) { v += dpp<0xB1>(v); v += dpp<0x4E>(v); v += dpp<0x141>(v); return v; }
DI float swap16_add(float x) { auto r = __builtin_amdgcn_permlane16_swap(__float_as_uint(x), __float_as_uint(x), false, false); return __uint_as_float(r[0]) + __uint_as_float(r[1]); }
DI float swap32_add(float x) { auto r = __builtin_amdgcn_permlane32_swap(__float_as_uint(x), __float_as_uint(x), false, false); return __uint_as_float(r[0]) + __uint_as_float(r[1]); }
DI float sum32(float v) { v = sum8(v); v += dpp<0x140>(v); return swap16_add(v); }
DI u16 f2bf(float f) { return (u16)(pk(f, f) & 0xffffu); }
DI float fmaxx(float a, float b) { return __builtin_elementwise_maximum(a, b); }
DI float fmax3(float a, float b, float c) { return __builtin_elementwise_maximum(__builtin_elementwise_maximum(a, b), c); }

DI void zdecode(int z, int& lr, int& cc) {
  const int w = z / 144, y = z - w * 144;
  if (y < 128) { const int tp = y >> 5; lr = (y >> 2) & 7; cc = 18 * w + 4 * tp + (y & 3); }
  else { const int y2 = y - 128; lr = y2 >> 1; cc = 18 * w + 16 + (y2 & 1); }
}

template <int IMM> DI void dma(unsigned voff, const char* g) { asm volatile("global_load_lds_dwordx4 %0, %1 offset:%2" :: "v"(voff), "s"(g), "i"(IMM) : "memory"); }
DI void setm0(unsigned a) { asm volatile("s_mov_b32 m0, %0" :: "s"(a) : "memory"); }
DI void touch(const char* p, unsigned voff, unsigned ldsdummy) {
  asm volatile("s_mov_b32 m0, %0\n\ts_nop 0\n\tglobal_load_lds_dword %1, %2" :: "s"(ldsdummy), "v"(voff), "s"(p) : "memory");
}
DI b4 ldT(unsigned base, int off) { return __builtin_amdgcn_ds_read_tr16_b64_v4bf16((LAS b4*)(size_t)(base + off)); }
DI b8 ldA(unsigned base, int off) { return *(LAS b8*)(size_t)(base + off); }
DI b8 cat(const b4& lo, const b4& hi) { return __builtin_shufflevector(lo, hi, 0, 1, 2, 3, 4, 5, 6, 7); }
template <bool AG, bool ZERO> DI void mfma16(f4& c, const b8& a, const b8& b) {
  if constexpr (AG) {
    if constexpr (ZERO) asm volatile("v_mfma_f32_16x16x32_bf16 %0, %1, %2, 0" : "=a"(c) : "v"(a), "v"(b));
    else asm volatile("v_mfma_f32_16x16x32_bf16 %0, %1, %2, %0" : "+a"(c) : "v"(a), "v"(b));
  } else {
    if constexpr (ZERO) asm volatile("v_mfma_f32_16x16x32_bf16 %0, %1, %2, 0" : "=&v"(c) : "v"(a), "v"(b));
    else asm volatile("v_mfma_f32_16x16x32_bf16 %0, %1, %2, %0" : "+v"(c) : "v"(a), "v"(b));
  }
}
template <bool AG, bool ZERO> DI void mfma32(f16v& c, const b8& a, const b8& b) {
  if constexpr (AG) {
    if constexpr (ZERO) asm volatile("v_mfma_f32_32x32x16_bf16 %0, %1, %2, 0" : "=a"(c) : "v"(a), "v"(b));
    else asm volatile("v_mfma_f32_32x32x16_bf16 %0, %1, %2, %0" : "+a"(c) : "v"(a), "v"(b));
  } else {
    if constexpr (ZERO) asm volatile("v_mfma_f32_32x32x16_bf16 %0, %1, %2, 0" : "=&v"(c) : "v"(a), "v"(b));
    else asm volatile("v_mfma_f32_32x32x16_bf16 %0, %1, %2, %0" : "+v"(c) : "v"(a), "v"(b));
  }
}
template <int NVG> DI void settle(f4 (&acc)[9][4]) {
  asm volatile("s_nop 7\n\ts_nop 7" ::: "memory");
#pragma unroll
  for (int t = 0; t < 9; ++t)
#pragma unroll
    for (int m = 0; m < 4; ++m) { if (t * 4 + m < 36 - NVG) asm volatile("" : "+a"(acc[t][m])); else asm volatile("" : "+v"(acc[t][m])); }
}

struct GS {
  b4 wlo[2][9], whi[2][9];
  b8 af[2][4];
  unsigned wbase, ring;
};
DI void issue_all(const GS& s, int slot, const unsigned (&voff)[9], const char* gp) {
  const unsigned sl = s.ring + slot * SLOT;
  setm0(sl + 4096); asm volatile("s_nop 0");
  dma<-4096>(voff[0], gp); dma<-3072>(voff[1], gp); dma<-2048>(voff[2], gp); dma<-1024>(voff[3], gp);
  dma<0>(voff[4], gp); dma<1024>(voff[5], gp); dma<2048>(voff[6], gp); dma<3072>(voff[7], gp);
  setm0(sl + 8192); asm volatile("s_nop 0"); dma<0>(voff[8], gp);
}
template <int PAR, int AP, bool ZERO, bool RDW, bool RDA, bool DMA, int VM, int NVG, typename PF>
DI void step(GS& s, f4 (&acc)[9][4], const unsigned (&voff)[9], const char* gp, unsigned ab, PF&& pf) {
  const unsigned sl = s.ring + (PAR ^ 1) * SLOT;
  const unsigned wb = s.wbase + (PAR ^ 1) * SLOT;
  auto mm = [&](auto ic) {
    constexpr int I = decltype(ic)::value, t = I >> 2, m = I & 3;
    mfma16<(I < 36 - NVG), ZERO>(acc[t][m], cat(s.wlo[PAR][t], s.whi[PAR][t]), s.af[AP][m]);
  };
  auto rd = [&](auto ic) {
    constexpr int I = decltype(ic)::value;
    if constexpr (I < 4) { if constexpr (RDA) s.af[AP ^ 1][I] = ldA(ab, I * 16 * ROWB); }
    else if constexpr (RDW) {
      if constexpr (((I - 4) & 1) == 0) s.wlo[PAR ^ 1][(I - 4) >> 1] = ldT(wb, 32 * ((I - 4) >> 1));
      else s.whi[PAR ^ 1][(I - 4) >> 1] = ldT(wb, 32 * ((I - 4) >> 1) + 4608);
    }
  };
  auto gap = [&](auto gc) {
    constexpr int G = decltype(gc)::value;
    mm(IC<2 * G>{});
    if constexpr (G == 0 && RDW) asm volatile("s_waitcnt vmcnt(%0)" :: "i"(VM) : "memory");
    if constexpr (G <= 10) rd(IC<2 * G>{});
    if constexpr (DMA) {
      if constexpr (G == 12) { asm volatile("s_waitcnt lgkmcnt(0)" ::: "memory"); setm0(sl + 4096); }
      if constexpr (G >= 13 && G <= 16) dma<(2 * (G - 13) - 4) * 1024>(voff[2 * (G - 13)], gp);
      if constexpr (G == 17) dma<0>(voff[8], gp);
    }
    GAP();
    mm(IC<2 * G + 1>{});
    if constexpr (G <= 10) rd(IC<2 * G + 1>{});
    if constexpr (DMA) {
      if constexpr (G >= 13 && G <= 16) dma<(2 * (G - 13) + 1 - 4) * 1024>(voff[2 * (G - 13) + 1], gp);
      if constexpr (G == 16) setm0(sl + 8192);
    }
    if constexpr (G == 17) pf();
    GAP();
  };
  UNROLL(18, gap);
}
DI void read_w(GS& s, int par) {
#pragma unroll
  for (int t = 0; t < 9; ++t) { s.wlo[par][t] = ldT(s.wbase + par * SLOT, 32 * t); s.whi[par][t] = ldT(s.wbase + par * SLOT, 32 * t + 4608); }
}
DI void read_a(GS& s, int set, unsigned ab) {
#pragma unroll
  for (int m = 0; m < 4; ++m) s.af[set][m] = ldA(ab, m * 16 * ROWB);
}
template <int NS, int NEXT> DI void gemm_rows(GS& s, f4 (&acc)[9][4], const unsigned (&voff)[9], const char* gW, unsigned ab_e, unsigned ab_o,
                                              const unsigned (&nvoff)[9], const char* nx0, const char* nx1, const char* nx2) {
  constexpr size_t KS = (size_t)32 * ROWB;
  auto no = [&]() {};
  step<0, 0, true, true, true, true, 9, 0>(s, acc, voff, gW + 3 * KS, ab_o, no);
  step<1, 1, false, true, true, true, 9, 0>(s, acc, voff, gW + 4 * KS, ab_e + 128, no);
  for (int k = 2; k < NS - 4; k += 2) {
    step<0, 0, false, true, true, true, 9, 0>(s, acc, voff, gW + (size_t)(k + 3) * KS, ab_o + 128 * (k >> 1), no);
    step<1, 1, false, true, true, true, 9, 0>(s, acc, voff, gW + (size_t)(k + 4) * KS, ab_e + 128 * ((k + 2) >> 1), no);
  }
  step<0, 0, false, true, true, true, 9, 0>(s, acc, voff, gW + (size_t)(NS - 1) * KS, ab_o + 128 * ((NS - 4) >> 1), no);
  step<1, 1, false, true, true, NEXT != 0, 9, 0>(s, acc, nvoff, nx0, ab_e + 128 * ((NS - 2) >> 1), no);
  step<0, 0, false, true, true, NEXT != 0, NEXT != 0 ? 9 : 0, 0>(s, acc, nvoff, nx1, ab_o + 128 * ((NS - 2) >> 1), no);
  step<1, 1, false, NEXT == 1, false, NEXT == 1, 9, 0>(s, acc, nvoff, nx2, 0, no);
}
DI void store_partials(const f4 (&acc)[9][4], __amdgpu_buffer_rsrc_t rs, unsigned base, unsigned dstride, int w, int lane) {
  const int G = lane >> 4, c = lane & 15, lr = c & 7, dh = c >> 3;
#pragma unroll
  for (int m = 0; m < 4; ++m) {
    const unsigned dst = base + (2 * m + dh) * dstride + w * 2304 + lr * 64 + (2 * (G & 1) + (G >> 1)) * 16;
#pragma unroll
    for (int tp = 0; tp < 4; ++tp) {
      const f4 a = acc[2 * tp][m], b = acc[2 * tp + 1][m];
      auto r0 = __builtin_amdgcn_permlane16_swap(pk(a[0], a[1]), pk(b[0], b[1]), false, false);
      auto r1 = __builtin_amdgcn_permlane16_swap(pk(a[2], a[3]), pk(b[2], b[3]), false, false);
      __builtin_amdgcn_raw_buffer_store_b128((u4){r0[0], r1[0], r0[1], r1[1]}, rs, dst + tp * 512, 0, 16);
    }
  }
#pragma unroll
  for (int mp = 0; mp < 2; ++mp) {
    const f4 a = acc[8][2 * mp], b = acc[8][2 * mp + 1];
    auto r0 = __builtin_amdgcn_permlane16_swap(pk(a[0], a[1]), pk(b[0], b[1]), false, false);
    auto r1 = __builtin_amdgcn_permlane16_swap(pk(a[2], a[3]), pk(b[2], b[3]), false, false);
    const unsigned dst = base + (2 * (2 * mp + (G & 1)) + dh) * dstride + w * 2304 + 2048 + lr * 32 + (G >> 1) * 16;
    __builtin_amdgcn_raw_buffer_store_b128((u4){r0[0], r1[0], r0[1], r1[1]}, rs, dst, 0, 16);
  }
}

constexpr unsigned SPIN_CAP = 1u << 17;
DI void publish(u64* flag, u64 tag) {
  asm volatile("s_waitcnt vmcnt(0)" ::: "memory");
  BAR();
  if (threadIdx.x == 0) __hip_atomic_store((gu64*)flag, tag, __ATOMIC_RELAXED, __HIP_MEMORY_SCOPE_AGENT);
}
DI void await8(u64* flags8, u64 tag, unsigned* tmo, int w, int lane) {
  if (w == 0) {
    for (unsigned spins = 0;; ++spins) {
      const u64 v = __hip_atomic_load((gu64*)(flags8 + (lane & 7) * FLAG_STRIDE), __ATOMIC_RELAXED, __HIP_MEMORY_SCOPE_AGENT);
      if (__all(v == tag)) break;
      if (spins > SPIN_CAP) { if (lane == 0) __hip_atomic_store((gu32*)tmo, 1u, __ATOMIC_RELAXED, __HIP_MEMORY_SCOPE_AGENT); break; }
      __builtin_amdgcn_s_sleep(1);
    }
  }
  BAR();
}
template <int P0, int NP, bool F32> DI void gather_mod(char* ws, u64 tag, unsigned ldsb, unsigned* tmo, int tid) {
  constexpr int N = (NP + 255) / 256;
  const __amdgpu_buffer_rsrc_t rM = __builtin_amdgcn_make_buffer_rsrc(ws + WS_MODG, 0, 6 * D * 8, 0x00020000);
  const u64 gtag = tag << 16;
  const unsigned thi = (unsigned)(gtag >> 32), tlo = (unsigned)gtag & 0xffff0000u;
  u4 gr[N];
  for (unsigned spins = 0;; ++spins) {
    bool ok = true;
#pragma unroll
    for (int i = 0; i < N; ++i) gr[i] = __builtin_amdgcn_raw_buffer_load_b128(rM, (P0 + (tid + 256 * i < NP ? tid + 256 * i : (tid & 63))) * 16, 0, 16);
#pragma unroll
    for (int i = 0; i < N; ++i) ok &= gr[i][1] == thi && gr[i][3] == thi && (gr[i][0] & 0xffff0000u) == tlo && (gr[i][2] & 0xffff0000u) == tlo;
    if (__all(ok)) break;
    if (spins > SPIN_CAP / 4) { if ((tid & 63) == 0) __hip_atomic_store((gu32*)tmo, 2u, __ATOMIC_RELAXED, __HIP_MEMORY_SCOPE_AGENT); break; }
    __builtin_amdgcn_s_sleep(2);
  }
#pragma unroll
  for (int i = 0; i < N; ++i)
    if (tid + 256 * i < NP) {
      const int idx = P0 + tid + 256 * i;
      if constexpr (F32) {
        f2 v = {bflo(gr[i][0]), bflo(gr[i][2])};
        if (idx >= D / 2) v = rb2(v + 1.f);
        *(LAS f2*)(size_t)(ldsb + LDS_X1 + 1024 + idx * 8) = v;
      } else *(LAS unsigned*)(size_t)(ldsb + LDS_MOD + idx * 4) = (gr[i][0] & 0xffffu) | (gr[i][2] << 16);
    }
}
DI u4 gated_residual(const u4 (&v)[8], const u4& res, const u4& g) {
  u4 o;
#pragma unroll
  for (int e = 0; e < 4; ++e) {
    f2 s = bf2(v[0][e]);
#pragma unroll
    for (int j = 1; j < 8; ++j) s += bf2(v[j][e]);
    const unsigned t = pk(bf2(pk(s)) * bf2(g[e]));
    o[e] = pk(bf2(res[e]) + bf2(t));
  }
  return o;
}

constexpr float QSCALE = 0.08838834764831845f * 1.4426950408889634f;
constexpr float THR = 40.0f;
struct QS {
  b4 wlo[2][8], whi[2][8];
  b8 hf[2][4];
  unsigned wb[4];
  unsigned hb[4];
  unsigned ring;
};
template <int PAR, bool ZERO, bool RD, bool DMA, int VM, typename PF>
DI void qstep(QS& s, f16v (&acc)[4][2], const unsigned (&voff)[8], const char* gp, unsigned hoff, PF&& pf) {
  const unsigned sl = s.ring + (PAR ^ 1) * QSLOT;
  auto rd = [&](auto ic) {
    constexpr int I = decltype(ic)::value;
    if constexpr (RD) {
      if constexpr (I < 4) {
        s.hf[PAR ^ 1][I] = ldA(s.hb[2 * (PAR ^ 1) + (I >> 1)] + hoff, (I & 1) * 32 * ROWB);
      } else {
        constexpr int J = I - 4, f = J >> 1, t = J & 1, ks = f >> 2, dt = f & 3;
        const b4 v = ldT(s.wb[dt], (PAR ^ 1) * QSLOT + 4096 * ks + 1024 * t);
        if constexpr (t == 0) s.wlo[PAR ^ 1][f] = v; else s.whi[PAR ^ 1][f] = v;
      }
    }
  };
  auto gap = [&](auto gc) {
    constexpr int G = decltype(gc)::value, ks = G >> 3, dt = (G >> 1) & 3, qb = G & 1, f = ks * 4 + dt;
    mfma32<false, ZERO && ks == 0>(acc[dt][qb], cat(s.wlo[PAR][f], s.whi[PAR][f]), s.hf[PAR][ks * 2 + qb]);
    if constexpr (G == 0 && RD) asm volatile("s_waitcnt vmcnt(%0)" :: "i"(VM) : "memory");
    if constexpr (G <= 6) { rd(IC<3 * G>{}); rd(IC<3 * G + 1>{}); if constexpr (3 * G + 2 < 20) rd(IC<3 * G + 2>{}); }
    if constexpr (DMA) {
      if constexpr (G == 9) { asm volatile("s_waitcnt lgkmcnt(0)" ::: "memory"); setm0(sl + 4096); }
      if constexpr (G >= 10 && G <= 13) { dma<(2 * (G - 10) - 4) * 1024>(voff[2 * (G - 10)], gp); dma<(2 * (G - 10) + 1 - 4) * 1024>(voff[2 * (G - 10) + 1], gp); }
    }
    if constexpr (G == 15) pf();
    GAP();
  };
  UNROLL(16, gap);
}

struct AT {
  f16v S[2][2][2];
  f16v negm[2];
  f16v O[4][2];
  b8 q[2][8];
  b8 P[2][4];
  float m[2], l[2], rs0[2], rs1[2], fct[2], pm0[2], pm1[2];
  float one;
  unsigned kb_[2], vb_[2];
  unsigned voff[8];
  unsigned m0k, m0v;
  const char *gk, *gv;
  bool pend;
};
template <int MODE> DI void qk(f16v& s, const b8& k, const b8& q, const f16v& negm) {
  if constexpr (MODE == 0) asm volatile("v_mfma_f32_32x32x16_bf16 %0, %1, %2, 0" : "=&v"(s) : "v"(k), "a"(q));
  else if constexpr (MODE == 1) asm volatile("v_mfma_f32_32x32x16_bf16 %0, %1, %2, %3" : "=&v"(s) : "v"(k), "a"(q), "v"(negm));
  else asm volatile("v_mfma_f32_32x32x16_bf16 %0, %1, %2, %0" : "+v"(s) : "v"(k), "a"(q));
}
template <int XP, int KB, int E> DI void sm_exp(AT& a) { constexpr int qb = E >> 4, r = E & 15; a.S[XP][KB][qb][r] = __builtin_amdgcn_exp2f(a.S[XP][KB][qb][r]); }
template <int XP, int KB, int E> DI void sm_add(AT& a) {
  constexpr int qb = E >> 4, r = E & 15;
  if constexpr (r & 1) a.rs1[qb] = __builtin_fmaf(a.S[XP][KB][qb][r], a.one, a.rs1[qb]); else a.rs0[qb] += a.S[XP][KB][qb][r];
}
template <int XP, int C> DI void sm_pack(AT& a) {
  constexpr int kb = C >> 4, qb = (C >> 3) & 1, s = (C >> 2) & 1, p = C & 3;
  u4 w = __builtin_bit_cast(u4, a.P[qb][kb * 2 + s]);
  w[p] = pk(a.S[XP][kb][qb][8 * s + 2 * p], a.S[XP][kb][qb][8 * s + 2 * p + 1]);
  a.P[qb][kb * 2 + s] = __builtin_bit_cast(b8, w);
  if constexpr (p == 3) asm volatile("" : "+v"(a.P[qb][kb * 2 + s]));
}
template <int X, bool FIRST, bool DK, bool DV, typename PF> DI void phaseA(AT& a, int t, PF&& pf) {
  constexpr int XP = X ^ 1;
  const unsigned kslot = X * 16384;
  b8 kf[16];
  auto kread = [&](auto ic) { constexpr int I = decltype(ic)::value, ks = I >> 1, kb = I & 1; kf[I] = ldA(a.kb_[ks & 1], kslot + 8192 * kb + 512 * (ks >> 1)); };
  kread(IC<0>{}); kread(IC<1>{}); kread(IC<2>{}); kread(IC<3>{});
  const char* gk = a.gk + (size_t)(t + 1) * 16384; const char* gv = a.gv + (size_t)t * 16384;
  pf();
  auto gap = [&](auto gc) {
    constexpr int G = decltype(gc)::value, ks = G >> 2, kb = (G >> 1) & 1, qb = G & 1, I = G >> 1;
    qk<(ks == 0) ? (FIRST ? 0 : 1) : 2>(a.S[X][kb][qb], kf[I], a.q[qb][ks], a.negm[qb]);
    if constexpr ((G & 1) == 0 && I + 4 < 16) kread(IC<I + 4>{});
    if constexpr (DK) { if constexpr (G == 0) setm0(a.m0k + (X ^ 1) * 16384); if constexpr ((G & 1) && G < 16) dma<((G >> 1) - 4) * 1024>(a.voff[G >> 1], gk); }
    if constexpr (DV) { if constexpr (G == 16) setm0(a.m0v + X * 16384); if constexpr ((G & 1) && G > 16) dma<(((G - 16) >> 1) - 4) * 1024>(a.voff[(G - 16) >> 1], gv); }
    if constexpr (!FIRST) {
      if constexpr (G < 16) { sm_exp<XP, 1, 2 * G>(a); sm_exp<XP, 1, 2 * G + 1>(a); sm_pack<XP, G>(a); }
      else { sm_pack<XP, G>(a); sm_add<XP, 1, 2 * (G - 16)>(a); sm_add<XP, 1, 2 * (G - 16) + 1>(a); }
    }
    GAP();
  };
  UNROLL(32, gap);
  if constexpr (!FIRST) {
#pragma unroll
    for (int qb = 0; qb < 2; ++qb) { a.l[qb] += a.rs0[qb] + a.rs1[qb]; asm volatile("" : "+v"(a.l[qb])); }
  }
}
DI float swap32_max(float x) { auto r = __builtin_amdgcn_permlane32_swap(__float_as_uint(x), __float_as_uint(x), false, false); return fmaxx(__uint_as_float(r[0]), __uint_as_float(r[1])); }
template <int X, bool NOPV, bool ZERO> DI void phaseB(AT& a) {
  const unsigned vslot = (X ^ 1) * 16384;
  b4 vlo[16], vhi[16];
  auto vread = [&](auto ic) {
    constexpr int I = decltype(ic)::value, k4 = I >> 2, db = I & 3;
    vlo[I] = ldT(a.vb_[0], vslot + 2048 * (2 * k4) + 512 * db);
    vhi[I] = ldT(a.vb_[1], vslot + 2048 * (2 * k4 + 1) + 512 * db);
  };
  if constexpr (!NOPV) { vread(IC<0>{}); vread(IC<1>{}); vread(IC<2>{}); }
  float rmax[2];
  auto pv = [&](auto gc) {
    constexpr int G = decltype(gc)::value, I = G >> 1, k4 = I >> 2, db = I & 3, qb = G & 1;
    if constexpr (!NOPV) {
      mfma32<true, ZERO && k4 == 0>(a.O[db][qb], cat(vlo[I], vhi[I]), a.P[qb][k4]);
      if constexpr ((G & 1) == 0 && I + 3 < 16) vread(IC<I + 3>{});
    }
  };
  auto gap1 = [&](auto gc) {
    constexpr int G = decltype(gc)::value;
    pv(gc);
    if constexpr (G >= 4 && G < 12) {
      constexpr int i = G - 4, q = i >> 2, r = 4 * (i & 3);
      if constexpr ((i & 3) == 0) { a.pm0[q] = fmax3(a.S[X][0][q][0], a.S[X][0][q][1], a.S[X][1][q][0]); a.pm1[q] = fmax3(a.S[X][0][q][2], a.S[X][0][q][3], a.S[X][1][q][1]);
                                    a.pm0[q] = fmax3(a.pm0[q], a.S[X][1][q][2], a.S[X][1][q][3]); }
      else { a.pm0[q] = fmax3(a.pm0[q], a.S[X][0][q][r], a.S[X][0][q][r + 1]); a.pm1[q] = fmax3(a.pm1[q], a.S[X][0][q][r + 2], a.S[X][0][q][r + 3]);
             a.pm0[q] = fmax3(a.pm0[q], a.S[X][1][q][r], a.S[X][1][q][r + 1]); a.pm1[q] = fmax3(a.pm1[q], a.S[X][1][q][r + 2], a.S[X][1][q][r + 3]); }
    }
    if constexpr (G == 12 || G == 13) { constexpr int q = G - 12; rmax[q] = swap32_max(fmaxx(a.pm0[q], a.pm1[q])); }
    GAP();
  };
  UNROLL(14, gap1);
  if constexpr (NOPV) {
#pragma unroll
    for (int q = 0; q < 2; ++q) {
      a.m[q] = rmax[q]; a.l[q] = 0.f; a.fct[q] = 1.f;
#pragma unroll
      for (int kb = 0; kb < 2; ++kb)
#pragma unroll
        for (int e = 0; e < 16; ++e) a.S[X][kb][q][e] -= rmax[q];
#pragma unroll
      for (int e = 0; e < 16; ++e) a.negm[q][e] = -rmax[q];
    }
  } else {
    const bool grew = (rmax[0] > THR) | (rmax[1] > THR);
    if (__builtin_expect(__any((int)grew), 0)) {
#pragma unroll
      for (int q = 0; q < 2; ++q) {
        const float dl = fmaxx(rmax[q], 0.f); a.m[q] += dl; a.fct[q] = __builtin_amdgcn_exp2f(-dl);
#pragma unroll
        for (int kb = 0; kb < 2; ++kb)
#pragma unroll
          for (int e = 0; e < 16; ++e) a.S[X][kb][q][e] -= dl;
#pragma unroll
        for (int e = 0; e < 16; ++e) a.negm[q][e] = -a.m[q];
      }
      a.pend = true;
    }
  }
  a.rs0[0] = a.rs0[1] = a.rs1[0] = a.rs1[1] = 0.f;
  GAP();
  auto gap2 = [&](auto gc) {
    constexpr int G = decltype(gc)::value + 14;
    pv(IC<G>{});
    if constexpr (G >= 15) {
      constexpr int e0 = 2 * (G - 15);
      if constexpr (e0 < 32) { sm_exp<X, 0, e0>(a); sm_exp<X, 0, e0 + 1>(a); }
      if constexpr (G >= 16) { sm_add<X, 0, e0 - 2>(a); sm_add<X, 0, e0 - 1>(a); }
    }
    GAP();
  };
  UNROLL(18, gap2);
  asm volatile("" : "+v"(a.S[X][0][0]), "+v"(a.S[X][0][1]), "+v"(a.rs0[0]), "+v"(a.rs0[1]), "+v"(a.rs1[0]), "+v"(a.rs1[1]));
  if constexpr (!NOPV) {
    if (__builtin_expect(a.pend, 0)) {
      asm volatile("s_nop 7\n\ts_nop 7" ::: "memory");
#pragma unroll
      for (int db = 0; db < 4; ++db)
#pragma unroll
        for (int q = 0; q < 2; ++q) {
          asm volatile("" : "+a"(a.O[db][q]));
          f16v o = a.O[db][q];
#pragma unroll
          for (int e = 0; e < 16; ++e) o[e] *= a.fct[q];
          a.O[db][q] = o;
          asm volatile("" : "+a"(a.O[db][q]));
        }
      a.l[0] *= a.fct[0]; a.l[1] *= a.fct[1]; a.pend = false;
      asm volatile("s_nop 1" ::: "memory");
    }
  }
}

struct Params { const u16 *x, *c, *wmod, *wq, *kc, *vc, *wo, *wg, *wu, *wd; u16* out; char* ws; float one; unsigned gap; };

__global__ __launch_bounds__(256, 1) void k_main(Params p) {
  extern __shared__ __attribute__((aligned(16))) char lds[];
  const int tid = threadIdx.x, w = __builtin_amdgcn_readfirstlane(tid >> 6), lane = tid & 63;
  const int j = blockIdx.x & 7, c = blockIdx.x >> 3;
  const unsigned ldsb = (unsigned)(size_t)lds;
  const int hh = lane >> 5, r32 = lane & 31;
  u64* const flags = (u64*)(p.ws + WS_FLAG);
  unsigned* const tmo = (unsigned*)(p.ws + WS_TMO);
  STAMP(0);
  PST(0);
  QS qs;
  unsigned voffQ[8];
#pragma unroll
  for (int i = 0; i < 8; ++i) voffQ[i] = (4 * i + (lane >> 4)) * 8192 + 16 * ((lane & 15) ^ ((lane >> 4) << 2)) - (i - 4) * 1024 + 4096;
  const char* gq = (const char*)p.wq + (size_t)(4 * j + w) * 256 - 4096;
  qs.ring = __builtin_amdgcn_readfirstlane(ldsb + ABYTES + w * 2 * QSLOT);
  constexpr size_t KS_Q = (size_t)32 * 8192;
  auto issueq = [&](int slot, const char* gp) {
    setm0(qs.ring + slot * QSLOT + 4096); asm volatile("s_nop 0");
    dma<-4096>(voffQ[0], gp); dma<-3072>(voffQ[1], gp); dma<-2048>(voffQ[2], gp); dma<-1024>(voffQ[3], gp);
    dma<0>(voffQ[4], gp); dma<1024>(voffQ[5], gp); dma<2048>(voffQ[6], gp); dma<3072>(voffQ[7], gp);
  };
  const int mg = blockIdx.x % 54, ms = blockIdx.x / 54;
  u4 wm[5]; u16 cval[5];
#pragma unroll
  for (int i = 0; i < 5; ++i) {
    const int r = 144 * (ms & 3) + (i < 4 || tid < 128 ? (tid >> 3) + 32 * i : 0);
    wm[i] = *(const u4*)(p.wmod + (size_t)r * (6 * D) + mg * 64 + (tid & 7) * 8); cval[i] = p.c[r];
  }
  const u64 e0 = __hip_atomic_load((gu64*)(p.ws + WS_EPOCH), __ATOMIC_RELAXED, __HIP_MEMORY_SCOPE_AGENT);
  AT a;
  u64 tag;
  {
    f16v Qt[4][2];
    QS& s = qs;
    {
      const int rr = lane >> 3, sub = lane & 7;
      u4 xv[2][9];
#pragma unroll
      for (int ps = 0; ps < 2; ++ps)
#pragma unroll
        for (int i = 0; i < 9; ++i) xv[ps][i] = *(const u4*)(p.x + (size_t)(c * 64 + 16 * w + 8 * ps + rr) * D + (sub + 8 * i) * 8);
      GAP();
      PST(1);
      tag = ((u64)__builtin_amdgcn_readfirstlane((unsigned)(e0 >> 32)) << 32 | (u64)__builtin_amdgcn_readfirstlane((unsigned)e0)) + 1;
      const u64 gtag = tag << 16;
      if (blockIdx.x < 216) {
        float acc[8] = {};
#pragma unroll
        for (int i = 0; i < 5; ++i) {
          const float xc = bf2f(cval[i]);
          float sc = rbf(xc * __builtin_amdgcn_rcpf(1.f + __expf(-xc)));
          if (i == 4 && tid >= 128) sc = 0.f;
#pragma unroll
          for (int e = 0; e < 4; ++e) { acc[2 * e] += sc * bflo(wm[i][e]); acc[2 * e + 1] += sc * bfhi(wm[i][e]); }
        }
        LAS float* red = (LAS float*)(size_t)(ldsb + LDS_X1);
#pragma unroll
        for (int e = 0; e < 8; ++e) {
          float t = acc[e];
          t += dpp<0x128>(t);
          t = swap32_add(swap16_add(t));
          if (lane < 8) red[w * 64 + lane * 8 + e] = t;
        }
        asm volatile("s_waitcnt lgkmcnt(0)" ::: "memory");
        BAR();
        if (w == 0) {
          const int pr = lane & 31;
          f2 v = {0.f, 0.f};
#pragma unroll
          for (int k = 0; k < 4; ++k) v += *(LAS f2*)(red + k * 64 + 2 * pr);
          const __amdgpu_buffer_rsrc_t rP = __builtin_amdgcn_make_buffer_rsrc(p.ws + WS_MODP + (size_t)mg * 1536, 0, 1536, 0x00020000);
          if (ms != 0) {
            if (lane < 32) __builtin_amdgcn_raw_buffer_store_b128((u4){__float_as_uint(v[0]), __float_as_uint(v[1]), (unsigned)tag, (unsigned)(tag >> 32)}, rP, (ms - 1) * 512 + pr * 16, 0, 16);
          } else {
            u4 gr[3];
            for (unsigned spins = 0;; ++spins) {
              bool ok = true;
#pragma unroll
              for (int k = 0; k < 3; ++k) gr[k] = __builtin_amdgcn_raw_buffer_load_b128(rP, k * 512 + pr * 16, 0, 16);
#pragma unroll
              for (int k = 0; k < 3; ++k) ok &= gr[k][2] == (unsigned)tag && gr[k][3] == (unsigned)(tag >> 32);
              if (__all(ok)) break;
              if (spins > SPIN_CAP) { if (lane == 0) __hip_atomic_store((gu32*)tmo, 3u, __ATOMIC_RELAXED, __HIP_MEMORY_SCOPE_AGENT); break; }
              __builtin_amdgcn_s_sleep(1);
            }
#pragma unroll
            for (int k = 0; k < 3; ++k) v += (f2){__uint_as_float(gr[k][0]), __uint_as_float(gr[k][1])};
            const u64 g0 = gtag | f2bf(v[0]), g1 = gtag | f2bf(v[1]);
            const __amdgpu_buffer_rsrc_t rG = __builtin_amdgcn_make_buffer_rsrc(p.ws + WS_MODG, 0, 6 * D * 8, 0x00020000);
            if (lane < 32) __builtin_amdgcn_raw_buffer_store_b128((u4){(unsigned)g0, (unsigned)(g0 >> 32), (unsigned)g1, (unsigned)(g1 >> 32)}, rG, (mg * 32 + pr) * 16, 0, 16);
          }
        }
      }
      STAMP(20);
      PST(2);
      f2 y[2][9][4];
      {
#pragma unroll
        for (int ps = 0; ps < 2; ++ps) {
          f2 a1 = {0.f, 0.f}, a2 = {0.f, 0.f};
#pragma unroll
          for (int i = 0; i < 9; ++i)
#pragma unroll
            for (int e = 0; e < 4; ++e) { y[ps][i][e] = bf2(xv[ps][i][e]); a1 += y[ps][i][e]; a2 = __builtin_elementwise_fma(y[ps][i][e], y[ps][i][e], a2); }
          float s1 = a1[0] + a1[1], s2 = a2[0] + a2[1];
          s1 = sum8(s1); s2 = sum8(s2);
          if (ps == 0) PST(3);
          if (ps == 1) {
            asm volatile("" : "+v"(s1), "+v"(s2));
            issueq(0, gq); issueq(1, gq + KS_Q);
            {
              const int li = tid < 144 ? tid : tid - 112;
              touch((const char*)p.wq + (size_t)512 * j * 2, (18 * c + (li >> 3)) * 8192 + (li & 7) * 128, __builtin_amdgcn_readfirstlane(ldsb + LDS_DUMMY));
            }
          }
          const float mean = s1 * (1.f / D), var = __builtin_fmaxf(__builtin_fmaf(-mean, mean, s2 * (1.f / D)), 0.f);
          const float mr = rbf(mean), rs = __builtin_amdgcn_rcpf(rbf(__builtin_amdgcn_sqrtf(rbf(rbf(var) + 1e-5f))));
          f2 mr2 = {mr, mr}, rs2 = {rs, rs}; asm volatile("" : "+v"(mr2), "+v"(rs2));
#pragma unroll
          for (int i = 0; i < 9; ++i) {
            f2 t[4];
#pragma unroll
            for (int e = 0; e < 4; ++e) t[e] = y[ps][i][e] - mr2;
#pragma unroll
            for (int e = 0; e < 4; ++e) t[e] = rb2(t[e]);
#pragma unroll
            for (int e = 0; e < 4; ++e) t[e] = t[e] * rs2;
#pragma unroll
            for (int e = 0; e < 4; ++e) y[ps][i][e] = rb2(t[e]);
            asm volatile("" : "+v"(y[ps][i][0]), "+v"(y[ps][i][1]), "+v"(y[ps][i][2]), "+v"(y[ps][i][3]));
          }
        }
      }
      STAMP(21);
      PST(4);
      gather_mod<0, 576, true>(p.ws, tag, ldsb, tmo, tid);
      STAMP(22);
      PST(5);
      asm volatile("s_waitcnt lgkmcnt(0)" ::: "memory");
      BAR();
      STAMP(23);
      PST(6);
      {
        const unsigned tb = ldsb + LDS_X1 + 1024 + sub * 32;
        f4 cst[2][4];
        auto ldc = [&](int i, f4 (&d)[4]) { d[0] = *(LAS f4*)(size_t)(tb + i * 256); d[1] = *(LAS f4*)(size_t)(tb + i * 256 + 16); d[2] = *(LAS f4*)(size_t)(tb + i * 256 + D * 4); d[3] = *(LAS f4*)(size_t)(tb + i * 256 + D * 4 + 16); };
        ldc(0, cst[0]);
        const unsigned ad0 = ldsb + (16 * w + rr) * ROWB + ((sub ^ ((rr >> 1) & 7)) * 16);
        const unsigned ad1 = ldsb + (16 * w + 8 + rr) * ROWB + ((sub ^ (((rr >> 1) + 4) & 7)) * 16);
#pragma unroll
        for (int i = 0; i < 9; ++i) {
          if (i + 1 < 9) ldc(i + 1, cst[(i + 1) & 1]);
          GAP();
          const f4 (&k)[4] = cst[i & 1];
          const f2 sh[4] = {{k[0][0], k[0][1]}, {k[0][2], k[0][3]}, {k[1][0], k[1][1]}, {k[1][2], k[1][3]}}, cc[4] = {{k[2][0], k[2][1]}, {k[2][2], k[2][3]}, {k[3][0], k[3][1]}, {k[3][2], k[3][3]}};
          f2 t[2][4]; u4 o[2];
#pragma unroll
          for (int ps = 0; ps < 2; ++ps)
#pragma unroll
            for (int e = 0; e < 4; ++e) t[ps][e] = y[ps][i][e] * cc[e];
#pragma unroll
          for (int ps = 0; ps < 2; ++ps)
#pragma unroll
            for (int e = 0; e < 4; ++e) t[ps][e] = rb2(t[ps][e]);
#pragma unroll
          for (int ps = 0; ps < 2; ++ps)
#pragma unroll
            for (int e = 0; e < 4; ++e) t[ps][e] = t[ps][e] + sh[e];
#pragma unroll
          for (int ps = 0; ps < 2; ++ps)
#pragma unroll
            for (int e = 0; e < 4; ++e) o[ps][e] = pk(t[ps][e]);
          *(LAS u4*)(size_t)(ad0 + i * 128) = o[0];
          *(LAS u4*)(size_t)(ad1 + i * 128) = o[1];
          GAP();
        }
      }
    }
    {
      const int blk = (lane >> 4) & 1, q = (lane & 15) >> 2, pp = lane & 3;
#pragma unroll
      for (int dt = 0; dt < 4; ++dt) s.wb[dt] = s.ring + (8 * hh + q) * 256 + 64 * (dt ^ q) + 32 * blk + 16 * (pp & 1) + 8 * (pp >> 1);
#pragma unroll
      for (int i = 0; i < 4; ++i) s.hb[i] = ldsb + r32 * ROWB + (((2 * i + hh) ^ ((r32 >> 1) & 7)) * 16);
    }
    asm volatile("s_waitcnt lgkmcnt(0)" ::: "memory");
    BAR();
    STAMP(1);
    PST(7);
#pragma unroll
    for (int f = 0; f < 8; ++f) { s.wlo[0][f] = ldT(s.wb[f & 3], 4096 * (f >> 2)); s.whi[0][f] = ldT(s.wb[f & 3], 4096 * (f >> 2) + 1024); }
#pragma unroll
    for (int i = 0; i < 4; ++i) s.hf[0][i] = ldA(s.hb[i >> 1], (i & 1) * 32 * ROWB);
    asm volatile("s_waitcnt lgkmcnt(0)" ::: "memory");
    issueq(0, gq + 2 * KS_Q);
    auto no = [&]() {};
    auto pfkv = [&]() {
      touch((const char*)(w < 2 ? p.kc : p.vc) + ((size_t)(2 * j + (w & 1)) * SK + (SK - WIN)) * DH * 2, (32 * c + (tid & 31)) * 128, __builtin_amdgcn_readfirstlane(ldsb + LDS_DUMMY));
    };
    qstep<0, true, true, true, 8>(s, Qt, voffQ, gq + 3 * KS_Q, 0, no);
    qstep<1, false, true, true, 8>(s, Qt, voffQ, gq + 4 * KS_Q, 128, pfkv);
    qstep<0, false, true, true, 9>(s, Qt, voffQ, gq + 5 * KS_Q, 128, no);
    qstep<1, false, true, true, 9>(s, Qt, voffQ, gq + 6 * KS_Q, 256, no);
    for (int n = 4; n < 14; n += 2) {
      qstep<0, false, true, true, 8>(s, Qt, voffQ, gq + (size_t)(n + 3) * KS_Q, 128 * ((n + 1) >> 1), no);
      qstep<1, false, true, true, 8>(s, Qt, voffQ, gq + (size_t)(n + 4) * KS_Q, 128 * ((n + 2) >> 1), no);
    }
    qstep<0, false, true, true, 8>(s, Qt, voffQ, gq + (size_t)17 * KS_Q, 128 * 7, no);
    qstep<1, false, true, false, 8>(s, Qt, voffQ, gq, 128 * 8, no);
    qstep<0, false, true, false, 0>(s, Qt, voffQ, gq, 128 * 8, no);
    qstep<1, false, false, false, 0>(s, Qt, voffQ, gq, 0, no);
    STAMP(2);
    PST(8);
    {
      const int kvl = w >> 1, half = w & 1;
      const unsigned kr = ldsb + kvl * 65536, vr = kr + 32768;
#pragma unroll
      for (int i = 0; i < 2; ++i) a.kb_[i] = kr + 2048 * (r32 >> 3) + 64 * (r32 & 7) + 16 * ((2 * i + hh) ^ ((r32 >> 2) & 3));
      const int blk = (lane >> 4) & 1, q = (lane & 15) >> 2, pp = lane & 3;
#pragma unroll
      for (int t = 0; t < 2; ++t) a.vb_[t] = vr + 64 * (4 * hh + q) + 16 * ((2 * blk + (pp >> 1)) ^ ((2 * t + hh) & 3)) + 8 * (pp & 1);
      const int st = lane >> 5, r7 = (lane >> 2) & 7, pos = lane & 3;
#pragma unroll
      for (int i = 0; i < 8; ++i) {
        const int rg = 4 * half + (i >> 1);
        a.voff[i] = rg * 2048 + r7 * 256 + (i & 1) * 128 + st * 64 + 16 * (pos ^ ((2 * (rg & 1) + (r7 >> 2)) & 3)) - (i - 4) * 1024 + 4096;
      }
      a.m0k = __builtin_amdgcn_readfirstlane(kr + 8192 * half + 4096); a.m0v = __builtin_amdgcn_readfirstlane(vr + 8192 * half + 4096);
      const size_t kvoff = ((size_t)(2 * j + kvl) * SK + (SK - WIN)) * DH * 2;
      a.gk = (const char*)p.kc + kvoff - 4096; a.gv = (const char*)p.vc + kvoff - 4096;
      a.pend = false; a.one = p.one;
    }
    asm volatile("s_waitcnt lgkmcnt(0)" ::: "memory");
    BAR();
    setm0(a.m0k); asm volatile("s_nop 0");
    dma<-4096>(a.voff[0], a.gk); dma<-3072>(a.voff[1], a.gk); dma<-2048>(a.voff[2], a.gk); dma<-1024>(a.voff[3], a.gk);
    dma<0>(a.voff[4], a.gk); dma<1024>(a.voff[5], a.gk); dma<2048>(a.voff[6], a.gk); dma<3072>(a.voff[7], a.gk);
    asm volatile("s_nop 7" ::: "memory");
#pragma unroll
    for (int dt = 0; dt < 4; ++dt)
#pragma unroll
      for (int qb = 0; qb < 2; ++qb) {
        asm volatile("" : "+v"(Qt[dt][qb]));
#pragma unroll
        for (int s2 = 0; s2 < 2; ++s2) {
          u4 w4;
#pragma unroll
          for (int e = 0; e < 4; ++e) w4[e] = pk((f2){Qt[dt][qb][8 * s2 + 2 * e], Qt[dt][qb][8 * s2 + 2 * e + 1]} * QSCALE);
          a.q[qb][2 * dt + s2] = __builtin_bit_cast(b8, w4);
          asm volatile("" : "+a"(a.q[qb][2 * dt + s2]));
        }
      }
  }
  STAMP(3);
  {
    auto tile_bar = [&]() { asm volatile("s_waitcnt vmcnt(0) lgkmcnt(0)" ::: "memory"); BAR(); };
    auto nopf = [&]() {};
    tile_bar(); phaseA<0, true, true, true>(a, 0, nopf); asm volatile("s_nop 7\n\ts_nop 3" ::: "memory"); GAP(); phaseB<0, true, false>(a);
    tile_bar(); phaseA<1, false, true, true>(a, 1, nopf); phaseB<1, false, true>(a);
    auto pfwo = [&]() {
      const int li = tid < 144 ? tid : tid - 112;
      touch((const char*)p.wo + (size_t)512 * j * ROWB, (16 * c) * ROWB + li * 128, __builtin_amdgcn_readfirstlane(ldsb + LDS_DUMMY));
    };
    for (int t = 2; t < 6; t += 2) {
      tile_bar(); phaseA<0, false, true, true>(a, t, [&]() { if (t == 2) pfwo(); }); phaseB<0, false, false>(a);
      tile_bar(); phaseA<1, false, true, true>(a, t + 1, nopf); phaseB<1, false, false>(a);
    }
    tile_bar(); phaseA<0, false, true, true>(a, 6, nopf); phaseB<0, false, false>(a);
    tile_bar(); phaseA<1, false, false, true>(a, 7, nopf); phaseB<1, false, false>(a);
    { auto f1 = [&](auto ic) { sm_exp<1, 1, decltype(ic)::value>(a); }; UNROLL(32, f1);
      auto f2_ = [&](auto ic) { sm_pack<1, decltype(ic)::value>(a); }; UNROLL(32, f2_);
      auto f3 = [&](auto ic) { sm_add<1, 1, decltype(ic)::value>(a); }; UNROLL(32, f3); }
#pragma unroll
    for (int qb = 0; qb < 2; ++qb) a.l[qb] += a.rs0[qb] + a.rs1[qb];
    tile_bar();
    const unsigned vslot = 16384;
    b4 vlo[16], vhi[16];
#pragma unroll
    for (int I = 0; I < 16; ++I) { vlo[I] = ldT(a.vb_[0], vslot + 2048 * (2 * (I >> 2)) + 512 * (I & 3)); vhi[I] = ldT(a.vb_[1], vslot + 2048 * (2 * (I >> 2) + 1) + 512 * (I & 3)); }
    asm volatile("s_nop 1" ::: "memory"); GAP();
#pragma unroll
    for (int G = 0; G < 32; ++G) { const int I = G >> 1; mfma32<true, false>(a.O[I & 3][G & 1], cat(vlo[I], vhi[I]), a.P[G & 1][I >> 2]); }
  }
  STAMP(4);
  int tid2 = threadIdx.x; asm volatile("" : "+v"(tid2));
  {
  const int tid = tid2, lane = tid2 & 63, hh = lane >> 5, r32 = lane & 31;
  GS s;
  unsigned voffB[9];
#pragma unroll
  for (int i = 0; i < 9; ++i) { const int idx = i * 64 + lane, imm = i < 8 ? (i - 4) * 1024 : 0; voffB[i] = (idx / 18) * ROWB + (idx % 18) * 16 - imm + 4096; }
  s.ring = __builtin_amdgcn_readfirstlane(ldsb + ABYTES + w * 2 * SLOT);
  const int g4 = lane >> 4, cl = lane & 15;
  s.wbase = s.ring + (4 * g4 + ((lane & 15) >> 2)) * 288 + 8 * (lane & 3);
  const unsigned sw = (cl >> 1) & 7;
  const unsigned ab_e = ldsb + cl * ROWB + ((g4 ^ sw) * 16), ab_o = ldsb + cl * ROWB + (((4 + g4) ^ sw) * 16);
  const __amdgpu_buffer_rsrc_t rX = __builtin_amdgcn_make_buffer_rsrc(p.ws + WS_X + (size_t)c * 64 * SLICE, 0, 64 * SLICE, 0x00020000);
  unsigned voffA[9];
#pragma unroll
  for (int i = 0; i < 9; ++i) { const int idx = i * 64 + lane, imm = i < 8 ? (i - 4) * 1024 : 0; voffA[i] = (idx / 18) * (DF * 2) + (idx % 18) * 16 - imm + 4096; }
  const char* gG = (const char*)p.wg + (size_t)(576 * j + 144 * w) * 2 - 4096;
  const char* gU = (const char*)p.wu + (size_t)(576 * j + 144 * w) * 2 - 4096;
  const char* gD = (const char*)p.wd + (size_t)(576 * j) * ROWB + 288 * w - 4096;
  f4 aG[9][4];
  {
    const char* gO = (const char*)p.wo + (size_t)(512 * j) * ROWB + 288 * w - 4096;
    asm volatile("s_waitcnt lgkmcnt(0)" ::: "memory");
    BAR();
    issue_all(s, 0, voffB, gO); issue_all(s, 1, voffB, gO + (size_t)32 * ROWB);
    asm volatile("s_nop 7" ::: "memory");
#pragma unroll
    for (int qb = 0; qb < 2; ++qb) {
      const float il = __builtin_amdgcn_rcpf(swap32_add(a.l[qb]));
      const int row = 32 * qb + r32; const unsigned swr = (r32 >> 1) & 7;
#pragma unroll
      for (int db = 0; db < 4; ++db) {
        asm volatile("" : "+a"(a.O[db][qb]));
        const f16v o = a.O[db][qb];
#pragma unroll
        for (int u = 0; u < 2; ++u) {
          const u4 wv = {pk((f2){o[4 * u], o[4 * u + 1]} * il), pk((f2){o[4 * u + 2], o[4 * u + 3]} * il), pk((f2){o[4 * u + 8], o[4 * u + 9]} * il), pk((f2){o[4 * u + 10], o[4 * u + 11]} * il)};
          *(LAS u4*)(size_t)(ldsb + row * ROWB + (((4 * (4 * w + db) + 2 * u + hh) ^ swr) * 16)) = wv;
        }
      }
    }
    asm volatile("s_waitcnt vmcnt(9) lgkmcnt(0)" ::: "memory");
    BAR();
    STAMP(5);
    read_w(s, 0); read_a(s, 0, ab_e);
    asm volatile("s_waitcnt lgkmcnt(0)" ::: "memory");
    issue_all(s, 0, voffB, gO + (size_t)2 * 32 * ROWB);
    gemm_rows<16, 2>(s, aG, voffB, gO, ab_e, ab_o, voffA, gG, gU, gG);
    STAMP(6);
    settle<0>(aG);
    store_partials(aG, rX, j * SLICE, 8 * SLICE, w, lane);
  }
  {
    int zlr[3], zcc[3];
    publish(flags + (0 * 256 + blockIdx.x) * FLAG_STRIDE, tag);
    STAMP(7);
    const int t3 = tid < 192 ? tid : tid - 64;
    u4 xr[3], gv[3];
#pragma unroll
    for (int i = 0; i < 3; ++i) {
      zdecode(t3 + 192 * i, zlr[i], zcc[i]);
      xr[i] = *(const u4*)(p.x + (size_t)(c * 64 + j * 8 + zlr[i]) * D + zcc[i] * 8);
    }
    gather_mod<576, 1152, false>(p.ws, tag, ldsb, tmo, tid);
    asm volatile("s_waitcnt lgkmcnt(0)" ::: "memory");
    await8(flags + (0 * 256 + c * 8) * FLAG_STRIDE, tag, tmo, w, lane);
    const int lrow = 2 * w + hh, grow = j * 8 + lrow;
    u2 cbv[3][2], sbv[3][2];
#pragma unroll
    for (int i = 0; i < 3; ++i) {
      gv[i] = *(LAS u4*)(size_t)(ldsb + LDS_MOD + (2 * D + zcc[i] * 8) * 2);
      const int pos = (i < 2 || r32 < 8) ? r32 + 32 * i : r32;
      const int q = pos ^ ((grow >> 1) & 7), k0 = 32 * (q >> 2) + 4 * (q & 3);
#pragma unroll
      for (int h = 0; h < 2; ++h) { cbv[i][h] = *(LAS u2*)(size_t)(ldsb + LDS_MOD + (4 * D + k0 + 16 * h) * 2); sbv[i][h] = *(LAS u2*)(size_t)(ldsb + LDS_MOD + (3 * D + k0 + 16 * h) * 2); }
    }
    STAMP(8);
    {
      u4 v[3][8];
#pragma unroll
      for (int i = 0; i < 3; ++i)
#pragma unroll
        for (int jj = 0; jj < 8; ++jj) v[i][jj] = __builtin_amdgcn_raw_buffer_load_b128(rX, (j * 8 + jj) * SLICE + (t3 + 192 * i) * 16, 0, AUX_X);
#pragma unroll
      for (int i = 0; i < 3; ++i) {
        const u4 x1 = gated_residual(v[i], xr[i], gv[i]);
        if (tid < 192) {
          *(LAS u4*)(size_t)(ldsb + LDS_X1 + (tid + 192 * i) * 16) = x1;
          LAS f4* dst = (LAS f4*)(size_t)(ldsb + (zlr[i] * D + zcc[i] * 8) * 4);
          const f2 a0 = bf2(x1[0]), a1 = bf2(x1[1]), a2 = bf2(x1[2]), a3 = bf2(x1[3]);
          dst[0] = (f4){a0[0], a0[1], a1[0], a1[1]}; dst[1] = (f4){a2[0], a2[1], a3[0], a3[1]};
        }
      }
    }
    asm volatile("s_waitcnt lgkmcnt(0)" ::: "memory");
    BAR();
    STAMP(9);
    {
      f4 v[3][2];
      f2 sum = {0.f, 0.f};
#pragma unroll
      for (int i = 0; i < 3; ++i) {
        const bool on = (i < 2 || r32 < 8);
        const int pos = on ? r32 + 32 * i : r32;
        const int q = pos ^ ((grow >> 1) & 7), k0 = 32 * (q >> 2) + 4 * (q & 3);
#pragma unroll
        for (int h = 0; h < 2; ++h) {
          v[i][h] = *(LAS f4*)(size_t)(ldsb + (lrow * D + k0 + 16 * h) * 4);
          if (on) sum += (f2){v[i][h][0] + v[i][h][1], v[i][h][2] + v[i][h][3]};
        }
      }
      float sm = sum[0] + sum[1];
      sm = sum32(sm);
      const float mean = sm * (1.f / D);
      float q2 = 0.f;
#pragma unroll
      for (int i = 0; i < 3; ++i)
        if (i < 2 || r32 < 8) {
#pragma unroll
          for (int h = 0; h < 2; ++h)
#pragma unroll
            for (int e = 0; e < 4; ++e) { const float dl = v[i][h][e] - mean; q2 += dl * dl; }
        }
      q2 = sum32(q2);
      const float mr = rbf(mean), rs = __builtin_amdgcn_rcpf(rbf(__builtin_amdgcn_sqrtf(rbf(rbf(q2 * (1.f / D)) + 1e-5f))));
      const __amdgpu_buffer_rsrc_t rH = __builtin_amdgcn_make_buffer_rsrc(p.ws + WS_H2 + (size_t)c * ABYTES, 0, ABYTES, 0x00020000);
#pragma unroll
      for (int i = 0; i < 3; ++i) {
        u4 o;
#pragma unroll
        for (int h = 0; h < 2; ++h)
#pragma unroll
          for (int e = 0; e < 2; ++e) {
            unsigned t = pk((f2){v[i][h][2 * e], v[i][h][2 * e + 1]} - mr);
            t = pk(bf2(t) * rs);
            t = pk(bf2(t) * bf2(pk(bf2(cbv[i][h][e]) + 1.f)));
            o[2 * h + e] = pk(bf2(t) + bf2(sbv[i][h][e]));
          }
        if (i < 2 || r32 < 8) __builtin_amdgcn_raw_buffer_store_b128(o, rH, grow * ROWB + (r32 + 32 * i) * 16, 0, 16);
      }
      publish(flags + (1 * 256 + blockIdx.x) * FLAG_STRIDE, tag);
      STAMP(10);
      if (w != 0) {
        const unsigned dummy = __builtin_amdgcn_readfirstlane(ldsb + LDS_DUMMY + 0 * w);
        int li = tid - 64; li = li < 162 ? li : li - 94;
        const int r = 18 * c + li / 9, l = li % 9;
        touch((const char*)p.wg + (size_t)576 * j * 2, r * (DF * 2) + l * 128, dummy);
        touch((const char*)p.wu + (size_t)576 * j * 2, r * (DF * 2) + l * 128, dummy);
      }
      await8(flags + (1 * 256 + c * 8) * FLAG_STRIDE, tag, tmo, w, lane);
      STAMP(11);
      u4 hv[18];
#pragma unroll
      for (int i = 0; i < 18; ++i) hv[i] = __builtin_amdgcn_raw_buffer_load_b128(rH, (i * 256 + tid) * 16, 0, AUX_H);
#pragma unroll
      for (int i = 0; i < 18; ++i) *(LAS u4*)(size_t)(ldsb + (i * 256 + tid) * 16) = hv[i];
    }
    asm volatile("s_waitcnt vmcnt(0) lgkmcnt(0)" ::: "memory");
    BAR();
  }
  STAMP(12);
  {
    constexpr size_t KS_GU = (size_t)32 * DF * 2, KS_D = (size_t)32 * ROWB;
    read_w(s, 0); read_a(s, 0, ab_e);
    asm volatile("s_waitcnt lgkmcnt(0)" ::: "memory");
    issue_all(s, 0, voffA, gG + KS_GU);
    f4 aU[9][4];
    auto no = [&]() {};
    auto pfd = [&]() {
      const unsigned dummy = __builtin_amdgcn_readfirstlane(ldsb + LDS_DUMMY + 0 * w);
      int li = tid; li = li < 162 ? li : li - 94;
      touch((const char*)p.wd + (size_t)576 * j * ROWB, (18 * c) * ROWB + li * 128, dummy);
    };
    auto kpair = [&](auto zc, auto lastc, auto vmc, int k, auto&& pf) {
      constexpr bool Z = decltype(zc)::value, LAST = decltype(lastc)::value; constexpr int VM = decltype(vmc)::value;
      const char* u1 = gU + (size_t)(k + 1) * KS_GU; const char* g2 = gG + (size_t)(k + 2) * KS_GU;
      step<0, 0, Z, true, false, true, 9, 0>(s, aG, voffA, u1, 0, no);
      if constexpr (!LAST) {
        step<1, 0, Z, true, true, true, 9, 8>(s, aU, voffA, g2, ab_o + 128 * (k >> 1), pf);
        step<0, 1, false, true, false, true, VM, 0>(s, aG, voffA, u1 + KS_GU, 0, no);
        step<1, 1, false, true, true, true, VM, 8>(s, aU, voffA, g2 + KS_GU, ab_e + 128 * ((k + 2) >> 1), no);
      } else {
        step<1, 0, Z, true, true, true, 9, 8>(s, aU, voffB, gD, ab_o + 128 * (k >> 1), no);
        step<0, 1, false, true, false, true, 9, 0>(s, aG, voffB, gD + KS_D, 0, no);
        step<1, 1, false, true, false, true, 9, 8>(s, aU, voffB, gD + 2 * KS_D, 0, no);
      }
    };
    kpair(std::true_type{}, std::false_type{}, IC<9>{}, 0, no);
    for (int k = 2; k < PFD_K; k += 2) kpair(std::false_type{}, std::false_type{}, IC<9>{}, k, no);
    kpair(std::false_type{}, std::false_type{}, IC<10>{}, PFD_K, pfd);
    for (int k = PFD_K + 2; k < 16; k += 2) kpair(std::false_type{}, std::false_type{}, IC<9>{}, k, no);
    kpair(std::false_type{}, std::true_type{}, IC<9>{}, 16, no);
    STAMP(13);
    settle<0>(aG); settle<8>(aU);
    u2 act[9][4];
#pragma unroll
    for (int t = 0; t < 9; ++t)
#pragma unroll
      for (int m = 0; m < 4; ++m)
#pragma unroll
        for (int e = 0; e < 2; ++e) {
          const f2 x = {aG[t][m][2 * e], aG[t][m][2 * e + 1]}, u = {aU[t][m][2 * e], aU[t][m][2 * e + 1]};
          const f2 z = x * (x * x * -0.10294324f + -2.3022082f);
          const f2 dn = (f2){__builtin_amdgcn_exp2f(z[0]), __builtin_amdgcn_exp2f(z[1])} + 1.f;
          act[t][m][e] = pk((x * u) * (f2){__builtin_amdgcn_rcpf(dn[0]), __builtin_amdgcn_rcpf(dn[1])});
        }
    asm volatile("s_waitcnt lgkmcnt(0)" ::: "memory");
    BAR();
#pragma unroll
    for (int t = 0; t < 9; ++t) {
      const int tt = 9 * w + t, ks = tt >> 1, ah = tt & 1;
      const unsigned ad = ldsb + cl * ROWB + (((4 * ks + g4) ^ sw) * 16) + 8 * ah;
#pragma unroll
      for (int m = 0; m < 4; ++m) *(LAS u2*)(size_t)(ad + m * 16 * ROWB) = act[t][m];
    }
    asm volatile("s_waitcnt lgkmcnt(0)" ::: "memory");
    BAR();
    STAMP(14);
    read_a(s, 0, ab_e);
    gemm_rows<18, 0>(s, aG, voffB, gD, ab_e, ab_o, voffB, gD, gD, gD);
    STAMP(15);
  }
  {
    int tid3 = threadIdx.x; asm volatile("" : "+v"(tid3));
    const int tid = tid3, lane = tid3 & 63;
    settle<0>(aG);
    store_partials(aG, rX, j * 8 * SLICE, SLICE, w, lane);
    publish(flags + (2 * 256 + blockIdx.x) * FLAG_STRIDE, tag);
    STAMP(16);
    const int t3 = tid < 192 ? tid : tid - 64;
    int zlr[3], zcc[3];
    u4 gv[3], x1[3];
#pragma unroll
    for (int i = 0; i < 3; ++i) {
      zdecode(t3 + 192 * i, zlr[i], zcc[i]);
      gv[i] = *(LAS u4*)(size_t)(ldsb + LDS_MOD + (5 * D + zcc[i] * 8) * 2);
      x1[i] = *(LAS u4*)(size_t)(ldsb + LDS_X1 + (t3 + 192 * i) * 16);
    }
    if (blockIdx.x == 0 && w == 1) {
      for (unsigned spins = 0; spins <= SPIN_CAP; ++spins) {
        bool ok = true;
#pragma unroll
        for (int i = 0; i < 4; ++i) ok &= __hip_atomic_load((gu64*)(flags + (lane + 64 * i) * FLAG_STRIDE), __ATOMIC_RELAXED, __HIP_MEMORY_SCOPE_AGENT) == tag;
        if (__all(ok)) break;
        __builtin_amdgcn_s_sleep(4);
      }
      if (lane == 0) __hip_atomic_store((gu64*)(p.ws + WS_EPOCH), tag, __ATOMIC_RELAXED, __HIP_MEMORY_SCOPE_AGENT);
    }
    await8(flags + (2 * 256 + c * 8) * FLAG_STRIDE, tag, tmo, w, lane);
    STAMP(17);
    u4 v[3][8];
#pragma unroll
    for (int i = 0; i < 3; ++i)
#pragma unroll
      for (int jj = 0; jj < 8; ++jj) v[i][jj] = __builtin_amdgcn_raw_buffer_load_b128(rX, (jj * 8 + j) * SLICE + (t3 + 192 * i) * 16, 0, AUX_X);
#pragma unroll
    for (int i = 0; i < 3; ++i) {
      const u4 o = gated_residual(v[i], x1[i], gv[i]);
      if (tid < 192) *(u4*)(p.out + (size_t)(c * 64 + j * 8 + zlr[i]) * D + zcc[i] * 8) = o;
    }
  }
  STAMP(18);
  }
}

extern "C" void kernel_launch(void* const* d_in, const int* in_sizes, int n_in, void* d_out, int out_size, void* d_ws, size_t ws_size, hipStream_t stream) {
  Params p;
  p.x = (const u16*)d_in[0]; p.kc = (const u16*)d_in[1]; p.vc = (const u16*)d_in[2]; p.wq = (const u16*)d_in[3]; p.wo = (const u16*)d_in[4];
  p.wg = (const u16*)d_in[6]; p.wu = (const u16*)d_in[7]; p.wd = (const u16*)d_in[8];
  p.c = (const u16*)d_in[9]; p.wmod = (const u16*)d_in[10];
  p.out = (u16*)d_out; p.ws = (char*)d_ws;
 p.one = 1.0f; p.gap = 0u;
  (void)hipFuncSetAttribute((const void*)k_main, hipFuncAttributeMaxDynamicSharedMemorySize, LDS_BYTES);
  k_main<<<256, 256, LDS_BYTES, stream>>>(p);
}
```

```cpp
#include <hip/hip_runtime.h>
#include <stdint.h>
#include <utility>
#include <type_traits>

typedef unsigned short u16;
typedef unsigned long long u64;
typedef __bf16 b2 __attribute__((ext_vector_type(2)));
typedef __bf16 b4 __attribute__((ext_vector_type(4)));
typedef __bf16 b8 __attribute__((ext_vector_type(8)));
typedef float f2 __attribute__((ext_vector_type(2)));
typedef float f4 __attribute__((ext_vector_type(4)));
typedef float f16v __attribute__((ext_vector_type(16)));
typedef unsigned u2 __attribute__((ext_vector_type(2)));
typedef unsigned u4 __attribute__((ext_vector_type(4)));
typedef __attribute__((address_space(1))) unsigned gu32;
typedef __attribute__((address_space(1))) unsigned long long gu64;
#define LAS __attribute__((address_space(3)))
#define DI __device__ __forceinline__
#define GAP() __builtin_amdgcn_sched_barrier(0)
#define BAR() do { asm volatile("" ::: "memory"); __builtin_amdgcn_s_barrier(); asm volatile("" ::: "memory"); } while (0)
template <int V> using IC = std::integral_constant<int, V>;
#define AUX_X 18
#define AUX_H 18
#define PFD_K 14
#define UNROLL(N, F) [&]<int... I_>(std::integer_sequence<int, I_...>) { (F(IC<I_>{}), ...); }(std::make_integer_sequence<int, N>{})

constexpr int S = 2048, D = 576, DH = 128, SK = 2048, WIN = 512, DF = 4608;
constexpr int ROWB = D * 2;
constexpr int ABYTES = 64 * ROWB;
constexpr int SLICE = 8 * ROWB;
constexpr int SLOT = 9216;
constexpr int QSLOT = 8192;
constexpr int LDS_DUMMY = ABYTES + 8 * SLOT;
constexpr int LDS_X1 = LDS_DUMMY + 256;
constexpr int LDS_MOD = LDS_X1 + SLICE;
constexpr int LDS_BYTES = LDS_MOD + 6 * D * 2;
static_assert(LDS_BYTES <= 163840, "LDS");
constexpr size_t WS_EPOCH = 0;
constexpr size_t WS_TMO = 256;
constexpr size_t WS_MODG = 4096;
constexpr size_t WS_MODP = 81920;
constexpr size_t WS_FLAG = 32768;
constexpr int FLAG_STRIDE = 8;
constexpr size_t WS_X = 262144;
constexpr size_t WS_H2 = WS_X + (size_t)32 * 64 * SLICE;
constexpr size_t WS_DBG = 200u << 20;
#define STAMP(i)
#define STAMP_FLUSH()
#define PST(i)

DI float bf2f(u16 v) { return __uint_as_float(((unsigned)v) << 16); }
DI float bflo(unsigned w) { return __uint_as_float(w << 16); }
DI float bfhi(unsigned w) { return __uint_as_float(w & 0xffff0000u); }
DI f2 bf2(unsigned w) { return (f2){bflo(w), bfhi(w)}; }
DI unsigned pk(float lo, float hi) { f2 v = {lo, hi}; b2 b = __builtin_convertvector(v, b2); return __builtin_bit_cast(unsigned, b); }
DI unsigned pk(f2 v) { b2 b = __builtin_convertvector(v, b2); return __builtin_bit_cast(unsigned, b); }
DI float rbf(float f) { return bflo(pk(f, f)); }
DI f2 rb2(f2 v) { return (f2){__uint_as_float(pk(0.f, v[0])), __uint_as_float(pk(0.f, v[1]))}; }
template <int CTRL> DI float dpp(float v) { return __uint_as_float(__builtin_amdgcn_update_dpp(0u, __float_as_uint(v), CTRL, 0xf, 0xf, false)); }
DI float sum8(float v) { v += dpp<0xB1>(v); v += dpp<0x4E>(v); v += dpp<0x141>(v); return v; }
DI float swap16_add(float x) { auto r = __builtin_amdgcn_permlane16_swap(__float_as_uint(x), __float_as_uint(x), false, false); return __uint_as_float(r[0]) + __uint_as_float(r[1]); }
DI float swap32_add(float x) { auto r = __builtin_amdgcn_permlane32_swap(__float_as_uint(x), __float_as_uint(x), false, false); return __uint_as_float(r[0]) + __uint_as_float(r[1]); }
DI float sum32(float v) { v = sum8(v); v += dpp<0x140>(v); return swap16_add(v); }
DI u16 f2bf(float f) { return (u16)(pk(f, f) & 0xffffu); }
DI float fmaxx(float a, float b) { return __builtin_elementwise_maximum(a, b); }
DI float fmax3(float a, float b, float c) { return __builtin_elementwise_maximum(__builtin_elementwise_maximum(a, b), c); }

DI void zdecode(int z, int& lr, int& cc) {
  const int w = z / 144, y = z - w * 144;
  if (y < 128) { const int tp = y >> 5; lr = (y >> 2) & 7; cc = 18 * w + 4 * tp + (y & 3); }
  else { const int y2 = y - 128; lr = y2 >> 1; cc = 18 * w + 16 + (y2 & 1); }
}

template <int IMM> DI void dma(unsigned voff, const char* g) { asm volatile("global_load_lds_dwordx4 %0, %1 offset:%2" :: "v"(voff), "s"(g), "i"(IMM) : "memory"); }
DI void setm0(unsigned a) { asm volatile("s_mov_b32 m0, %0" :: "s"(a) : "memory"); }
DI void touch(const char* p, unsigned voff, unsigned ldsdummy) {
  asm volatile("s_mov_b32 m0, %0\n\ts_nop 0\n\tglobal_load_lds_dword %1, %2" :: "s"(ldsdummy), "v"(voff), "s"(p) : "memory");
}
DI void touchp(const char* a, unsigned ldsdummy) {
  asm volatile("s_mov_b32 m0, %0\n\ts_nop 0\n\tglobal_load_lds_dword %1, off" :: "s"(ldsdummy), "v"(a) : "memory");
}
DI b4 ldT(unsigned base, int off) { return __builtin_amdgcn_ds_read_tr16_b64_v4bf16((LAS b4*)(size_t)(base + off)); }
DI b8 ldA(unsigned base, int off) { return *(LAS b8*)(size_t)(base + off); }
DI b8 cat(const b4& lo, const b4& hi) { return __builtin_shufflevector(lo, hi, 0, 1, 2, 3, 4, 5, 6, 7); }
template <bool AG, bool ZERO> DI void mfma16(f4& c, const b8& a, const b8& b) {
  if constexpr (AG) {
    if constexpr (ZERO) asm volatile("v_mfma_f32_16x16x32_bf16 %0, %1, %2, 0" : "=a"(c) : "v"(a), "v"(b));
    else asm volatile("v_mfma_f32_16x16x32_bf16 %0, %1, %2, %0" : "+a"(c) : "v"(a), "v"(b));
  } else {
    if constexpr (ZERO) asm volatile("v_mfma_f32_16x16x32_bf16 %0, %1, %2, 0" : "=&v"(c) : "v"(a), "v"(b));
    else asm volatile("v_mfma_f32_16x16x32_bf16 %0, %1, %2, %0" : "+v"(c) : "v"(a), "v"(b));
  }
}
template <bool AG, bool ZERO> DI void mfma32(f16v& c, const b8& a, const b8& b) {
  if constexpr (AG) {
    if constexpr (ZERO) asm volatile("v_mfma_f32_32x32x16_bf16 %0, %1, %2, 0" : "=a"(c) : "v"(a), "v"(b));
    else asm volatile("v_mfma_f32_32x32x16_bf16 %0, %1, %2, %0" : "+a"(c) : "v"(a), "v"(b));
  } else {
    if constexpr (ZERO) asm volatile("v_mfma_f32_32x32x16_bf16 %0, %1, %2, 0" : "=&v"(c) : "v"(a), "v"(b));
    else asm volatile("v_mfma_f32_32x32x16_bf16 %0, %1, %2, %0" : "+v"(c) : "v"(a), "v"(b));
  }
}
template <int NVG> DI void settle(f4 (&acc)[9][4]) {
  asm volatile("s_nop 7\n\ts_nop 7" ::: "memory");
#pragma unroll
  for (int t = 0; t < 9; ++t)
#pragma unroll
    for (int m = 0; m < 4; ++m) { if (t * 4 + m < 36 - NVG) asm volatile("" : "+a"(acc[t][m])); else asm volatile("" : "+v"(acc[t][m])); }
}

struct GS {
  b4 wlo[2][9], whi[2][9];
  b8 af[2][4];
  unsigned wbase, ring;
};
DI void issue_all(const GS& s, int slot, const unsigned (&voff)[9], const char* gp) {
  const unsigned sl = s.ring + slot * SLOT;
  setm0(sl + 4096); asm volatile("s_nop 0");
  dma<-4096>(voff[0], gp); dma<-3072>(voff[1], gp); dma<-2048>(voff[2], gp); dma<-1024>(voff[3], gp);
  dma<0>(voff[4], gp); dma<1024>(voff[5], gp); dma<2048>(voff[6], gp); dma<3072>(voff[7], gp);
  setm0(sl + 8192); asm volatile("s_nop 0"); dma<0>(voff[8], gp);
}
template <int PAR, int AP, bool ZERO, bool RDW, bool RDA, bool DMA, int VM, int NVG, typename PF>
DI void step(GS& s, f4 (&acc)[9][4], const unsigned (&voff)[9], const char* gp, unsigned ab, PF&& pf) {
  const unsigned sl = s.ring + (PAR ^ 1) * SLOT;
  const unsigned wb = s.wbase + (PAR ^ 1) * SLOT;
  auto mm = [&](auto ic) {
    constexpr int I = decltype(ic)::value, t = I >> 2, m = I & 3;
    mfma16<(I < 36 - NVG), ZERO>(acc[t][m], cat(s.wlo[PAR][t], s.whi[PAR][t]), s.af[AP][m]);
  };
  auto rd = [&](auto ic) {
    constexpr int I = decltype(ic)::value;
    if constexpr (I < 4) { if constexpr (RDA) s.af[AP ^ 1][I] = ldA(ab, I * 16 * ROWB); }
    else if constexpr (RDW) {
      if constexpr (((I - 4) & 1) == 0) s.wlo[PAR ^ 1][(I - 4) >> 1] = ldT(wb, 32 * ((I - 4) >> 1));
      else s.whi[PAR ^ 1][(I - 4) >> 1] = ldT(wb, 32 * ((I - 4) >> 1) + 4608);
    }
  };
  auto gap = [&](auto gc) {
    constexpr int G = decltype(gc)::value;
    mm(IC<2 * G>{});
    if constexpr (G == 0 && RDW) asm volatile("s_waitcnt vmcnt(%0)" :: "i"(VM) : "memory");
    if constexpr (G <= 10) rd(IC<2 * G>{});
    if constexpr (DMA) {
      if constexpr (G == 12) { asm volatile("s_waitcnt lgkmcnt(0)" ::: "memory"); setm0(sl + 4096); }
      if constexpr (G >= 13 && G <= 16) dma<(2 * (G - 13) - 4) * 1024>(voff[2 * (G - 13)], gp);
      if constexpr (G == 17) dma<0>(voff[8], gp);
    }
    GAP();
    mm(IC<2 * G + 1>{});
    if constexpr (G <= 10) rd(IC<2 * G + 1>{});
    if constexpr (DMA) {
      if constexpr (G >= 13 && G <= 16) dma<(2 * (G - 13) + 1 - 4) * 1024>(voff[2 * (G - 13) + 1], gp);
      if constexpr (G == 16) setm0(sl + 8192);
    }
    if constexpr (G == 17) pf();
    GAP();
  };
  UNROLL(18, gap);
}
DI void read_w(GS& s, int par) {
#pragma unroll
  for (int t = 0; t < 9; ++t) { s.wlo[par][t] = ldT(s.wbase + par * SLOT, 32 * t); s.whi[par][t] = ldT(s.wbase + par * SLOT, 32 * t + 4608); }
}
DI void read_a(GS& s, int set, unsigned ab) {
#pragma unroll
  for (int m = 0; m < 4; ++m) s.af[set][m] = ldA(ab, m * 16 * ROWB);
}
template <int NS, int NEXT, int NPF, typename PF> DI void gemm_rows(GS& s, f4 (&acc)[9][4], const unsigned (&voff)[9], const char* gW, unsigned ab_e, unsigned ab_o,
                                              const unsigned (&nvoff)[9], const char* nx0, const char* nx1, const char* nx2, PF&& pf) {
  constexpr size_t KS = (size_t)32 * ROWB;
  auto no = [&]() {};
  step<0, 0, true, true, true, true, 9, 0>(s, acc, voff, gW + 3 * KS, ab_o, no);
  step<1, 1, false, true, true, true, 9, 0>(s, acc, voff, gW + 4 * KS, ab_e + 128, pf);
  step<0, 0, false, true, true, true, 9 + NPF, 0>(s, acc, voff, gW + 5 * KS, ab_o + 128, no);
  step<1, 1, false, true, true, true, 9 + NPF, 0>(s, acc, voff, gW + 6 * KS, ab_e + 256, no);
  for (int k = 4; k < NS - 4; k += 2) {
    step<0, 0, false, true, true, true, 9, 0>(s, acc, voff, gW + (size_t)(k + 3) * KS, ab_o + 128 * (k >> 1), no);
    step<1, 1, false, true, true, true, 9, 0>(s, acc, voff, gW + (size_t)(k + 4) * KS, ab_e + 128 * ((k + 2) >> 1), no);
  }
  step<0, 0, false, true, true, true, 9, 0>(s, acc, voff, gW + (size_t)(NS - 1) * KS, ab_o + 128 * ((NS - 4) >> 1), no);
  step<1, 1, false, true, true, NEXT != 0, 9, 0>(s, acc, nvoff, nx0, ab_e + 128 * ((NS - 2) >> 1), no);
  step<0, 0, false, true, true, NEXT != 0, NEXT != 0 ? 9 : 0, 0>(s, acc, nvoff, nx1, ab_o + 128 * ((NS - 2) >> 1), no);
  step<1, 1, false, NEXT == 1, false, NEXT == 1, 9, 0>(s, acc, nvoff, nx2, 0, no);
}
DI void store_partials(const f4 (&acc)[9][4], __amdgpu_buffer_rsrc_t rs, unsigned base, unsigned dstride, int w, int lane) {
  const int G = lane >> 4, c = lane & 15, lr = c & 7, dh = c >> 3;
#pragma unroll
  for (int m = 0; m < 4; ++m) {
    const unsigned dst = base + (2 * m + dh) * dstride + w * 2304 + lr * 64 + (2 * (G & 1) + (G >> 1)) * 16;
#pragma unroll
    for (int tp = 0; tp < 4; ++tp) {
      const f4 a = acc[2 * tp][m], b = acc[2 * tp + 1][m];
      auto r0 = __builtin_amdgcn_permlane16_swap(pk(a[0], a[1]), pk(b[0], b[1]), false, false);
      auto r1 = __builtin_amdgcn_permlane16_swap(pk(a[2], a[3]), pk(b[2], b[3]), false, false);
      __builtin_amdgcn_raw_buffer_store_b128((u4){r0[0], r1[0], r0[1], r1[1]}, rs, dst + tp * 512, 0, 16);
    }
  }
#pragma unroll
  for (int mp = 0; mp < 2; ++mp) {
    const f4 a = acc[8][2 * mp], b = acc[8][2 * mp + 1];
    auto r0 = __builtin_amdgcn_permlane16_swap(pk(a[0], a[1]), pk(b[0], b[1]), false, false);
    auto r1 = __builtin_amdgcn_permlane16_swap(pk(a[2], a[3]), pk(b[2], b[3]), false, false);
    const unsigned dst = base + (2 * (2 * mp + (G & 1)) + dh) * dstride + w * 2304 + 2048 + lr * 32 + (G >> 1) * 16;
    __builtin_amdgcn_raw_buffer_store_b128((u4){r0[0], r1[0], r0[1], r1[1]}, rs, dst, 0, 16);
  }
}

constexpr unsigned SPIN_CAP = 1u << 17;
DI void publish(u64* flag, u64 tag) {
  asm volatile("s_waitcnt vmcnt(0)" ::: "memory");
  BAR();
  if (threadIdx.x == 0) __hip_atomic_store((gu64*)flag, tag, __ATOMIC_RELAXED, __HIP_MEMORY_SCOPE_AGENT);
}
DI void await8(u64* flags8, u64 tag, unsigned* tmo, int w, int lane) {
  if (w == 0) {
    for (unsigned spins = 0;; ++spins) {
      const u64 v = __hip_atomic_load((gu64*)(flags8 + (lane & 7) * FLAG_STRIDE), __ATOMIC_RELAXED, __HIP_MEMORY_SCOPE_AGENT);
      if (__all(v == tag)) break;
      if (spins > SPIN_CAP) { if (lane == 0) __hip_atomic_store((gu32*)tmo, 1u, __ATOMIC_RELAXED, __HIP_MEMORY_SCOPE_AGENT); break; }
      __builtin_amdgcn_s_sleep(1);
    }
  }
  BAR();
}
template <int P0, int NP, bool F32, int NT> DI void gather_mod(char* ws, u64 tag, unsigned ldsb, unsigned* tmo, int tid) {
  constexpr int N = (NP + NT - 1) / NT;
  const __amdgpu_buffer_rsrc_t rM = __builtin_amdgcn_make_buffer_rsrc(ws + WS_MODG, 0, 6 * D * 8, 0x00020000);
  const u64 gtag = tag << 16;
  const unsigned thi = (unsigned)(gtag >> 32), tlo = (unsigned)gtag & 0xffff0000u;
  u4 gr[N];
  for (unsigned spins = 0;; ++spins) {
    bool ok = true;
#pragma unroll
    for (int i = 0; i < N; ++i) gr[i] = __builtin_amdgcn_raw_buffer_load_b128(rM, (P0 + (tid + NT * i < NP ? tid + NT * i : (tid & 63))) * 16, 0, 16);
#pragma unroll
    for (int i = 0; i < N; ++i) ok &= gr[i][1] == thi && gr[i][3] == thi && (gr[i][0] & 0xffff0000u) == tlo && (gr[i][2] & 0xffff0000u) == tlo;
    if (__all(ok)) break;
    if (spins > SPIN_CAP / 4) { if ((tid & 63) == 0) __hip_atomic_store((gu32*)tmo, 2u, __ATOMIC_RELAXED, __HIP_MEMORY_SCOPE_AGENT); break; }
    __builtin_amdgcn_s_sleep(2);
  }
#pragma unroll
  for (int i = 0; i < N; ++i)
    if (tid + NT * i < NP) {
      const int idx = P0 + tid + NT * i;
      if constexpr (F32) {
        f2 v = {bflo(gr[i][0]), bflo(gr[i][2])};
        if (idx >= D / 2) v = rb2(v + 1.f);
        *(LAS f2*)(size_t)(ldsb + LDS_X1 + 1024 + idx * 8) = v;
      } else *(LAS unsigned*)(size_t)(ldsb + LDS_MOD + idx * 4) = (gr[i][0] & 0xffffu) | (gr[i][2] << 16);
    }
}
DI u4 gated_residual(const u4 (&v)[8], const u4& res, const u4& g) {
  u4 o;
#pragma unroll
  for (int e = 0; e < 4; ++e) {
    f2 s = bf2(v[0][e]);
#pragma unroll
    for (int j = 1; j < 8; ++j) s += bf2(v[j][e]);
    const unsigned t = pk(bf2(pk(s)) * bf2(g[e]));
    o[e] = pk(bf2(res[e]) + bf2(t));
  }
  return o;
}

constexpr float QSCALE = 0.08838834764831845f * 1.4426950408889634f;
constexpr float THR = 40.0f;
struct QS {
  b4 wlo[2][8], whi[2][8];
  b8 hf[2][4];
  unsigned wb[4];
  unsigned hb[4];
  unsigned ring;
};
template <int PAR, bool ZERO, bool RD, bool DMA, int VM, typename PF>
DI void qstep(QS& s, f16v (&acc)[4][2], const unsigned (&voff)[8], const char* gp, unsigned hoff, PF&& pf) {
  const unsigned sl = s.ring + (PAR ^ 1) * QSLOT;
  auto rd = [&](auto ic) {
    constexpr int I = decltype(ic)::value;
    if constexpr (RD) {
      if constexpr (I < 4) {
        s.hf[PAR ^ 1][I] = ldA(s.hb[2 * (PAR ^ 1) + (I >> 1)] + hoff, (I & 1) * 32 * ROWB);
      } else {
        constexpr int J = I - 4, f = J >> 1, t = J & 1, ks = f >> 2, dt = f & 3;
        const b4 v = ldT(s.wb[dt], (PAR ^ 1) * QSLOT + 4096 * ks + 1024 * t);
        if constexpr (t == 0) s.wlo[PAR ^ 1][f] = v; else s.whi[PAR ^ 1][f] = v;
      }
    }
  };
  auto gap = [&](auto gc) {
    constexpr int G = decltype(gc)::value, ks = G >> 3, dt = (G >> 1) & 3, qb = G & 1, f = ks * 4 + dt;
    mfma32<false, ZERO && ks == 0>(acc[dt][qb], cat(s.wlo[PAR][f], s.whi[PAR][f]), s.hf[PAR][ks * 2 + qb]);
    if constexpr (G == 0 && RD) asm volatile("s_waitcnt vmcnt(%0)" :: "i"(VM) : "memory");
    if constexpr (G <= 6) { rd(IC<3 * G>{}); rd(IC<3 * G + 1>{}); if constexpr (3 * G + 2 < 20) rd(IC<3 * G + 2>{}); }
    if constexpr (DMA) {
      if constexpr (G == 9) { asm volatile("s_waitcnt lgkmcnt(0)" ::: "memory"); setm0(sl + 4096); }
      if constexpr (G >= 10 && G <= 13) { dma<(2 * (G - 10) - 4) * 1024>(voff[2 * (G - 10)], gp); dma<(2 * (G - 10) + 1 - 4) * 1024>(voff[2 * (G - 10) + 1], gp); }
    }
    if constexpr (G == 15) pf();
    GAP();
  };
  UNROLL(16, gap);
}

struct AT {
  f16v S[2][2][2];
  f16v negm[2];
  f16v O[4][2];
  b8 q[2][8];
  b8 P[2][4];
  float m[2], l[2], rs0[2], rs1[2], fct[2], pm0[2], pm1[2];
  float one;
  unsigned kb_[2], vb_[2];
  unsigned voff[8];
  unsigned m0k, m0v;
  const char *gk, *gv;
  bool pend;
};
template <int MODE> DI void qk(f16v& s, const b8& k, const b8& q, const f16v& negm) {
  if constexpr (MODE == 0) asm volatile("v_mfma_f32_32x32x16_bf16 %0, %1, %2, 0" : "=&v"(s) : "v"(k), "a"(q));
  else if constexpr (MODE == 1) asm volatile("v_mfma_f32_32x32x16_bf16 %0, %1, %2, %3" : "=&v"(s) : "v"(k), "a"(q), "v"(negm));
  else asm volatile("v_mfma_f32_32x32x16_bf16 %0, %1, %2, %0" : "+v"(s) : "v"(k), "a"(q));
}
template <int XP, int KB, int E> DI void sm_exp(AT& a) { constexpr int qb = E >> 4, r = E & 15; a.S[XP][KB][qb][r] = __builtin_amdgcn_exp2f(a.S[XP][KB][qb][r]); }
template <int XP, int KB, int E> DI void sm_add(AT& a) {
  constexpr int qb = E >> 4, r = E & 15;
  if constexpr (r & 1) a.rs1[qb] = __builtin_fmaf(a.S[XP][KB][qb][r], a.one, a.rs1[qb]); else a.rs0[qb] += a.S[XP][KB][qb][r];
}
template <int XP, int C> DI void sm_pack(AT& a) {
  constexpr int kb = C >> 4, qb = (C >> 3) & 1, s = (C >> 2) & 1, p = C & 3;
  u4 w = __builtin_bit_cast(u4, a.P[qb][kb * 2 + s]);
  w[p] = pk(a.S[XP][kb][qb][8 * s + 2 * p], a.S[XP][kb][qb][8 * s + 2 * p + 1]);
  a.P[qb][kb * 2 + s] = __builtin_bit_cast(b8, w);
  if constexpr (p == 3) asm volatile("" : "+v"(a.P[qb][kb * 2 + s]));
}
template <int X, bool FIRST, bool DK, bool DV, typename PF> DI void phaseA(AT& a, int t, PF&& pf) {
  constexpr int XP = X ^ 1;
  const unsigned kslot = X * 16384;
  b8 kf[16];
  auto kread = [&](auto ic) { constexpr int I = decltype(ic)::value, ks = I >> 1, kb = I & 1; kf[I] = ldA(a.kb_[ks & 1], kslot + 8192 * kb + 512 * (ks >> 1)); };
  kread(IC<0>{}); kread(IC<1>{}); kread(IC<2>{}); kread(IC<3>{});
  const char* gk = a.gk + (size_t)(t + 1) * 16384; const char* gv = a.gv + (size_t)t * 16384;
  auto gap = [&](auto gc) {
    constexpr int G = decltype(gc)::value, ks = G >> 2, kb = (G >> 1) & 1, qb = G & 1, I = G >> 1;
    qk<(ks == 0) ? (FIRST ? 0 : 1) : 2>(a.S[X][kb][qb], kf[I], a.q[qb][ks], a.negm[qb]);
    if constexpr ((G & 1) == 0 && I + 4 < 16) kread(IC<I + 4>{});
    if constexpr (DK) { if constexpr (G == 0) setm0(a.m0k + (X ^ 1) * 16384); if constexpr ((G & 1) && G < 16) dma<((G >> 1) - 4) * 1024>(a.voff[G >> 1], gk); }
    if constexpr (DV) { if constexpr (G == 16) setm0(a.m0v + X * 16384); if constexpr ((G & 1) && G > 16) dma<(((G - 16) >> 1) - 4) * 1024>(a.voff[(G - 16) >> 1], gv); }
    if constexpr (!FIRST) {
      if constexpr (G < 16) { sm_exp<XP, 1, 2 * G>(a); sm_exp<XP, 1, 2 * G + 1>(a); sm_pack<XP, G>(a); }
      else { sm_pack<XP, G>(a); sm_add<XP, 1, 2 * (G - 16)>(a); sm_add<XP, 1, 2 * (G - 16) + 1>(a); }
    }
    GAP();
  };
  UNROLL(32, gap);
  pf();
  if constexpr (!FIRST) {
#pragma unroll
    for (int qb = 0; qb < 2; ++qb) { a.l[qb] += a.rs0[qb] + a.rs1[qb]; asm volatile("" : "+v"(a.l[qb])); }
  }
}
DI float swap32_max(float x) { auto r = __builtin_amdgcn_permlane32_swap(__float_as_uint(x), __float_as_uint(x), false, false); return fmaxx(__uint_as_float(r[0]), __uint_as_float(r[1])); }
template <int X, bool NOPV, bool ZERO> DI void phaseB(AT& a) {
  const unsigned vslot = (X ^ 1) * 16384;
  b4 vlo[16], vhi[16];
  auto vread = [&](auto ic) {
    constexpr int I = decltype(ic)::value, k4 = I >> 2, db = I & 3;
    vlo[I] = ldT(a.vb_[0], vslot + 2048 * (2 * k4) + 512 * db);
    vhi[I] = ldT(a.vb_[1], vslot + 2048 * (2 * k4 + 1) + 512 * db);
  };
  if constexpr (!NOPV) { vread(IC<0>{}); vread(IC<1>{}); vread(IC<2>{}); }
  float rmax[2];
  auto pv = [&](auto gc) {
    constexpr int G = decltype(gc)::value, I = G >> 1, k4 = I >> 2, db = I & 3, qb = G & 1;
    if constexpr (!NOPV) {
      mfma32<true, ZERO && k4 == 0>(a.O[db][qb], cat(vlo[I], vhi[I]), a.P[qb][k4]);
      if constexpr ((G & 1) == 0 && I + 3 < 16) vread(IC<I + 3>{});
    }
  };
  auto gap1 = [&](auto gc) {
    constexpr int G = decltype(gc)::value;
    pv(gc);
    if constexpr (G >= 4 && G < 12) {
      constexpr int i = G - 4, q = i >> 2, r = 4 * (i & 3);
      if constexpr ((i & 3) == 0) { a.pm0[q] = fmax3(a.S[X][0][q][0], a.S[X][0][q][1], a.S[X][1][q][0]); a.pm1[q] = fmax3(a.S[X][0][q][2], a.S[X][0][q][3], a.S[X][1][q][1]);
                                    a.pm0[q] = fmax3(a.pm0[q], a.S[X][1][q][2], a.S[X][1][q][3]); }
      else { a.pm0[q] = fmax3(a.pm0[q], a.S[X][0][q][r], a.S[X][0][q][r + 1]); a.pm1[q] = fmax3(a.pm1[q], a.S[X][0][q][r + 2], a.S[X][0][q][r + 3]);
             a.pm0[q] = fmax3(a.pm0[q], a.S[X][1][q][r], a.S[X][1][q][r + 1]); a.pm1[q] = fmax3(a.pm1[q], a.S[X][1][q][r + 2], a.S[X][1][q][r + 3]); }
    }
    if constexpr (G == 12 || G == 13) { constexpr int q = G - 12; rmax[q] = swap32_max(fmaxx(a.pm0[q], a.pm1[q])); }
    GAP();
  };
  UNROLL(14, gap1);
  if constexpr (NOPV) {
#pragma unroll
    for (int q = 0; q < 2; ++q) {
      a.m[q] = rmax[q]; a.l[q] = 0.f; a.fct[q] = 1.f;
#pragma unroll
      for (int kb = 0; kb < 2; ++kb)
#pragma unroll
        for (int e = 0; e < 16; ++e) a.S[X][kb][q][e] -= rmax[q];
#pragma unroll
      for (int e = 0; e < 16; ++e) a.negm[q][e] = -rmax[q];
    }
  } else {
    const bool grew = (rmax[0] > THR) | (rmax[1] > THR);
    if (__builtin_expect(__any((int)grew), 0)) {
#pragma unroll
      for (int q = 0; q < 2; ++q) {
        const float dl = fmaxx(rmax[q], 0.f); a.m[q] += dl; a.fct[q] = __builtin_amdgcn_exp2f(-dl);
#pragma unroll
        for (int kb = 0; kb < 2; ++kb)
#pragma unroll
          for (int e = 0; e < 16; ++e) a.S[X][kb][q][e] -= dl;
#pragma unroll
        for (int e = 0; e < 16; ++e) a.negm[q][e] = -a.m[q];
      }
      a.pend = true;
    }
  }
  a.rs0[0] = a.rs0[1] = a.rs1[0] = a.rs1[1] = 0.f;
  GAP();
  auto gap2 = [&](auto gc) {
    constexpr int G = decltype(gc)::value + 14;
    pv(IC<G>{});
    if constexpr (G >= 15) {
      constexpr int e0 = 2 * (G - 15);
      if constexpr (e0 < 32) { sm_exp<X, 0, e0>(a); sm_exp<X, 0, e0 + 1>(a); }
      if constexpr (G >= 16) { sm_add<X, 0, e0 - 2>(a); sm_add<X, 0, e0 - 1>(a); }
    }
    GAP();
  };
  UNROLL(18, gap2);
  asm volatile("" : "+v"(a.S[X][0][0]), "+v"(a.S[X][0][1]), "+v"(a.rs0[0]), "+v"(a.rs0[1]), "+v"(a.rs1[0]), "+v"(a.rs1[1]));
  if constexpr (!NOPV) {
    if (__builtin_expect(a.pend, 0)) {
      asm volatile("s_nop 7\n\ts_nop 7" ::: "memory");
#pragma unroll
      for (int db = 0; db < 4; ++db)
#pragma unroll
        for (int q = 0; q < 2; ++q) {
          asm volatile("" : "+a"(a.O[db][q]));
          f16v o = a.O[db][q];
#pragma unroll
          for (int e = 0; e < 16; ++e) o[e] *= a.fct[q];
          a.O[db][q] = o;
          asm volatile("" : "+a"(a.O[db][q]));
        }
      a.l[0] *= a.fct[0]; a.l[1] *= a.fct[1]; a.pend = false;
      asm volatile("s_nop 1" ::: "memory");
    }
  }
}

struct Params { const u16 *x, *c, *wmod, *wq, *kc, *vc, *wo, *wg, *wu, *wd; u16* out; char* ws; float one; unsigned gap; };

__global__ __launch_bounds__(256, 1) void k_main(Params p) {
  extern __shared__ __attribute__((aligned(16))) char lds[];
  const int tid = threadIdx.x, w = __builtin_amdgcn_readfirstlane(tid >> 6), lane = tid & 63;
  const int j = blockIdx.x & 7, c = blockIdx.x >> 3;
  const unsigned ldsb = (unsigned)(size_t)lds;
  const int hh = lane >> 5, r32 = lane & 31;
  u64* const flags = (u64*)(p.ws + WS_FLAG);
  unsigned* const tmo = (unsigned*)(p.ws + WS_TMO);
  STAMP(0);
  PST(0);
  QS qs;
  unsigned voffQ[8];
#pragma unroll
  for (int i = 0; i < 8; ++i) voffQ[i] = (4 * i + (lane >> 4)) * 8192 + 16 * ((lane & 15) ^ ((lane >> 4) << 2)) - (i - 4) * 1024 + 4096;
  const char* gq = (const char*)p.wq + (size_t)(4 * j + w) * 256 - 4096;
  qs.ring = __builtin_amdgcn_readfirstlane(ldsb + ABYTES + w * 2 * QSLOT);
  constexpr size_t KS_Q = (size_t)32 * 8192;
  auto issueq = [&](int slot, const char* gp) {
    setm0(qs.ring + slot * QSLOT + 4096); asm volatile("s_nop 0");
    dma<-4096>(voffQ[0], gp); dma<-3072>(voffQ[1], gp); dma<-2048>(voffQ[2], gp); dma<-1024>(voffQ[3], gp);
    dma<0>(voffQ[4], gp); dma<1024>(voffQ[5], gp); dma<2048>(voffQ[6], gp); dma<3072>(voffQ[7], gp);
  };
  const int mg = blockIdx.x % 54, ms = blockIdx.x / 54;
  u4 wm[5]; u16 cval[5];
#pragma unroll
  for (int i = 0; i < 5; ++i) {
    const int r = 144 * (ms & 3) + (i < 4 || tid < 128 ? (tid >> 3) + 32 * i : 0);
    wm[i] = *(const u4*)(p.wmod + (size_t)r * (6 * D) + mg * 64 + (tid & 7) * 8); cval[i] = p.c[r];
  }
  const u64 e0 = __hip_atomic_load((gu64*)(p.ws + WS_EPOCH), __ATOMIC_RELAXED, __HIP_MEMORY_SCOPE_AGENT);
  AT a;
  u64 tag;
  {
    f16v Qt[4][2];
    QS& s = qs;
    {
      const int rr = lane >> 3, sub = lane & 7;
      u4 xv[2][9];
#pragma unroll
      for (int ps = 0; ps < 2; ++ps)
#pragma unroll
        for (int i = 0; i < 9; ++i) xv[ps][i] = *(const u4*)(p.x + (size_t)(c * 64 + 16 * w + 8 * ps + rr) * D + (sub + 8 * i) * 8);
      GAP();
      PST(1);
      tag = ((u64)__builtin_amdgcn_readfirstlane((unsigned)(e0 >> 32)) << 32 | (u64)__builtin_amdgcn_readfirstlane((unsigned)e0)) + 1;
      const u64 gtag = tag << 16;
      PST(9);
      if (blockIdx.x < 216) {
        float acc[8] = {};
#pragma unroll
        for (int i = 0; i < 5; ++i) {
          const float xc = bf2f(cval[i]);
          float sc = rbf(xc * __builtin_amdgcn_rcpf(1.f + __expf(-xc)));
          if (i == 4 && tid >= 128) sc = 0.f;
#pragma unroll
          for (int e = 0; e < 4; ++e) { acc[2 * e] += sc * bflo(wm[i][e]); acc[2 * e + 1] += sc * bfhi(wm[i][e]); }
        }
        LAS float* red = (LAS float*)(size_t)(ldsb + LDS_X1);
#pragma unroll
        for (int e = 0; e < 8; ++e) {
          float t = acc[e];
          t += dpp<0x128>(t);
          t = swap32_add(swap16_add(t));
          if (lane < 8) red[w * 64 + lane * 8 + e] = t;
        }
        PST(10);
        asm volatile("s_waitcnt lgkmcnt(0)" ::: "memory");
        BAR();
        PST(11);
        if (w == 0) {
          const int pr = lane & 31;
          f2 v = {0.f, 0.f};
#pragma unroll
          for (int k = 0; k < 4; ++k) v += *(LAS f2*)(red + k * 64 + 2 * pr);
          const __amdgpu_buffer_rsrc_t rP = __builtin_amdgcn_make_buffer_rsrc(p.ws + WS_MODP + (size_t)mg * 1536, 0, 1536, 0x00020000);
          if (ms != 0) {
            if (lane < 32) __builtin_amdgcn_raw_buffer_store_b128((u4){__float_as_uint(v[0]), __float_as_uint(v[1]), (unsigned)tag, (unsigned)(tag >> 32)}, rP, (ms - 1) * 512 + pr * 16, 0, 16);
          } else {
            u4 gr[3];
            for (unsigned spins = 0;; ++spins) {
              bool ok = true;
#pragma unroll
              for (int k = 0; k < 3; ++k) gr[k] = __builtin_amdgcn_raw_buffer_load_b128(rP, k * 512 + pr * 16, 0, 16);
#pragma unroll
              for (int k = 0; k < 3; ++k) ok &= gr[k][2] == (unsigned)tag && gr[k][3] == (unsigned)(tag >> 32);
              if (__all(ok)) break;
              if (spins > SPIN_CAP) { if (lane == 0) __hip_atomic_store((gu32*)tmo, 3u, __ATOMIC_RELAXED, __HIP_MEMORY_SCOPE_AGENT); break; }
              __builtin_amdgcn_s_sleep(1);
            }
#pragma unroll
            for (int k = 0; k < 3; ++k) v += (f2){__uint_as_float(gr[k][0]), __uint_as_float(gr[k][1])};
            const u64 g0 = gtag | f2bf(v[0]), g1 = gtag | f2bf(v[1]);
            const __amdgpu_buffer_rsrc_t rG = __builtin_amdgcn_make_buffer_rsrc(p.ws + WS_MODG, 0, 6 * D * 8, 0x00020000);
            if (lane < 32) __builtin_amdgcn_raw_buffer_store_b128((u4){(unsigned)g0, (unsigned)(g0 >> 32), (unsigned)g1, (unsigned)(g1 >> 32)}, rG, (mg * 32 + pr) * 16, 0, 16);
          }
        }
      }
      STAMP(20);
      PST(2);
      f2 y[2][9][4];
      {
#pragma unroll
        for (int ps = 0; ps < 2; ++ps) {
          f2 a1 = {0.f, 0.f}, a2 = {0.f, 0.f};
#pragma unroll
          for (int i = 0; i < 9; ++i)
#pragma unroll
            for (int e = 0; e < 4; ++e) { y[ps][i][e] = bf2(xv[ps][i][e]); a1 += y[ps][i][e]; a2 = __builtin_elementwise_fma(y[ps][i][e], y[ps][i][e], a2); }
          float s1 = a1[0] + a1[1], s2 = a2[0] + a2[1];
          s1 = sum8(s1); s2 = sum8(s2);
          if (ps == 0) PST(3);
          if (ps == 1) {
            asm volatile("" : "+v"(s1), "+v"(s2));
            issueq(0, gq); issueq(1, gq + KS_Q);
            const int li = tid < 144 ? tid : tid - 112;
            touch((const char*)p.wq + (size_t)512 * j * 2, (18 * c + (li >> 3)) * 8192 + (li & 7) * 128, __builtin_amdgcn_readfirstlane(ldsb + LDS_DUMMY));
          }
          const float mean = s1 * (1.f / D), var = __builtin_fmaxf(__builtin_fmaf(-mean, mean, s2 * (1.f / D)), 0.f);
          const float mr = rbf(mean), rs = __builtin_amdgcn_rcpf(rbf(__builtin_amdgcn_sqrtf(rbf(rbf(var) + 1e-5f))));
          f2 mr2 = {mr, mr}, rs2 = {rs, rs}; asm volatile("" : "+v"(mr2), "+v"(rs2));
#pragma unroll
          for (int i = 0; i < 9; ++i) {
            f2 t[4];
#pragma unroll
            for (int e = 0; e < 4; ++e) t[e] = y[ps][i][e] - mr2;
#pragma unroll
            for (int e = 0; e < 4; ++e) t[e] = rb2(t[e]);
#pragma unroll
            for (int e = 0; e < 4; ++e) t[e] = t[e] * rs2;
#pragma unroll
            for (int e = 0; e < 4; ++e) y[ps][i][e] = rb2(t[e]);
            asm volatile("" : "+v"(y[ps][i][0]), "+v"(y[ps][i][1]), "+v"(y[ps][i][2]), "+v"(y[ps][i][3]));
          }
        }
      }
      STAMP(21);
      PST(4);
      gather_mod<0, 576, true, 256>(p.ws, tag, ldsb, tmo, tid);
      STAMP(22);
      PST(5);
      asm volatile("s_waitcnt lgkmcnt(0)" ::: "memory");
      BAR();
      STAMP(23);
      PST(6);
      {
        const unsigned tb = ldsb + LDS_X1 + 1024 + sub * 32;
        f4 cst[2][4];
        auto ldc = [&](int i, f4 (&d)[4]) { d[0] = *(LAS f4*)(size_t)(tb + i * 256); d[1] = *(LAS f4*)(size_t)(tb + i * 256 + 16); d[2] = *(LAS f4*)(size_t)(tb + i * 256 + D * 4); d[3] = *(LAS f4*)(size_t)(tb + i * 256 + D * 4 + 16); };
        ldc(0, cst[0]);
        const unsigned ad0 = ldsb + (16 * w + rr) * ROWB + ((sub ^ ((rr >> 1) & 7)) * 16);
        const unsigned ad1 = ldsb + (16 * w + 8 + rr) * ROWB + ((sub ^ (((rr >> 1) + 4) & 7)) * 16);
#pragma unroll
        for (int i = 0; i < 9; ++i) {
          if (i + 1 < 9) ldc(i + 1, cst[(i + 1) & 1]);
          GAP();
          const f4 (&k)[4] = cst[i & 1];
          const f2 sh[4] = {{k[0][0], k[0][1]}, {k[0][2], k[0][3]}, {k[1][0], k[1][1]}, {k[1][2], k[1][3]}}, cc[4] = {{k[2][0], k[2][1]}, {k[2][2], k[2][3]}, {k[3][0], k[3][1]}, {k[3][2], k[3][3]}};
          f2 t[2][4]; u4 o[2];
#pragma unroll
          for (int ps = 0; ps < 2; ++ps)
#pragma unroll
            for (int e = 0; e < 4; ++e) t[ps][e] = y[ps][i][e] * cc[e];
#pragma unroll
          for (int ps = 0; ps < 2; ++ps)
#pragma unroll
            for (int e = 0; e < 4; ++e) t[ps][e] = rb2(t[ps][e]);
#pragma unroll
          for (int ps = 0; ps < 2; ++ps)
#pragma unroll
            for (int e = 0; e < 4; ++e) t[ps][e] = t[ps][e] + sh[e];
#pragma unroll
          for (int ps = 0; ps < 2; ++ps)
#pragma unroll
            for (int e = 0; e < 4; ++e) o[ps][e] = pk(t[ps][e]);
          *(LAS u4*)(size_t)(ad0 + i * 128) = o[0];
          *(LAS u4*)(size_t)(ad1 + i * 128) = o[1];
          GAP();
        }
      }
    }
    {
      const int blk = (lane >> 4) & 1, q = (lane & 15) >> 2, pp = lane & 3;
#pragma unroll
      for (int dt = 0; dt < 4; ++dt) s.wb[dt] = s.ring + (8 * hh + q) * 256 + 64 * (dt ^ q) + 32 * blk + 16 * (pp & 1) + 8 * (pp >> 1);
#pragma unroll
      for (int i = 0; i < 4; ++i) s.hb[i] = ldsb + r32 * ROWB + (((2 * i + hh) ^ ((r32 >> 1) & 7)) * 16);
    }
    asm volatile("s_waitcnt lgkmcnt(0)" ::: "memory");
    BAR();
    STAMP(1);
    PST(7);
#pragma unroll
    for (int f = 0; f < 8; ++f) { s.wlo[0][f] = ldT(s.wb[f & 3], 4096 * (f >> 2)); s.whi[0][f] = ldT(s.wb[f & 3], 4096 * (f >> 2) + 1024); }
#pragma unroll
    for (int i = 0; i < 4; ++i) s.hf[0][i] = ldA(s.hb[i >> 1], (i & 1) * 32 * ROWB);
    asm volatile("s_waitcnt lgkmcnt(0)" ::: "memory");
    issueq(0, gq + 2 * KS_Q);
    auto no = [&]() {};
    auto pfkv = [&](int h) {
      if (lane < 16) touch((const char*)(w < 2 ? p.kc : p.vc) + ((size_t)(2 * j + (w & 1)) * SK + (SK - WIN)) * DH * 2, (32 * c + 16 * h + lane) * 128, __builtin_amdgcn_readfirstlane(ldsb + LDS_DUMMY));
    };
    qstep<0, true, true, true, 8>(s, Qt, voffQ, gq + 3 * KS_Q, 0, no);
    qstep<1, false, true, true, 8>(s, Qt, voffQ, gq + 4 * KS_Q, 128, [&]() { pfkv(0); });
    qstep<0, false, true, true, 9>(s, Qt, voffQ, gq + 5 * KS_Q, 128, no);
    qstep<1, false, true, true, 9>(s, Qt, voffQ, gq + 6 * KS_Q, 256, [&]() { pfkv(1); });
    qstep<0, false, true, true, 9>(s, Qt, voffQ, gq + 7 * KS_Q, 256, no);
    qstep<1, false, true, true, 9>(s, Qt, voffQ, gq + 8 * KS_Q, 384, no);
    for (int n = 6; n < 14; n += 2) {
      qstep<0, false, true, true, 8>(s, Qt, voffQ, gq + (size_t)(n + 3) * KS_Q, 128 * ((n + 1) >> 1), no);
      qstep<1, false, true, true, 8>(s, Qt, voffQ, gq + (size_t)(n + 4) * KS_Q, 128 * ((n + 2) >> 1), no);
    }
    qstep<0, false, true, true, 8>(s, Qt, voffQ, gq + (size_t)17 * KS_Q, 128 * 7, no);
    qstep<1, false, true, false, 8>(s, Qt, voffQ, gq, 128 * 8, no);
    qstep<0, false, true, false, 0>(s, Qt, voffQ, gq, 128 * 8, no);
    qstep<1, false, false, false, 0>(s, Qt, voffQ, gq, 0, no);
    STAMP(2);
    PST(8);
    {
      const int kvl = w >> 1, half = w & 1;
      const unsigned kr = ldsb + kvl * 65536, vr = kr + 32768;
#pragma unroll
      for (int i = 0; i < 2; ++i) a.kb_[i] = kr + 2048 * (r32 >> 3) + 64 * (r32 & 7) + 16 * ((2 * i + hh) ^ ((r32 >> 2) & 3));
      const int blk = (lane >> 4) & 1, q = (lane & 15) >> 2, pp = lane & 3;
#pragma unroll
      for (int t = 0; t < 2; ++t) a.vb_[t] = vr + 64 * (4 * hh + q) + 16 * ((2 * blk + (pp >> 1)) ^ ((2 * t + hh) & 3)) + 8 * (pp & 1);
      const int st = lane >> 5, r7 = (lane >> 2) & 7, pos = lane & 3;
#pragma unroll
      for (int i = 0; i < 8; ++i) {
        const int rg = 4 * half + (i >> 1);
        a.voff[i] = rg * 2048 + r7 * 256 + (i & 1) * 128 + st * 64 + 16 * (pos ^ ((2 * (rg & 1) + (r7 >> 2)) & 3)) - (i - 4) * 1024 + 4096;
      }
      a.m0k = __builtin_amdgcn_readfirstlane(kr + 8192 * half + 4096); a.m0v = __builtin_amdgcn_readfirstlane(vr + 8192 * half + 4096);
      const size_t kvoff = ((size_t)(2 * j + kvl) * SK + (SK - WIN)) * DH * 2;
      a.gk = (const char*)p.kc + kvoff - 4096; a.gv = (const char*)p.vc + kvoff - 4096;
      a.pend = false; a.one = p.one;
    }
    asm volatile("s_waitcnt lgkmcnt(0)" ::: "memory");
    BAR();
    setm0(a.m0k); asm volatile("s_nop 0");
    dma<-4096>(a.voff[0], a.gk); dma<-3072>(a.voff[1], a.gk); dma<-2048>(a.voff[2], a.gk); dma<-1024>(a.voff[3], a.gk);
    dma<0>(a.voff[4], a.gk); dma<1024>(a.voff[5], a.gk); dma<2048>(a.voff[6], a.gk); dma<3072>(a.voff[7], a.gk);
    asm volatile("s_nop 7" ::: "memory");
#pragma unroll
    for (int dt = 0; dt < 4; ++dt)
#pragma unroll
      for (int qb = 0; qb < 2; ++qb) {
        asm volatile("" : "+v"(Qt[dt][qb]));
#pragma unroll
        for (int s2 = 0; s2 < 2; ++s2) {
          u4 w4;
#pragma unroll
          for (int e = 0; e < 4; ++e) w4[e] = pk((f2){Qt[dt][qb][8 * s2 + 2 * e], Qt[dt][qb][8 * s2 + 2 * e + 1]} * QSCALE);
          a.q[qb][2 * dt + s2] = __builtin_bit_cast(b8, w4);
          asm volatile("" : "+a"(a.q[qb][2 * dt + s2]));
        }
      }
  }
  STAMP(3);
  {
    auto tile_bar = [&]() { asm volatile("s_waitcnt vmcnt(0) lgkmcnt(0)" ::: "memory"); BAR(); };
    const char* pfp = w < 2 ? (const char*)(w == 0 ? p.wg : p.wu) + (size_t)576 * j * 2 + (size_t)(18 * c + lane / 9) * (DF * 2) + (lane % 9) * 128
                            : (const char*)p.wo + (size_t)512 * j * ROWB + (16 * c) * ROWB + (12 * (w - 2) + lane) * 128;
    const int pfs = w < 2 ? 3 * DF * 2 : 24 * 128, pfn = w < 2 ? 27 : 12;
    auto pft = [&](int) { if (lane < pfn) touchp(pfp, __builtin_amdgcn_readfirstlane(ldsb + LDS_DUMMY)); pfp += pfs; };
    auto tile_bar1 = [&]() { asm volatile("s_waitcnt vmcnt(1) lgkmcnt(0)" ::: "memory"); BAR(); };
    tile_bar(); phaseA<0, true, true, true>(a, 0, [&]() {}); asm volatile("s_nop 7\n\ts_nop 3" ::: "memory"); GAP(); phaseB<0, true, false>(a);
    tile_bar(); phaseA<1, false, true, true>(a, 1, [&]() { pft(1); }); phaseB<1, false, true>(a);
    for (int t = 2; t < 6; t += 2) {
      tile_bar1(); phaseA<0, false, true, true>(a, t, [&]() { pft(t); }); phaseB<0, false, false>(a);
      tile_bar1(); phaseA<1, false, true, true>(a, t + 1, [&]() { pft(t + 1); }); phaseB<1, false, false>(a);
    }
    tile_bar1(); phaseA<0, false, true, true>(a, 6, [&]() { pft(6); }); phaseB<0, false, false>(a);
    tile_bar1(); phaseA<1, false, false, true>(a, 7, [&]() {}); phaseB<1, false, false>(a);
    { auto f1 = [&](auto ic) { sm_exp<1, 1, decltype(ic)::value>(a); }; UNROLL(32, f1);
      auto f2_ = [&](auto ic) { sm_pack<1, decltype(ic)::value>(a); }; UNROLL(32, f2_);
      auto f3 = [&](auto ic) { sm_add<1, 1, decltype(ic)::value>(a); }; UNROLL(32, f3); }
#pragma unroll
    for (int qb = 0; qb < 2; ++qb) a.l[qb] += a.rs0[qb] + a.rs1[qb];
    tile_bar();
    const unsigned vslot = 16384;
    b4 vlo[16], vhi[16];
#pragma unroll
    for (int I = 0; I < 16; ++I) { vlo[I] = ldT(a.vb_[0], vslot + 2048 * (2 * (I >> 2)) + 512 * (I & 3)); vhi[I] = ldT(a.vb_[1], vslot + 2048 * (2 * (I >> 2) + 1) + 512 * (I & 3)); }
    asm volatile("s_nop 1" ::: "memory"); GAP();
#pragma unroll
    for (int G = 0; G < 32; ++G) { const int I = G >> 1; mfma32<true, false>(a.O[I & 3][G & 1], cat(vlo[I], vhi[I]), a.P[G & 1][I >> 2]); }
  }
  STAMP(4);
  int tid2 = threadIdx.x; asm volatile("" : "+v"(tid2));
  {
  const int tid = tid2, lane = tid2 & 63, hh = lane >> 5, r32 = lane & 31;
  GS s;
  unsigned voffB[9];
#pragma unroll
  for (int i = 0; i < 9; ++i) { const int idx = i * 64 + lane, imm = i < 8 ? (i - 4) * 1024 : 0; voffB[i] = (idx / 18) * ROWB + (idx % 18) * 16 - imm + 4096; }
  s.ring = __builtin_amdgcn_readfirstlane(ldsb + ABYTES + w * 2 * SLOT);
  const int g4 = lane >> 4, cl = lane & 15;
  s.wbase = s.ring + (4 * g4 + ((lane & 15) >> 2)) * 288 + 8 * (lane & 3);
  const unsigned sw = (cl >> 1) & 7;
  const unsigned ab_e = ldsb + cl * ROWB + ((g4 ^ sw) * 16), ab_o = ldsb + cl * ROWB + (((4 + g4) ^ sw) * 16);
  const __amdgpu_buffer_rsrc_t rX = __builtin_amdgcn_make_buffer_rsrc(p.ws + WS_X + (size_t)c * 64 * SLICE, 0, 64 * SLICE, 0x00020000);
  unsigned voffA[9];
#pragma unroll
  for (int i = 0; i < 9; ++i) { const int idx = i * 64 + lane, imm = i < 8 ? (i - 4) * 1024 : 0; voffA[i] = (idx / 18) * (DF * 2) + (idx % 18) * 16 - imm + 4096; }
  const char* gG = (const char*)p.wg + (size_t)(576 * j + 144 * w) * 2 - 4096;
  const char* gU = (const char*)p.wu + (size_t)(576 * j + 144 * w) * 2 - 4096;
  const char* gD = (const char*)p.wd + (size_t)(576 * j) * ROWB + 288 * w - 4096;
  f4 aG[9][4];
  {
    const char* gO = (const char*)p.wo + (size_t)(512 * j) * ROWB + 288 * w - 4096;
    asm volatile("s_waitcnt lgkmcnt(0)" ::: "memory");
    BAR();
    issue_all(s, 0, voffB, gO); issue_all(s, 1, voffB, gO + (size_t)32 * ROWB);
    asm volatile("s_nop 7" ::: "memory");
#pragma unroll
    for (int qb = 0; qb < 2; ++qb) {
      const float il = __builtin_amdgcn_rcpf(swap32_add(a.l[qb]));
      const int row = 32 * qb + r32; const unsigned swr = (r32 >> 1) & 7;
#pragma unroll
      for (int db = 0; db < 4; ++db) {
        asm volatile("" : "+a"(a.O[db][qb]));
        const f16v o = a.O[db][qb];
#pragma unroll
        for (int u = 0; u < 2; ++u) {
          const u4 wv = {pk((f2){o[4 * u], o[4 * u + 1]} * il), pk((f2){o[4 * u + 2], o[4 * u + 3]} * il), pk((f2){o[4 * u + 8], o[4 * u + 9]} * il), pk((f2){o[4 * u + 10], o[4 * u + 11]} * il)};
          *(LAS u4*)(size_t)(ldsb + row * ROWB + (((4 * (4 * w + db) + 2 * u + hh) ^ swr) * 16)) = wv;
        }
      }
    }
    asm volatile("s_waitcnt vmcnt(9) lgkmcnt(0)" ::: "memory");
    BAR();
    STAMP(5);
    read_w(s, 0); read_a(s, 0, ab_e);
    asm volatile("s_waitcnt lgkmcnt(0)" ::: "memory");
    issue_all(s, 0, voffB, gO + (size_t)2 * 32 * ROWB);
    gemm_rows<16, 2, 0>(s, aG, voffB, gO, ab_e, ab_o, voffA, gG, gU, gG, [&]() {});
    STAMP(6);
    settle<0>(aG);
    store_partials(aG, rX, j * SLICE, 8 * SLICE, w, lane);
  }
  {
    int zlr[3], zcc[3];
    publish(flags + (0 * 256 + blockIdx.x) * FLAG_STRIDE, tag);
    STAMP(7);
    const int t3 = tid < 192 ? tid : tid - 64;
    u4 xr[3], gv[3];
#pragma unroll
    for (int i = 0; i < 3; ++i) {
      zdecode(t3 + 192 * i, zlr[i], zcc[i]);
      xr[i] = *(const u4*)(p.x + (size_t)(c * 64 + j * 8 + zlr[i]) * D + zcc[i] * 8);
    }
    gather_mod<576, 1152, false, 256>(p.ws, tag, ldsb, tmo, tid);
    asm volatile("s_waitcnt lgkmcnt(0)" ::: "memory");
    await8(flags + (0 * 256 + c * 8) * FLAG_STRIDE, tag, tmo, w, lane);
    const int lrow = 2 * w + hh, grow = j * 8 + lrow;
    u2 cbv[3][2], sbv[3][2];
#pragma unroll
    for (int i = 0; i < 3; ++i) {
      gv[i] = *(LAS u4*)(size_t)(ldsb + LDS_MOD + (2 * D + zcc[i] * 8) * 2);
      const int pos = (i < 2 || r32 < 8) ? r32 + 32 * i : r32;
      const int q = pos ^ ((grow >> 1) & 7), k0 = 32 * (q >> 2) + 4 * (q & 3);
#pragma unroll
      for (int h = 0; h < 2; ++h) { cbv[i][h] = *(LAS u2*)(size_t)(ldsb + LDS_MOD + (4 * D + k0 + 16 * h) * 2); sbv[i][h] = *(LAS u2*)(size_t)(ldsb + LDS_MOD + (3 * D + k0 + 16 * h) * 2); }
    }
    STAMP(8);
    {
      u4 v[3][8];
#pragma unroll
      for (int i = 0; i < 3; ++i)
#pragma unroll
        for (int jj = 0; jj < 8; ++jj) v[i][jj] = __builtin_amdgcn_raw_buffer_load_b128(rX, (j * 8 + jj) * SLICE + (t3 + 192 * i) * 16, 0, AUX_X);
#pragma unroll
      for (int i = 0; i < 3; ++i) {
        const u4 x1 = gated_residual(v[i], xr[i], gv[i]);
        if (tid < 192) {
          *(LAS u4*)(size_t)(ldsb + LDS_X1 + (tid + 192 * i) * 16) = x1;
          LAS f4* dst = (LAS f4*)(size_t)(ldsb + (zlr[i] * D + zcc[i] * 8) * 4);
          const f2 a0 = bf2(x1[0]), a1 = bf2(x1[1]), a2 = bf2(x1[2]), a3 = bf2(x1[3]);
          dst[0] = (f4){a0[0], a0[1], a1[0], a1[1]}; dst[1] = (f4){a2[0], a2[1], a3[0], a3[1]};
        }
      }
    }
    asm volatile("s_waitcnt lgkmcnt(0)" ::: "memory");
    BAR();
    STAMP(9);
    {
      f4 v[3][2];
      f2 sum = {0.f, 0.f};
#pragma unroll
      for (int i = 0; i < 3; ++i) {
        const bool on = (i < 2 || r32 < 8);
        const int pos = on ? r32 + 32 * i : r32;
        const int q = pos ^ ((grow >> 1) & 7), k0 = 32 * (q >> 2) + 4 * (q & 3);
#pragma unroll
        for (int h = 0; h < 2; ++h) {
          v[i][h] = *(LAS f4*)(size_t)(ldsb + (lrow * D + k0 + 16 * h) * 4);
          if (on) sum += (f2){v[i][h][0] + v[i][h][1], v[i][h][2] + v[i][h][3]};
        }
      }
      float sm = sum[0] + sum[1];
      sm = sum32(sm);
      const float mean = sm * (1.f / D);
      float q2 = 0.f;
#pragma unroll
      for (int i = 0; i < 3; ++i)
        if (i < 2 || r32 < 8) {
#pragma unroll
          for (int h = 0; h < 2; ++h)
#pragma unroll
            for (int e = 0; e < 4; ++e) { const float dl = v[i][h][e] - mean; q2 += dl * dl; }
        }
      q2 = sum32(q2);
      const float mr = rbf(mean), rs = __builtin_amdgcn_rcpf(rbf(__builtin_amdgcn_sqrtf(rbf(rbf(q2 * (1.f / D)) + 1e-5f))));
      const __amdgpu_buffer_rsrc_t rH = __builtin_amdgcn_make_buffer_rsrc(p.ws + WS_H2 + (size_t)c * ABYTES, 0, ABYTES, 0x00020000);
#pragma unroll
      for (int i = 0; i < 3; ++i) {
        u4 o;
#pragma unroll
        for (int h = 0; h < 2; ++h)
#pragma unroll
          for (int e = 0; e < 2; ++e) {
            unsigned t = pk((f2){v[i][h][2 * e], v[i][h][2 * e + 1]} - mr);
            t = pk(bf2(t) * rs);
            t = pk(bf2(t) * bf2(pk(bf2(cbv[i][h][e]) + 1.f)));
            o[2 * h + e] = pk(bf2(t) + bf2(sbv[i][h][e]));
          }
        if (i < 2 || r32 < 8) __builtin_amdgcn_raw_buffer_store_b128(o, rH, grow * ROWB + (r32 + 32 * i) * 16, 0, 16);
      }
      publish(flags + (1 * 256 + blockIdx.x) * FLAG_STRIDE, tag);
      STAMP(10);
      if (w != 0) {
        const unsigned dummy = __builtin_amdgcn_readfirstlane(ldsb + LDS_DUMMY);
        int li = tid - 64; li = li < 162 ? li : li - 94;
        const int r = 18 * c + li / 9, l = li % 9;
        touch((const char*)p.wg + (size_t)576 * j * 2, r * (DF * 2) + l * 128, dummy);
        touch((const char*)p.wu + (size_t)576 * j * 2, r * (DF * 2) + l * 128, dummy);
      }
      await8(flags + (1 * 256 + c * 8) * FLAG_STRIDE, tag, tmo, w, lane);
      STAMP(11);
      u4 hv[18];
#pragma unroll
      for (int i = 0; i < 18; ++i) hv[i] = __builtin_amdgcn_raw_buffer_load_b128(rH, (i * 256 + tid) * 16, 0, AUX_H);
#pragma unroll
      for (int i = 0; i < 18; ++i) *(LAS u4*)(size_t)(ldsb + (i * 256 + tid) * 16) = hv[i];
    }
    asm volatile("s_waitcnt vmcnt(0) lgkmcnt(0)" ::: "memory");
    BAR();
  }
  STAMP(12);
  {
    constexpr size_t KS_GU = (size_t)32 * DF * 2, KS_D = (size_t)32 * ROWB;
    read_w(s, 0); read_a(s, 0, ab_e);
    asm volatile("s_waitcnt lgkmcnt(0)" ::: "memory");
    issue_all(s, 0, voffA, gG + KS_GU);
    f4 aU[9][4];
    auto no = [&]() {};
    auto pfd = [&](int h) {
      int li = 64 * h + 16 * w + lane; li = li < 162 ? li : 161;
      if (lane < 16) touch((const char*)p.wd + (size_t)576 * j * ROWB, (18 * c) * ROWB + li * 128, __builtin_amdgcn_readfirstlane(ldsb + LDS_DUMMY));
    };
    auto kpair = [&](auto zc, auto lastc, auto vmc, int k, auto&& pf) {
      constexpr bool Z = decltype(zc)::value, LAST = decltype(lastc)::value; constexpr int VM = decltype(vmc)::value;
      const char* u1 = gU + (size_t)(k + 1) * KS_GU; const char* g2 = gG + (size_t)(k + 2) * KS_GU;
      step<0, 0, Z, true, false, true, 9, 0>(s, aG, voffA, u1, 0, no);
      if constexpr (!LAST) {
        step<1, 0, Z, true, true, true, 9, 8>(s, aU, voffA, g2, ab_o + 128 * (k >> 1), pf);
        step<0, 1, false, true, false, true, VM, 0>(s, aG, voffA, u1 + KS_GU, 0, no);
        step<1, 1, false, true, true, true, VM, 8>(s, aU, voffA, g2 + KS_GU, ab_e + 128 * ((k + 2) >> 1), no);
      } else {
        step<1, 0, Z, true, true, true, 9, 8>(s, aU, voffB, gD, ab_o + 128 * (k >> 1), no);
        step<0, 1, false, true, false, true, 9, 0>(s, aG, voffB, gD + KS_D, 0, no);
        step<1, 1, false, true, false, true, 9, 8>(s, aU, voffB, gD + 2 * KS_D, 0, no);
      }
    };
    kpair(std::true_type{}, std::false_type{}, IC<9>{}, 0, no);
    for (int k = 2; k < 8; k += 2) kpair(std::false_type{}, std::false_type{}, IC<9>{}, k, no);
    for (int k = 8; k < 14; k += 2) kpair(std::false_type{}, std::false_type{}, IC<10>{}, k, [&]() { pfd((k - 8) >> 1); });
    kpair(std::false_type{}, std::false_type{}, IC<9>{}, 14, no);
    kpair(std::false_type{}, std::true_type{}, IC<9>{}, 16, no);
    STAMP(13);
    settle<0>(aG); settle<8>(aU);
    u2 act[9][4];
#pragma unroll
    for (int t = 0; t < 9; ++t)
#pragma unroll
      for (int m = 0; m < 4; ++m)
#pragma unroll
        for (int e = 0; e < 2; ++e) {
          const f2 x = {aG[t][m][2 * e], aG[t][m][2 * e + 1]}, u = {aU[t][m][2 * e], aU[t][m][2 * e + 1]};
          const f2 z = x * (x * x * -0.10294324f + -2.3022082f);
          const f2 dn = (f2){__builtin_amdgcn_exp2f(z[0]), __builtin_amdgcn_exp2f(z[1])} + 1.f;
          act[t][m][e] = pk((x * u) * (f2){__builtin_amdgcn_rcpf(dn[0]), __builtin_amdgcn_rcpf(dn[1])});
        }
    asm volatile("s_waitcnt lgkmcnt(0)" ::: "memory");
    BAR();
#pragma unroll
    for (int t = 0; t < 9; ++t) {
      const int tt = 9 * w + t, ks = tt >> 1, ah = tt & 1;
      const unsigned ad = ldsb + cl * ROWB + (((4 * ks + g4) ^ sw) * 16) + 8 * ah;
#pragma unroll
      for (int m = 0; m < 4; ++m) *(LAS u2*)(size_t)(ad + m * 16 * ROWB) = act[t][m];
    }
    asm volatile("s_waitcnt lgkmcnt(0)" ::: "memory");
    BAR();
    STAMP(14);
    read_a(s, 0, ab_e);
    gemm_rows<18, 0, 0>(s, aG, voffB, gD, ab_e, ab_o, voffB, gD, gD, gD, [&]() {});
    STAMP(15);
  }
  {
    int tid3 = threadIdx.x; asm volatile("" : "+v"(tid3));
    const int tid = tid3, lane = tid3 & 63;
    settle<0>(aG);
    store_partials(aG, rX, j * 8 * SLICE, SLICE, w, lane);
    publish(flags + (2 * 256 + blockIdx.x) * FLAG_STRIDE, tag);
    STAMP(16);
    const int t3 = tid < 192 ? tid : tid - 64;
    int zlr[3], zcc[3];
    u4 gv[3], x1[3];
#pragma unroll
    for (int i = 0; i < 3; ++i) {
      zdecode(t3 + 192 * i, zlr[i], zcc[i]);
      gv[i] = *(LAS u4*)(size_t)(ldsb + LDS_MOD + (5 * D + zcc[i] * 8) * 2);
      x1[i] = *(LAS u4*)(size_t)(ldsb + LDS_X1 + (t3 + 192 * i) * 16);
    }
    if (blockIdx.x == 0 && w == 1) {
      for (unsigned spins = 0; spins <= SPIN_CAP; ++spins) {
        bool ok = true;
#pragma unroll
        for (int i = 0; i < 4; ++i) ok &= __hip_atomic_load((gu64*)(flags + (lane + 64 * i) * FLAG_STRIDE), __ATOMIC_RELAXED, __HIP_MEMORY_SCOPE_AGENT) == tag;
        if (__all(ok)) break;
        __builtin_amdgcn_s_sleep(4);
      }
      if (lane == 0) __hip_atomic_store((gu64*)(p.ws + WS_EPOCH), tag, __ATOMIC_RELAXED, __HIP_MEMORY_SCOPE_AGENT);
    }
    await8(flags + (2 * 256 + c * 8) * FLAG_STRIDE, tag, tmo, w, lane);
    STAMP(17);
    u4 v[3][8];
#pragma unroll
    for (int i = 0; i < 3; ++i)
#pragma unroll
      for (int jj = 0; jj < 8; ++jj) v[i][jj] = __builtin_amdgcn_raw_buffer_load_b128(rX, (jj * 8 + j) * SLICE + (t3 + 192 * i) * 16, 0, AUX_X);
#pragma unroll
    for (int i = 0; i < 3; ++i) {
      const u4 o = gated_residual(v[i], x1[i], gv[i]);
      if (tid < 192) *(u4*)(p.out + (size_t)(c * 64 + j * 8 + zlr[i]) * D + zcc[i] * 8) = o;
    }
  }
  STAMP(18);
  asm volatile("s_waitcnt lgkmcnt(0)" ::: "memory");
  STAMP_FLUSH();
  }
}

extern "C" void kernel_launch(void* const* d_in, const int* in_sizes, int n_in, void* d_out, int out_size, void* d_ws, size_t ws_size, hipStream_t stream) {
  Params p;
  p.x = (const u16*)d_in[0]; p.kc = (const u16*)d_in[1]; p.vc = (const u16*)d_in[2]; p.wq = (const u16*)d_in[3]; p.wo = (const u16*)d_in[4];
  p.wg = (const u16*)d_in[6]; p.wu = (const u16*)d_in[7]; p.wd = (const u16*)d_in[8];
  p.c = (const u16*)d_in[9]; p.wmod = (const u16*)d_in[10];
  p.out = (u16*)d_out; p.ws = (char*)d_ws;
 p.one = 1.0f; p.gap = 0u;
  (void)hipFuncSetAttribute((const void*)k_main, hipFuncAttributeMaxDynamicSharedMemorySize, LDS_BYTES);
  k_main<<<256, 256, LDS_BYTES, stream>>>(p);
}
```

```cpp
#include <hip/hip_runtime.h>
#include <stdint.h>
#include <utility>
#include <type_traits>

typedef unsigned short u16;
typedef unsigned long long u64;
typedef __bf16 b2 __attribute__((ext_vector_type(2)));
typedef __bf16 b4 __attribute__((ext_vector_type(4)));
typedef __bf16 b8 __attribute__((ext_vector_type(8)));
typedef float f2 __attribute__((ext_vector_type(2)));
typedef float f4 __attribute__((ext_vector_type(4)));
typedef float f16v __attribute__((ext_vector_type(16)));
typedef unsigned u2 __attribute__((ext_vector_type(2)));
typedef unsigned u4 __attribute__((ext_vector_type(4)));
typedef __attribute__((address_space(1))) unsigned gu32;
typedef __attribute__((address_space(1))) unsigned long long gu64;
#define LAS __attribute__((address_space(3)))
#define DI __device__ __forceinline__
#define GAP() __builtin_amdgcn_sched_barrier(0)
#define BAR() do { asm volatile("" ::: "memory"); __builtin_amdgcn_s_barrier(); asm volatile("" ::: "memory"); } while (0)
template <int V> using IC = std::integral_constant<int, V>;
#define AUX_X 18
#define AUX_H 18
#define PFD_K 14
#define UNROLL(N, F) [&]<int... I_>(std::integer_sequence<int, I_...>) { (F(IC<I_>{}), ...); }(std::make_integer_sequence<int, N>{})

constexpr int S = 2048, D = 576, DH = 128, SK = 2048, WIN = 512, DF = 4608;
constexpr int ROWB = D * 2;
constexpr int ABYTES = 64 * ROWB;
constexpr int SLICE = 8 * ROWB;
constexpr int SLOT = 9216;
constexpr int QSLOT = 8192;
constexpr int LDS_DUMMY = ABYTES + 8 * SLOT;
constexpr int LDS_X1 = LDS_DUMMY + 256;
constexpr int LDS_MOD = LDS_X1 + SLICE;
constexpr int LDS_BYTES = LDS_MOD + 6 * D * 2;
static_assert(LDS_BYTES <= 163840, "LDS");
constexpr size_t WS_EPOCH = 0;
constexpr size_t WS_TMO = 256;
constexpr size_t WS_MODG = 4096;
constexpr size_t WS_MODP = 131072;
constexpr size_t WS_FLAG = 32768;
constexpr int FLAG_STRIDE = 8;
constexpr size_t WS_X = 1048576;
constexpr size_t WS_H2 = WS_X + (size_t)32 * 64 * SLICE;
constexpr size_t WS_DBG = 200u << 20;
#define STAMP(i)
#define STAMP_FLUSH()
#define PST(i)

DI float bf2f(u16 v) { return __uint_as_float(((unsigned)v) << 16); }
DI float bflo(unsigned w) { return __uint_as_float(w << 16); }
DI float bfhi(unsigned w) { return __uint_as_float(w & 0xffff0000u); }
DI f2 bf2(unsigned w) { return (f2){bflo(w), bfhi(w)}; }
DI unsigned pk(float lo, float hi) { f2 v = {lo, hi}; b2 b = __builtin_convertvector(v, b2); return __builtin_bit_cast(unsigned, b); }
DI unsigned pk(f2 v) { b2 b = __builtin_convertvector(v, b2); return __builtin_bit_cast(unsigned, b); }
DI float rbf(float f) { return bflo(pk(f, f)); }
DI f2 rb2(f2 v) { return (f2){__uint_as_float(pk(0.f, v[0])), __uint_as_float(pk(0.f, v[1]))}; }
template <int CTRL> DI float dpp(float v) { return __uint_as_float(__builtin_amdgcn_update_dpp(0u, __float_as_uint(v), CTRL, 0xf, 0xf, false)); }
DI float sum8(float v) { v += dpp<0xB1>(v); v += dpp<0x4E>(v); v += dpp<0x141>(v); return v; }
DI float swap16_add(float x) { auto r = __builtin_amdgcn_permlane16_swap(__float_as_uint(x), __float_as_uint(x), false, false); return __uint_as_float(r[0]) + __uint_as_float(r[1]); }
DI float swap32_add(float x) { auto r = __builtin_amdgcn_permlane32_swap(__float_as_uint(x), __float_as_uint(x), false, false); return __uint_as_float(r[0]) + __uint_as_float(r[1]); }
DI float sum32(float v) { v = sum8(v); v += dpp<0x140>(v); return swap16_add(v); }
DI u16 f2bf(float f) { return (u16)(pk(f, f) & 0xffffu); }
DI float fmaxx(float a, float b) { return __builtin_elementwise_maximum(a, b); }
DI float fmax3(float a, float b, float c) { return __builtin_elementwise_maximum(__builtin_elementwise_maximum(a, b), c); }

DI void zdecode(int z, int& lr, int& cc) {
  const int w = z / 144, y = z - w * 144;
  if (y < 128) { const int tp = y >> 5; lr = (y >> 2) & 7; cc = 18 * w + 4 * tp + (y & 3); }
  else { const int y2 = y - 128; lr = y2 >> 1; cc = 18 * w + 16 + (y2 & 1); }
}

template <int IMM> DI void dma(unsigned voff, const char* g) { asm volatile("global_load_lds_dwordx4 %0, %1 offset:%2" :: "v"(voff), "s"(g), "i"(IMM) : "memory"); }
DI void setm0(unsigned a) { asm volatile("s_mov_b32 m0, %0" :: "s"(a) : "memory"); }
DI void touch(const char* p, unsigned voff, unsigned ldsdummy) {
  asm volatile("s_mov_b32 m0, %0\n\ts_nop 0\n\tglobal_load_lds_dword %1, %2" :: "s"(ldsdummy), "v"(voff), "s"(p) : "memory");
}
DI void touchp(const char* a, unsigned ldsdummy) {
  asm volatile("s_mov_b32 m0, %0\n\ts_nop 0\n\tglobal_load_lds_dword %1, off" :: "s"(ldsdummy), "v"(a) : "memory");
}
DI b4 ldT(unsigned base, int off) { return __builtin_amdgcn_ds_read_tr16_b64_v4bf16((LAS b4*)(size_t)(base + off)); }
DI b8 ldA(unsigned base, int off) { return *(LAS b8*)(size_t)(base + off); }
DI b8 cat(const b4& lo, const b4& hi) { return __builtin_shufflevector(lo, hi, 0, 1, 2, 3, 4, 5, 6, 7); }
template <bool AG, bool ZERO> DI void mfma16(f4& c, const b8& a, const b8& b) {
  if constexpr (AG) {
    if constexpr (ZERO) asm volatile("v_mfma_f32_16x16x32_bf16 %0, %1, %2, 0" : "=a"(c) : "v"(a), "v"(b));
    else asm volatile("v_mfma_f32_16x16x32_bf16 %0, %1, %2, %0" : "+a"(c) : "v"(a), "v"(b));
  } else {
    if constexpr (ZERO) asm volatile("v_mfma_f32_16x16x32_bf16 %0, %1, %2, 0" : "=&v"(c) : "v"(a), "v"(b));
    else asm volatile("v_mfma_f32_16x16x32_bf16 %0, %1, %2, %0" : "+v"(c) : "v"(a), "v"(b));
  }
}
template <bool AG, bool ZERO> DI void mfma32(f16v& c, const b8& a, const b8& b) {
  if constexpr (AG) {
    if constexpr (ZERO) asm volatile("v_mfma_f32_32x32x16_bf16 %0, %1, %2, 0" : "=a"(c) : "v"(a), "v"(b));
    else asm volatile("v_mfma_f32_32x32x16_bf16 %0, %1, %2, %0" : "+a"(c) : "v"(a), "v"(b));
  } else {
    if constexpr (ZERO) asm volatile("v_mfma_f32_32x32x16_bf16 %0, %1, %2, 0" : "=&v"(c) : "v"(a), "v"(b));
    else asm volatile("v_mfma_f32_32x32x16_bf16 %0, %1, %2, %0" : "+v"(c) : "v"(a), "v"(b));
  }
}
template <int NVG> DI void settle(f4 (&acc)[9][4]) {
  asm volatile("s_nop 7\n\ts_nop 7" ::: "memory");
#pragma unroll
  for (int t = 0; t < 9; ++t)
#pragma unroll
    for (int m = 0; m < 4; ++m) { if (t * 4 + m < 36 - NVG) asm volatile("" : "+a"(acc[t][m])); else asm volatile("" : "+v"(acc[t][m])); }
}

struct GS {
  b4 wlo[2][9], whi[2][9];
  b8 af[2][4];
  unsigned wbase, ring;
};
DI void issue_all(const GS& s, int slot, const unsigned (&voff)[9], const char* gp) {
  const unsigned sl = s.ring + slot * SLOT;
  setm0(sl + 4096); asm volatile("s_nop 0");
  dma<-4096>(voff[0], gp); dma<-3072>(voff[1], gp); dma<-2048>(voff[2], gp); dma<-1024>(voff[3], gp);
  dma<0>(voff[4], gp); dma<1024>(voff[5], gp); dma<2048>(voff[6], gp); dma<3072>(voff[7], gp);
  setm0(sl + 8192); asm volatile("s_nop 0"); dma<0>(voff[8], gp);
}
template <int PAR, int AP, bool ZERO, bool RDW, bool RDA, bool DMA, int VM, int NVG, typename PF>
DI void step(GS& s, f4 (&acc)[9][4], const unsigned (&voff)[9], const char* gp, unsigned ab, PF&& pf) {
  const unsigned sl = s.ring + (PAR ^ 1) * SLOT;
  const unsigned wb = s.wbase + (PAR ^ 1) * SLOT;
  auto mm = [&](auto ic) {
    constexpr int I = decltype(ic)::value, t = I >> 2, m = I & 3;
    mfma16<(I < 36 - NVG), ZERO>(acc[t][m], cat(s.wlo[PAR][t], s.whi[PAR][t]), s.af[AP][m]);
  };
  auto rd = [&](auto ic) {
    constexpr int I = decltype(ic)::value;
    if constexpr (I < 4) { if constexpr (RDA) s.af[AP ^ 1][I] = ldA(ab, I * 16 * ROWB); }
    else if constexpr (RDW) {
      if constexpr (((I - 4) & 1) == 0) s.wlo[PAR ^ 1][(I - 4) >> 1] = ldT(wb, 32 * ((I - 4) >> 1));
      else s.whi[PAR ^ 1][(I - 4) >> 1] = ldT(wb, 32 * ((I - 4) >> 1) + 4608);
    }
  };
  auto gap = [&](auto gc) {
    constexpr int G = decltype(gc)::value;
    mm(IC<2 * G>{});
    if constexpr (G == 0 && RDW) asm volatile("s_waitcnt vmcnt(%0)" :: "i"(VM) : "memory");
    if constexpr (G <= 10) rd(IC<2 * G>{});
    if constexpr (DMA) {
      if constexpr (G == 12) { asm volatile("s_waitcnt lgkmcnt(0)" ::: "memory"); setm0(sl + 4096); }
      if constexpr (G >= 13 && G <= 16) dma<(2 * (G - 13) - 4) * 1024>(voff[2 * (G - 13)], gp);
      if constexpr (G == 17) dma<0>(voff[8], gp);
    }
    GAP();
    mm(IC<2 * G + 1>{});
    if constexpr (G <= 10) rd(IC<2 * G + 1>{});
    if constexpr (DMA) {
      if constexpr (G >= 13 && G <= 16) dma<(2 * (G - 13) + 1 - 4) * 1024>(voff[2 * (G - 13) + 1], gp);
      if constexpr (G == 16) setm0(sl + 8192);
    }
    if constexpr (G == 17) pf();
    GAP();
  };
  UNROLL(18, gap);
}
DI void read_w(GS& s, int par) {
#pragma unroll
  for (int t = 0; t < 9; ++t) { s.wlo[par][t] = ldT(s.wbase + par * SLOT, 32 * t); s.whi[par][t] = ldT(s.wbase + par * SLOT, 32 * t + 4608); }
}
DI void read_a(GS& s, int set, unsigned ab) {
#pragma unroll
  for (int m = 0; m < 4; ++m) s.af[set][m] = ldA(ab, m * 16 * ROWB);
}
struct Epi { __amdgpu_buffer_rsrc_t rs; unsigned base, dstride, ownlds; int own, w, lane; };
template <int U> DI void epi_unit(f4 (&acc)[9][4], const Epi& e) {
  const int G = e.lane >> 4, c = e.lane & 15, lr = c & 7, dh = c >> 3;
  constexpr int ta = U < 16 ? 2 * (U >> 2) : 8, tb = U < 16 ? ta + 1 : 8, ma = U < 16 ? (U & 3) : 2 * (U - 16), mb = U < 16 ? ma : ma + 1;
  asm volatile("" : "+a"(acc[ta][ma]), "+a"(acc[tb][mb]));
  const f4 a = acc[ta][ma], b = acc[tb][mb];
  auto r0 = __builtin_amdgcn_permlane16_swap(pk(a[0], a[1]), pk(b[0], b[1]), false, false);
  auto r1 = __builtin_amdgcn_permlane16_swap(pk(a[2], a[3]), pk(b[2], b[3]), false, false);
  const u4 v = {r0[0], r1[0], r0[1], r1[1]};
  const int d = U < 16 ? 2 * ma + dh : 2 * (ma + (G & 1)) + dh;
  const unsigned off = U < 16 ? e.w * 2304 + (U >> 2) * 512 + lr * 64 + (2 * (G & 1) + (G >> 1)) * 16 : e.w * 2304 + 2048 + lr * 32 + (G >> 1) * 16;
  __builtin_amdgcn_raw_buffer_store_b128(v, e.rs, d == e.own ? 0x7ffffff0u : e.base + d * e.dstride + off, 0, 16);
  if ((U < 16 ? ma : ma >> 1) == (U < 16 ? e.own >> 1 : e.own >> 2)) { if (d == e.own) *(LAS u4*)(size_t)(e.ownlds + off) = v; }
}
template <bool NEXT, int VM> DI void final2(GS& s, f4 (&acc)[9][4], const unsigned (&nvoff)[9], const char* nx1, unsigned ab, const Epi& e) {
  const unsigned sl = s.ring + SLOT, wb = s.wbase + SLOT;
  asm volatile("s_waitcnt vmcnt(%0)" :: "i"(VM) : "memory");
#pragma unroll
  for (int m = 0; m < 4; ++m) s.af[1][m] = ldA(ab, m * 16 * ROWB);
  auto rdw = [&](auto ic) { constexpr int t = decltype(ic)::value; s.wlo[1][t] = ldT(wb, 32 * t); s.whi[1][t] = ldT(wb, 32 * t + 4608); };
  rdw(IC<0>{}); rdw(IC<1>{});
  GAP();
  auto grp = [&](auto gc) {
    constexpr int G = decltype(gc)::value, t = G >> 1, st = G & 1;
    auto mm = [&](auto mc) { constexpr int m = decltype(mc)::value; mfma16<true, false>(acc[t][m], cat(s.wlo[st][t], s.whi[st][t]), s.af[st][m]); };
    mm(IC<0>{});
    if constexpr (st == 0 && t + 2 < 9) rdw(IC<t + 2>{});
    if constexpr (NEXT && G == 15) { asm volatile("s_waitcnt lgkmcnt(0)" ::: "memory"); setm0(sl + 4096); }
    GAP();
    constexpr int U = G >= 5 ? G - 5 : -1;
    mm(IC<1>{});
    if constexpr (U >= 0 && U < 16 && (U >> 2) * 4 + 5 <= G) epi_unit<(U >= 0 && U < 16) ? U : 0>(acc, e);
    GAP();
    mm(IC<2>{});
    if constexpr (NEXT && G == 16) { dma<-4096>(nvoff[0], nx1); dma<-3072>(nvoff[1], nx1); dma<-2048>(nvoff[2], nx1); dma<-1024>(nvoff[3], nx1); }
    if constexpr (NEXT && G == 17) { dma<0>(nvoff[4], nx1); dma<1024>(nvoff[5], nx1); dma<2048>(nvoff[6], nx1); dma<3072>(nvoff[7], nx1); setm0(sl + 8192); }
    GAP();
    mm(IC<3>{});
    GAP();
  };
  UNROLL(18, grp);
  if constexpr (NEXT) dma<0>(nvoff[8], nx1);
  asm volatile("s_nop 7" ::: "memory");
  epi_unit<13>(acc, e); epi_unit<14>(acc, e); epi_unit<15>(acc, e); epi_unit<16>(acc, e); epi_unit<17>(acc, e);
}
template <int NS, int NEXT, int NPF, typename PF> DI void gemm_rows(GS& s, f4 (&acc)[9][4], const unsigned (&voff)[9], const char* gW, unsigned ab_e, unsigned ab_o,
                                              const unsigned (&nvoff)[9], const char* nx0, const char* nx1, const Epi& e, PF&& pf) {
  constexpr size_t KS = (size_t)32 * ROWB;
  auto no = [&]() {};
  step<0, 0, true, true, true, true, 9, 0>(s, acc, voff, gW + 3 * KS, ab_o, no);
  step<1, 1, false, true, true, true, 9, 0>(s, acc, voff, gW + 4 * KS, ab_e + 128, pf);
  step<0, 0, false, true, true, true, 9 + NPF, 0>(s, acc, voff, gW + 5 * KS, ab_o + 128, no);
  step<1, 1, false, true, true, true, 9 + NPF, 0>(s, acc, voff, gW + 6 * KS, ab_e + 256, no);
  for (int k = 4; k < NS - 4; k += 2) {
    step<0, 0, false, true, true, true, 9, 0>(s, acc, voff, gW + (size_t)(k + 3) * KS, ab_o + 128 * (k >> 1), no);
    step<1, 1, false, true, true, true, 9, 0>(s, acc, voff, gW + (size_t)(k + 4) * KS, ab_e + 128 * ((k + 2) >> 1), no);
  }
  step<0, 0, false, true, true, true, 9, 0>(s, acc, voff, gW + (size_t)(NS - 1) * KS, ab_o + 128 * ((NS - 4) >> 1), no);
  step<1, 1, false, true, true, NEXT != 0, 9, 0>(s, acc, nvoff, nx0, ab_e + 128 * ((NS - 2) >> 1), no);
  final2<NEXT != 0, NEXT != 0 ? 9 : 0>(s, acc, nvoff, nx1, ab_o + 128 * ((NS - 2) >> 1), e);
}

constexpr unsigned SPIN_CAP = 1u << 17;
DI void publish(u64* flag, u64 tag) {
  asm volatile("s_waitcnt vmcnt(0)" ::: "memory");
  BAR();
  if (threadIdx.x == 0) __hip_atomic_store((gu64*)flag, tag, __ATOMIC_RELAXED, __HIP_MEMORY_SCOPE_AGENT);
}
DI void await8(u64* flags8, u64 tag, unsigned* tmo, int w, int lane) {
  if (w == 0) {
    for (unsigned spins = 0;; ++spins) {
      const u64 v = __hip_atomic_load((gu64*)(flags8 + (lane & 7) * FLAG_STRIDE), __ATOMIC_RELAXED, __HIP_MEMORY_SCOPE_AGENT);
      if (__all(v == tag)) break;
      if (spins > SPIN_CAP) { if (lane == 0) __hip_atomic_store((gu32*)tmo, 1u, __ATOMIC_RELAXED, __HIP_MEMORY_SCOPE_AGENT); break; }
      __builtin_amdgcn_s_sleep(1);
    }
  }
  BAR();
}
template <int P0, int NP, bool F32, int NT> DI void gather_mod(char* ws, u64 tag, unsigned ldsb, unsigned* tmo, int tid) {
  constexpr int N = (NP + NT - 1) / NT;
  const __amdgpu_buffer_rsrc_t rM = __builtin_amdgcn_make_buffer_rsrc(ws + WS_MODG, 0, 6 * D * 8, 0x00020000);
  const u64 gtag = tag << 16;
  const unsigned thi = (unsigned)(gtag >> 32), tlo = (unsigned)gtag & 0xffff0000u;
  u4 gr[N];
  for (unsigned spins = 0;; ++spins) {
    bool ok = true;
#pragma unroll
    for (int i = 0; i < N; ++i) gr[i] = __builtin_amdgcn_raw_buffer_load_b128(rM, (P0 + (tid + NT * i < NP ? tid + NT * i : (tid & 63))) * 16, 0, 16);
#pragma unroll
    for (int i = 0; i < N; ++i) ok &= gr[i][1] == thi && gr[i][3] == thi && (gr[i][0] & 0xffff0000u) == tlo && (gr[i][2] & 0xffff0000u) == tlo;
    if (__all(ok)) break;
    if (spins > SPIN_CAP / 8) { if ((tid & 63) == 0) __hip_atomic_store((gu32*)tmo, 2u, __ATOMIC_RELAXED, __HIP_MEMORY_SCOPE_AGENT); break; }
    __builtin_amdgcn_s_sleep(8);
  }
#pragma unroll
  for (int i = 0; i < N; ++i)
    if (tid + NT * i < NP) {
      const int idx = P0 + tid + NT * i;
      if (F32 && idx < D) {
        f2 v = {bflo(gr[i][0]), bflo(gr[i][2])};
        if (idx >= D / 2) v = rb2(v + 1.f);
        *(LAS f2*)(size_t)(ldsb + LDS_X1 + 1024 + idx * 8) = v;
      } else *(LAS unsigned*)(size_t)(ldsb + LDS_MOD + idx * 4) = (gr[i][0] & 0xffffu) | (gr[i][2] << 16);
    }
}
DI u4 gated_residual(const u4 (&v)[8], const u4& res, const u4& g) {
  u4 o;
#pragma unroll
  for (int e = 0; e < 4; ++e) {
    f2 s = bf2(v[0][e]);
#pragma unroll
    for (int j = 1; j < 8; ++j) s += bf2(v[j][e]);
    const unsigned t = pk(bf2(pk(s)) * bf2(g[e]));
    o[e] = pk(bf2(res[e]) + bf2(t));
  }
  return o;
}

constexpr float QSCALE = 0.08838834764831845f * 1.4426950408889634f;
constexpr float THR = 40.0f;
struct QS {
  b4 wlo[2][8], whi[2][8];
  b8 hf[2][4];
  unsigned wb[4];
  unsigned hb[4];
  unsigned ring;
};
template <int PAR, bool ZERO, bool RD, bool DMA, int VM, typename PF>
DI void qstep(QS& s, f16v (&acc)[4][2], const unsigned (&voff)[8], const char* gp, unsigned hoff, PF&& pf) {
  const unsigned sl = s.ring + (PAR ^ 1) * QSLOT;
  auto rd = [&](auto ic) {
    constexpr int I = decltype(ic)::value;
    if constexpr (RD) {
      if constexpr (I < 4) {
        s.hf[PAR ^ 1][I] = ldA(s.hb[2 * (PAR ^ 1) + (I >> 1)] + hoff, (I & 1) * 32 * ROWB);
      } else {
        constexpr int J = I - 4, f = J >> 1, t = J & 1, ks = f >> 2, dt = f & 3;
        const b4 v = ldT(s.wb[dt], (PAR ^ 1) * QSLOT + 4096 * ks + 1024 * t);
        if constexpr (t == 0) s.wlo[PAR ^ 1][f] = v; else s.whi[PAR ^ 1][f] = v;
      }
    }
  };
  auto gap = [&](auto gc) {
    constexpr int G = decltype(gc)::value, ks = G >> 3, dt = (G >> 1) & 3, qb = G & 1, f = ks * 4 + dt;
    mfma32<false, ZERO && ks == 0>(acc[dt][qb], cat(s.wlo[PAR][f], s.whi[PAR][f]), s.hf[PAR][ks * 2 + qb]);
    if constexpr (G == 0 && RD) asm volatile("s_waitcnt vmcnt(%0)" :: "i"(VM) : "memory");
    if constexpr (G <= 6) { rd(IC<3 * G>{}); rd(IC<3 * G + 1>{}); if constexpr (3 * G + 2 < 20) rd(IC<3 * G + 2>{}); }
    if constexpr (DMA) {
      if constexpr (G == 9) { asm volatile("s_waitcnt lgkmcnt(0)" ::: "memory"); setm0(sl + 4096); }
      if constexpr (G >= 10 && G <= 13) { dma<(2 * (G - 10) - 4) * 1024>(voff[2 * (G - 10)], gp); dma<(2 * (G - 10) + 1 - 4) * 1024>(voff[2 * (G - 10) + 1], gp); }
    }
    if constexpr (G == 15) pf();
    GAP();
  };
  UNROLL(16, gap);
}

struct AT {
  f16v S[2][2][2];
  f16v negm[2];
  f16v O[4][2];
  b8 q[2][8];
  b8 P[2][4];
  float m[2], l[2], rs0[2], rs1[2], fct[2], pm0[2], pm1[2];
  float one;
  unsigned kb_[2], vb_[2];
  unsigned voff[8];
  unsigned m0k, m0v;
  const char *gk, *gv;
  bool pend;
};
template <int MODE> DI void qk(f16v& s, const b8& k, const b8& q, const f16v& negm) {
  if constexpr (MODE == 0) asm volatile("v_mfma_f32_32x32x16_bf16 %0, %1, %2, 0" : "=&v"(s) : "v"(k), "a"(q));
  else if constexpr (MODE == 1) asm volatile("v_mfma_f32_32x32x16_bf16 %0, %1, %2, %3" : "=&v"(s) : "v"(k), "a"(q), "v"(negm));
  else asm volatile("v_mfma_f32_32x32x16_bf16 %0, %1, %2, %0" : "+v"(s) : "v"(k), "a"(q));
}
template <int XP, int KB, int E> DI void sm_exp(AT& a) { constexpr int qb = E >> 4, r = E & 15; a.S[XP][KB][qb][r] = __builtin_amdgcn_exp2f(a.S[XP][KB][qb][r]); }
template <int XP, int KB, int E> DI void sm_add(AT& a) {
  constexpr int qb = E >> 4, r = E & 15;
  if constexpr (r & 1) a.rs1[qb] = __builtin_fmaf(a.S[XP][KB][qb][r], a.one, a.rs1[qb]); else a.rs0[qb] += a.S[XP][KB][qb][r];
}
template <int XP, int C> DI void sm_pack(AT& a) {
  constexpr int kb = C >> 4, qb = (C >> 3) & 1, s = (C >> 2) & 1, p = C & 3;
  u4 w = __builtin_bit_cast(u4, a.P[qb][kb * 2 + s]);
  w[p] = pk(a.S[XP][kb][qb][8 * s + 2 * p], a.S[XP][kb][qb][8 * s + 2 * p + 1]);
  a.P[qb][kb * 2 + s] = __builtin_bit_cast(b8, w);
  if constexpr (p == 3) asm volatile("" : "+v"(a.P[qb][kb * 2 + s]));
}
template <int X, bool FIRST, bool DK, bool DV, typename PF> DI void phaseA(AT& a, int t, PF&& pf) {
  constexpr int XP = X ^ 1;
  const unsigned kslot = X * 16384;
  b8 kf[16];
  auto kread = [&](auto ic) { constexpr int I = decltype(ic)::value, ks = I >> 1, kb = I & 1; kf[I] = ldA(a.kb_[ks & 1], kslot + 8192 * kb + 512 * (ks >> 1)); };
  kread(IC<0>{}); kread(IC<1>{}); kread(IC<2>{}); kread(IC<3>{});
  const char* gk = a.gk + (size_t)(t + 1) * 16384; const char* gv = a.gv + (size_t)t * 16384;
  auto gap = [&](auto gc) {
    constexpr int G = decltype(gc)::value, ks = G >> 2, kb = (G >> 1) & 1, qb = G & 1, I = G >> 1;
    qk<(ks == 0) ? (FIRST ? 0 : 1) : 2>(a.S[X][kb][qb], kf[I], a.q[qb][ks], a.negm[qb]);
    if constexpr ((G & 1) == 0 && I + 4 < 16) kread(IC<I + 4>{});
    if constexpr (DK) { if constexpr (G == 0) setm0(a.m0k + (X ^ 1) * 16384); if constexpr ((G & 1) && G < 16) dma<((G >> 1) - 4) * 1024>(a.voff[G >> 1], gk); }
    if constexpr (DV) { if constexpr (G == 16) setm0(a.m0v + X * 16384); if constexpr ((G & 1) && G > 16) dma<(((G - 16) >> 1) - 4) * 1024>(a.voff[(G - 16) >> 1], gv); }
    if constexpr (!FIRST) {
      if constexpr (G < 16) { sm_exp<XP, 1, 2 * G>(a); sm_exp<XP, 1, 2 * G + 1>(a); sm_pack<XP, G>(a); }
      else { sm_pack<XP, G>(a); sm_add<XP, 1, 2 * (G - 16)>(a); sm_add<XP, 1, 2 * (G - 16) + 1>(a); }
    }
    GAP();
  };
  UNROLL(32, gap);
  pf();
  if constexpr (!FIRST) {
#pragma unroll
    for (int qb = 0; qb < 2; ++qb) { a.l[qb] += a.rs0[qb] + a.rs1[qb]; asm volatile("" : "+v"(a.l[qb])); }
  }
}
DI float swap32_max(float x) { auto r = __builtin_amdgcn_permlane32_swap(__float_as_uint(x), __float_as_uint(x), false, false); return fmaxx(__uint_as_float(r[0]), __uint_as_float(r[1])); }
template <int X, bool NOPV, bool ZERO> DI void phaseB(AT& a) {
  const unsigned vslot = (X ^ 1) * 16384;
  b4 vlo[16], vhi[16];
  auto vread = [&](auto ic) {
    constexpr int I = decltype(ic)::value, k4 = I >> 2, db = I & 3;
    vlo[I] = ldT(a.vb_[0], vslot + 2048 * (2 * k4) + 512 * db);
    vhi[I] = ldT(a.vb_[1], vslot + 2048 * (2 * k4 + 1) + 512 * db);
  };
  if constexpr (!NOPV) { vread(IC<0>{}); vread(IC<1>{}); vread(IC<2>{}); }
  float rmax[2];
  auto pv = [&](auto gc) {
    constexpr int G = decltype(gc)::value, I = G >> 1, k4 = I >> 2, db = I & 3, qb = G & 1;
    if constexpr (!NOPV) {
      mfma32<true, ZERO && k4 == 0>(a.O[db][qb], cat(vlo[I], vhi[I]), a.P[qb][k4]);
      if constexpr ((G & 1) == 0 && I + 3 < 16) vread(IC<I + 3>{});
    }
  };
  auto gap1 = [&](auto gc) {
    constexpr int G = decltype(gc)::value;
    pv(gc);
    if constexpr (G >= 4 && G < 12) {
      constexpr int i = G - 4, q = i >> 2, r = 4 * (i & 3);
      if constexpr ((i & 3) == 0) { a.pm0[q] = fmax3(a.S[X][0][q][0], a.S[X][0][q][1], a.S[X][1][q][0]); a.pm1[q] = fmax3(a.S[X][0][q][2], a.S[X][0][q][3], a.S[X][1][q][1]);
                                    a.pm0[q] = fmax3(a.pm0[q], a.S[X][1][q][2], a.S[X][1][q][3]); }
      else { a.pm0[q] = fmax3(a.pm0[q], a.S[X][0][q][r], a.S[X][0][q][r + 1]); a.pm1[q] = fmax3(a.pm1[q], a.S[X][0][q][r + 2], a.S[X][0][q][r + 3]);
             a.pm0[q] = fmax3(a.pm0[q], a.S[X][1][q][r], a.S[X][1][q][r + 1]); a.pm1[q] = fmax3(a.pm1[q], a.S[X][1][q][r + 2], a.S[X][1][q][r + 3]); }
    }
    if constexpr (G == 12 || G == 13) { constexpr int q = G - 12; rmax[q] = swap32_max(fmaxx(a.pm0[q], a.pm1[q])); }
    GAP();
  };
  UNROLL(14, gap1);
  if constexpr (NOPV) {
#pragma unroll
    for (int q = 0; q < 2; ++q) {
      a.m[q] = rmax[q]; a.l[q] = 0.f; a.fct[q] = 1.f;
#pragma unroll
      for (int kb = 0; kb < 2; ++kb)
#pragma unroll
        for (int e = 0; e < 16; ++e) a.S[X][kb][q][e] -= rmax[q];
#pragma unroll
      for (int e = 0; e < 16; ++e) a.negm[q][e] = -rmax[q];
    }
  } else {
    const bool grew = (rmax[0] > THR) | (rmax[1] > THR);
    if (__builtin_expect(__any((int)grew), 0)) {
#pragma unroll
      for (int q = 0; q < 2; ++q) {
        const float dl = fmaxx(rmax[q], 0.f); a.m[q] += dl; a.fct[q] = __builtin_amdgcn_exp2f(-dl);
#pragma unroll
        for (int kb = 0; kb < 2; ++kb)
#pragma unroll
          for (int e = 0; e < 16; ++e) a.S[X][kb][q][e] -= dl;
#pragma unroll
        for (int e = 0; e < 16; ++e) a.negm[q][e] = -a.m[q];
      }
      a.pend = true;
    }
  }
  a.rs0[0] = a.rs0[1] = a.rs1[0] = a.rs1[1] = 0.f;
  GAP();
  auto gap2 = [&](auto gc) {
    constexpr int G = decltype(gc)::value + 14;
    pv(IC<G>{});
    if constexpr (G >= 15) {
      constexpr int e0 = 2 * (G - 15);
      if constexpr (e0 < 32) { sm_exp<X, 0, e0>(a); sm_exp<X, 0, e0 + 1>(a); }
      if constexpr (G >= 16) { sm_add<X, 0, e0 - 2>(a); sm_add<X, 0, e0 - 1>(a); }
    }
    GAP();
  };
  UNROLL(18, gap2);
  asm volatile("" : "+v"(a.S[X][0][0]), "+v"(a.S[X][0][1]), "+v"(a.rs0[0]), "+v"(a.rs0[1]), "+v"(a.rs1[0]), "+v"(a.rs1[1]));
  if constexpr (!NOPV) {
    if (__builtin_expect(a.pend, 0)) {
      asm volatile("s_nop 7\n\ts_nop 7" ::: "memory");
#pragma unroll
      for (int db = 0; db < 4; ++db)
#pragma unroll
        for (int q = 0; q < 2; ++q) {
          asm volatile("" : "+a"(a.O[db][q]));
          f16v o = a.O[db][q];
#pragma unroll
          for (int e = 0; e < 16; ++e) o[e] *= a.fct[q];
          a.O[db][q] = o;
          asm volatile("" : "+a"(a.O[db][q]));
        }
      a.l[0] *= a.fct[0]; a.l[1] *= a.fct[1]; a.pend = false;
      asm volatile("s_nop 1" ::: "memory");
    }
  }
}

struct Params { const u16 *x, *c, *wmod, *wq, *kc, *vc, *wo, *wg, *wu, *wd; u16* out; char* ws; float one; unsigned gap; };

__global__ __launch_bounds__(256, 1) void k_main(Params p) {
  extern __shared__ __attribute__((aligned(16))) char lds[];
  const int tid = threadIdx.x, w = __builtin_amdgcn_readfirstlane(tid >> 6), lane = tid & 63;
  const int j = blockIdx.x & 7, c = blockIdx.x >> 3;
  const unsigned ldsb = (unsigned)(size_t)lds;
  const int hh = lane >> 5, r32 = lane & 31;
  u64* const flags = (u64*)(p.ws + WS_FLAG);
  unsigned* const tmo = (unsigned*)(p.ws + WS_TMO);
  STAMP(0);
  PST(0);
  QS qs;
  unsigned voffQ[8];
#pragma unroll
  for (int i = 0; i < 8; ++i) voffQ[i] = (4 * i + (lane >> 4)) * 8192 + 16 * ((lane & 15) ^ ((lane >> 4) << 2)) - (i - 4) * 1024 + 4096;
  const char* gq = (const char*)p.wq + (size_t)(4 * j + w) * 256 - 4096;
  qs.ring = __builtin_amdgcn_readfirstlane(ldsb + ABYTES + w * 2 * QSLOT);
  constexpr size_t KS_Q = (size_t)32 * 8192;
  auto issueq = [&](int slot, const char* gp) {
    setm0(qs.ring + slot * QSLOT + 4096); asm volatile("s_nop 0");
    dma<-4096>(voffQ[0], gp); dma<-3072>(voffQ[1], gp); dma<-2048>(voffQ[2], gp); dma<-1024>(voffQ[3], gp);
    dma<0>(voffQ[4], gp); dma<1024>(voffQ[5], gp); dma<2048>(voffQ[6], gp); dma<3072>(voffQ[7], gp);
  };
  const int mg = blockIdx.x % 54, ms = blockIdx.x / 54;
  u4 wm[5]; u16 cval[5];
#pragma unroll
  for (int i = 0; i < 5; ++i) {
    const int r = 144 * (ms & 3) + (i < 4 || tid < 128 ? (tid >> 3) + 32 * i : 0);
    wm[i] = *(const u4*)(p.wmod + (size_t)r * (6 * D) + mg * 64 + (tid & 7) * 8); cval[i] = p.c[r];
  }
  const u64 e0 = __hip_atomic_load((gu64*)(p.ws + WS_EPOCH), __ATOMIC_RELAXED, __HIP_MEMORY_SCOPE_AGENT);
  AT a;
  u64 tag;
  {
    f16v Qt[4][2];
    QS& s = qs;
    {
      const int rr = lane >> 3, sub = lane & 7;
      tag = ((u64)__builtin_amdgcn_readfirstlane((unsigned)(e0 >> 32)) << 32 | (u64)__builtin_amdgcn_readfirstlane((unsigned)e0)) + 1;
      const u64 gtag = tag << 16;
      PST(9);
      const __amdgpu_buffer_rsrc_t rP = __builtin_amdgcn_make_buffer_rsrc(p.ws + WS_MODP + (size_t)mg * 8192, 0, 8192, 0x00020000);
      if (blockIdx.x < 216) {
        float acc[8] = {};
#pragma unroll
        for (int i = 0; i < 5; ++i) {
          const float xc = bf2f(cval[i]);
          float sc = rbf(xc * __builtin_amdgcn_rcpf(1.f + __expf(-xc)));
          if (i == 4 && tid >= 128) sc = 0.f;
#pragma unroll
          for (int e = 0; e < 4; ++e) { acc[2 * e] += sc * bflo(wm[i][e]); acc[2 * e + 1] += sc * bfhi(wm[i][e]); }
        }
#pragma unroll
        for (int e = 0; e < 8; ++e) { float t = acc[e]; t += dpp<0x128>(t); acc[e] = swap32_add(swap16_add(t)); }
        const int k = (lane >> 3) & 3;
        const float v0 = k == 0 ? acc[0] : k == 1 ? acc[2] : k == 2 ? acc[4] : acc[6], v1 = k == 0 ? acc[1] : k == 1 ? acc[3] : k == 2 ? acc[5] : acc[7];
        if (lane < 32) __builtin_amdgcn_raw_buffer_store_b128((u4){__float_as_uint(v0), __float_as_uint(v1), (unsigned)tag, (unsigned)(tag >> 32)}, rP, (4 * ms + w) * 512 + (4 * (lane & 7) + k) * 16, 0, 16);
      }
      PST(10);
      if (blockIdx.x < 54 && w == 0) {
        const int pr = lane & 31, hf = lane >> 5;
        u4 gr[8];
        for (unsigned spins = 0;; ++spins) {
          bool ok = true;
#pragma unroll
          for (int k = 0; k < 8; ++k) gr[k] = __builtin_amdgcn_raw_buffer_load_b128(rP, (8 * hf + k) * 512 + pr * 16, 0, 16);
#pragma unroll
          for (int k = 0; k < 8; ++k) ok &= gr[k][2] == (unsigned)tag && gr[k][3] == (unsigned)(tag >> 32);
          if (__all(ok)) break;
          if (spins > SPIN_CAP) { if (lane == 0) __hip_atomic_store((gu32*)tmo, 3u, __ATOMIC_RELAXED, __HIP_MEMORY_SCOPE_AGENT); break; }
          __builtin_amdgcn_s_sleep(1);
        }
        f2 v = {0.f, 0.f};
#pragma unroll
        for (int k = 0; k < 8; ++k) v += (f2){__uint_as_float(gr[k][0]), __uint_as_float(gr[k][1])};
        v[0] = swap32_add(v[0]); v[1] = swap32_add(v[1]);
        const u64 g0 = gtag | f2bf(v[0]), g1 = gtag | f2bf(v[1]);
        const __amdgpu_buffer_rsrc_t rG = __builtin_amdgcn_make_buffer_rsrc(p.ws + WS_MODG, 0, 6 * D * 8, 0x00020000);
        if (lane < 32) __builtin_amdgcn_raw_buffer_store_b128((u4){(unsigned)g0, (unsigned)(g0 >> 32), (unsigned)g1, (unsigned)(g1 >> 32)}, rG, (mg * 32 + pr) * 16, 0, 16);
      }
      PST(11);
      u4 xv[2][9];
      GAP();
#pragma unroll
      for (int ps = 0; ps < 2; ++ps)
#pragma unroll
        for (int i = 0; i < 9; ++i) xv[ps][i] = *(const u4*)(p.x + (size_t)(c * 64 + 16 * w + 8 * ps + rr) * D + (sub + 8 * i) * 8);
      GAP();
      PST(1);
      STAMP(20);
      PST(2);
      f2 y[2][9][4];
      {
#pragma unroll
        for (int ps = 0; ps < 2; ++ps) {
          f2 a1 = {0.f, 0.f}, a2 = {0.f, 0.f};
#pragma unroll
          for (int i = 0; i < 9; ++i)
#pragma unroll
            for (int e = 0; e < 4; ++e) { y[ps][i][e] = bf2(xv[ps][i][e]); a1 += y[ps][i][e]; a2 = __builtin_elementwise_fma(y[ps][i][e], y[ps][i][e], a2); }
          float s1 = a1[0] + a1[1], s2 = a2[0] + a2[1];
          s1 = sum8(s1); s2 = sum8(s2);
          if (ps == 0) PST(3);
          const float mean = s1 * (1.f / D), var = __builtin_fmaxf(__builtin_fmaf(-mean, mean, s2 * (1.f / D)), 0.f);
          const float mr = rbf(mean), rs = __builtin_amdgcn_rcpf(rbf(__builtin_amdgcn_sqrtf(rbf(rbf(var) + 1e-5f))));
          f2 mr2 = {mr, mr}, rs2 = {rs, rs}; asm volatile("" : "+v"(mr2), "+v"(rs2));
#pragma unroll
          for (int i = 0; i < 9; ++i) {
            f2 t[4];
#pragma unroll
            for (int e = 0; e < 4; ++e) t[e] = y[ps][i][e] - mr2;
#pragma unroll
            for (int e = 0; e < 4; ++e) t[e] = rb2(t[e]);
#pragma unroll
            for (int e = 0; e < 4; ++e) t[e] = t[e] * rs2;
#pragma unroll
            for (int e = 0; e < 4; ++e) y[ps][i][e] = rb2(t[e]);
            asm volatile("" : "+v"(y[ps][i][0]), "+v"(y[ps][i][1]), "+v"(y[ps][i][2]), "+v"(y[ps][i][3]));
          }
        }
      }
      STAMP(21);
      PST(4);
      gather_mod<0, 1728, true, 256>(p.ws, tag, ldsb, tmo, tid);
      STAMP(22);
      PST(5);
      issueq(0, gq); issueq(1, gq + KS_Q);
      {
        const int li = tid < 144 ? tid : tid - 112;
        touch((const char*)p.wq + (size_t)512 * j * 2, (18 * c + (li >> 3)) * 8192 + (li & 7) * 128, __builtin_amdgcn_readfirstlane(ldsb + LDS_DUMMY));
      }
      asm volatile("s_waitcnt lgkmcnt(0)" ::: "memory");
      BAR();
      STAMP(23);
      PST(6);
      {
        const unsigned tb = ldsb + LDS_X1 + 1024 + sub * 32;
        f4 cst[2][4];
        auto ldc = [&](int i, f4 (&d)[4]) { d[0] = *(LAS f4*)(size_t)(tb + i * 256); d[1] = *(LAS f4*)(size_t)(tb + i * 256 + 16); d[2] = *(LAS f4*)(size_t)(tb + i * 256 + D * 4); d[3] = *(LAS f4*)(size_t)(tb + i * 256 + D * 4 + 16); };
        ldc(0, cst[0]);
        const unsigned ad0 = ldsb + (16 * w + rr) * ROWB + ((sub ^ ((rr >> 1) & 7)) * 16);
        const unsigned ad1 = ldsb + (16 * w + 8 + rr) * ROWB + ((sub ^ (((rr >> 1) + 4) & 7)) * 16);
#pragma unroll
        for (int i = 0; i < 9; ++i) {
          if (i + 1 < 9) ldc(i + 1, cst[(i + 1) & 1]);
          GAP();
          const f4 (&k)[4] = cst[i & 1];
          const f2 sh[4] = {{k[0][0], k[0][1]}, {k[0][2], k[0][3]}, {k[1][0], k[1][1]}, {k[1][2], k[1][3]}}, cc[4] = {{k[2][0], k[2][1]}, {k[2][2], k[2][3]}, {k[3][0], k[3][1]}, {k[3][2], k[3][3]}};
          f2 t[2][4]; u4 o[2];
#pragma unroll
          for (int ps = 0; ps < 2; ++ps)
#pragma unroll
            for (int e = 0; e < 4; ++e) t[ps][e] = y[ps][i][e] * cc[e];
#pragma unroll
          for (int ps = 0; ps < 2; ++ps)
#pragma unroll
            for (int e = 0; e < 4; ++e) t[ps][e] = rb2(t[ps][e]);
#pragma unroll
          for (int ps = 0; ps < 2; ++ps)
#pragma unroll
            for (int e = 0; e < 4; ++e) t[ps][e] = t[ps][e] + sh[e];
#pragma unroll
          for (int ps = 0; ps < 2; ++ps)
#pragma unroll
            for (int e = 0; e < 4; ++e) o[ps][e] = pk(t[ps][e]);
          *(LAS u4*)(size_t)(ad0 + i * 128) = o[0];
          *(LAS u4*)(size_t)(ad1 + i * 128) = o[1];
          GAP();
        }
      }
    }
    {
      const int blk = (lane >> 4) & 1, q = (lane & 15) >> 2, pp = lane & 3;
#pragma unroll
      for (int dt = 0; dt < 4; ++dt) s.wb[dt] = s.ring + (8 * hh + q) * 256 + 64 * (dt ^ q) + 32 * blk + 16 * (pp & 1) + 8 * (pp >> 1);
#pragma unroll
      for (int i = 0; i < 4; ++i) s.hb[i] = ldsb + r32 * ROWB + (((2 * i + hh) ^ ((r32 >> 1) & 7)) * 16);
    }
    asm volatile("s_waitcnt vmcnt(9) lgkmcnt(0)" ::: "memory");
    BAR();
    STAMP(1);
    PST(7);
#pragma unroll
    for (int f = 0; f < 8; ++f) { s.wlo[0][f] = ldT(s.wb[f & 3], 4096 * (f >> 2)); s.whi[0][f] = ldT(s.wb[f & 3], 4096 * (f >> 2) + 1024); }
#pragma unroll
    for (int i = 0; i < 4; ++i) s.hf[0][i] = ldA(s.hb[i >> 1], (i & 1) * 32 * ROWB);
    asm volatile("s_waitcnt lgkmcnt(0)" ::: "memory");
    issueq(0, gq + 2 * KS_Q);
    auto no = [&]() {};
    auto pfkv = [&](int h) {
      if (lane < 16) touch((const char*)(w < 2 ? p.kc : p.vc) + ((size_t)(2 * j + (w & 1)) * SK + (SK - WIN)) * DH * 2, (32 * c + 16 * h + lane) * 128, __builtin_amdgcn_readfirstlane(ldsb + LDS_DUMMY));
    };
    qstep<0, true, true, true, 9>(s, Qt, voffQ, gq + 3 * KS_Q, 0, no);
    qstep<1, false, true, true, 8>(s, Qt, voffQ, gq + 4 * KS_Q, 128, [&]() { pfkv(0); });
    qstep<0, false, true, true, 9>(s, Qt, voffQ, gq + 5 * KS_Q, 128, no);
    qstep<1, false, true, true, 9>(s, Qt, voffQ, gq + 6 * KS_Q, 256, [&]() { pfkv(1); });
    qstep<0, false, true, true, 9>(s, Qt, voffQ, gq + 7 * KS_Q, 256, no);
    qstep<1, false, true, true, 9>(s, Qt, voffQ, gq + 8 * KS_Q, 384, no);
    for (int n = 6; n < 14; n += 2) {
      qstep<0, false, true, true, 8>(s, Qt, voffQ, gq + (size_t)(n + 3) * KS_Q, 128 * ((n + 1) >> 1), no);
      qstep<1, false, true, true, 8>(s, Qt, voffQ, gq + (size_t)(n + 4) * KS_Q, 128 * ((n + 2) >> 1), no);
    }
    qstep<0, false, true, true, 8>(s, Qt, voffQ, gq + (size_t)17 * KS_Q, 128 * 7, no);
    qstep<1, false, true, false, 8>(s, Qt, voffQ, gq, 128 * 8, no);
    qstep<0, false, true, false, 0>(s, Qt, voffQ, gq, 128 * 8, no);
    qstep<1, false, false, false, 0>(s, Qt, voffQ, gq, 0, no);
    STAMP(2);
    PST(8);
    {
      const int kvl = w >> 1, half = w & 1;
      const unsigned kr = ldsb + kvl * 65536, vr = kr + 32768;
#pragma unroll
      for (int i = 0; i < 2; ++i) a.kb_[i] = kr + 2048 * (r32 >> 3) + 64 * (r32 & 7) + 16 * ((2 * i + hh) ^ ((r32 >> 2) & 3));
      const int blk = (lane >> 4) & 1, q = (lane & 15) >> 2, pp = lane & 3;
#pragma unroll
      for (int t = 0; t < 2; ++t) a.vb_[t] = vr + 64 * (4 * hh + q) + 16 * ((2 * blk + (pp >> 1)) ^ ((2 * t + hh) & 3)) + 8 * (pp & 1);
      const int st = lane >> 5, r7 = (lane >> 2) & 7, pos = lane & 3;
#pragma unroll
      for (int i = 0; i < 8; ++i) {
        const int rg = 4 * half + (i >> 1);
        a.voff[i] = rg * 2048 + r7 * 256 + (i & 1) * 128 + st * 64 + 16 * (pos ^ ((2 * (rg & 1) + (r7 >> 2)) & 3)) - (i - 4) * 1024 + 4096;
      }
      a.m0k = __builtin_amdgcn_readfirstlane(kr + 8192 * half + 4096); a.m0v = __builtin_amdgcn_readfirstlane(vr + 8192 * half + 4096);
      const size_t kvoff = ((size_t)(2 * j + kvl) * SK + (SK - WIN)) * DH * 2;
      a.gk = (const char*)p.kc + kvoff - 4096; a.gv = (const char*)p.vc + kvoff - 4096;
      a.pend = false; a.one = p.one;
    }
    asm volatile("s_waitcnt lgkmcnt(0)" ::: "memory");
    BAR();
    setm0(a.m0k); asm volatile("s_nop 0");
    dma<-4096>(a.voff[0], a.gk); dma<-3072>(a.voff[1], a.gk); dma<-2048>(a.voff[2], a.gk); dma<-1024>(a.voff[3], a.gk);
    dma<0>(a.voff[4], a.gk); dma<1024>(a.voff[5], a.gk); dma<2048>(a.voff[6], a.gk); dma<3072>(a.voff[7], a.gk);
    asm volatile("s_nop 7" ::: "memory");
#pragma unroll
    for (int dt = 0; dt < 4; ++dt)
#pragma unroll
      for (int qb = 0; qb < 2; ++qb) {
        asm volatile("" : "+v"(Qt[dt][qb]));
#pragma unroll
        for (int s2 = 0; s2 < 2; ++s2) {
          u4 w4;
#pragma unroll
          for (int e = 0; e < 4; ++e) w4[e] = pk((f2){Qt[dt][qb][8 * s2 + 2 * e], Qt[dt][qb][8 * s2 + 2 * e + 1]} * QSCALE);
          a.q[qb][2 * dt + s2] = __builtin_bit_cast(b8, w4);
          asm volatile("" : "+a"(a.q[qb][2 * dt + s2]));
        }
      }
  }
  STAMP(3);
  {
    auto tile_bar = [&]() { asm volatile("s_waitcnt vmcnt(0) lgkmcnt(0)" ::: "memory"); BAR(); };
    const char* pfp = w < 2 ? (const char*)(w == 0 ? p.wg : p.wu) + (size_t)576 * j * 2 + (size_t)(18 * c + lane / 9) * (DF * 2) + (lane % 9) * 128
                            : (const char*)p.wo + (size_t)512 * j * ROWB + (16 * c) * ROWB + (12 * (w - 2) + lane) * 128;
    const int pfs = w < 2 ? 3 * DF * 2 : 24 * 128, pfn = w < 2 ? 27 : 12;
    auto pft = [&](int) { if (lane < pfn) touchp(pfp, __builtin_amdgcn_readfirstlane(ldsb + LDS_DUMMY)); pfp += pfs; };
    auto tile_bar1 = [&]() { asm volatile("s_waitcnt vmcnt(1) lgkmcnt(0)" ::: "memory"); BAR(); };
    tile_bar(); phaseA<0, true, true, true>(a, 0, [&]() {}); asm volatile("s_nop 7\n\ts_nop 3" ::: "memory"); GAP(); phaseB<0, true, false>(a);
    tile_bar(); phaseA<1, false, true, true>(a, 1, [&]() { pft(1); }); phaseB<1, false, true>(a);
    for (int t = 2; t < 6; t += 2) {
      tile_bar1(); phaseA<0, false, true, true>(a, t, [&]() { pft(t); }); phaseB<0, false, false>(a);
      tile_bar1(); phaseA<1, false, true, true>(a, t + 1, [&]() { pft(t + 1); }); phaseB<1, false, false>(a);
    }
    tile_bar1(); phaseA<0, false, true, true>(a, 6, [&]() { pft(6); }); phaseB<0, false, false>(a);
    tile_bar1(); phaseA<1, false, false, true>(a, 7, [&]() {}); phaseB<1, false, false>(a);
    { auto f1 = [&](auto ic) { sm_exp<1, 1, decltype(ic)::value>(a); }; UNROLL(32, f1);
      auto f2_ = [&](auto ic) { sm_pack<1, decltype(ic)::value>(a); }; UNROLL(32, f2_);
      auto f3 = [&](auto ic) { sm_add<1, 1, decltype(ic)::value>(a); }; UNROLL(32, f3); }
#pragma unroll
    for (int qb = 0; qb < 2; ++qb) a.l[qb] += a.rs0[qb] + a.rs1[qb];
    tile_bar();
    const unsigned vslot = 16384;
    b4 vlo[16], vhi[16];
#pragma unroll
    for (int I = 0; I < 16; ++I) { vlo[I] = ldT(a.vb_[0], vslot + 2048 * (2 * (I >> 2)) + 512 * (I & 3)); vhi[I] = ldT(a.vb_[1], vslot + 2048 * (2 * (I >> 2) + 1) + 512 * (I & 3)); }
    asm volatile("s_nop 1" ::: "memory"); GAP();
#pragma unroll
    for (int G = 0; G < 32; ++G) { const int I = G >> 1; mfma32<true, false>(a.O[I & 3][G & 1], cat(vlo[I], vhi[I]), a.P[G & 1][I >> 2]); }
  }
  STAMP(4);
  int tid2 = threadIdx.x; asm volatile("" : "+v"(tid2));
  {
  const int tid = tid2, lane = tid2 & 63, hh = lane >> 5, r32 = lane & 31;
  GS s;
  unsigned voffB[9];
#pragma unroll
  for (int i = 0; i < 9; ++i) { const int idx = i * 64 + lane, imm = i < 8 ? (i - 4) * 1024 : 0; voffB[i] = (idx / 18) * ROWB + (idx % 18) * 16 - imm + 4096; }
  s.ring = __builtin_amdgcn_readfirstlane(ldsb + ABYTES + w * 2 * SLOT);
  const int g4 = lane >> 4, cl = lane & 15;
  s.wbase = s.ring + (4 * g4 + ((lane & 15) >> 2)) * 288 + 8 * (lane & 3);
  const unsigned sw = (cl >> 1) & 7;
  const unsigned ab_e = ldsb + cl * ROWB + ((g4 ^ sw) * 16), ab_o = ldsb + cl * ROWB + (((4 + g4) ^ sw) * 16);
  const __amdgpu_buffer_rsrc_t rX = __builtin_amdgcn_make_buffer_rsrc(p.ws + WS_X + (size_t)c * 64 * SLICE, 0, 64 * SLICE, 0x00020000);
  unsigned voffA[9];
#pragma unroll
  for (int i = 0; i < 9; ++i) { const int idx = i * 64 + lane, imm = i < 8 ? (i - 4) * 1024 : 0; voffA[i] = (idx / 18) * (DF * 2) + (idx % 18) * 16 - imm + 4096; }
  const char* gG = (const char*)p.wg + (size_t)(576 * j + 144 * w) * 2 - 4096;
  const char* gU = (const char*)p.wu + (size_t)(576 * j + 144 * w) * 2 - 4096;
  const char* gD = (const char*)p.wd + (size_t)(576 * j) * ROWB + 288 * w - 4096;
  f4 aG[9][4];
  {
    const char* gO = (const char*)p.wo + (size_t)(512 * j) * ROWB + 288 * w - 4096;
    asm volatile("s_waitcnt lgkmcnt(0)" ::: "memory");
    BAR();
    issue_all(s, 0, voffB, gO); issue_all(s, 1, voffB, gO + (size_t)32 * ROWB);
    asm volatile("s_nop 7" ::: "memory");
#pragma unroll
    for (int qb = 0; qb < 2; ++qb) {
      const float il = __builtin_amdgcn_rcpf(swap32_add(a.l[qb]));
      const int row = 32 * qb + r32; const unsigned swr = (r32 >> 1) & 7;
#pragma unroll
      for (int db = 0; db < 4; ++db) {
        asm volatile("" : "+a"(a.O[db][qb]));
        const f16v o = a.O[db][qb];
#pragma unroll
        for (int u = 0; u < 2; ++u) {
          const u4 wv = {pk((f2){o[4 * u], o[4 * u + 1]} * il), pk((f2){o[4 * u + 2], o[4 * u + 3]} * il), pk((f2){o[4 * u + 8], o[4 * u + 9]} * il), pk((f2){o[4 * u + 10], o[4 * u + 11]} * il)};
          *(LAS u4*)(size_t)(ldsb + row * ROWB + (((4 * (4 * w + db) + 2 * u + hh) ^ swr) * 16)) = wv;
        }
      }
    }
    asm volatile("s_waitcnt vmcnt(9) lgkmcnt(0)" ::: "memory");
    BAR();
    STAMP(5);
    read_w(s, 0); read_a(s, 0, ab_e);
    asm volatile("s_waitcnt lgkmcnt(0)" ::: "memory");
    issue_all(s, 0, voffB, gO + (size_t)2 * 32 * ROWB);
    const Epi e1 = {rX, (unsigned)(j * SLICE), 8u * SLICE, ldsb + LDS_X1, j, w, lane};
    gemm_rows<16, 2, 0>(s, aG, voffB, gO, ab_e, ab_o, voffA, gG, gU, e1, [&]() {});
    STAMP(6);
  }
  {
    int zlr[3], zcc[3];
    const int t3 = tid < 192 ? tid : tid - 64;
    u4 xr[3], gv[3];
#pragma unroll
    for (int i = 0; i < 3; ++i) {
      zdecode(t3 + 192 * i, zlr[i], zcc[i]);
      xr[i] = *(const u4*)(p.x + (size_t)(c * 64 + j * 8 + zlr[i]) * D + zcc[i] * 8);
      gv[i] = *(LAS u4*)(size_t)(ldsb + LDS_MOD + (2 * D + zcc[i] * 8) * 2);
    }
    const int lrow = 2 * w + hh, grow = j * 8 + lrow;
    u2 cbv[3][2], sbv[3][2];
#pragma unroll
    for (int i = 0; i < 3; ++i) {
      const int pos = (i < 2 || r32 < 8) ? r32 + 32 * i : r32;
      const int q = pos ^ ((grow >> 1) & 7), k0 = 32 * (q >> 2) + 4 * (q & 3);
#pragma unroll
      for (int h = 0; h < 2; ++h) { cbv[i][h] = *(LAS u2*)(size_t)(ldsb + LDS_MOD + (4 * D + k0 + 16 * h) * 2); sbv[i][h] = *(LAS u2*)(size_t)(ldsb + LDS_MOD + (3 * D + k0 + 16 * h) * 2); }
    }
    asm volatile("s_waitcnt lgkmcnt(0)" ::: "memory");
    publish(flags + (0 * 256 + blockIdx.x) * FLAG_STRIDE, tag);
    STAMP(7);
    await8(flags + (0 * 256 + c * 8) * FLAG_STRIDE, tag, tmo, w, lane);
    STAMP(8);
    {
      u4 v[3][8];
#pragma unroll
      for (int i = 0; i < 3; ++i) {
#pragma unroll
        for (int jj = 0; jj < 7; ++jj) v[i][jj] = __builtin_amdgcn_raw_buffer_load_b128(rX, (j * 8 + jj + (jj >= j)) * SLICE + (t3 + 192 * i) * 16, 0, AUX_X);
      }
#pragma unroll
      for (int i = 0; i < 3; ++i) v[i][7] = *(LAS u4*)(size_t)(ldsb + LDS_X1 + (t3 + 192 * i) * 16);
#pragma unroll
      for (int i = 0; i < 3; ++i) {
        const u4 x1 = gated_residual(v[i], xr[i], gv[i]);
        if (tid < 192) {
          *(LAS u4*)(size_t)(ldsb + LDS_X1 + (tid + 192 * i) * 16) = x1;
          LAS f4* dst = (LAS f4*)(size_t)(ldsb + (zlr[i] * D + zcc[i] * 8) * 4);
          const f2 a0 = bf2(x1[0]), a1 = bf2(x1[1]), a2 = bf2(x1[2]), a3 = bf2(x1[3]);
          dst[0] = (f4){a0[0], a0[1], a1[0], a1[1]}; dst[1] = (f4){a2[0], a2[1], a3[0], a3[1]};
        }
      }
    }
    asm volatile("s_waitcnt lgkmcnt(0)" ::: "memory");
    BAR();
    STAMP(9);
    {
      f4 v[3][2];
      f2 sum = {0.f, 0.f};
#pragma unroll
      for (int i = 0; i < 3; ++i) {
        const bool on = (i < 2 || r32 < 8);
        const int pos = on ? r32 + 32 * i : r32;
        const int q = pos ^ ((grow >> 1) & 7), k0 = 32 * (q >> 2) + 4 * (q & 3);
#pragma unroll
        for (int h = 0; h < 2; ++h) {
          v[i][h] = *(LAS f4*)(size_t)(ldsb + (lrow * D + k0 + 16 * h) * 4);
          if (on) sum += (f2){v[i][h][0] + v[i][h][1], v[i][h][2] + v[i][h][3]};
        }
      }
      float sm = sum[0] + sum[1];
      sm = sum32(sm);
      const float mean = sm * (1.f / D);
      float q2 = 0.f;
#pragma unroll
      for (int i = 0; i < 3; ++i)
        if (i < 2 || r32 < 8) {
#pragma unroll
          for (int h = 0; h < 2; ++h)
#pragma unroll
            for (int e = 0; e < 4; ++e) { const float dl = v[i][h][e] - mean; q2 += dl * dl; }
        }
      q2 = sum32(q2);
      const float mr = rbf(mean), rs = __builtin_amdgcn_rcpf(rbf(__builtin_amdgcn_sqrtf(rbf(rbf(q2 * (1.f / D)) + 1e-5f))));
      const __amdgpu_buffer_rsrc_t rH = __builtin_amdgcn_make_buffer_rsrc(p.ws + WS_H2 + (size_t)c * ABYTES, 0, ABYTES, 0x00020000);
      u4 oh[3];
#pragma unroll
      for (int i = 0; i < 3; ++i) {
        u4& o = oh[i];
#pragma unroll
        for (int h = 0; h < 2; ++h)
#pragma unroll
          for (int e = 0; e < 2; ++e) {
            unsigned t = pk((f2){v[i][h][2 * e], v[i][h][2 * e + 1]} - mr);
            t = pk(bf2(t) * rs);
            t = pk(bf2(t) * bf2(pk(bf2(cbv[i][h][e]) + 1.f)));
            o[2 * h + e] = pk(bf2(t) + bf2(sbv[i][h][e]));
          }
        if (i < 2 || r32 < 8) __builtin_amdgcn_raw_buffer_store_b128(o, rH, grow * ROWB + (r32 + 32 * i) * 16, 0, 16);
      }
      publish(flags + (1 * 256 + blockIdx.x) * FLAG_STRIDE, tag);
      STAMP(10);
      if (w != 0) {
        const unsigned dummy = __builtin_amdgcn_readfirstlane(ldsb + LDS_DUMMY);
        int li = tid - 64; li = li < 162 ? li : li - 94;
        const int r = 18 * c + li / 9, l = li % 9;
        touch((const char*)p.wg + (size_t)576 * j * 2, r * (DF * 2) + l * 128, dummy);
        touch((const char*)p.wu + (size_t)576 * j * 2, r * (DF * 2) + l * 128, dummy);
      }
      await8(flags + (1 * 256 + c * 8) * FLAG_STRIDE, tag, tmo, w, lane);
      STAMP(11);
      u4 hv[16];
#pragma unroll
      for (int i = 0; i < 16; ++i) { const int y = i * 256 + tid < 4032 ? i * 256 + tid : tid; hv[i] = __builtin_amdgcn_raw_buffer_load_b128(rH, (y + (y >= j * 576 ? 576 : 0)) * 16, 0, AUX_H); }
#pragma unroll
      for (int i = 0; i < 3; ++i) if (i < 2 || r32 < 8) *(LAS u4*)(size_t)(ldsb + grow * ROWB + (r32 + 32 * i) * 16) = oh[i];
#pragma unroll
      for (int i = 0; i < 16; ++i) { const int y = i * 256 + tid; if (i < 15 || y < 4032) *(LAS u4*)(size_t)(ldsb + (y + (y >= j * 576 ? 576 : 0)) * 16) = hv[i]; }
    }
    asm volatile("s_waitcnt vmcnt(0) lgkmcnt(0)" ::: "memory");
    BAR();
  }
  STAMP(12);
  {
    constexpr size_t KS_GU = (size_t)32 * DF * 2, KS_D = (size_t)32 * ROWB;
    read_w(s, 0); read_a(s, 0, ab_e);
    asm volatile("s_waitcnt lgkmcnt(0)" ::: "memory");
    issue_all(s, 0, voffA, gG + KS_GU);
    f4 aU[9][4];
    auto no = [&]() {};
    auto pfd = [&](int h) {
      int li = 64 * h + 16 * w + lane; li = li < 162 ? li : 161;
      if (lane < 16) touch((const char*)p.wd + (size_t)576 * j * ROWB, (18 * c) * ROWB + li * 128, __builtin_amdgcn_readfirstlane(ldsb + LDS_DUMMY));
    };
    auto kpair = [&](auto zc, auto lastc, auto vmc, int k, auto&& pf) {
      constexpr bool Z = decltype(zc)::value, LAST = decltype(lastc)::value; constexpr int VM = decltype(vmc)::value;
      const char* u1 = gU + (size_t)(k + 1) * KS_GU; const char* g2 = gG + (size_t)(k + 2) * KS_GU;
      step<0, 0, Z, true, false, true, 9, 0>(s, aG, voffA, u1, 0, no);
      if constexpr (!LAST) {
        step<1, 0, Z, true, true, true, 9, 8>(s, aU, voffA, g2, ab_o + 128 * (k >> 1), pf);
        step<0, 1, false, true, false, true, VM, 0>(s, aG, voffA, u1 + KS_GU, 0, no);
        step<1, 1, false, true, true, true, VM, 8>(s, aU, voffA, g2 + KS_GU, ab_e + 128 * ((k + 2) >> 1), no);
      } else {
        step<1, 0, Z, true, true, true, 9, 8>(s, aU, voffB, gD, ab_o + 128 * (k >> 1), no);
        step<0, 1, false, true, false, true, 9, 0>(s, aG, voffB, gD + KS_D, 0, no);
        step<1, 1, false, true, false, true, 9, 8>(s, aU, voffB, gD + 2 * KS_D, 0, no);
      }
    };
    kpair(std::true_type{}, std::false_type{}, IC<9>{}, 0, no);
    for (int k = 2; k < 8; k += 2) kpair(std::false_type{}, std::false_type{}, IC<9>{}, k, no);
    for (int k = 8; k < 14; k += 2) kpair(std::false_type{}, std::false_type{}, IC<10>{}, k, [&]() { pfd((k - 8) >> 1); });
    kpair(std::false_type{}, std::false_type{}, IC<9>{}, 14, no);
    kpair(std::false_type{}, std::true_type{}, IC<9>{}, 16, no);
    STAMP(13);
    settle<0>(aG); settle<8>(aU);
    u2 act[9][4];
#pragma unroll
    for (int t = 0; t < 9; ++t)
#pragma unroll
      for (int m = 0; m < 4; ++m)
#pragma unroll
        for (int e = 0; e < 2; ++e) {
          const f2 x = {aG[t][m][2 * e], aG[t][m][2 * e + 1]}, u = {aU[t][m][2 * e], aU[t][m][2 * e + 1]};
          const f2 z = x * (x * x * -0.10294324f + -2.3022082f);
          const f2 dn = (f2){__builtin_amdgcn_exp2f(z[0]), __builtin_amdgcn_exp2f(z[1])} + 1.f;
          act[t][m][e] = pk((x * u) * (f2){__builtin_amdgcn_rcpf(dn[0]), __builtin_amdgcn_rcpf(dn[1])});
        }
    asm volatile("s_waitcnt lgkmcnt(0)" ::: "memory");
    BAR();
#pragma unroll
    for (int t = 0; t < 9; ++t) {
      const int tt = 9 * w + t, ks = tt >> 1, ah = tt & 1;
      const unsigned ad = ldsb + cl * ROWB + (((4 * ks + g4) ^ sw) * 16) + 8 * ah;
#pragma unroll
      for (int m = 0; m < 4; ++m) *(LAS u2*)(size_t)(ad + m * 16 * ROWB) = act[t][m];
    }
    asm volatile("s_waitcnt lgkmcnt(0)" ::: "memory");
    BAR();
    STAMP(14);
    read_a(s, 0, ab_e);
    const Epi e2 = {rX, (unsigned)(j * 8 * SLICE), (unsigned)SLICE, s.ring - w * 2304, j, w, lane};
    gemm_rows<18, 0, 0>(s, aG, voffB, gD, ab_e, ab_o, voffB, gD, gD, e2, [&]() {});
    STAMP(15);
  }
  {
    int tid3 = threadIdx.x; asm volatile("" : "+v"(tid3));
    const int tid = tid3, lane = tid3 & 63;
    asm volatile("s_waitcnt lgkmcnt(0)" ::: "memory");
    publish(flags + (2 * 256 + blockIdx.x) * FLAG_STRIDE, tag);
    STAMP(16);
    const int t3 = tid < 192 ? tid : tid - 64;
    int zlr[3], zcc[3];
    u4 gv[3], x1[3];
#pragma unroll
    for (int i = 0; i < 3; ++i) {
      zdecode(t3 + 192 * i, zlr[i], zcc[i]);
      gv[i] = *(LAS u4*)(size_t)(ldsb + LDS_MOD + (5 * D + zcc[i] * 8) * 2);
      x1[i] = *(LAS u4*)(size_t)(ldsb + LDS_X1 + (t3 + 192 * i) * 16);
    }
    if (blockIdx.x == 0 && w == 1) {
      for (unsigned spins = 0; spins <= SPIN_CAP; ++spins) {
        bool ok = true;
#pragma unroll
        for (int i = 0; i < 4; ++i) ok &= __hip_atomic_load((gu64*)(flags + (lane + 64 * i) * FLAG_STRIDE), __ATOMIC_RELAXED, __HIP_MEMORY_SCOPE_AGENT) == tag;
        if (__all(ok)) break;
        __builtin_amdgcn_s_sleep(4);
      }
      if (lane == 0) __hip_atomic_store((gu64*)(p.ws + WS_EPOCH), tag, __ATOMIC_RELAXED, __HIP_MEMORY_SCOPE_AGENT);
    }
    await8(flags + (2 * 256 + c * 8) * FLAG_STRIDE, tag, tmo, w, lane);
    STAMP(17);
    u4 v[3][8];
#pragma unroll
    for (int i = 0; i < 3; ++i)
#pragma unroll
      for (int jj = 0; jj < 7; ++jj) v[i][jj] = __builtin_amdgcn_raw_buffer_load_b128(rX, ((jj + (jj >= j)) * 8 + j) * SLICE + (t3 + 192 * i) * 16, 0, AUX_X);
#pragma unroll
    for (int i = 0; i < 3; ++i) { const int z = t3 + 192 * i, zw = z / 144; v[i][7] = *(LAS u4*)(size_t)(ldsb + ABYTES + zw * 2 * SLOT + (z - zw * 144) * 16); }
#pragma unroll
    for (int i = 0; i < 3; ++i) {
      const u4 o = gated_residual(v[i], x1[i], gv[i]);
      if (tid < 192) *(u4*)(p.out + (size_t)(c * 64 + j * 8 + zlr[i]) * D + zcc[i] * 8) = o;
    }
  }
  STAMP(18);
  asm volatile("s_waitcnt lgkmcnt(0)" ::: "memory");
  STAMP_FLUSH();
  }
}

extern "C" void kernel_launch(void* const* d_in, const int* in_sizes, int n_in, void* d_out, int out_size, void* d_ws, size_t ws_size, hipStream_t stream) {
  Params p;
  p.x = (const u16*)d_in[0]; p.kc = (const u16*)d_in[1]; p.vc = (const u16*)d_in[2]; p.wq = (const u16*)d_in[3]; p.wo = (const u16*)d_in[4];
  p.wg = (const u16*)d_in[6]; p.wu = (const u16*)d_in[7]; p.wd = (const u16*)d_in[8];
  p.c = (const u16*)d_in[9]; p.wmod = (const u16*)d_in[10];
  p.out = (u16*)d_out; p.ws = (char*)d_ws;
 p.one = 1.0f; p.gap = 0u;
  (void)hipFuncSetAttribute((const void*)k_main, hipFuncAttributeMaxDynamicSharedMemorySize, LDS_BYTES);
  k_main<<<256, 256, LDS_BYTES, stream>>>(p);
}
```

```cpp
#include <hip/hip_runtime.h>
#include <stdint.h>
#include <utility>
#include <type_traits>

typedef unsigned short u16;
typedef unsigned long long u64;
typedef __bf16 b2 __attribute__((ext_vector_type(2)));
typedef __bf16 b4 __attribute__((ext_vector_type(4)));
typedef __bf16 b8 __attribute__((ext_vector_type(8)));
typedef float f2 __attribute__((ext_vector_type(2)));
typedef float f4 __attribute__((ext_vector_type(4)));
typedef float f16v __attribute__((ext_vector_type(16)));
typedef unsigned u2 __attribute__((ext_vector_type(2)));
typedef unsigned u4 __attribute__((ext_vector_type(4)));
typedef __attribute__((address_space(1))) unsigned gu32;
typedef __attribute__((address_space(1))) unsigned long long gu64;
#define LAS __attribute__((address_space(3)))
#define DI __device__ __forceinline__
#define GAP() __builtin_amdgcn_sched_barrier(0)
#define LGKM0() __builtin_amdgcn_s_waitcnt(0xC07F)
#define BAR() do { asm volatile("" ::: "memory"); __builtin_amdgcn_s_barrier(); asm volatile("" ::: "memory"); } while (0)
template <int V> using IC = std::integral_constant<int, V>;
#define AUX_X 18
#define AUX_H 18
#define PFD_K 14
#define UNROLL(N, F) [&]<int... I_>(std::integer_sequence<int, I_...>) __attribute__((always_inline)) { (F(IC<I_>{}), ...); }(std::make_integer_sequence<int, N>{})

constexpr int S = 2048, D = 576, DH = 128, SK = 2048, WIN = 512, DF = 4608;
constexpr int ROWB = D * 2;
constexpr int ABYTES = 64 * ROWB;
constexpr int SLICE = 8 * ROWB;
constexpr int SLOT = 9216;
constexpr int QSLOT = 8192;
constexpr int LDS_DUMMY = ABYTES + 8 * SLOT;
constexpr int LDS_X1 = LDS_DUMMY + 256;
constexpr int LDS_MOD = LDS_X1 + SLICE;
constexpr int LDS_BYTES = LDS_MOD + 6 * D * 2;
static_assert(LDS_BYTES <= 163840, "LDS");
constexpr size_t WS_EPOCH = 0;
constexpr size_t WS_TMO = 256;
constexpr size_t WS_MODG = 4096;
constexpr size_t WS_MODP = 131072;
constexpr size_t WS_FLAG = 32768;
constexpr int FLAG_STRIDE = 8;
constexpr size_t WS_X = 1048576;
constexpr size_t WS_H2 = WS_X + (size_t)32 * 64 * SLICE;
constexpr size_t WS_DBG = 200u << 20;
#define STAMP(i)
#define STAMP_FLUSH()
#define PST(i)

DI float bf2f(u16 v) { return __uint_as_float(((unsigned)v) << 16); }
DI float bflo(unsigned w) { return __uint_as_float(w << 16); }
DI float bfhi(unsigned w) { return __uint_as_float(w & 0xffff0000u); }
DI f2 bf2(unsigned w) { return (f2){bflo(w), bfhi(w)}; }
DI unsigned pk(float lo, float hi) { f2 v = {lo, hi}; b2 b = __builtin_convertvector(v, b2); return __builtin_bit_cast(unsigned, b); }
DI unsigned pk(f2 v) { b2 b = __builtin_convertvector(v, b2); return __builtin_bit_cast(unsigned, b); }
DI float rbf(float f) { return bflo(pk(f, f)); }
DI f2 rb2(f2 v) { return (f2){__uint_as_float(pk(0.f, v[0])), __uint_as_float(pk(0.f, v[1]))}; }
template <int CTRL> DI float dpp(float v) { return __uint_as_float(__builtin_amdgcn_update_dpp(0u, __float_as_uint(v), CTRL, 0xf, 0xf, false)); }
DI float sum8(float v) { v += dpp<0xB1>(v); v += dpp<0x4E>(v); v += dpp<0x141>(v); return v; }
DI float swap16_add(float x) { auto r = __builtin_amdgcn_permlane16_swap(__float_as_uint(x), __float_as_uint(x), false, false); return __uint_as_float(r[0]) + __uint_as_float(r[1]); }
DI float swap32_add(float x) { auto r = __builtin_amdgcn_permlane32_swap(__float_as_uint(x), __float_as_uint(x), false, false); return __uint_as_float(r[0]) + __uint_as_float(r[1]); }
DI float sum32(float v) { v = sum8(v); v += dpp<0x140>(v); return swap16_add(v); }
DI u16 f2bf(float f) { return (u16)(pk(f, f) & 0xffffu); }
DI float fmaxx(float a, float b) { return __builtin_elementwise_maximum(a, b); }
DI float fmax3(float a, float b, float c) { return __builtin_elementwise_maximum(__builtin_elementwise_maximum(a, b), c); }

DI void zdecode(int z, int& lr, int& cc) {
  const int w = z / 144, y = z - w * 144;
  if (y < 128) { const int tp = y >> 5; lr = (y >> 2) & 7; cc = 18 * w + 4 * tp + (y & 3); }
  else { const int y2 = y - 128; lr = y2 >> 1; cc = 18 * w + 16 + (y2 & 1); }
}

template <int IMM> DI void dma(unsigned voff, const char* g) { asm volatile("global_load_lds_dwordx4 %0, %1 offset:%2" :: "v"(voff), "s"(g), "i"(IMM) : "memory"); }
DI void setm0(unsigned a) { asm volatile("s_mov_b32 m0, %0" :: "s"(a) : "memory"); }
DI void touch(const char* p, unsigned voff, unsigned ldsdummy) {
  asm volatile("s_mov_b32 m0, %0\n\ts_nop 0\n\tglobal_load_lds_dword %1, %2" :: "s"(ldsdummy), "v"(voff), "s"(p) : "memory");
}
DI void touchp(const char* a, unsigned ldsdummy) {
  asm volatile("s_mov_b32 m0, %0\n\ts_nop 0\n\tglobal_load_lds_dword %1, off" :: "s"(ldsdummy), "v"(a) : "memory");
}
DI b4 ldT(unsigned base, int off) { return __builtin_amdgcn_ds_read_tr16_b64_v4bf16((LAS b4*)(size_t)(base + off)); }
DI b8 ldA(unsigned base, int off) { return *(LAS b8*)(size_t)(base + off); }
DI b8 cat(const b4& lo, const b4& hi) { return __builtin_shufflevector(lo, hi, 0, 1, 2, 3, 4, 5, 6, 7); }
template <bool AG, bool ZERO> DI void mfma16(f4& c, const b8& a, const b8& b) {
  if constexpr (AG) {
    if constexpr (ZERO) asm volatile("v_mfma_f32_16x16x32_bf16 %0, %1, %2, 0" : "=a"(c) : "v"(a), "v"(b));
    else asm volatile("v_mfma_f32_16x16x32_bf16 %0, %1, %2, %0" : "+a"(c) : "v"(a), "v"(b));
  } else {
    if constexpr (ZERO) asm volatile("v_mfma_f32_16x16x32_bf16 %0, %1, %2, 0" : "=&v"(c) : "v"(a), "v"(b));
    else asm volatile("v_mfma_f32_16x16x32_bf16 %0, %1, %2, %0" : "+v"(c) : "v"(a), "v"(b));
  }
}
template <bool AG, bool ZERO> DI void mfma32(f16v& c, const b8& a, const b8& b) {
  if constexpr (AG) {
    if constexpr (ZERO) asm volatile("v_mfma_f32_32x32x16_bf16 %0, %1, %2, 0" : "=a"(c) : "v"(a), "v"(b));
    else asm volatile("v_mfma_f32_32x32x16_bf16 %0, %1, %2, %0" : "+a"(c) : "v"(a), "v"(b));
  } else {
    if constexpr (ZERO) asm volatile("v_mfma_f32_32x32x16_bf16 %0, %1, %2, 0" : "=&v"(c) : "v"(a), "v"(b));
    else asm volatile("v_mfma_f32_32x32x16_bf16 %0, %1, %2, %0" : "+v"(c) : "v"(a), "v"(b));
  }
}
template <int NVG> DI void settle(f4 (&acc)[9][4]) {
  asm volatile("s_nop 7\n\ts_nop 7" ::: "memory");
#pragma unroll
  for (int t = 0; t < 9; ++t)
#pragma unroll
    for (int m = 0; m < 4; ++m) { if (t * 4 + m < 36 - NVG) asm volatile("" : "+a"(acc[t][m])); else asm volatile("" : "+v"(acc[t][m])); }
}

struct GS {
  b4 wlo[2][9], whi[2][9];
  b8 af[2][4];
  unsigned wbase, ring;
};
DI void issue_all(const GS& s, int slot, const unsigned (&voff)[9], const char* gp) {
  const unsigned sl = s.ring + slot * SLOT;
  setm0(sl + 4096); asm volatile("s_nop 0");
  dma<-4096>(voff[0], gp); dma<-3072>(voff[1], gp); dma<-2048>(voff[2], gp); dma<-1024>(voff[3], gp);
  dma<0>(voff[4], gp); dma<1024>(voff[5], gp); dma<2048>(voff[6], gp); dma<3072>(voff[7], gp);
  setm0(sl + 8192); asm volatile("s_nop 0"); dma<0>(voff[8], gp);
}
template <int PAR, int AP, bool ZERO, bool RDW, bool RDA, bool DMA, int VM, int NVG, typename PF>
DI void step(GS& s, f4 (&acc)[9][4], const unsigned (&voff)[9], const char* gp, unsigned ab, PF&& pf) {
  const unsigned sl = s.ring + (PAR ^ 1) * SLOT;
  const unsigned wb = s.wbase + (PAR ^ 1) * SLOT;
  auto mm = [&](auto ic) __attribute__((always_inline)) {
    constexpr int I = decltype(ic)::value, t = I >> 2, m = I & 3;
    mfma16<(I < 36 - NVG), ZERO>(acc[t][m], cat(s.wlo[PAR][t], s.whi[PAR][t]), s.af[AP][m]);
  };
  auto rd = [&](auto ic) __attribute__((always_inline)) {
    constexpr int I = decltype(ic)::value;
    if constexpr (I < 4) { if constexpr (RDA) s.af[AP ^ 1][I] = ldA(ab, I * 16 * ROWB); }
    else if constexpr (RDW) {
      if constexpr (((I - 4) & 1) == 0) s.wlo[PAR ^ 1][(I - 4) >> 1] = ldT(wb, 32 * ((I - 4) >> 1));
      else s.whi[PAR ^ 1][(I - 4) >> 1] = ldT(wb, 32 * ((I - 4) >> 1) + 4608);
    }
  };
  auto gap = [&](auto gc) __attribute__((always_inline)) {
    constexpr int G = decltype(gc)::value;
    mm(IC<2 * G>{});
    if constexpr (G == 0 && RDW) asm volatile("s_waitcnt vmcnt(%0)" :: "i"(VM) : "memory");
    if constexpr (G <= 10) rd(IC<2 * G>{});
    if constexpr (DMA) {
      if constexpr (G == 12) { LGKM0(); setm0(sl + 4096); }
      if constexpr (G >= 13 && G <= 16) dma<(2 * (G - 13) - 4) * 1024>(voff[2 * (G - 13)], gp);
      if constexpr (G == 17) dma<0>(voff[8], gp);
    }
    GAP();
    mm(IC<2 * G + 1>{});
    if constexpr (G <= 10) rd(IC<2 * G + 1>{});
    if constexpr (DMA) {
      if constexpr (G >= 13 && G <= 16) dma<(2 * (G - 13) + 1 - 4) * 1024>(voff[2 * (G - 13) + 1], gp);
      if constexpr (G == 16) setm0(sl + 8192);
    }
    if constexpr (G == 17) pf();
    GAP();
  };
  UNROLL(18, gap);
}
DI void read_w(GS& s, int par) {
#pragma unroll
  for (int t = 0; t < 9; ++t) { s.wlo[par][t] = ldT(s.wbase + par * SLOT, 32 * t); s.whi[par][t] = ldT(s.wbase + par * SLOT, 32 * t + 4608); }
}
DI void read_a(GS& s, int set, unsigned ab) {
#pragma unroll
  for (int m = 0; m < 4; ++m) s.af[set][m] = ldA(ab, m * 16 * ROWB);
}
struct Epi { __amdgpu_buffer_rsrc_t rs; unsigned base, dstride, ownlds; int own, w, lane; };
template <int U> DI void epi_unit(f4 (&acc)[9][4], const Epi& e) {
  const int G = e.lane >> 4, c = e.lane & 15, lr = c & 7, dh = c >> 3;
  constexpr int ta = U < 16 ? 2 * (U >> 2) : 8, tb = U < 16 ? ta + 1 : 8, ma = U < 16 ? (U & 3) : 2 * (U - 16), mb = U < 16 ? ma : ma + 1;
  asm volatile("" : "+a"(acc[ta][ma]), "+a"(acc[tb][mb]));
  const f4 a = acc[ta][ma], b = acc[tb][mb];
  auto r0 = __builtin_amdgcn_permlane16_swap(pk(a[0], a[1]), pk(b[0], b[1]), false, false);
  auto r1 = __builtin_amdgcn_permlane16_swap(pk(a[2], a[3]), pk(b[2], b[3]), false, false);
  const u4 v = {r0[0], r1[0], r0[1], r1[1]};
  const int d = U < 16 ? 2 * ma + dh : 2 * (ma + (G & 1)) + dh;
  const unsigned off = U < 16 ? e.w * 2304 + (U >> 2) * 512 + lr * 64 + (2 * (G & 1) + (G >> 1)) * 16 : e.w * 2304 + 2048 + lr * 32 + (G >> 1) * 16;
  __builtin_amdgcn_raw_buffer_store_b128(v, e.rs, d == e.own ? 0x7ffffff0u : e.base + d * e.dstride + off, 0, 16);
  if ((U < 16 ? ma : ma >> 1) == (U < 16 ? e.own >> 1 : e.own >> 2)) { if (d == e.own) *(LAS u4*)(size_t)(e.ownlds + off) = v; }
}
template <bool NEXT, int VM> DI void final2(GS& s, f4 (&acc)[9][4], const unsigned (&nvoff)[9], const char* nx1, unsigned ab, const Epi& e) {
  const unsigned sl = s.ring + SLOT, wb = s.wbase + SLOT;
  asm volatile("s_waitcnt vmcnt(%0)" :: "i"(VM) : "memory");
#pragma unroll
  for (int m = 0; m < 4; ++m) s.af[1][m] = ldA(ab, m * 16 * ROWB);
  auto rdw = [&](auto ic) __attribute__((always_inline)) { constexpr int t = decltype(ic)::value; s.wlo[1][t] = ldT(wb, 32 * t); s.whi[1][t] = ldT(wb, 32 * t + 4608); };
  rdw(IC<0>{}); rdw(IC<1>{});
  GAP();
  auto grp = [&](auto gc) __attribute__((always_inline)) {
    constexpr int G = decltype(gc)::value, t = G >> 1, st = G & 1;
    auto mm = [&](auto mc) __attribute__((always_inline)) { constexpr int m = decltype(mc)::value; mfma16<true, false>(acc[t][m], cat(s.wlo[st][t], s.whi[st][t]), s.af[st][m]); };
    mm(IC<0>{});
    if constexpr (st == 0 && t + 2 < 9) rdw(IC<t + 2>{});
    if constexpr (NEXT && G == 15) { LGKM0(); setm0(sl + 4096); }
    GAP();
    constexpr int U = G >= 5 ? G - 5 : -1;
    mm(IC<1>{});
    if constexpr (U >= 0 && U < 16 && (U >> 2) * 4 + 5 <= G) epi_unit<(U >= 0 && U < 16) ? U : 0>(acc, e);
    GAP();
    mm(IC<2>{});
    if constexpr (NEXT && G == 16) { dma<-4096>(nvoff[0], nx1); dma<-3072>(nvoff[1], nx1); dma<-2048>(nvoff[2], nx1); dma<-1024>(nvoff[3], nx1); }
    if constexpr (NEXT && G == 17) { dma<0>(nvoff[4], nx1); dma<1024>(nvoff[5], nx1); dma<2048>(nvoff[6], nx1); dma<3072>(nvoff[7], nx1); setm0(sl + 8192); }
    GAP();
    mm(IC<3>{});
    GAP();
  };
  UNROLL(18, grp);
  if constexpr (NEXT) dma<0>(nvoff[8], nx1);
  asm volatile("s_nop 7" ::: "memory");
  epi_unit<13>(acc, e); epi_unit<14>(acc, e); epi_unit<15>(acc, e); epi_unit<16>(acc, e); epi_unit<17>(acc, e);
}
template <int NS, int NEXT, int NPF, typename PF> DI void gemm_rows(GS& s, f4 (&acc)[9][4], const unsigned (&voff)[9], const char* gW, unsigned ab_e, unsigned ab_o,
                                              const unsigned (&nvoff)[9], const char* nx0, const char* nx1, const Epi& e, PF&& pf) {
  constexpr size_t KS = (size_t)32 * ROWB;
  auto no = [&]() __attribute__((always_inline)) {};
  step<0, 0, true, true, true, true, 9, 0>(s, acc, voff, gW + 3 * KS, ab_o, no);
  step<1, 1, false, true, true, true, 9, 0>(s, acc, voff, gW + 4 * KS, ab_e + 128, pf);
  step<0, 0, false, true, true, true, 9 + NPF, 0>(s, acc, voff, gW + 5 * KS, ab_o + 128, no);
  step<1, 1, false, true, true, true, 9 + NPF, 0>(s, acc, voff, gW + 6 * KS, ab_e + 256, no);
  for (int k = 4; k < NS - 4; k += 2) {
    step<0, 0, false, true, true, true, 9, 0>(s, acc, voff, gW + (size_t)(k + 3) * KS, ab_o + 128 * (k >> 1), no);
    step<1, 1, false, true, true, true, 9, 0>(s, acc, voff, gW + (size_t)(k + 4) * KS, ab_e + 128 * ((k + 2) >> 1), no);
  }
  step<0, 0, false, true, true, true, 9, 0>(s, acc, voff, gW + (size_t)(NS - 1) * KS, ab_o + 128 * ((NS - 4) >> 1), no);
  step<1, 1, false, true, true, NEXT != 0, 9, 0>(s, acc, nvoff, nx0, ab_e + 128 * ((NS - 2) >> 1), no);
  final2<NEXT != 0, NEXT != 0 ? 9 : 0>(s, acc, nvoff, nx1, ab_o + 128 * ((NS - 2) >> 1), e);
}

constexpr unsigned SPIN_CAP = 1u << 17;
DI void publish(u64* flag, u64 tag) {
  asm volatile("s_waitcnt vmcnt(0)" ::: "memory");
  BAR();
  if (threadIdx.x == 0) __hip_atomic_store((gu64*)flag, tag, __ATOMIC_RELAXED, __HIP_MEMORY_SCOPE_AGENT);
}
DI void await8(u64* flags8, u64 tag, unsigned* tmo, int w, int lane) {
  if (w == 0) {
    for (unsigned spins = 0;; ++spins) {
      const u64 v = __hip_atomic_load((gu64*)(flags8 + (lane & 7) * FLAG_STRIDE), __ATOMIC_RELAXED, __HIP_MEMORY_SCOPE_AGENT);
      if (__all(v == tag)) break;
      if (spins > SPIN_CAP) { if (lane == 0) __hip_atomic_store((gu32*)tmo, 1u, __ATOMIC_RELAXED, __HIP_MEMORY_SCOPE_AGENT); break; }
      __builtin_amdgcn_s_sleep(1);
    }
  }
  BAR();
}
template <int P0, int NP, bool F32, int NT> DI void gather_mod(char* ws, u64 tag, unsigned ldsb, unsigned* tmo, int tid) {
  constexpr int N = (NP + NT - 1) / NT;
  const __amdgpu_buffer_rsrc_t rM = __builtin_amdgcn_make_buffer_rsrc(ws + WS_MODG, 0, 6 * D * 8, 0x00020000);
  const u64 gtag = tag << 16;
  const unsigned thi = (unsigned)(gtag >> 32), tlo = (unsigned)gtag & 0xffff0000u;
  u4 gr[N];
  for (unsigned spins = 0;; ++spins) {
    bool ok = true;
#pragma unroll
    for (int i = 0; i < N; ++i) gr[i] = __builtin_amdgcn_raw_buffer_load_b128(rM, (P0 + (tid + NT * i < NP ? tid + NT * i : (tid & 63))) * 16, 0, 16);
#pragma unroll
    for (int i = 0; i < N; ++i) ok &= gr[i][1] == thi && gr[i][3] == thi && (gr[i][0] & 0xffff0000u) == tlo && (gr[i][2] & 0xffff0000u) == tlo;
    if (__all(ok)) break;
    if (spins > SPIN_CAP / 8) { if ((tid & 63) == 0) __hip_atomic_store((gu32*)tmo, 2u, __ATOMIC_RELAXED, __HIP_MEMORY_SCOPE_AGENT); break; }
    __builtin_amdgcn_s_sleep(8);
  }
#pragma unroll
  for (int i = 0; i < N; ++i)
    if (tid + NT * i < NP) {
      const int idx = P0 + tid + NT * i;
      if (F32 && idx < D) {
        f2 v = {bflo(gr[i][0]), bflo(gr[i][2])};
        if (idx >= D / 2) v = rb2(v + 1.f);
        *(LAS f2*)(size_t)(ldsb + LDS_X1 + 1024 + idx * 8) = v;
      } else *(LAS unsigned*)(size_t)(ldsb + LDS_MOD + idx * 4) = (gr[i][0] & 0xffffu) | (gr[i][2] << 16);
    }
}
DI u4 gated_residual(const u4 (&v)[8], const u4& res, const u4& g) {
  u4 o;
#pragma unroll
  for (int e = 0; e < 4; ++e) {
    f2 s = bf2(v[0][e]);
#pragma unroll
    for (int j = 1; j < 8; ++j) s += bf2(v[j][e]);
    const unsigned t = pk(bf2(pk(s)) * bf2(g[e]));
    o[e] = pk(bf2(res[e]) + bf2(t));
  }
  return o;
}

constexpr float QSCALE = 0.08838834764831845f * 1.4426950408889634f;
constexpr float THR = 40.0f;
struct QS {
  b4 wlo[2][8], whi[2][8];
  b8 hf[2][4];
  unsigned wb[4];
  unsigned hb[4];
  unsigned ring;
};
template <int PAR, bool ZERO, bool RD, bool DMA, int VM, typename PF>
DI void qstep(QS& s, f16v (&acc)[4][2], const unsigned (&voff)[8], const char* gp, unsigned hoff, PF&& pf) {
  const unsigned sl = s.ring + (PAR ^ 1) * QSLOT;
  auto rd = [&](auto ic) __attribute__((always_inline)) {
    constexpr int I = decltype(ic)::value;
    if constexpr (RD) {
      if constexpr (I < 4) {
        s.hf[PAR ^ 1][I] = ldA(s.hb[2 * (PAR ^ 1) + (I >> 1)] + hoff, (I & 1) * 32 * ROWB);
      } else {
        constexpr int J = I - 4, f = J >> 1, t = J & 1, ks = f >> 2, dt = f & 3;
        const b4 v = ldT(s.wb[dt], (PAR ^ 1) * QSLOT + 4096 * ks + 1024 * t);
        if constexpr (t == 0) s.wlo[PAR ^ 1][f] = v; else s.whi[PAR ^ 1][f] = v;
      }
    }
  };
  auto gap = [&](auto gc) __attribute__((always_inline)) {
    constexpr int G = decltype(gc)::value, ks = G >> 3, dt = (G >> 1) & 3, qb = G & 1, f = ks * 4 + dt;
    mfma32<false, ZERO && ks == 0>(acc[dt][qb], cat(s.wlo[PAR][f], s.whi[PAR][f]), s.hf[PAR][ks * 2 + qb]);
    if constexpr (G == 0 && RD) asm volatile("s_waitcnt vmcnt(%0)" :: "i"(VM) : "memory");
    if constexpr (G <= 6) { rd(IC<3 * G>{}); rd(IC<3 * G + 1>{}); if constexpr (3 * G + 2 < 20) rd(IC<3 * G + 2>{}); }
    if constexpr (DMA) {
      if constexpr (G == 9) { LGKM0(); setm0(sl + 4096); }
      if constexpr (G >= 10 && G <= 13) { dma<(2 * (G - 10) - 4) * 1024>(voff[2 * (G - 10)], gp); dma<(2 * (G - 10) + 1 - 4) * 1024>(voff[2 * (G - 10) + 1], gp); }
    }
    if constexpr (G == 15) pf();
    GAP();
  };
  UNROLL(16, gap);
}

struct AT {
  f16v S[2][2][2];
  f16v negm[2];
  f16v O[4][2];
  b8 q[2][8];
  b8 P[2][4];
  float m[2], l[2], rs0[2], rs1[2], fct[2], pm0[2], pm1[2];
  float one;
  unsigned kb_[2], vb_[2];
  unsigned voff[8];
  unsigned m0k, m0v;
  const char *gk, *gv;
  bool pend;
};
template <int MODE> DI void qk(f16v& s, const b8& k, const b8& q, const f16v& negm) {
  if constexpr (MODE == 0) asm volatile("v_mfma_f32_32x32x16_bf16 %0, %1, %2, 0" : "=&v"(s) : "v"(k), "a"(q));
  else if constexpr (MODE == 1) asm volatile("v_mfma_f32_32x32x16_bf16 %0, %1, %2, %3" : "=&v"(s) : "v"(k), "a"(q), "v"(negm));
  else asm volatile("v_mfma_f32_32x32x16_bf16 %0, %1, %2, %0" : "+v"(s) : "v"(k), "a"(q));
}
template <int XP, int KB, int E> DI void sm_exp(AT& a) { constexpr int qb = E >> 4, r = E & 15; a.S[XP][KB][qb][r] = __builtin_amdgcn_exp2f(a.S[XP][KB][qb][r]); }
template <int XP, int KB, int E> DI void sm_add(AT& a) {
  constexpr int qb = E >> 4, r = E & 15;
  if constexpr (r & 1) a.rs1[qb] = __builtin_fmaf(a.S[XP][KB][qb][r], a.one, a.rs1[qb]); else a.rs0[qb] += a.S[XP][KB][qb][r];
}
template <int XP, int C> DI void sm_pack(AT& a) {
  constexpr int kb = C >> 4, qb = (C >> 3) & 1, s = (C >> 2) & 1, p = C & 3;
  u4 w = __builtin_bit_cast(u4, a.P[qb][kb * 2 + s]);
  w[p] = pk(a.S[XP][kb][qb][8 * s + 2 * p], a.S[XP][kb][qb][8 * s + 2 * p + 1]);
  a.P[qb][kb * 2 + s] = __builtin_bit_cast(b8, w);
  if constexpr (p == 3) asm volatile("" : "+v"(a.P[qb][kb * 2 + s]));
}
template <int X, bool FIRST, bool DK, bool DV, typename PF> DI void phaseA(AT& a, int t, PF&& pf) {
  constexpr int XP = X ^ 1;
  const unsigned kslot = X * 16384;
  b8 kf[16];
  auto kread = [&](auto ic) __attribute__((always_inline)) { constexpr int I = decltype(ic)::value, ks = I >> 1, kb = I & 1; kf[I] = ldA(a.kb_[ks & 1], kslot + 8192 * kb + 512 * (ks >> 1)); };
  kread(IC<0>{}); kread(IC<1>{}); kread(IC<2>{}); kread(IC<3>{});
  const char* gk = a.gk + (size_t)(t + 1) * 16384; const char* gv = a.gv + (size_t)t * 16384;
  auto gap = [&](auto gc) __attribute__((always_inline)) {
    constexpr int G = decltype(gc)::value, ks = G >> 2, kb = (G >> 1) & 1, qb = G & 1, I = G >> 1;
    qk<(ks == 0) ? (FIRST ? 0 : 1) : 2>(a.S[X][kb][qb], kf[I], a.q[qb][ks], a.negm[qb]);
    if constexpr ((G & 1) == 0 && I + 4 < 16) kread(IC<I + 4>{});
    if constexpr (DK) { if constexpr (G == 0) setm0(a.m0k + (X ^ 1) * 16384); if constexpr ((G & 1) && G < 16) dma<((G >> 1) - 4) * 1024>(a.voff[G >> 1], gk); }
    if constexpr (DV) { if constexpr (G == 16) setm0(a.m0v + X * 16384); if constexpr ((G & 1) && G > 16) dma<(((G - 16) >> 1) - 4) * 1024>(a.voff[(G - 16) >> 1], gv); }
    if constexpr (!FIRST) {
      if constexpr (G < 16) { sm_exp<XP, 1, 2 * G>(a); sm_exp<XP, 1, 2 * G + 1>(a); sm_pack<XP, G>(a); }
      else { sm_pack<XP, G>(a); sm_add<XP, 1, 2 * (G - 16)>(a); sm_add<XP, 1, 2 * (G - 16) + 1>(a); }
    }
    GAP();
  };
  UNROLL(32, gap);
  pf();
  if constexpr (!FIRST) {
#pragma unroll
    for (int qb = 0; qb < 2; ++qb) { a.l[qb] += a.rs0[qb] + a.rs1[qb]; asm volatile("" : "+v"(a.l[qb])); }
  }
}
DI float swap32_max(float x) { auto r = __builtin_amdgcn_permlane32_swap(__float_as_uint(x), __float_as_uint(x), false, false); return fmaxx(__uint_as_float(r[0]), __uint_as_float(r[1])); }
template <int X, bool NOPV, bool ZERO> DI void phaseB(AT& a) {
  const unsigned vslot = (X ^ 1) * 16384;
  b4 vlo[16], vhi[16];
  auto vread = [&](auto ic) __attribute__((always_inline)) {
    constexpr int I = decltype(ic)::value, k4 = I >> 2, db = I & 3;
    vlo[I] = ldT(a.vb_[0], vslot + 2048 * (2 * k4) + 512 * db);
    vhi[I] = ldT(a.vb_[1], vslot + 2048 * (2 * k4 + 1) + 512 * db);
  };
  if constexpr (!NOPV) { vread(IC<0>{}); vread(IC<1>{}); vread(IC<2>{}); }
  float rmax[2];
  auto pv = [&](auto gc) __attribute__((always_inline)) {
    constexpr int G = decltype(gc)::value, I = G >> 1, k4 = I >> 2, db = I & 3, qb = G & 1;
    if constexpr (!NOPV) {
      mfma32<true, ZERO && k4 == 0>(a.O[db][qb], cat(vlo[I], vhi[I]), a.P[qb][k4]);
      if constexpr ((G & 1) == 0 && I + 3 < 16) vread(IC<I + 3>{});
    }
  };
  auto gap1 = [&](auto gc) __attribute__((always_inline)) {
    constexpr int G = decltype(gc)::value;
    pv(gc);
    if constexpr (G >= 4 && G < 12) {
      constexpr int i = G - 4, q = i >> 2, r = 4 * (i & 3);
      if constexpr ((i & 3) == 0) { a.pm0[q] = fmax3(a.S[X][0][q][0], a.S[X][0][q][1], a.S[X][1][q][0]); a.pm1[q] = fmax3(a.S[X][0][q][2], a.S[X][0][q][3], a.S[X][1][q][1]);
                                    a.pm0[q] = fmax3(a.pm0[q], a.S[X][1][q][2], a.S[X][1][q][3]); }
      else { a.pm0[q] = fmax3(a.pm0[q], a.S[X][0][q][r], a.S[X][0][q][r + 1]); a.pm1[q] = fmax3(a.pm1[q], a.S[X][0][q][r + 2], a.S[X][0][q][r + 3]);
             a.pm0[q] = fmax3(a.pm0[q], a.S[X][1][q][r], a.S[X][1][q][r + 1]); a.pm1[q] = fmax3(a.pm1[q], a.S[X][1][q][r + 2], a.S[X][1][q][r + 3]); }
    }
    if constexpr (G == 12 || G == 13) { constexpr int q = G - 12; rmax[q] = swap32_max(fmaxx(a.pm0[q], a.pm1[q])); }
    GAP();
  };
  UNROLL(14, gap1);
  if constexpr (NOPV) {
#pragma unroll
    for (int q = 0; q < 2; ++q) {
      a.m[q] = rmax[q]; a.l[q] = 0.f; a.fct[q] = 1.f;
#pragma unroll
      for (int kb = 0; kb < 2; ++kb)
#pragma unroll
        for (int e = 0; e < 16; ++e) a.S[X][kb][q][e] -= rmax[q];
#pragma unroll
      for (int e = 0; e < 16; ++e) a.negm[q][e] = -rmax[q];
    }
  } else {
    const bool grew = (rmax[0] > THR) | (rmax[1] > THR);
    if (__builtin_expect(__any((int)grew), 0)) {
#pragma unroll
      for (int q = 0; q < 2; ++q) {
        const float dl = fmaxx(rmax[q], 0.f); a.m[q] += dl; a.fct[q] = __builtin_amdgcn_exp2f(-dl);
#pragma unroll
        for (int kb = 0; kb < 2; ++kb)
#pragma unroll
          for (int e = 0; e < 16; ++e) a.S[X][kb][q][e] -= dl;
#pragma unroll
        for (int e = 0; e < 16; ++e) a.negm[q][e] = -a.m[q];
      }
      a.pend = true;
    }
  }
  a.rs0[0] = a.rs0[1] = a.rs1[0] = a.rs1[1] = 0.f;
  GAP();
  auto gap2 = [&](auto gc) __attribute__((always_inline)) {
    constexpr int G = decltype(gc)::value + 14;
    pv(IC<G>{});
    if constexpr (G >= 15) {
      constexpr int e0 = 2 * (G - 15);
      if constexpr (e0 < 32) { sm_exp<X, 0, e0>(a); sm_exp<X, 0, e0 + 1>(a); }
      if constexpr (G >= 16) { sm_add<X, 0, e0 - 2>(a); sm_add<X, 0, e0 - 1>(a); }
    }
    GAP();
  };
  UNROLL(18, gap2);
  asm volatile("" : "+v"(a.S[X][0][0]), "+v"(a.S[X][0][1]), "+v"(a.rs0[0]), "+v"(a.rs0[1]), "+v"(a.rs1[0]), "+v"(a.rs1[1]));
  if constexpr (!NOPV) {
    if (__builtin_expect(a.pend, 0)) {
      asm volatile("s_nop 7\n\ts_nop 7" ::: "memory");
#pragma unroll
      for (int db = 0; db < 4; ++db)
#pragma unroll
        for (int q = 0; q < 2; ++q) {
          asm volatile("" : "+a"(a.O[db][q]));
          f16v o = a.O[db][q];
#pragma unroll
          for (int e = 0; e < 16; ++e) o[e] *= a.fct[q];
          a.O[db][q] = o;
          asm volatile("" : "+a"(a.O[db][q]));
        }
      a.l[0] *= a.fct[0]; a.l[1] *= a.fct[1]; a.pend = false;
      asm volatile("s_nop 1" ::: "memory");
    }
  }
}

template <int X, bool FIRST, bool DK, bool DV, typename PF> DI void fastA(AT& a, int t, PF&& pf) {
  constexpr int XP = X ^ 1;
  const unsigned kslot = X * 16384;
  b8 kf[16];
  auto kread = [&](auto ic) __attribute__((always_inline)) { constexpr int I = decltype(ic)::value, ks = I >> 1, kb = I & 1; kf[I] = ldA(a.kb_[ks & 1], kslot + 8192 * kb + 512 * (ks >> 1)); };
  kread(IC<0>{}); kread(IC<1>{}); kread(IC<2>{}); kread(IC<3>{});
  const char* gk = a.gk + (size_t)(t + 1) * 16384; const char* gv = a.gv + (size_t)t * 16384;
  auto gap = [&](auto gc) __attribute__((always_inline)) {
    constexpr int G = decltype(gc)::value, ks = G >> 2, kb = (G >> 1) & 1, qb = G & 1, I = G >> 1;
    qk<(ks == 0) ? (FIRST ? 0 : 1) : 2>(a.S[X][kb][qb], kf[I], a.q[qb][ks], a.negm[qb]);
    if constexpr ((G & 1) == 0 && I + 4 < 16) kread(IC<I + 4>{});
    if constexpr (DK) { if constexpr (G == 0) setm0(a.m0k + (X ^ 1) * 16384); if constexpr ((G & 1) && G < 16) dma<((G >> 1) - 4) * 1024>(a.voff[G >> 1], gk); }
    if constexpr (DV) { if constexpr (G == 16) setm0(a.m0v + X * 16384); if constexpr ((G & 1) && G > 16) dma<(((G - 16) >> 1) - 4) * 1024>(a.voff[(G - 16) >> 1], gv); }
    if constexpr (!FIRST) {
      sm_exp<XP, 1, G>(a);
      if constexpr (G >= 1) sm_add<XP, 1, G - 1>(a);
      if constexpr (G >= 3 && (G & 1)) sm_pack<XP, 16 + (G - 3) / 2>(a);
    }
    GAP();
  };
  UNROLL(32, gap);
  pf();
  if constexpr (!FIRST) {
    sm_add<XP, 1, 31>(a); sm_pack<XP, 31>(a);
#pragma unroll
    for (int qb = 0; qb < 2; ++qb) { a.l[qb] += a.rs0[qb] + a.rs1[qb]; asm volatile("" : "+v"(a.l[qb])); }
  }
}
constexpr int fb_e(int G) { return G < 2 ? 0 : G - 2 + (G > 5) + (G > 9); }
constexpr int dr_e(int n) { return n < 8 ? n : n < 16 ? n + 8 : n < 24 ? n - 8 : n; }
template <int X, int MODE> DI void fastB(AT& a) {
  constexpr bool ZERO = MODE == 1, DRAIN = MODE == 3;
  if constexpr (MODE == 2) {
#pragma unroll
    for (int q = 0; q < 2; ++q) {
      float p0 = fmax3(a.S[X][0][q][0], a.S[X][1][q][0], a.S[X][0][q][2]), p1 = fmax3(a.S[X][0][q][1], a.S[X][1][q][1], a.S[X][0][q][3]);
      p0 = fmaxx(p0, a.S[X][1][q][2]); p1 = fmaxx(p1, a.S[X][1][q][3]);
#pragma unroll
      for (int r = 4; r < 16; r += 2) { p0 = fmax3(p0, a.S[X][0][q][r], a.S[X][1][q][r]); p1 = fmax3(p1, a.S[X][0][q][r + 1], a.S[X][1][q][r + 1]); }
      const float mx = swap32_max(fmaxx(p0, p1));
      f2 m2 = {mx, mx}; asm volatile("" : "+v"(m2));
      a.l[q] = 0.f;
#pragma unroll
      for (int kb = 0; kb < 2; ++kb)
#pragma unroll
        for (int e = 0; e < 16; e += 2) { const f2 v = (f2){a.S[X][kb][q][e], a.S[X][kb][q][e + 1]} - m2; a.S[X][kb][q][e] = v[0]; a.S[X][kb][q][e + 1] = v[1]; }
#pragma unroll
      for (int e = 0; e < 16; ++e) a.negm[q][e] = -mx;
    }
    a.rs0[0] = a.rs0[1] = a.rs1[0] = a.rs1[1] = 0.f;
    { auto f = [&](auto ic) __attribute__((always_inline)) { sm_exp<X, 0, decltype(ic)::value>(a); }; UNROLL(32, f); }
    { auto f = [&](auto ic) __attribute__((always_inline)) { sm_add<X, 0, decltype(ic)::value>(a); }; UNROLL(32, f); }
    { auto f = [&](auto ic) __attribute__((always_inline)) { sm_pack<X, decltype(ic)::value>(a); }; UNROLL(16, f); }
  } else {
    const unsigned vslot = (DRAIN ? X : X ^ 1) * 16384;
    b4 vlo[16], vhi[16];
    auto vread = [&](auto ic) __attribute__((always_inline)) {
      constexpr int I = decltype(ic)::value, k4 = I >> 2, db = I & 3;
      vlo[I] = ldT(a.vb_[0], vslot + 2048 * (2 * k4) + 512 * db);
      vhi[I] = ldT(a.vb_[1], vslot + 2048 * (2 * k4 + 1) + 512 * db);
    };
    vread(IC<0>{}); vread(IC<1>{}); vread(IC<2>{});
    if constexpr (!DRAIN) a.rs0[0] = a.rs0[1] = a.rs1[0] = a.rs1[1] = 0.f;
    auto gap = [&](auto gc) __attribute__((always_inline)) {
      constexpr int G = decltype(gc)::value, I = G >> 1, k4 = I >> 2, db = I & 3, qb = G & 1;
      mfma32<true, ZERO && k4 == 0>(a.O[db][qb], cat(vlo[I], vhi[I]), a.P[qb][k4]);
      if constexpr ((G & 1) == 0 && I + 3 < 16) vread(IC<I + 3>{});
      if constexpr (DRAIN) {
        if constexpr (G < 16) { sm_exp<X, 1, dr_e(2 * G)>(a); sm_exp<X, 1, dr_e(2 * G) + 1>(a); }
        if constexpr (G >= 2 && G < 18) sm_pack<X, 16 + dr_e(2 * (G - 2)) / 2>(a);
        if constexpr (G >= 16) { sm_add<X, 1, dr_e(2 * (G - 16))>(a); sm_add<X, 1, dr_e(2 * (G - 16)) + 1>(a); }
      } else {
        if constexpr (fb_e(G) < fb_e(G + 1)) sm_exp<X, 0, fb_e(G)>(a);
        if constexpr (fb_e(G) + 1 < fb_e(G + 1)) sm_exp<X, 0, fb_e(G) + 1>(a);
        if constexpr (G >= 3 && fb_e(G - 1) < fb_e(G)) sm_add<X, 0, G >= 3 ? fb_e(G - 1) : 0>(a);
        if constexpr (G >= 3 && fb_e(G - 1) + 1 < fb_e(G)) sm_add<X, 0, G >= 3 ? fb_e(G - 1) + 1 : 0>(a);
        if constexpr (G >= 16 && G <= 29) sm_pack<X, G >= 16 ? G - 16 : 0>(a);
        if constexpr (G == 31) sm_pack<X, 14>(a);
      }
      GAP();
    };
    UNROLL(32, gap);
    if constexpr (!DRAIN) {
      sm_add<X, 0, 31>(a);
      static_assert(fb_e(32) == 32 && fb_e(28) >= 28 && fb_e(30) >= 30, "schedule");
      sm_pack<X, 15>(a);
      asm volatile("" : "+v"(a.S[X][0][0]), "+v"(a.S[X][0][1]), "+v"(a.rs0[0]), "+v"(a.rs0[1]), "+v"(a.rs1[0]), "+v"(a.rs1[1]));
    } else {
#pragma unroll
      for (int qb = 0; qb < 2; ++qb) a.l[qb] += a.rs0[qb] + a.rs1[qb];
    }
  }
}

struct Params { const u16 *x, *c, *wmod, *wq, *kc, *vc, *wo, *wg, *wu, *wd; u16* out; char* ws; float one; unsigned gap; };

__global__ __launch_bounds__(256, 1) void k_main(Params p) {
  extern __shared__ __attribute__((aligned(16))) char lds[];
  const int tid = threadIdx.x, w = __builtin_amdgcn_readfirstlane(tid >> 6), lane = tid & 63;
  const int j = blockIdx.x & 7, c = blockIdx.x >> 3;
  const unsigned ldsb = (unsigned)(size_t)lds;
  const int hh = lane >> 5, r32 = lane & 31;
  u64* const flags = (u64*)(p.ws + WS_FLAG);
  unsigned* const tmo = (unsigned*)(p.ws + WS_TMO);
  STAMP(0);
  PST(0);
  QS qs;
  unsigned voffQ[8];
#pragma unroll
  for (int i = 0; i < 8; ++i) voffQ[i] = (4 * i + (lane >> 4)) * 8192 + 16 * ((lane & 15) ^ ((lane >> 4) << 2)) - (i - 4) * 1024 + 4096;
  const char* gq = (const char*)p.wq + (size_t)(4 * j + w) * 256 - 4096;
  qs.ring = __builtin_amdgcn_readfirstlane(ldsb + ABYTES + w * 2 * QSLOT);
  constexpr size_t KS_Q = (size_t)32 * 8192;
  auto issueq = [&](int slot, const char* gp) __attribute__((always_inline)) {
    setm0(qs.ring + slot * QSLOT + 4096); asm volatile("s_nop 0");
    dma<-4096>(voffQ[0], gp); dma<-3072>(voffQ[1], gp); dma<-2048>(voffQ[2], gp); dma<-1024>(voffQ[3], gp);
    dma<0>(voffQ[4], gp); dma<1024>(voffQ[5], gp); dma<2048>(voffQ[6], gp); dma<3072>(voffQ[7], gp);
  };
  const int mg = blockIdx.x % 54, ms = blockIdx.x / 54;
  u4 wm[5]; u16 cval[5];
#pragma unroll
  for (int i = 0; i < 5; ++i) {
    const int r = 144 * (ms & 3) + (i < 4 || tid < 128 ? (tid >> 3) + 32 * i : 0);
    wm[i] = *(const u4*)(p.wmod + (size_t)r * (6 * D) + mg * 64 + (tid & 7) * 8); cval[i] = p.c[r];
  }
  const u64 e0 = __hip_atomic_load((gu64*)(p.ws + WS_EPOCH), __ATOMIC_RELAXED, __HIP_MEMORY_SCOPE_AGENT);
  AT a;
  u64 tag;
  {
    f16v Qt[4][2];
    QS& s = qs;
    {
      const int rr = lane >> 3, sub = lane & 7;
      tag = ((u64)(unsigned)__builtin_amdgcn_readfirstlane((unsigned)(e0 >> 32)) << 32 | (u64)(unsigned)__builtin_amdgcn_readfirstlane((unsigned)e0)) + 1;
      const u64 gtag = tag << 16;
      PST(9);
      const __amdgpu_buffer_rsrc_t rP = __builtin_amdgcn_make_buffer_rsrc(p.ws + WS_MODP + (size_t)mg * 8192, 0, 8192, 0x00020000);
      if (blockIdx.x < 216) {
        float acc[8] = {};
#pragma unroll
        for (int i = 0; i < 5; ++i) {
          const float xc = bf2f(cval[i]);
          float sc = rbf(xc * __builtin_amdgcn_rcpf(1.f + __expf(-xc)));
          if (i == 4 && tid >= 128) sc = 0.f;
#pragma unroll
          for (int e = 0; e < 4; ++e) { acc[2 * e] += sc * bflo(wm[i][e]); acc[2 * e + 1] += sc * bfhi(wm[i][e]); }
        }
#pragma unroll
        for (int e = 0; e < 8; ++e) { float t = acc[e]; t += dpp<0x128>(t); acc[e] = swap32_add(swap16_add(t)); }
        const int k = (lane >> 3) & 3;
        const float v0 = k == 0 ? acc[0] : k == 1 ? acc[2] : k == 2 ? acc[4] : acc[6], v1 = k == 0 ? acc[1] : k == 1 ? acc[3] : k == 2 ? acc[5] : acc[7];
        if (lane < 32) __builtin_amdgcn_raw_buffer_store_b128((u4){__float_as_uint(v0), __float_as_uint(v1), (unsigned)tag, (unsigned)(tag >> 32)}, rP, (4 * ms + w) * 512 + (4 * (lane & 7) + k) * 16, 0, 16);
      }
      PST(10);
      if (blockIdx.x < 54 && w == 0) {
        const int pr = lane & 31, hf = lane >> 5;
        u4 gr[8];
        for (unsigned spins = 0;; ++spins) {
          bool ok = true;
#pragma unroll
          for (int k = 0; k < 8; ++k) gr[k] = __builtin_amdgcn_raw_buffer_load_b128(rP, (8 * hf + k) * 512 + pr * 16, 0, 16);
#pragma unroll
          for (int k = 0; k < 8; ++k) ok &= gr[k][2] == (unsigned)tag && gr[k][3] == (unsigned)(tag >> 32);
          if (__all(ok)) break;
          if (spins > SPIN_CAP) { if (lane == 0) __hip_atomic_store((gu32*)tmo, 3u, __ATOMIC_RELAXED, __HIP_MEMORY_SCOPE_AGENT); break; }
          __builtin_amdgcn_s_sleep(1);
        }
        f2 v = {0.f, 0.f};
#pragma unroll
        for (int k = 0; k < 8; ++k) v += (f2){__uint_as_float(gr[k][0]), __uint_as_float(gr[k][1])};
        v[0] = swap32_add(v[0]); v[1] = swap32_add(v[1]);
        const u64 g0 = gtag | f2bf(v[0]), g1 = gtag | f2bf(v[1]);
        const __amdgpu_buffer_rsrc_t rG = __builtin_amdgcn_make_buffer_rsrc(p.ws + WS_MODG, 0, 6 * D * 8, 0x00020000);
        if (lane < 32) __builtin_amdgcn_raw_buffer_store_b128((u4){(unsigned)g0, (unsigned)(g0 >> 32), (unsigned)g1, (unsigned)(g1 >> 32)}, rG, (mg * 32 + pr) * 16, 0, 16);
      }
      PST(11);
      u4 xv[2][9];
      GAP();
#pragma unroll
      for (int ps = 0; ps < 2; ++ps)
#pragma unroll
        for (int i = 0; i < 9; ++i) xv[ps][i] = *(const u4*)(p.x + (size_t)(c * 64 + 16 * w + 8 * ps + rr) * D + (sub + 8 * i) * 8);
      GAP();
      PST(1);
      STAMP(20);
      PST(2);
      f2 y[2][9][4];
      {
#pragma unroll
        for (int ps = 0; ps < 2; ++ps) {
          f2 a1 = {0.f, 0.f}, a2 = {0.f, 0.f};
#pragma unroll
          for (int i = 0; i < 9; ++i)
#pragma unroll
            for (int e = 0; e < 4; ++e) { y[ps][i][e] = bf2(xv[ps][i][e]); a1 += y[ps][i][e]; a2 = __builtin_elementwise_fma(y[ps][i][e], y[ps][i][e], a2); }
          float s1 = a1[0] + a1[1], s2 = a2[0] + a2[1];
          s1 = sum8(s1); s2 = sum8(s2);
          if (ps == 0) PST(3);
          const float mean = s1 * (1.f / D), var = __builtin_fmaxf(__builtin_fmaf(-mean, mean, s2 * (1.f / D)), 0.f);
          const float mr = rbf(mean), rs = __builtin_amdgcn_rcpf(rbf(__builtin_amdgcn_sqrtf(rbf(rbf(var) + 1e-5f))));
          f2 mr2 = {mr, mr}, rs2 = {rs, rs}; asm volatile("" : "+v"(mr2), "+v"(rs2));
#pragma unroll
          for (int i = 0; i < 9; ++i) {
            f2 t[4];
#pragma unroll
            for (int e = 0; e < 4; ++e) t[e] = y[ps][i][e] - mr2;
#pragma unroll
            for (int e = 0; e < 4; ++e) t[e] = rb2(t[e]);
#pragma unroll
            for (int e = 0; e < 4; ++e) t[e] = t[e] * rs2;
#pragma unroll
            for (int e = 0; e < 4; ++e) y[ps][i][e] = rb2(t[e]);
            asm volatile("" : "+v"(y[ps][i][0]), "+v"(y[ps][i][1]), "+v"(y[ps][i][2]), "+v"(y[ps][i][3]));
          }
        }
      }
      STAMP(21);
      PST(4);
      gather_mod<0, 1728, true, 256>(p.ws, tag, ldsb, tmo, tid);
      STAMP(22);
      PST(5);
      issueq(0, gq); issueq(1, gq + KS_Q);
      {
        const int li = tid < 144 ? tid : tid - 112;
        touch((const char*)p.wq + (size_t)512 * j * 2, (18 * c + (li >> 3)) * 8192 + (li & 7) * 128, __builtin_amdgcn_readfirstlane(ldsb + LDS_DUMMY));
      }
      asm volatile("s_waitcnt lgkmcnt(0)" ::: "memory");
      BAR();
      STAMP(23);
      PST(6);
      {
        const unsigned tb = ldsb + LDS_X1 + 1024 + sub * 32;
        f4 cst[2][4];
        auto ldc = [&](int i, f4 (&d)[4]) { d[0] = *(LAS f4*)(size_t)(tb + i * 256); d[1] = *(LAS f4*)(size_t)(tb + i * 256 + 16); d[2] = *(LAS f4*)(size_t)(tb + i * 256 + D * 4); d[3] = *(LAS f4*)(size_t)(tb + i * 256 + D * 4 + 16); };
        ldc(0, cst[0]);
        const unsigned ad0 = ldsb + (16 * w + rr) * ROWB + ((sub ^ ((rr >> 1) & 7)) * 16);
        const unsigned ad1 = ldsb + (16 * w + 8 + rr) * ROWB + ((sub ^ (((rr >> 1) + 4) & 7)) * 16);
#pragma unroll
        for (int i = 0; i < 9; ++i) {
          if (i + 1 < 9) ldc(i + 1, cst[(i + 1) & 1]);
          GAP();
          const f4 (&k)[4] = cst[i & 1];
          const f2 sh[4] = {{k[0][0], k[0][1]}, {k[0][2], k[0][3]}, {k[1][0], k[1][1]}, {k[1][2], k[1][3]}}, cc[4] = {{k[2][0], k[2][1]}, {k[2][2], k[2][3]}, {k[3][0], k[3][1]}, {k[3][2], k[3][3]}};
          f2 t[2][4]; u4 o[2];
#pragma unroll
          for (int ps = 0; ps < 2; ++ps)
#pragma unroll
            for (int e = 0; e < 4; ++e) t[ps][e] = y[ps][i][e] * cc[e];
#pragma unroll
          for (int ps = 0; ps < 2; ++ps)
#pragma unroll
            for (int e = 0; e < 4; ++e) t[ps][e] = rb2(t[ps][e]);
#pragma unroll
          for (int ps = 0; ps < 2; ++ps)
#pragma unroll
            for (int e = 0; e < 4; ++e) t[ps][e] = t[ps][e] + sh[e];
#pragma unroll
          for (int ps = 0; ps < 2; ++ps)
#pragma unroll
            for (int e = 0; e < 4; ++e) o[ps][e] = pk(t[ps][e]);
          *(LAS u4*)(size_t)(ad0 + i * 128) = o[0];
          *(LAS u4*)(size_t)(ad1 + i * 128) = o[1];
          GAP();
        }
      }
    }
    {
      const int blk = (lane >> 4) & 1, q = (lane & 15) >> 2, pp = lane & 3;
#pragma unroll
      for (int dt = 0; dt < 4; ++dt) s.wb[dt] = s.ring + (8 * hh + q) * 256 + 64 * (dt ^ q) + 32 * blk + 16 * (pp & 1) + 8 * (pp >> 1);
#pragma unroll
      for (int i = 0; i < 4; ++i) s.hb[i] = ldsb + r32 * ROWB + (((2 * i + hh) ^ ((r32 >> 1) & 7)) * 16);
    }
    asm volatile("s_waitcnt vmcnt(9) lgkmcnt(0)" ::: "memory");
    BAR();
    STAMP(1);
    PST(7);
#pragma unroll
    for (int f = 0; f < 8; ++f) { s.wlo[0][f] = ldT(s.wb[f & 3], 4096 * (f >> 2)); s.whi[0][f] = ldT(s.wb[f & 3], 4096 * (f >> 2) + 1024); }
#pragma unroll
    for (int i = 0; i < 4; ++i) s.hf[0][i] = ldA(s.hb[i >> 1], (i & 1) * 32 * ROWB);
    asm volatile("s_waitcnt lgkmcnt(0)" ::: "memory");
    issueq(0, gq + 2 * KS_Q);
    auto no = [&]() __attribute__((always_inline)) {};
    auto pfkv = [&](int h) __attribute__((always_inline)) {
      if (lane < 16) touch((const char*)(w < 2 ? p.kc : p.vc) + ((size_t)(2 * j + (w & 1)) * SK + (SK - WIN)) * DH * 2, (32 * c + 16 * h + lane) * 128, __builtin_amdgcn_readfirstlane(ldsb + LDS_DUMMY));
    };
    qstep<0, true, true, true, 9>(s, Qt, voffQ, gq + 3 * KS_Q, 0, no);
    qstep<1, false, true, true, 8>(s, Qt, voffQ, gq + 4 * KS_Q, 128, [&]() __attribute__((always_inline)) { pfkv(0); });
    qstep<0, false, true, true, 9>(s, Qt, voffQ, gq + 5 * KS_Q, 128, no);
    qstep<1, false, true, true, 9>(s, Qt, voffQ, gq + 6 * KS_Q, 256, [&]() __attribute__((always_inline)) { pfkv(1); });
    qstep<0, false, true, true, 9>(s, Qt, voffQ, gq + 7 * KS_Q, 256, no);
    qstep<1, false, true, true, 9>(s, Qt, voffQ, gq + 8 * KS_Q, 384, no);
    for (int n = 6; n < 14; n += 2) {
      qstep<0, false, true, true, 8>(s, Qt, voffQ, gq + (size_t)(n + 3) * KS_Q, 128 * ((n + 1) >> 1), no);
      qstep<1, false, true, true, 8>(s, Qt, voffQ, gq + (size_t)(n + 4) * KS_Q, 128 * ((n + 2) >> 1), no);
    }
    qstep<0, false, true, true, 8>(s, Qt, voffQ, gq + (size_t)17 * KS_Q, 128 * 7, no);
    qstep<1, false, true, false, 8>(s, Qt, voffQ, gq, 128 * 8, no);
    qstep<0, false, true, false, 0>(s, Qt, voffQ, gq, 128 * 8, no);
    qstep<1, false, false, false, 0>(s, Qt, voffQ, gq, 0, no);
    STAMP(2);
    PST(8);
    {
      const int kvl = w >> 1, half = w & 1;
      const unsigned kr = ldsb + kvl * 65536, vr = kr + 32768;
#pragma unroll
      for (int i = 0; i < 2; ++i) a.kb_[i] = kr + 2048 * (r32 >> 3) + 64 * (r32 & 7) + 16 * ((2 * i + hh) ^ ((r32 >> 2) & 3));
      const int blk = (lane >> 4) & 1, q = (lane & 15) >> 2, pp = lane & 3;
#pragma unroll
      for (int t = 0; t < 2; ++t) a.vb_[t] = vr + 64 * (4 * hh + q) + 16 * ((2 * blk + (pp >> 1)) ^ ((2 * t + hh) & 3)) + 8 * (pp & 1);
      const int st = lane >> 5, r7 = (lane >> 2) & 7, pos = lane & 3;
#pragma unroll
      for (int i = 0; i < 8; ++i) {
        const int rg = 4 * half + (i >> 1);
        a.voff[i] = rg * 2048 + r7 * 256 + (i & 1) * 128 + st * 64 + 16 * (pos ^ ((2 * (rg & 1) + (r7 >> 2)) & 3)) - (i - 4) * 1024 + 4096;
      }
      a.m0k = __builtin_amdgcn_readfirstlane(kr + 8192 * half + 4096); a.m0v = __builtin_amdgcn_readfirstlane(vr + 8192 * half + 4096);
      const size_t kvoff = ((size_t)(2 * j + kvl) * SK + (SK - WIN)) * DH * 2;
      a.gk = (const char*)p.kc + kvoff - 4096; a.gv = (const char*)p.vc + kvoff - 4096;
      a.pend = false; a.one = p.one;
    }
    asm volatile("s_waitcnt lgkmcnt(0)" ::: "memory");
    BAR();
    setm0(a.m0k); asm volatile("s_nop 0");
    dma<-4096>(a.voff[0], a.gk); dma<-3072>(a.voff[1], a.gk); dma<-2048>(a.voff[2], a.gk); dma<-1024>(a.voff[3], a.gk);
    dma<0>(a.voff[4], a.gk); dma<1024>(a.voff[5], a.gk); dma<2048>(a.voff[6], a.gk); dma<3072>(a.voff[7], a.gk);
    asm volatile("s_nop 7" ::: "memory");
#pragma unroll
    for (int dt = 0; dt < 4; ++dt)
#pragma unroll
      for (int qb = 0; qb < 2; ++qb) {
        asm volatile("" : "+v"(Qt[dt][qb]));
#pragma unroll
        for (int s2 = 0; s2 < 2; ++s2) {
          u4 w4;
#pragma unroll
          for (int e = 0; e < 4; ++e) w4[e] = pk((f2){Qt[dt][qb][8 * s2 + 2 * e], Qt[dt][qb][8 * s2 + 2 * e + 1]} * QSCALE);
          a.q[qb][2 * dt + s2] = __builtin_bit_cast(b8, w4);
          asm volatile("" : "+a"(a.q[qb][2 * dt + s2]));
        }
      }
  }
  STAMP(3);
  {
    auto tile_bar = [&]() __attribute__((always_inline)) { asm volatile("s_waitcnt vmcnt(0) lgkmcnt(0)" ::: "memory"); BAR(); };
    const char* pfp = lane >= 32 ? p.ws + WS_X + (size_t)c * 64 * SLICE + (size_t)j * SLICE + (18 * w + lane - 32) * 128
                    : w < 2 ? (const char*)(w == 0 ? p.wg : p.wu) + (size_t)576 * j * 2 + (size_t)(18 * c + lane / 9 - 3) * (DF * 2) + (lane % 9) * 128
                            : (const char*)p.wo + (size_t)512 * j * ROWB + (16 * c) * ROWB + (12 * (w - 2) + lane - 24) * 128;
    const int pfs = lane >= 32 ? 8 * SLICE : w < 2 ? 3 * DF * 2 : 24 * 128, pfn = w < 2 ? 27 : 12;
    auto pft = [&](int t) __attribute__((always_inline)) {
      const bool on = lane < pfn && t >= 1 && t <= 6;
      touchp(on ? pfp : (const char*)p.wo, __builtin_amdgcn_readfirstlane(ldsb + LDS_DUMMY));
      pfp += pfs;
    };
    auto tile_bar1 = [&]() __attribute__((always_inline)) { asm volatile("s_waitcnt vmcnt(1) lgkmcnt(0)" ::: "memory"); BAR(); };
    LAS unsigned* flag = (LAS unsigned*)(size_t)(ldsb + LDS_X1);
    if (tid == 0) *flag = 0u;
    auto k0 = [&]() __attribute__((always_inline)) {
      setm0(a.m0k); asm volatile("s_nop 0");
      dma<-4096>(a.voff[0], a.gk); dma<-3072>(a.voff[1], a.gk); dma<-2048>(a.voff[2], a.gk); dma<-1024>(a.voff[3], a.gk);
      dma<0>(a.voff[4], a.gk); dma<1024>(a.voff[5], a.gk); dma<2048>(a.voff[6], a.gk); dma<3072>(a.voff[7], a.gk);
    };
    tile_bar(); fastA<0, true, true, true>(a, 0, [&]() __attribute__((always_inline)) { pft(0); }); asm volatile("s_nop 7\n\ts_nop 3" ::: "memory"); GAP(); fastB<0, 2>(a);
    tile_bar1(); fastA<1, false, true, true>(a, 1, [&]() __attribute__((always_inline)) { pft(1); }); fastB<1, 1>(a);
    for (int t = 2; t < 6; t += 2) {
      tile_bar1(); fastA<0, false, true, true>(a, t, [&]() __attribute__((always_inline)) { pft(t); }); fastB<0, 0>(a);
      tile_bar1(); fastA<1, false, true, true>(a, t + 1, [&]() __attribute__((always_inline)) { pft(t + 1); }); fastB<1, 0>(a);
    }
    tile_bar1(); fastA<0, false, true, true>(a, 6, [&]() __attribute__((always_inline)) { pft(6); }); fastB<0, 0>(a);
    tile_bar1(); fastA<1, false, false, true>(a, 7, [&]() __attribute__((always_inline)) { pft(7); }); fastB<1, 0>(a);
    tile_bar1();
    fastB<1, 3>(a);
    {
      const bool bad = !(a.l[0] < 0x1p40f) || !(a.l[1] < 0x1p40f);
      if (__any((int)bad) && lane == 0) *flag = 1u;
      asm volatile("s_waitcnt lgkmcnt(0)" ::: "memory"); BAR();
      unsigned redo = __builtin_amdgcn_readfirstlane(*(volatile LAS unsigned*)flag); asm volatile("" : "+s"(redo));
      if (__builtin_expect(redo != 0u, 0)) {
        auto nopf = [&]() __attribute__((always_inline)) {};
        a.pend = false;
        auto upt = [&](const char* q) __attribute__((always_inline)) { const u64 v = (u64)q; return (const char*)((u64)(unsigned)__builtin_amdgcn_readfirstlane((unsigned)(v >> 32)) << 32 | (u64)(unsigned)__builtin_amdgcn_readfirstlane((unsigned)v)); };
        a.gk = upt(a.gk); a.gv = upt(a.gv); a.m0k = __builtin_amdgcn_readfirstlane(a.m0k); a.m0v = __builtin_amdgcn_readfirstlane(a.m0v);
        k0();
        tile_bar(); phaseA<0, true, true, true>(a, 0, nopf); asm volatile("s_nop 7\n\ts_nop 3" ::: "memory"); GAP(); phaseB<0, true, false>(a);
        tile_bar(); phaseA<1, false, true, true>(a, 1, nopf); phaseB<1, false, true>(a);
        for (int t = 2; t < 6; t += 2) {
          tile_bar(); phaseA<0, false, true, true>(a, t, nopf); phaseB<0, false, false>(a);
          tile_bar(); phaseA<1, false, true, true>(a, t + 1, nopf); phaseB<1, false, false>(a);
        }
        tile_bar(); phaseA<0, false, true, true>(a, 6, nopf); phaseB<0, false, false>(a);
        tile_bar(); phaseA<1, false, false, true>(a, 7, nopf); phaseB<1, false, false>(a);
        { auto f1 = [&](auto ic) __attribute__((always_inline)) { sm_exp<1, 1, decltype(ic)::value>(a); }; UNROLL(32, f1);
          auto f2_ = [&](auto ic) __attribute__((always_inline)) { sm_pack<1, decltype(ic)::value>(a); }; UNROLL(32, f2_);
          auto f3 = [&](auto ic) __attribute__((always_inline)) { sm_add<1, 1, decltype(ic)::value>(a); }; UNROLL(32, f3); }
#pragma unroll
        for (int qb = 0; qb < 2; ++qb) a.l[qb] += a.rs0[qb] + a.rs1[qb];
        tile_bar();
        const unsigned vslot = 16384;
        b4 vlo[16], vhi[16];
#pragma unroll
        for (int I = 0; I < 16; ++I) { vlo[I] = ldT(a.vb_[0], vslot + 2048 * (2 * (I >> 2)) + 512 * (I & 3)); vhi[I] = ldT(a.vb_[1], vslot + 2048 * (2 * (I >> 2) + 1) + 512 * (I & 3)); }
        asm volatile("s_nop 1" ::: "memory"); GAP();
#pragma unroll
        for (int G = 0; G < 32; ++G) { const int I = G >> 1; mfma32<true, false>(a.O[I & 3][G & 1], cat(vlo[I], vhi[I]), a.P[G & 1][I >> 2]); }
        asm volatile("s_waitcnt vmcnt(0) lgkmcnt(0)" ::: "memory"); BAR();
      }
    }
  }
  STAMP(4);
  int tid2 = threadIdx.x; asm volatile("" : "+v"(tid2));
  {
  const int tid = tid2, lane = tid2 & 63, hh = lane >> 5, r32 = lane & 31;
  GS s;
  unsigned voffB[9];
#pragma unroll
  for (int i = 0; i < 9; ++i) { const int idx = i * 64 + lane, imm = i < 8 ? (i - 4) * 1024 : 0; voffB[i] = (idx / 18) * ROWB + (idx % 18) * 16 - imm + 4096; }
  s.ring = __builtin_amdgcn_readfirstlane(ldsb + ABYTES + w * 2 * SLOT);
  const int g4 = lane >> 4, cl = lane & 15;
  s.wbase = s.ring + (4 * g4 + ((lane & 15) >> 2)) * 288 + 8 * (lane & 3);
  const unsigned sw = (cl >> 1) & 7;
  const unsigned ab_e = ldsb + cl * ROWB + ((g4 ^ sw) * 16), ab_o = ldsb + cl * ROWB + (((4 + g4) ^ sw) * 16);
  const __amdgpu_buffer_rsrc_t rX = __builtin_amdgcn_make_buffer_rsrc(p.ws + WS_X + (size_t)c * 64 * SLICE, 0, 64 * SLICE, 0x00020000);
  unsigned voffA[9];
#pragma unroll
  for (int i = 0; i < 9; ++i) { const int idx = i * 64 + lane, imm = i < 8 ? (i - 4) * 1024 : 0; voffA[i] = (idx / 18) * (DF * 2) + (idx % 18) * 16 - imm + 4096; }
  const char* gG = (const char*)p.wg + (size_t)(576 * j + 144 * w) * 2 - 4096;
  const char* gU = (const char*)p.wu + (size_t)(576 * j + 144 * w) * 2 - 4096;
  const char* gD = (const char*)p.wd + (size_t)(576 * j) * ROWB + 288 * w - 4096;
  f4 aG[9][4];
  {
    const char* gO = (const char*)p.wo + (size_t)(512 * j) * ROWB + 288 * w - 4096;
    issue_all(s, 0, voffB, gO); issue_all(s, 1, voffB, gO + (size_t)32 * ROWB);
    asm volatile("s_nop 7" ::: "memory");
#pragma unroll
    for (int qb = 0; qb < 2; ++qb) {
      const float il = __builtin_amdgcn_rcpf(swap32_add(a.l[qb]));
      const int row = 32 * qb + r32; const unsigned swr = (r32 >> 1) & 7;
#pragma unroll
      for (int db = 0; db < 4; ++db) {
        asm volatile("" : "+a"(a.O[db][qb]));
        const f16v o = a.O[db][qb];
#pragma unroll
        for (int u = 0; u < 2; ++u) {
          const u4 wv = {pk((f2){o[4 * u], o[4 * u + 1]} * il), pk((f2){o[4 * u + 2], o[4 * u + 3]} * il), pk((f2){o[4 * u + 8], o[4 * u + 9]} * il), pk((f2){o[4 * u + 10], o[4 * u + 11]} * il)};
          *(LAS u4*)(size_t)(ldsb + row * ROWB + (((4 * (4 * w + db) + 2 * u + hh) ^ swr) * 16)) = wv;
        }
      }
    }
    asm volatile("s_waitcnt vmcnt(9) lgkmcnt(0)" ::: "memory");
    BAR();
    STAMP(5);
    read_w(s, 0); read_a(s, 0, ab_e);
    asm volatile("s_waitcnt lgkmcnt(0)" ::: "memory");
    issue_all(s, 0, voffB, gO + (size_t)2 * 32 * ROWB);
    const Epi e1 = {rX, (unsigned)(j * SLICE), 8u * SLICE, ldsb + LDS_X1, j, w, lane};
    gemm_rows<16, 2, 0>(s, aG, voffB, gO, ab_e, ab_o, voffA, gG, gU, e1, [&]() __attribute__((always_inline)) {});
    STAMP(6);
  }
  {
    int zlr[3], zcc[3];
    const int t3 = tid < 192 ? tid : tid - 64;
    u4 xr[3], gv[3];
#pragma unroll
    for (int i = 0; i < 3; ++i) {
      zdecode(t3 + 192 * i, zlr[i], zcc[i]);
      xr[i] = *(const u4*)(p.x + (size_t)(c * 64 + j * 8 + zlr[i]) * D + zcc[i] * 8);
      gv[i] = *(LAS u4*)(size_t)(ldsb + LDS_MOD + (2 * D + zcc[i] * 8) * 2);
    }
    const int lrow = 2 * w + hh, grow = j * 8 + lrow;
    u2 cbv[3][2], sbv[3][2];
#pragma unroll
    for (int i = 0; i < 3; ++i) {
      const int pos = (i < 2 || r32 < 8) ? r32 + 32 * i : r32;
      const int q = pos ^ ((grow >> 1) & 7), k0 = 32 * (q >> 2) + 4 * (q & 3);
#pragma unroll
      for (int h = 0; h < 2; ++h) { cbv[i][h] = *(LAS u2*)(size_t)(ldsb + LDS_MOD + (4 * D + k0 + 16 * h) * 2); sbv[i][h] = *(LAS u2*)(size_t)(ldsb + LDS_MOD + (3 * D + k0 + 16 * h) * 2); }
    }
    asm volatile("s_waitcnt lgkmcnt(0)" ::: "memory");
    publish(flags + (0 * 256 + blockIdx.x) * FLAG_STRIDE, tag);
    STAMP(7);
    await8(flags + (0 * 256 + c * 8) * FLAG_STRIDE, tag, tmo, w, lane);
    STAMP(8);
    {
      u4 v[3][8];
#pragma unroll
      for (int i = 0; i < 3; ++i) {
#pragma unroll
        for (int jj = 0; jj < 7; ++jj) v[i][jj] = __builtin_amdgcn_raw_buffer_load_b128(rX, (j * 8 + jj + (jj >= j)) * SLICE + (t3 + 192 * i) * 16, 0, AUX_X);
      }
#pragma unroll
      for (int i = 0; i < 3; ++i) v[i][7] = *(LAS u4*)(size_t)(ldsb + LDS_X1 + (t3 + 192 * i) * 16);
#pragma unroll
      for (int i = 0; i < 3; ++i) {
        const u4 x1 = gated_residual(v[i], xr[i], gv[i]);
        if (tid < 192) {
          *(LAS u4*)(size_t)(ldsb + LDS_X1 + (tid + 192 * i) * 16) = x1;
          LAS f4* dst = (LAS f4*)(size_t)(ldsb + (zlr[i] * D + zcc[i] * 8) * 4);
          const f2 a0 = bf2(x1[0]), a1 = bf2(x1[1]), a2 = bf2(x1[2]), a3 = bf2(x1[3]);
          dst[0] = (f4){a0[0], a0[1], a1[0], a1[1]}; dst[1] = (f4){a2[0], a2[1], a3[0], a3[1]};
        }
      }
    }
    asm volatile("s_waitcnt lgkmcnt(0)" ::: "memory");
    BAR();
    STAMP(9);
    {
      f4 v[3][2];
      f2 sum = {0.f, 0.f};
#pragma unroll
      for (int i = 0; i < 3; ++i) {
        const bool on = (i < 2 || r32 < 8);
        const int pos = on ? r32 + 32 * i : r32;
        const int q = pos ^ ((grow >> 1) & 7), k0 = 32 * (q >> 2) + 4 * (q & 3);
#pragma unroll
        for (int h = 0; h < 2; ++h) {
          v[i][h] = *(LAS f4*)(size_t)(ldsb + (lrow * D + k0 + 16 * h) * 4);
          if (on) sum += (f2){v[i][h][0] + v[i][h][1], v[i][h][2] + v[i][h][3]};
        }
      }
      float sm = sum[0] + sum[1];
      sm = sum32(sm);
      const float mean = sm * (1.f / D);
      float q2 = 0.f;
#pragma unroll
      for (int i = 0; i < 3; ++i)
        if (i < 2 || r32 < 8) {
#pragma unroll
          for (int h = 0; h < 2; ++h)
#pragma unroll
            for (int e = 0; e < 4; ++e) { const float dl = v[i][h][e] - mean; q2 += dl * dl; }
        }
      q2 = sum32(q2);
      const float mr = rbf(mean), rs = __builtin_amdgcn_rcpf(rbf(__builtin_amdgcn_sqrtf(rbf(rbf(q2 * (1.f / D)) + 1e-5f))));
      const __amdgpu_buffer_rsrc_t rH = __builtin_amdgcn_make_buffer_rsrc(p.ws + WS_H2 + (size_t)c * ABYTES, 0, ABYTES, 0x00020000);
      u4 oh[3];
#pragma unroll
      for (int i = 0; i < 3; ++i) {
        u4& o = oh[i];
#pragma unroll
        for (int h = 0; h < 2; ++h)
#pragma unroll
          for (int e = 0; e < 2; ++e) {
            unsigned t = pk((f2){v[i][h][2 * e], v[i][h][2 * e + 1]} - mr);
            t = pk(bf2(t) * rs);
            t = pk(bf2(t) * bf2(pk(bf2(cbv[i][h][e]) + 1.f)));
            o[2 * h + e] = pk(bf2(t) + bf2(sbv[i][h][e]));
          }
        if (i < 2 || r32 < 8) __builtin_amdgcn_raw_buffer_store_b128(o, rH, grow * ROWB + (r32 + 32 * i) * 16, 0, 16);
      }
      publish(flags + (1 * 256 + blockIdx.x) * FLAG_STRIDE, tag);
      STAMP(10);
      if (w != 0) {
        const unsigned dummy = __builtin_amdgcn_readfirstlane(ldsb + LDS_DUMMY);
        int li = tid - 64; li = li < 162 ? li : li - 94;
        const int r = 18 * c + li / 9, l = li % 9;
        touch((const char*)p.wg + (size_t)576 * j * 2, r * (DF * 2) + l * 128, dummy);
        touch((const char*)p.wu + (size_t)576 * j * 2, r * (DF * 2) + l * 128, dummy);
      }
      await8(flags + (1 * 256 + c * 8) * FLAG_STRIDE, tag, tmo, w, lane);
      STAMP(11);
      u4 hv[16];
#pragma unroll
      for (int i = 0; i < 16; ++i) { const int y = i * 256 + tid < 4032 ? i * 256 + tid : tid; hv[i] = __builtin_amdgcn_raw_buffer_load_b128(rH, (y + (y >= j * 576 ? 576 : 0)) * 16, 0, AUX_H); }
#pragma unroll
      for (int i = 0; i < 3; ++i) if (i < 2 || r32 < 8) *(LAS u4*)(size_t)(ldsb + grow * ROWB + (r32 + 32 * i) * 16) = oh[i];
#pragma unroll
      for (int i = 0; i < 16; ++i) { const int y = i * 256 + tid; if (i < 15 || y < 4032) *(LAS u4*)(size_t)(ldsb + (y + (y >= j * 576 ? 576 : 0)) * 16) = hv[i]; }
    }
    asm volatile("s_waitcnt vmcnt(0) lgkmcnt(0)" ::: "memory");
    BAR();
  }
  STAMP(12);
  {
    constexpr size_t KS_GU = (size_t)32 * DF * 2, KS_D = (size_t)32 * ROWB;
    read_w(s, 0); read_a(s, 0, ab_e);
    asm volatile("s_waitcnt lgkmcnt(0)" ::: "memory");
    issue_all(s, 0, voffA, gG + KS_GU);
    f4 aU[9][4];
    auto no = [&]() __attribute__((always_inline)) {};
    auto pfd = [&](int h) __attribute__((always_inline)) {
      int li = 64 * h + 16 * w + lane; li = li < 162 ? li : 161;
      if (lane < 16) touch((const char*)p.wd + (size_t)576 * j * ROWB, (18 * c) * ROWB + li * 128, __builtin_amdgcn_readfirstlane(ldsb + LDS_DUMMY));
    };
    auto kpair = [&](auto zc, auto lastc, auto vmc, int k, auto&& pf) __attribute__((always_inline)) {
      constexpr bool Z = decltype(zc)::value, LAST = decltype(lastc)::value; constexpr int VM = decltype(vmc)::value;
      const char* u1 = gU + (size_t)(k + 1) * KS_GU; const char* g2 = gG + (size_t)(k + 2) * KS_GU;
      step<0, 0, Z, true, false, true, 9, 0>(s, aG, voffA, u1, 0, no);
      if constexpr (!LAST) {
        step<1, 0, Z, true, true, true, 9, 8>(s, aU, voffA, g2, ab_o + 128 * (k >> 1), pf);
        step<0, 1, false, true, false, true, VM, 0>(s, aG, voffA, u1 + KS_GU, 0, no);
        step<1, 1, false, true, true, true, VM, 8>(s, aU, voffA, g2 + KS_GU, ab_e + 128 * ((k + 2) >> 1), no);
      } else {
        step<1, 0, Z, true, true, true, 9, 8>(s, aU, voffB, gD, ab_o + 128 * (k >> 1), no);
        step<0, 1, false, true, false, true, 9, 0>(s, aG, voffB, gD + KS_D, 0, no);
        step<1, 1, false, true, false, true, 9, 8>(s, aU, voffB, gD + 2 * KS_D, 0, no);
      }
    };
    kpair(std::true_type{}, std::false_type{}, IC<9>{}, 0, no);
    for (int k = 2; k < 8; k += 2) kpair(std::false_type{}, std::false_type{}, IC<9>{}, k, no);
    for (int k = 8; k < 14; k += 2) kpair(std::false_type{}, std::false_type{}, IC<10>{}, k, [&]() __attribute__((always_inline)) { pfd((k - 8) >> 1); });
    kpair(std::false_type{}, std::false_type{}, IC<9>{}, 14, no);
    kpair(std::false_type{}, std::true_type{}, IC<9>{}, 16, no);
    STAMP(13);
    settle<0>(aG); settle<8>(aU);
    u2 act[9][4];
#pragma unroll
    for (int t = 0; t < 9; ++t)
#pragma unroll
      for (int m = 0; m < 4; ++m)
#pragma unroll
        for (int e = 0; e < 2; ++e) {
          const f2 x = {aG[t][m][2 * e], aG[t][m][2 * e + 1]}, u = {aU[t][m][2 * e], aU[t][m][2 * e + 1]};
          const f2 z = x * (x * x * -0.10294324f + -2.3022082f);
          const f2 dn = (f2){__builtin_amdgcn_exp2f(z[0]), __builtin_amdgcn_exp2f(z[1])} + 1.f;
          act[t][m][e] = pk((x * u) * (f2){__builtin_amdgcn_rcpf(dn[0]), __builtin_amdgcn_rcpf(dn[1])});
        }
    asm volatile("s_waitcnt lgkmcnt(0)" ::: "memory");
    BAR();
#pragma unroll
    for (int t = 0; t < 9; ++t) {
      const int tt = 9 * w + t, ks = tt >> 1, ah = tt & 1;
      const unsigned ad = ldsb + cl * ROWB + (((4 * ks + g4) ^ sw) * 16) + 8 * ah;
#pragma unroll
      for (int m = 0; m < 4; ++m) *(LAS u2*)(size_t)(ad + m * 16 * ROWB) = act[t][m];
    }
    asm volatile("s_waitcnt lgkmcnt(0)" ::: "memory");
    BAR();
    STAMP(14);
    read_a(s, 0, ab_e);
    const Epi e2 = {rX, (unsigned)(j * 8 * SLICE), (unsigned)SLICE, s.ring - w * 2304, j, w, lane};
    gemm_rows<18, 0, 0>(s, aG, voffB, gD, ab_e, ab_o, voffB, gD, gD, e2, [&]() __attribute__((always_inline)) {});
    STAMP(15);
  }
  {
    int tid3 = threadIdx.x; asm volatile("" : "+v"(tid3));
    const int tid = tid3, lane = tid3 & 63;
    asm volatile("s_waitcnt lgkmcnt(0)" ::: "memory");
    publish(flags + (2 * 256 + blockIdx.x) * FLAG_STRIDE, tag);
    STAMP(16);
    const int t3 = tid < 192 ? tid : tid - 64;
    int zlr[3], zcc[3];
    u4 gv[3], x1[3];
#pragma unroll
    for (int i = 0; i < 3; ++i) {
      zdecode(t3 + 192 * i, zlr[i], zcc[i]);
      gv[i] = *(LAS u4*)(size_t)(ldsb + LDS_MOD + (5 * D + zcc[i] * 8) * 2);
      x1[i] = *(LAS u4*)(size_t)(ldsb + LDS_X1 + (t3 + 192 * i) * 16);
    }
    if (blockIdx.x == 0 && w == 1) {
      for (unsigned spins = 0; spins <= SPIN_CAP; ++spins) {
        bool ok = true;
#pragma unroll
        for (int i = 0; i < 4; ++i) ok &= __hip_atomic_load((gu64*)(flags + (lane + 64 * i) * FLAG_STRIDE), __ATOMIC_RELAXED, __HIP_MEMORY_SCOPE_AGENT) == tag;
        if (__all(ok)) break;
        __builtin_amdgcn_s_sleep(4);
      }
      if (lane == 0) __hip_atomic_store((gu64*)(p.ws + WS_EPOCH), tag, __ATOMIC_RELAXED, __HIP_MEMORY_SCOPE_AGENT);
    }
    await8(flags + (2 * 256 + c * 8) * FLAG_STRIDE, tag, tmo, w, lane);
    STAMP(17);
    u4 v[3][8];
#pragma unroll
    for (int i = 0; i < 3; ++i)
#pragma unroll
      for (int jj = 0; jj < 7; ++jj) v[i][jj] = __builtin_amdgcn_raw_buffer_load_b128(rX, ((jj + (jj >= j)) * 8 + j) * SLICE + (t3 + 192 * i) * 16, 0, AUX_X);
#pragma unroll
    for (int i = 0; i < 3; ++i) { const int z = t3 + 192 * i, zw = z / 144; v[i][7] = *(LAS u4*)(size_t)(ldsb + ABYTES + zw * 2 * SLOT + (z - zw * 144) * 16); }
#pragma unroll
    for (int i = 0; i < 3; ++i) {
      const u4 o = gated_residual(v[i], x1[i], gv[i]);
      if (tid < 192) *(u4*)(p.out + (size_t)(c * 64 + j * 8 + zlr[i]) * D + zcc[i] * 8) = o;
    }
  }
  STAMP(18);
  asm volatile("s_waitcnt lgkmcnt(0)" ::: "memory");
  STAMP_FLUSH();
  }
}

extern "C" void kernel_launch(void* const* d_in, const int* in_sizes, int n_in, void* d_out, int out_size, void* d_ws, size_t ws_size, hipStream_t stream) {
  Params p;
  p.x = (const u16*)d_in[0]; p.kc = (const u16*)d_in[1]; p.vc = (const u16*)d_in[2]; p.wq = (const u16*)d_in[3]; p.wo = (const u16*)d_in[4];
  p.wg = (const u16*)d_in[6]; p.wu = (const u16*)d_in[7]; p.wd = (const u16*)d_in[8];
  p.c = (const u16*)d_in[9]; p.wmod = (const u16*)d_in[10];
  p.out = (u16*)d_out; p.ws = (char*)d_ws;
 p.one = 1.0f; p.gap = 0u;
  (void)hipFuncSetAttribute((const void*)k_main, hipFuncAttributeMaxDynamicSharedMemorySize, LDS_BYTES);
  k_main<<<256, 256, LDS_BYTES, stream>>>(p);
}
```

```cpp
#include <hip/hip_runtime.h>
#include <stdint.h>
#include <utility>
#include <type_traits>

typedef unsigned short u16;
typedef unsigned long long u64;
typedef __bf16 b2 __attribute__((ext_vector_type(2)));
typedef __bf16 b4 __attribute__((ext_vector_type(4)));
typedef __bf16 b8 __attribute__((ext_vector_type(8)));
typedef float f2 __attribute__((ext_vector_type(2)));
typedef float f4 __attribute__((ext_vector_type(4)));
typedef float f16v __attribute__((ext_vector_type(16)));
typedef unsigned u2 __attribute__((ext_vector_type(2)));
typedef unsigned u4 __attribute__((ext_vector_type(4)));
typedef __attribute__((address_space(1))) unsigned gu32;
typedef __attribute__((address_space(1))) unsigned long long gu64;
#define LAS __attribute__((address_space(3)))
#define DI __device__ __forceinline__
#define GAP() __builtin_amdgcn_sched_barrier(0)
#define LGKM0() __builtin_amdgcn_s_waitcnt(0xC07F)
#define BAR() do { asm volatile("" ::: "memory"); __builtin_amdgcn_s_barrier(); asm volatile("" ::: "memory"); } while (0)
template <int V> using IC = std::integral_constant<int, V>;
#define AUX_X 18
#define AUX_H 18
#ifndef NVU
#define NVU 8
#endif
#define PFD_K 14
#define UNROLL(N, F) [&]<int... I_>(std::integer_sequence<int, I_...>) __attribute__((always_inline)) { (F(IC<I_>{}), ...); }(std::make_integer_sequence<int, N>{})

constexpr int S = 2048, D = 576, DH = 128, SK = 2048, WIN = 512, DF = 4608;
constexpr int ROWB = D * 2;
constexpr int ABYTES = 64 * ROWB;
constexpr int SLICE = 8 * ROWB;
constexpr int SLOT = 9216;
constexpr int QSLOT = 8192;
constexpr int LDS_DUMMY = ABYTES + 8 * SLOT;
constexpr int LDS_X1 = LDS_DUMMY + 256;
constexpr int LDS_MOD = LDS_X1 + SLICE;
constexpr int LDS_BYTES = LDS_MOD + 6 * D * 2;
static_assert(LDS_BYTES <= 163840, "LDS");
constexpr size_t WS_EPOCH = 0;
constexpr size_t WS_TMO = 256;
constexpr size_t WS_MODG = 4096;
constexpr size_t WS_MODP = 131072;
constexpr size_t WS_FLAG = 32768;
constexpr int FLAG_STRIDE = 8;
constexpr size_t WS_X = 1048576;
constexpr size_t WS_H2 = WS_X + (size_t)32 * 64 * SLICE;
constexpr size_t WS_DBG = 200u << 20;
#define STAMP(i)
#define STAMP_FLUSH()
#define PST(i)

DI float bf2f(u16 v) { return __uint_as_float(((unsigned)v) << 16); }
DI float bflo(unsigned w) { return __uint_as_float(w << 16); }
DI float bfhi(unsigned w) { return __uint_as_float(w & 0xffff0000u); }
DI f2 bf2(unsigned w) { return (f2){bflo(w), bfhi(w)}; }
DI unsigned pk(float lo, float hi) { f2 v = {lo, hi}; b2 b = __builtin_convertvector(v, b2); return __builtin_bit_cast(unsigned, b); }
DI unsigned pk(f2 v) { b2 b = __builtin_convertvector(v, b2); return __builtin_bit_cast(unsigned, b); }
DI float rbf(float f) { return bflo(pk(f, f)); }
DI f2 rb2(f2 v) { return (f2){__uint_as_float(pk(0.f, v[0])), __uint_as_float(pk(0.f, v[1]))}; }
template <int CTRL> DI float dpp(float v) { return __uint_as_float(__builtin_amdgcn_update_dpp(0u, __float_as_uint(v), CTRL, 0xf, 0xf, false)); }
DI float sum8(float v) { v += dpp<0xB1>(v); v += dpp<0x4E>(v); v += dpp<0x141>(v); return v; }
DI float swap16_add(float x) { auto r = __builtin_amdgcn_permlane16_swap(__float_as_uint(x), __float_as_uint(x), false, false); return __uint_as_float(r[0]) + __uint_as_float(r[1]); }
DI float swap32_add(float x) { auto r = __builtin_amdgcn_permlane32_swap(__float_as_uint(x), __float_as_uint(x), false, false); return __uint_as_float(r[0]) + __uint_as_float(r[1]); }
DI float sum32(float v) { v = sum8(v); v += dpp<0x140>(v); return swap16_add(v); }
DI u16 f2bf(float f) { return (u16)(pk(f, f) & 0xffffu); }
DI float fmaxx(float a, float b) { return __builtin_elementwise_maximum(a, b); }
DI float fmax3(float a, float b, float c) { return __builtin_elementwise_maximum(__builtin_elementwise_maximum(a, b), c); }

DI void zdecode(int z, int& lr, int& cc) {
  const int w = z / 144, y = z - w * 144;
  if (y < 128) { const int tp = y >> 5; lr = (y >> 2) & 7; cc = 18 * w + 4 * tp + (y & 3); }
  else { const int y2 = y - 128; lr = y2 >> 1; cc = 18 * w + 16 + (y2 & 1); }
}

template <int IMM> DI void dma(unsigned voff, const char* g) { asm volatile("global_load_lds_dwordx4 %0, %1 offset:%2" :: "v"(voff), "s"(g), "i"(IMM) : "memory"); }
DI void setm0(unsigned a) { asm volatile("s_mov_b32 m0, %0" :: "s"(a) : "memory"); }
DI void touch(const char* p, unsigned voff, unsigned ldsdummy) {
  asm volatile("s_mov_b32 m0, %0\n\ts_nop 0\n\tglobal_load_lds_dword %1, %2" :: "s"(ldsdummy), "v"(voff), "s"(p) : "memory");
}
DI void touchp(const char* a, unsigned ldsdummy) {
  asm volatile("s_mov_b32 m0, %0\n\ts_nop 0\n\tglobal_load_lds_dword %1, off" :: "s"(ldsdummy), "v"(a) : "memory");
}
DI b4 ldT(unsigned base, int off) { return __builtin_amdgcn_ds_read_tr16_b64_v4bf16((LAS b4*)(size_t)(base + off)); }
DI b8 ldA(unsigned base, int off) { return *(LAS b8*)(size_t)(base + off); }
DI b8 cat(const b4& lo, const b4& hi) { return __builtin_shufflevector(lo, hi, 0, 1, 2, 3, 4, 5, 6, 7); }
template <bool AG, bool ZERO> DI void mfma16(f4& c, const b8& a, const b8& b) {
  if constexpr (AG) {
    if constexpr (ZERO) asm volatile("v_mfma_f32_16x16x32_bf16 %0, %1, %2, 0" : "=a"(c) : "v"(a), "v"(b));
    else asm volatile("v_mfma_f32_16x16x32_bf16 %0, %1, %2, %0" : "+a"(c) : "v"(a), "v"(b));
  } else {
    if constexpr (ZERO) asm volatile("v_mfma_f32_16x16x32_bf16 %0, %1, %2, 0" : "=&v"(c) : "v"(a), "v"(b));
    else asm volatile("v_mfma_f32_16x16x32_bf16 %0, %1, %2, %0" : "+v"(c) : "v"(a), "v"(b));
  }
}
template <bool AG, bool ZERO> DI void mfma32(f16v& c, const b8& a, const b8& b) {
  if constexpr (AG) {
    if constexpr (ZERO) asm volatile("v_mfma_f32_32x32x16_bf16 %0, %1, %2, 0" : "=a"(c) : "v"(a), "v"(b));
    else asm volatile("v_mfma_f32_32x32x16_bf16 %0, %1, %2, %0" : "+a"(c) : "v"(a), "v"(b));
  } else {
    if constexpr (ZERO) asm volatile("v_mfma_f32_32x32x16_bf16 %0, %1, %2, 0" : "=&v"(c) : "v"(a), "v"(b));
    else asm volatile("v_mfma_f32_32x32x16_bf16 %0, %1, %2, %0" : "+v"(c) : "v"(a), "v"(b));
  }
}
template <int NVG> DI void settle(f4 (&acc)[9][4]) {
  asm volatile("s_nop 7\n\ts_nop 7" ::: "memory");
#pragma unroll
  for (int t = 0; t < 9; ++t)
#pragma unroll
    for (int m = 0; m < 4; ++m) { if (t * 4 + m < 36 - NVG) asm volatile("" : "+a"(acc[t][m])); else asm volatile("" : "+v"(acc[t][m])); }
}

struct GS {
  b4 wlo[2][9], whi[2][9];
  b8 af[2][4];
  unsigned wbase, ring;
};
DI void issue_all(const GS& s, int slot, const unsigned (&voff)[9], const char* gp) {
  const unsigned sl = s.ring + slot * SLOT;
  setm0(sl + 4096); asm volatile("s_nop 0");
  dma<-4096>(voff[0], gp); dma<-3072>(voff[1], gp); dma<-2048>(voff[2], gp); dma<-1024>(voff[3], gp);
  dma<0>(voff[4], gp); dma<1024>(voff[5], gp); dma<2048>(voff[6], gp); dma<3072>(voff[7], gp);
  setm0(sl + 8192); asm volatile("s_nop 0"); dma<0>(voff[8], gp);
}
template <int PAR, int AP, bool ZERO, bool RDW, bool RDA, bool DMA, int VM, int NVG, typename PF>
DI void step(GS& s, f4 (&acc)[9][4], const unsigned (&voff)[9], const char* gp, unsigned ab, PF&& pf) {
  const unsigned sl = s.ring + (PAR ^ 1) * SLOT;
  const unsigned wb = s.wbase + (PAR ^ 1) * SLOT;
  auto mm = [&](auto ic) __attribute__((always_inline)) {
    constexpr int I = decltype(ic)::value, t = I >> 2, m = I & 3;
    mfma16<(I < 36 - NVG), ZERO>(acc[t][m], cat(s.wlo[PAR][t], s.whi[PAR][t]), s.af[AP][m]);
  };
  auto rd = [&](auto ic) __attribute__((always_inline)) {
    constexpr int I = decltype(ic)::value;
    if constexpr (I < 4) { if constexpr (RDA) s.af[AP ^ 1][I] = ldA(ab, I * 16 * ROWB); }
    else if constexpr (RDW) {
      if constexpr (((I - 4) & 1) == 0) s.wlo[PAR ^ 1][(I - 4) >> 1] = ldT(wb, 32 * ((I - 4) >> 1));
      else s.whi[PAR ^ 1][(I - 4) >> 1] = ldT(wb, 32 * ((I - 4) >> 1) + 4608);
    }
  };
  auto gap = [&](auto gc) __attribute__((always_inline)) {
    constexpr int G = decltype(gc)::value;
    mm(IC<2 * G>{});
    if constexpr (G == 0 && RDW) asm volatile("s_waitcnt vmcnt(%0)" :: "i"(VM) : "memory");
    if constexpr (G <= 10) rd(IC<2 * G>{});
    if constexpr (DMA) {
      if constexpr (G == 12) { LGKM0(); setm0(sl + 4096); }
      if constexpr (G >= 13 && G <= 16) dma<(2 * (G - 13) - 4) * 1024>(voff[2 * (G - 13)], gp);
      if constexpr (G == 17) dma<0>(voff[8], gp);
    }
    GAP();
    mm(IC<2 * G + 1>{});
    if constexpr (G <= 10) rd(IC<2 * G + 1>{});
    if constexpr (DMA) {
      if constexpr (G >= 13 && G <= 16) dma<(2 * (G - 13) + 1 - 4) * 1024>(voff[2 * (G - 13) + 1], gp);
      if constexpr (G == 16) setm0(sl + 8192);
    }
    if constexpr (G == 17) pf();
    GAP();
  };
  UNROLL(18, gap);
}
DI void read_w(GS& s, int par) {
#pragma unroll
  for (int t = 0; t < 9; ++t) { s.wlo[par][t] = ldT(s.wbase + par * SLOT, 32 * t); s.whi[par][t] = ldT(s.wbase + par * SLOT, 32 * t + 4608); }
}
DI void read_a(GS& s, int set, unsigned ab) {
#pragma unroll
  for (int m = 0; m < 4; ++m) s.af[set][m] = ldA(ab, m * 16 * ROWB);
}
struct Epi { __amdgpu_buffer_rsrc_t rs; unsigned base, dstride, ownlds; int own, w, lane; };
template <int U> DI void epi_unit(f4 (&acc)[9][4], const Epi& e) {
  const int G = e.lane >> 4, c = e.lane & 15, lr = c & 7, dh = c >> 3;
  constexpr int ta = U < 16 ? 2 * (U >> 2) : 8, tb = U < 16 ? ta + 1 : 8, ma = U < 16 ? (U & 3) : 2 * (U - 16), mb = U < 16 ? ma : ma + 1;
  asm volatile("" : "+a"(acc[ta][ma]), "+a"(acc[tb][mb]));
  const f4 a = acc[ta][ma], b = acc[tb][mb];
  auto r0 = __builtin_amdgcn_permlane16_swap(pk(a[0], a[1]), pk(b[0], b[1]), false, false);
  auto r1 = __builtin_amdgcn_permlane16_swap(pk(a[2], a[3]), pk(b[2], b[3]), false, false);
  const u4 v = {r0[0], r1[0], r0[1], r1[1]};
  const int d = U < 16 ? 2 * ma + dh : 2 * (ma + (G & 1)) + dh;
  const unsigned off = U < 16 ? e.w * 2304 + (U >> 2) * 512 + lr * 64 + (2 * (G & 1) + (G >> 1)) * 16 : e.w * 2304 + 2048 + lr * 32 + (G >> 1) * 16;
  __builtin_amdgcn_raw_buffer_store_b128(v, e.rs, d == e.own ? 0x7ffffff0u : e.base + d * e.dstride + off, 0, 16);
  if ((U < 16 ? ma : ma >> 1) == (U < 16 ? e.own >> 1 : e.own >> 2)) { if (d == e.own) *(LAS u4*)(size_t)(e.ownlds + off) = v; }
}
template <bool NEXT, int VM> DI void final2(GS& s, f4 (&acc)[9][4], const unsigned (&nvoff)[9], const char* nx1, unsigned ab, const Epi& e) {
  const unsigned sl = s.ring + SLOT, wb = s.wbase + SLOT;
  asm volatile("s_waitcnt vmcnt(%0)" :: "i"(VM) : "memory");
#pragma unroll
  for (int m = 0; m < 4; ++m) s.af[1][m] = ldA(ab, m * 16 * ROWB);
  auto rdw = [&](auto ic) __attribute__((always_inline)) { constexpr int t = decltype(ic)::value; s.wlo[1][t] = ldT(wb, 32 * t); s.whi[1][t] = ldT(wb, 32 * t + 4608); };
  rdw(IC<0>{}); rdw(IC<1>{});
  GAP();
  auto grp = [&](auto gc) __attribute__((always_inline)) {
    constexpr int G = decltype(gc)::value, t = G >> 1, st = G & 1;
    auto mm = [&](auto mc) __attribute__((always_inline)) { constexpr int m = decltype(mc)::value; mfma16<true, false>(acc[t][m], cat(s.wlo[st][t], s.whi[st][t]), s.af[st][m]); };
    mm(IC<0>{});
    if constexpr (st == 0 && t + 2 < 9) rdw(IC<t + 2>{});
    if constexpr (NEXT && G == 15) { LGKM0(); setm0(sl + 4096); }
    GAP();
    constexpr int U = G >= 5 ? G - 5 : -1;
    mm(IC<1>{});
    if constexpr (U >= 0 && U < 16 && (U >> 2) * 4 + 5 <= G) epi_unit<(U >= 0 && U < 16) ? U : 0>(acc, e);
    GAP();
    mm(IC<2>{});
    if constexpr (NEXT && G == 16) { dma<-4096>(nvoff[0], nx1); dma<-3072>(nvoff[1], nx1); dma<-2048>(nvoff[2], nx1); dma<-1024>(nvoff[3], nx1); }
    if constexpr (NEXT && G == 17) { dma<0>(nvoff[4], nx1); dma<1024>(nvoff[5], nx1); dma<2048>(nvoff[6], nx1); dma<3072>(nvoff[7], nx1); setm0(sl + 8192); }
    GAP();
    mm(IC<3>{});
    GAP();
  };
  UNROLL(18, grp);
  if constexpr (NEXT) dma<0>(nvoff[8], nx1);
  asm volatile("s_nop 7" ::: "memory");
  epi_unit<13>(acc, e); epi_unit<14>(acc, e); epi_unit<15>(acc, e); epi_unit<16>(acc, e); epi_unit<17>(acc, e);
}
template <int NS, int NEXT, int NPF, typename PF> DI void gemm_rows(GS& s, f4 (&acc)[9][4], const unsigned (&voff)[9], const char* gW, unsigned ab_e, unsigned ab_o,
                                              const unsigned (&nvoff)[9], const char* nx0, const char* nx1, const Epi& e, PF&& pf) {
  constexpr size_t KS = (size_t)32 * ROWB;
  auto no = [&]() __attribute__((always_inline)) {};
  step<0, 0, true, true, true, true, 9, 0>(s, acc, voff, gW + 3 * KS, ab_o, no);
  step<1, 1, false, true, true, true, 9, 0>(s, acc, voff, gW + 4 * KS, ab_e + 128, pf);
  step<0, 0, false, true, true, true, 9 + NPF, 0>(s, acc, voff, gW + 5 * KS, ab_o + 128, no);
  step<1, 1, false, true, true, true, 9 + NPF, 0>(s, acc, voff, gW + 6 * KS, ab_e + 256, no);
  for (int k = 4; k < NS - 4; k += 2) {
    step<0, 0, false, true, true, true, 9, 0>(s, acc, voff, gW + (size_t)(k + 3) * KS, ab_o + 128 * (k >> 1), no);
    step<1, 1, false, true, true, true, 9, 0>(s, acc, voff, gW + (size_t)(k + 4) * KS, ab_e + 128 * ((k + 2) >> 1), no);
  }
  step<0, 0, false, true, true, true, 9, 0>(s, acc, voff, gW + (size_t)(NS - 1) * KS, ab_o + 128 * ((NS - 4) >> 1), no);
  step<1, 1, false, true, true, NEXT != 0, 9, 0>(s, acc, nvoff, nx0, ab_e + 128 * ((NS - 2) >> 1), no);
  final2<NEXT != 0, NEXT != 0 ? 9 : 0>(s, acc, nvoff, nx1, ab_o + 128 * ((NS - 2) >> 1), e);
}

constexpr unsigned SPIN_CAP = 1u << 17;
DI void publish(u64* flag, u64 tag) {
  asm volatile("s_waitcnt vmcnt(0)" ::: "memory");
  BAR();
  if (threadIdx.x == 0) __hip_atomic_store((gu64*)flag, tag, __ATOMIC_RELAXED, __HIP_MEMORY_SCOPE_AGENT);
}
DI void await8(u64* flags8, u64 tag, unsigned* tmo, int w, int lane) {
  if (w == 0) {
    for (unsigned spins = 0;; ++spins) {
      const u64 v = __hip_atomic_load((gu64*)(flags8 + (lane & 7) * FLAG_STRIDE), __ATOMIC_RELAXED, __HIP_MEMORY_SCOPE_AGENT);
      if (__all(v == tag)) break;
      if (spins > SPIN_CAP) { if (lane == 0) __hip_atomic_store((gu32*)tmo, 1u, __ATOMIC_RELAXED, __HIP_MEMORY_SCOPE_AGENT); break; }
      __builtin_amdgcn_s_sleep(1);
    }
  }
  BAR();
}
template <int P0, int NP, bool F32, int NT> DI void gather_mod(char* ws, u64 tag, unsigned ldsb, unsigned* tmo, int tid) {
  constexpr int N = (NP + NT - 1) / NT;
  const __amdgpu_buffer_rsrc_t rM = __builtin_amdgcn_make_buffer_rsrc(ws + WS_MODG, 0, 6 * D * 8, 0x00020000);
  const u64 gtag = tag << 16;
  const unsigned thi = (unsigned)(gtag >> 32), tlo = (unsigned)gtag & 0xffff0000u;
  u4 gr[N];
  for (unsigned spins = 0;; ++spins) {
    bool ok = true;
#pragma unroll
    for (int i = 0; i < N; ++i) gr[i] = __builtin_amdgcn_raw_buffer_load_b128(rM, (P0 + (tid + NT * i < NP ? tid + NT * i : (tid & 63))) * 16, 0, 16);
#pragma unroll
    for (int i = 0; i < N; ++i) ok &= gr[i][1] == thi && gr[i][3] == thi && (gr[i][0] & 0xffff0000u) == tlo && (gr[i][2] & 0xffff0000u) == tlo;
    if (__all(ok)) break;
    if (spins > SPIN_CAP / 8) { if ((tid & 63) == 0) __hip_atomic_store((gu32*)tmo, 2u, __ATOMIC_RELAXED, __HIP_MEMORY_SCOPE_AGENT); break; }
    __builtin_amdgcn_s_sleep(8);
  }
#pragma unroll
  for (int i = 0; i < N; ++i)
    if (tid + NT * i < NP) {
      const int idx = P0 + tid + NT * i;
      if (F32 && idx < D) {
        f2 v = {bflo(gr[i][0]), bflo(gr[i][2])};
        if (idx >= D / 2) v = rb2(v + 1.f);
        *(LAS f2*)(size_t)(ldsb + LDS_X1 + 1024 + idx * 8) = v;
      } else *(LAS unsigned*)(size_t)(ldsb + LDS_MOD + idx * 4) = (gr[i][0] & 0xffffu) | (gr[i][2] << 16);
    }
}
DI u4 gated_residual(const u4 (&v)[8], const u4& res, const u4& g) {
  u4 o;
#pragma unroll
  for (int e = 0; e < 4; ++e) {
    f2 s = bf2(v[0][e]);
#pragma unroll
    for (int j = 1; j < 8; ++j) s += bf2(v[j][e]);
    const unsigned t = pk(bf2(pk(s)) * bf2(g[e]));
    o[e] = pk(bf2(res[e]) + bf2(t));
  }
  return o;
}

constexpr float QSCALE = 0.08838834764831845f * 1.4426950408889634f;
constexpr float THR = 40.0f;
struct QS {
  b4 wlo[2][8], whi[2][8];
  b8 hf[2][4];
  unsigned wb[4];
  unsigned hb[4];
  unsigned ring;
};
template <int PAR, bool ZERO, bool RD, bool DMA, int VM, typename PF>
DI void qstep(QS& s, f16v (&acc)[4][2], const unsigned (&voff)[8], const char* gp, unsigned hoff, PF&& pf) {
  const unsigned sl = s.ring + (PAR ^ 1) * QSLOT;
  auto rd = [&](auto ic) __attribute__((always_inline)) {
    constexpr int I = decltype(ic)::value;
    if constexpr (RD) {
      if constexpr (I < 4) {
        s.hf[PAR ^ 1][I] = ldA(s.hb[2 * (PAR ^ 1) + (I >> 1)] + hoff, (I & 1) * 32 * ROWB);
      } else {
        constexpr int J = I - 4, f = J >> 1, t = J & 1, ks = f >> 2, dt = f & 3;
        const b4 v = ldT(s.wb[dt], (PAR ^ 1) * QSLOT + 4096 * ks + 1024 * t);
        if constexpr (t == 0) s.wlo[PAR ^ 1][f] = v; else s.whi[PAR ^ 1][f] = v;
      }
    }
  };
  auto gap = [&](auto gc) __attribute__((always_inline)) {
    constexpr int G = decltype(gc)::value, ks = G >> 3, dt = (G >> 1) & 3, qb = G & 1, f = ks * 4 + dt;
    mfma32<false, ZERO && ks == 0>(acc[dt][qb], cat(s.wlo[PAR][f], s.whi[PAR][f]), s.hf[PAR][ks * 2 + qb]);
    if constexpr (G == 0 && RD) asm volatile("s_waitcnt vmcnt(%0)" :: "i"(VM) : "memory");
    if constexpr (G <= 6) { rd(IC<3 * G>{}); rd(IC<3 * G + 1>{}); if constexpr (3 * G + 2 < 20) rd(IC<3 * G + 2>{}); }
    if constexpr (DMA) {
      if constexpr (G == 9) { LGKM0(); setm0(sl + 4096); }
      if constexpr (G >= 10 && G <= 13) { dma<(2 * (G - 10) - 4) * 1024>(voff[2 * (G - 10)], gp); dma<(2 * (G - 10) + 1 - 4) * 1024>(voff[2 * (G - 10) + 1], gp); }
    }
    if constexpr (G == 15) pf();
    GAP();
  };
  UNROLL(16, gap);
}

struct AT {
  f16v S[2][2][2];
  f16v negm[2];
  f16v O[4][2];
  b8 q[2][8];
  b8 P[2][4];
  float m[2], l[2], rs0[2], rs1[2], fct[2], pm0[2], pm1[2];
  float one;
  unsigned kb_[2], vb_[2];
  unsigned voff[8];
  unsigned m0k, m0v;
  const char *gk, *gv;
  bool pend;
};
template <int MODE> DI void qk(f16v& s, const b8& k, const b8& q, const f16v& negm) {
  if constexpr (MODE == 0) asm volatile("v_mfma_f32_32x32x16_bf16 %0, %1, %2, 0" : "=&v"(s) : "v"(k), "a"(q));
  else if constexpr (MODE == 1) asm volatile("v_mfma_f32_32x32x16_bf16 %0, %1, %2, %3" : "=&v"(s) : "v"(k), "a"(q), "v"(negm));
  else asm volatile("v_mfma_f32_32x32x16_bf16 %0, %1, %2, %0" : "+v"(s) : "v"(k), "a"(q));
}
template <int XP, int KB, int E> DI void sm_exp(AT& a) { constexpr int qb = E >> 4, r = E & 15; a.S[XP][KB][qb][r] = __builtin_amdgcn_exp2f(a.S[XP][KB][qb][r]); }
template <int XP, int KB, int E> DI void sm_add(AT& a) {
  constexpr int qb = E >> 4, r = E & 15;
  if constexpr (r & 1) a.rs1[qb] = __builtin_fmaf(a.S[XP][KB][qb][r], a.one, a.rs1[qb]); else a.rs0[qb] += a.S[XP][KB][qb][r];
}
template <int XP, int C> DI void sm_pack(AT& a) {
  constexpr int kb = C >> 4, qb = (C >> 3) & 1, s = (C >> 2) & 1, p = C & 3;
  u4 w = __builtin_bit_cast(u4, a.P[qb][kb * 2 + s]);
  w[p] = pk(a.S[XP][kb][qb][8 * s + 2 * p], a.S[XP][kb][qb][8 * s + 2 * p + 1]);
  a.P[qb][kb * 2 + s] = __builtin_bit_cast(b8, w);
  if constexpr (p == 3) asm volatile("" : "+v"(a.P[qb][kb * 2 + s]));
}
template <int X, bool FIRST, bool DK, bool DV, typename PF> DI void phaseA(AT& a, int t, PF&& pf) {
  constexpr int XP = X ^ 1;
  const unsigned kslot = X * 16384;
  b8 kf[16];
  auto kread = [&](auto ic) __attribute__((always_inline)) { constexpr int I = decltype(ic)::value, ks = I >> 1, kb = I & 1; kf[I] = ldA(a.kb_[ks & 1], kslot + 8192 * kb + 512 * (ks >> 1)); };
  kread(IC<0>{}); kread(IC<1>{}); kread(IC<2>{}); kread(IC<3>{});
  const char* gk = a.gk + (size_t)(t + 1) * 16384; const char* gv = a.gv + (size_t)t * 16384;
  auto gap = [&](auto gc) __attribute__((always_inline)) {
    constexpr int G = decltype(gc)::value, ks = G >> 2, kb = (G >> 1) & 1, qb = G & 1, I = G >> 1;
    qk<(ks == 0) ? (FIRST ? 0 : 1) : 2>(a.S[X][kb][qb], kf[I], a.q[qb][ks], a.negm[qb]);
    if constexpr ((G & 1) == 0 && I + 4 < 16) kread(IC<I + 4>{});
    if constexpr (!FIRST && G == 8) asm volatile("" :: "v"(a.negm[0]), "v"(a.negm[1]));
    if constexpr (DK) { if constexpr (G == 0) setm0(a.m0k + (X ^ 1) * 16384); if constexpr ((G & 1) && G < 16) dma<((G >> 1) - 4) * 1024>(a.voff[G >> 1], gk); }
    if constexpr (DV) { if constexpr (G == 16) setm0(a.m0v + X * 16384); if constexpr ((G & 1) && G > 16) dma<(((G - 16) >> 1) - 4) * 1024>(a.voff[(G - 16) >> 1], gv); }
    if constexpr (!FIRST) {
      if constexpr (G < 16) { sm_exp<XP, 1, 2 * G>(a); sm_exp<XP, 1, 2 * G + 1>(a); sm_pack<XP, G>(a); }
      else { sm_pack<XP, G>(a); sm_add<XP, 1, 2 * (G - 16)>(a); sm_add<XP, 1, 2 * (G - 16) + 1>(a); }
    }
    GAP();
  };
  UNROLL(32, gap);
  pf();
  if constexpr (!FIRST) {
#pragma unroll
    for (int qb = 0; qb < 2; ++qb) { a.l[qb] += a.rs0[qb] + a.rs1[qb]; asm volatile("" : "+v"(a.l[qb])); }
  }
}
DI float swap32_max(float x) { auto r = __builtin_amdgcn_permlane32_swap(__float_as_uint(x), __float_as_uint(x), false, false); return fmaxx(__uint_as_float(r[0]), __uint_as_float(r[1])); }
template <int X, bool NOPV, bool ZERO> DI void phaseB(AT& a) {
  const unsigned vslot = (X ^ 1) * 16384;
  b4 vlo[16], vhi[16];
  auto vread = [&](auto ic) __attribute__((always_inline)) {
    constexpr int I = decltype(ic)::value, k4 = I >> 2, db = I & 3;
    vlo[I] = ldT(a.vb_[0], vslot + 2048 * (2 * k4) + 512 * db);
    vhi[I] = ldT(a.vb_[1], vslot + 2048 * (2 * k4 + 1) + 512 * db);
  };
  if constexpr (!NOPV) { vread(IC<0>{}); vread(IC<1>{}); vread(IC<2>{}); }
  float rmax[2];
  auto pv = [&](auto gc) __attribute__((always_inline)) {
    constexpr int G = decltype(gc)::value, I = G >> 1, k4 = I >> 2, db = I & 3, qb = G & 1;
    if constexpr (!NOPV) {
      mfma32<true, ZERO && k4 == 0>(a.O[db][qb], cat(vlo[I], vhi[I]), a.P[qb][k4]);
      if constexpr ((G & 1) == 0 && I + 3 < 16) vread(IC<I + 3>{});
    }
  };
  auto gap1 = [&](auto gc) __attribute__((always_inline)) {
    constexpr int G = decltype(gc)::value;
    pv(gc);
    if constexpr (G >= 4 && G < 12) {
      constexpr int i = G - 4, q = i >> 2, r = 4 * (i & 3);
      if constexpr ((i & 3) == 0) { a.pm0[q] = fmax3(a.S[X][0][q][0], a.S[X][0][q][1], a.S[X][1][q][0]); a.pm1[q] = fmax3(a.S[X][0][q][2], a.S[X][0][q][3], a.S[X][1][q][1]);
                                    a.pm0[q] = fmax3(a.pm0[q], a.S[X][1][q][2], a.S[X][1][q][3]); }
      else { a.pm0[q] = fmax3(a.pm0[q], a.S[X][0][q][r], a.S[X][0][q][r + 1]); a.pm1[q] = fmax3(a.pm1[q], a.S[X][0][q][r + 2], a.S[X][0][q][r + 3]);
             a.pm0[q] = fmax3(a.pm0[q], a.S[X][1][q][r], a.S[X][1][q][r + 1]); a.pm1[q] = fmax3(a.pm1[q], a.S[X][1][q][r + 2], a.S[X][1][q][r + 3]); }
    }
    if constexpr (G == 12 || G == 13) { constexpr int q = G - 12; rmax[q] = swap32_max(fmaxx(a.pm0[q], a.pm1[q])); }
    GAP();
  };
  UNROLL(14, gap1);
  if constexpr (NOPV) {
#pragma unroll
    for (int q = 0; q < 2; ++q) {
      a.m[q] = rmax[q]; a.l[q] = 0.f; a.fct[q] = 1.f;
#pragma unroll
      for (int kb = 0; kb < 2; ++kb)
#pragma unroll
        for (int e = 0; e < 16; ++e) a.S[X][kb][q][e] -= rmax[q];
#pragma unroll
      for (int e = 0; e < 16; ++e) a.negm[q][e] = -rmax[q];
      asm volatile("" : "+v"(a.negm[q]));
    }
  } else {
    const bool grew = (rmax[0] > THR) | (rmax[1] > THR);
    if (__builtin_expect(__any((int)grew), 0)) {
#pragma unroll
      for (int q = 0; q < 2; ++q) {
        const float dl = fmaxx(rmax[q], 0.f); a.m[q] += dl; a.fct[q] = __builtin_amdgcn_exp2f(-dl);
#pragma unroll
        for (int kb = 0; kb < 2; ++kb)
#pragma unroll
          for (int e = 0; e < 16; ++e) a.S[X][kb][q][e] -= dl;
#pragma unroll
        for (int e = 0; e < 16; ++e) a.negm[q][e] = -a.m[q];
        asm volatile("" : "+v"(a.negm[q]));
      }
      a.pend = true;
    }
  }
  a.rs0[0] = a.rs0[1] = a.rs1[0] = a.rs1[1] = 0.f;
  GAP();
  auto gap2 = [&](auto gc) __attribute__((always_inline)) {
    constexpr int G = decltype(gc)::value + 14;
    pv(IC<G>{});
    if constexpr (G >= 15) {
      constexpr int e0 = 2 * (G - 15);
      if constexpr (e0 < 32) { sm_exp<X, 0, e0>(a); sm_exp<X, 0, e0 + 1>(a); }
      if constexpr (G >= 16) { sm_add<X, 0, e0 - 2>(a); sm_add<X, 0, e0 - 1>(a); }
    }
    GAP();
  };
  UNROLL(18, gap2);
  asm volatile("" : "+v"(a.S[X][0][0]), "+v"(a.S[X][0][1]), "+v"(a.rs0[0]), "+v"(a.rs0[1]), "+v"(a.rs1[0]), "+v"(a.rs1[1]));
  if constexpr (!NOPV) {
    if (__builtin_expect(a.pend, 0)) {
      asm volatile("s_nop 7\n\ts_nop 7" ::: "memory");
#pragma unroll
      for (int db = 0; db < 4; ++db)
#pragma unroll
        for (int q = 0; q < 2; ++q) {
          asm volatile("" : "+a"(a.O[db][q]));
          f16v o = a.O[db][q];
#pragma unroll
          for (int e = 0; e < 16; ++e) o[e] *= a.fct[q];
          a.O[db][q] = o;
          asm volatile("" : "+a"(a.O[db][q]));
        }
      a.l[0] *= a.fct[0]; a.l[1] *= a.fct[1]; a.pend = false;
      asm volatile("s_nop 1" ::: "memory");
    }
  }
}

template <int X, bool FIRST, bool DK, bool DV, typename PF, typename PW> DI void fastA(AT& a, int t, PF&& pf, PW&& pw) {
  constexpr int XP = X ^ 1;
  const unsigned kslot = X * 16384;
  b8 kf[16];
  auto kread = [&](auto ic) __attribute__((always_inline)) { constexpr int I = decltype(ic)::value, ks = I >> 1, kb = I & 1; kf[I] = ldA(a.kb_[ks & 1], kslot + 8192 * kb + 512 * (ks >> 1)); };
  kread(IC<0>{}); kread(IC<1>{}); kread(IC<2>{}); kread(IC<3>{});
  const char* gk = a.gk + (size_t)(t + 1) * 16384; const char* gv = a.gv + (size_t)t * 16384;
  auto gap = [&](auto gc) __attribute__((always_inline)) {
    constexpr int G = decltype(gc)::value, ks = G >> 2, kb = (G >> 1) & 1, qb = G & 1, I = G >> 1;
    qk<(ks == 0) ? (FIRST ? 0 : 1) : 2>(a.S[X][kb][qb], kf[I], a.q[qb][ks], a.negm[qb]);
    if constexpr ((G & 1) == 0 && I + 4 < 16) kread(IC<I + 4>{});
    if constexpr (!FIRST && G == 8) asm volatile("" :: "v"(a.negm[0]), "v"(a.negm[1]));
    if constexpr (DK) { if constexpr (G == 0) setm0(a.m0k + (X ^ 1) * 16384); if constexpr ((G & 1) && G < 16) dma<((G >> 1) - 4) * 1024>(a.voff[G >> 1], gk); }
    if constexpr (DV) { if constexpr (G == 16) setm0(a.m0v + X * 16384); if constexpr ((G & 1) && G > 16) dma<(((G - 16) >> 1) - 4) * 1024>(a.voff[(G - 16) >> 1], gv); }
    if constexpr (G == 2 || G == 4 || G == 6) pw((G - 2) / 2);
    if constexpr (!FIRST) {
      sm_exp<XP, 1, G>(a);
      if constexpr (G >= 1) sm_add<XP, 1, G - 1>(a);
      if constexpr (G >= 3 && (G & 1)) sm_pack<XP, 16 + (G - 3) / 2>(a);
    }
    GAP();
  };
  UNROLL(32, gap);
  pf();
  if constexpr (!FIRST) {
    sm_add<XP, 1, 31>(a); sm_pack<XP, 31>(a);
#pragma unroll
    for (int qb = 0; qb < 2; ++qb) { a.l[qb] += a.rs0[qb] + a.rs1[qb]; asm volatile("" : "+v"(a.l[qb])); }
  }
}
constexpr int fb_e(int G) { return G < 2 ? 0 : G - 2 + (G > 5) + (G > 9); }
constexpr int dr_e(int n) { return n < 8 ? n : n < 16 ? n + 8 : n < 24 ? n - 8 : n; }
template <int X, int MODE> DI void fastB(AT& a) {
  constexpr bool ZERO = MODE == 1, DRAIN = MODE == 3;
  if constexpr (MODE == 2) {
#pragma unroll
    for (int q = 0; q < 2; ++q) {
      float p0 = fmax3(a.S[X][0][q][0], a.S[X][1][q][0], a.S[X][0][q][2]), p1 = fmax3(a.S[X][0][q][1], a.S[X][1][q][1], a.S[X][0][q][3]);
      p0 = fmaxx(p0, a.S[X][1][q][2]); p1 = fmaxx(p1, a.S[X][1][q][3]);
#pragma unroll
      for (int r = 4; r < 16; r += 2) { p0 = fmax3(p0, a.S[X][0][q][r], a.S[X][1][q][r]); p1 = fmax3(p1, a.S[X][0][q][r + 1], a.S[X][1][q][r + 1]); }
      const float mx = swap32_max(fmaxx(p0, p1));
      f2 m2 = {mx, mx}; asm volatile("" : "+v"(m2));
      a.l[q] = 0.f;
#pragma unroll
      for (int kb = 0; kb < 2; ++kb)
#pragma unroll
        for (int e = 0; e < 16; e += 2) { const f2 v = (f2){a.S[X][kb][q][e], a.S[X][kb][q][e + 1]} - m2; a.S[X][kb][q][e] = v[0]; a.S[X][kb][q][e + 1] = v[1]; }
#pragma unroll
      for (int e = 0; e < 16; ++e) a.negm[q][e] = -mx;
      asm volatile("" : "+v"(a.negm[q]));
    }
    a.rs0[0] = a.rs0[1] = a.rs1[0] = a.rs1[1] = 0.f;
    { auto f = [&](auto ic) __attribute__((always_inline)) { sm_exp<X, 0, decltype(ic)::value>(a); }; UNROLL(32, f); }
    { auto f = [&](auto ic) __attribute__((always_inline)) { sm_add<X, 0, decltype(ic)::value>(a); }; UNROLL(32, f); }
    { auto f = [&](auto ic) __attribute__((always_inline)) { sm_pack<X, decltype(ic)::value>(a); }; UNROLL(16, f); }
  } else {
    const unsigned vslot = (DRAIN ? X : X ^ 1) * 16384;
    b4 vlo[16], vhi[16];
    auto vread = [&](auto ic) __attribute__((always_inline)) {
      constexpr int I = decltype(ic)::value, k4 = I >> 2, db = I & 3;
      vlo[I] = ldT(a.vb_[0], vslot + 2048 * (2 * k4) + 512 * db);
      vhi[I] = ldT(a.vb_[1], vslot + 2048 * (2 * k4 + 1) + 512 * db);
    };
    vread(IC<0>{}); vread(IC<1>{}); vread(IC<2>{});
    if constexpr (!DRAIN) a.rs0[0] = a.rs0[1] = a.rs1[0] = a.rs1[1] = 0.f;
    auto gap = [&](auto gc) __attribute__((always_inline)) {
      constexpr int G = decltype(gc)::value, I = G >> 1, k4 = I >> 2, db = I & 3, qb = G & 1;
      mfma32<true, ZERO && k4 == 0>(a.O[db][qb], cat(vlo[I], vhi[I]), a.P[qb][k4]);
      if constexpr ((G & 1) == 0 && I + 3 < 16) vread(IC<I + 3>{});
      if constexpr (DRAIN) {
        if constexpr (G < 16) { sm_exp<X, 1, dr_e(2 * G)>(a); sm_exp<X, 1, dr_e(2 * G) + 1>(a); }
        if constexpr (G >= 2 && G < 18) sm_pack<X, 16 + dr_e(2 * (G - 2)) / 2>(a);
        if constexpr (G >= 16) { sm_add<X, 1, dr_e(2 * (G - 16))>(a); sm_add<X, 1, dr_e(2 * (G - 16)) + 1>(a); }
      } else {
        if constexpr (fb_e(G) < fb_e(G + 1)) sm_exp<X, 0, fb_e(G)>(a);
        if constexpr (fb_e(G) + 1 < fb_e(G + 1)) sm_exp<X, 0, fb_e(G) + 1>(a);
        if constexpr (G >= 3 && fb_e(G - 1) < fb_e(G)) sm_add<X, 0, G >= 3 ? fb_e(G - 1) : 0>(a);
        if constexpr (G >= 3 && fb_e(G - 1) + 1 < fb_e(G)) sm_add<X, 0, G >= 3 ? fb_e(G - 1) + 1 : 0>(a);
        if constexpr (G >= 16 && G <= 29) sm_pack<X, G >= 16 ? G - 16 : 0>(a);
        if constexpr (G == 31) sm_pack<X, 14>(a);
      }
      GAP();
    };
    UNROLL(32, gap);
    if constexpr (!DRAIN) {
      sm_add<X, 0, 31>(a);
      static_assert(fb_e(32) == 32 && fb_e(28) >= 28 && fb_e(30) >= 30, "schedule");
      sm_pack<X, 15>(a);
      asm volatile("" : "+v"(a.S[X][0][0]), "+v"(a.S[X][0][1]), "+v"(a.rs0[0]), "+v"(a.rs0[1]), "+v"(a.rs1[0]), "+v"(a.rs1[1]));
    } else {
#pragma unroll
      for (int qb = 0; qb < 2; ++qb) a.l[qb] += a.rs0[qb] + a.rs1[qb];
    }
  }
}

struct Params { const u16 *x, *c, *wmod, *wq, *kc, *vc, *wo, *wg, *wu, *wd; u16* out; char* ws; float one; unsigned gap; };

__global__ __launch_bounds__(256, 1) void k_main(Params p) {
  extern __shared__ __attribute__((aligned(16))) char lds[];
  const int tid = threadIdx.x, w = __builtin_amdgcn_readfirstlane(tid >> 6), lane = tid & 63;
  const int j = blockIdx.x & 7, c = blockIdx.x >> 3;
  const unsigned ldsb = (unsigned)(size_t)lds;
  const int hh = lane >> 5, r32 = lane & 31;
  u64* const flags = (u64*)(p.ws + WS_FLAG);
  unsigned* const tmo = (unsigned*)(p.ws + WS_TMO);
  STAMP(0);
  PST(0);
  QS qs;
  unsigned voffQ[8];
#pragma unroll
  for (int i = 0; i < 8; ++i) voffQ[i] = (4 * i + (lane >> 4)) * 8192 + 16 * ((lane & 15) ^ ((lane >> 4) << 2)) - (i - 4) * 1024 + 4096;
  const char* gq = (const char*)p.wq + (size_t)(4 * j + w) * 256 - 4096;
  qs.ring = __builtin_amdgcn_readfirstlane(ldsb + ABYTES + w * 2 * QSLOT);
  constexpr size_t KS_Q = (size_t)32 * 8192;
  auto issueq = [&](int slot, const char* gp) __attribute__((always_inline)) {
    setm0(qs.ring + slot * QSLOT + 4096); asm volatile("s_nop 0");
    dma<-4096>(voffQ[0], gp); dma<-3072>(voffQ[1], gp); dma<-2048>(voffQ[2], gp); dma<-1024>(voffQ[3], gp);
    dma<0>(voffQ[4], gp); dma<1024>(voffQ[5], gp); dma<2048>(voffQ[6], gp); dma<3072>(voffQ[7], gp);
  };
  const int mg = blockIdx.x % 54, ms = blockIdx.x / 54;
  u4 wm[5]; u16 cval[5];
#pragma unroll
  for (int i = 0; i < 5; ++i) {
    const int r = 144 * (ms & 3) + (i < 4 || tid < 128 ? (tid >> 3) + 32 * i : 0);
    wm[i] = *(const u4*)(p.wmod + (size_t)r * (6 * D) + mg * 64 + (tid & 7) * 8); cval[i] = p.c[r];
  }
  const u64 e0 = __hip_atomic_load((gu64*)(p.ws + WS_EPOCH), __ATOMIC_RELAXED, __HIP_MEMORY_SCOPE_AGENT);
  AT a;
  u64 tag;
  {
    f16v Qt[4][2];
    QS& s = qs;
    {
      const int rr = lane >> 3, sub = lane & 7;
      tag = ((u64)(unsigned)__builtin_amdgcn_readfirstlane((unsigned)(e0 >> 32)) << 32 | (u64)(unsigned)__builtin_amdgcn_readfirstlane((unsigned)e0)) + 1;
      const u64 gtag = tag << 16;
      PST(9);
      const __amdgpu_buffer_rsrc_t rP = __builtin_amdgcn_make_buffer_rsrc(p.ws + WS_MODP + (size_t)mg * 8192, 0, 8192, 0x00020000);
      if (blockIdx.x < 216) {
        float acc[8] = {};
#pragma unroll
        for (int i = 0; i < 5; ++i) {
          const float xc = bf2f(cval[i]);
          float sc = rbf(xc * __builtin_amdgcn_rcpf(1.f + __expf(-xc)));
          if (i == 4 && tid >= 128) sc = 0.f;
#pragma unroll
          for (int e = 0; e < 4; ++e) { acc[2 * e] += sc * bflo(wm[i][e]); acc[2 * e + 1] += sc * bfhi(wm[i][e]); }
        }
#pragma unroll
        for (int e = 0; e < 8; ++e) { float t = acc[e]; t += dpp<0x128>(t); acc[e] = swap32_add(swap16_add(t)); }
        const int k = (lane >> 3) & 3;
        const float v0 = k == 0 ? acc[0] : k == 1 ? acc[2] : k == 2 ? acc[4] : acc[6], v1 = k == 0 ? acc[1] : k == 1 ? acc[3] : k == 2 ? acc[5] : acc[7];
        if (lane < 32) __builtin_amdgcn_raw_buffer_store_b128((u4){__float_as_uint(v0), __float_as_uint(v1), (unsigned)tag, (unsigned)(tag >> 32)}, rP, (4 * ms + w) * 512 + (4 * (lane & 7) + k) * 16, 0, 16);
      }
      PST(10);
      if (blockIdx.x < 54 && w == 0) {
        const int pr = lane & 31, hf = lane >> 5;
        u4 gr[8];
        for (unsigned spins = 0;; ++spins) {
          bool ok = true;
#pragma unroll
          for (int k = 0; k < 8; ++k) gr[k] = __builtin_amdgcn_raw_buffer_load_b128(rP, (8 * hf + k) * 512 + pr * 16, 0, 16);
#pragma unroll
          for (int k = 0; k < 8; ++k) ok &= gr[k][2] == (unsigned)tag && gr[k][3] == (unsigned)(tag >> 32);
          if (__all(ok)) break;
          if (spins > SPIN_CAP) { if (lane == 0) __hip_atomic_store((gu32*)tmo, 3u, __ATOMIC_RELAXED, __HIP_MEMORY_SCOPE_AGENT); break; }
          __builtin_amdgcn_s_sleep(1);
        }
        f2 v = {0.f, 0.f};
#pragma unroll
        for (int k = 0; k < 8; ++k) v += (f2){__uint_as_float(gr[k][0]), __uint_as_float(gr[k][1])};
        v[0] = swap32_add(v[0]); v[1] = swap32_add(v[1]);
        const u64 g0 = gtag | f2bf(v[0]), g1 = gtag | f2bf(v[1]);
        const __amdgpu_buffer_rsrc_t rG = __builtin_amdgcn_make_buffer_rsrc(p.ws + WS_MODG, 0, 6 * D * 8, 0x00020000);
        if (lane < 32) __builtin_amdgcn_raw_buffer_store_b128((u4){(unsigned)g0, (unsigned)(g0 >> 32), (unsigned)g1, (unsigned)(g1 >> 32)}, rG, (mg * 32 + pr) * 16, 0, 16);
      }
      PST(11);
      u4 xv[2][9];
      GAP();
#pragma unroll
      for (int ps = 0; ps < 2; ++ps)
#pragma unroll
        for (int i = 0; i < 9; ++i) xv[ps][i] = *(const u4*)(p.x + (size_t)(c * 64 + 16 * w + 8 * ps + rr) * D + (sub + 8 * i) * 8);
      GAP();
      PST(1);
      STAMP(20);
      PST(2);
      f2 y[2][9][4];
      {
#pragma unroll
        for (int ps = 0; ps < 2; ++ps) {
          f2 a1 = {0.f, 0.f}, a2 = {0.f, 0.f};
#pragma unroll
          for (int i = 0; i < 9; ++i)
#pragma unroll
            for (int e = 0; e < 4; ++e) { y[ps][i][e] = bf2(xv[ps][i][e]); a1 += y[ps][i][e]; a2 = __builtin_elementwise_fma(y[ps][i][e], y[ps][i][e], a2); }
          float s1 = a1[0] + a1[1], s2 = a2[0] + a2[1];
          s1 = sum8(s1); s2 = sum8(s2);
          if (ps == 0) PST(3);
          const float mean = s1 * (1.f / D), var = __builtin_fmaxf(__builtin_fmaf(-mean, mean, s2 * (1.f / D)), 0.f);
          const float mr = rbf(mean), rs = __builtin_amdgcn_rcpf(rbf(__builtin_amdgcn_sqrtf(rbf(rbf(var) + 1e-5f))));
          f2 mr2 = {mr, mr}, rs2 = {rs, rs}; asm volatile("" : "+v"(mr2), "+v"(rs2));
#pragma unroll
          for (int i = 0; i < 9; ++i) {
            f2 t[4];
#pragma unroll
            for (int e = 0; e < 4; ++e) t[e] = y[ps][i][e] - mr2;
#pragma unroll
            for (int e = 0; e < 4; ++e) t[e] = rb2(t[e]);
#pragma unroll
            for (int e = 0; e < 4; ++e) t[e] = t[e] * rs2;
#pragma unroll
            for (int e = 0; e < 4; ++e) y[ps][i][e] = rb2(t[e]);
            asm volatile("" : "+v"(y[ps][i][0]), "+v"(y[ps][i][1]), "+v"(y[ps][i][2]), "+v"(y[ps][i][3]));
          }
        }
      }
      STAMP(21);
      PST(4);
      gather_mod<0, 1728, true, 256>(p.ws, tag, ldsb, tmo, tid);
      STAMP(22);
      PST(5);
      issueq(0, gq); issueq(1, gq + KS_Q);
      {
        const int li = tid < 144 ? tid : tid - 112;
        touch((const char*)p.wq + (size_t)512 * j * 2, (18 * c + (li >> 3)) * 8192 + (li & 7) * 128, __builtin_amdgcn_readfirstlane(ldsb + LDS_DUMMY));
      }
      asm volatile("s_waitcnt lgkmcnt(0)" ::: "memory");
      BAR();
      STAMP(23);
      PST(6);
      {
        const unsigned tb = ldsb + LDS_X1 + 1024 + sub * 32;
        f4 cst[2][4];
        auto ldc = [&](int i, f4 (&d)[4]) { d[0] = *(LAS f4*)(size_t)(tb + i * 256); d[1] = *(LAS f4*)(size_t)(tb + i * 256 + 16); d[2] = *(LAS f4*)(size_t)(tb + i * 256 + D * 4); d[3] = *(LAS f4*)(size_t)(tb + i * 256 + D * 4 + 16); };
        ldc(0, cst[0]);
        const unsigned ad0 = ldsb + (16 * w + rr) * ROWB + ((sub ^ ((rr >> 1) & 7)) * 16);
        const unsigned ad1 = ldsb + (16 * w + 8 + rr) * ROWB + ((sub ^ (((rr >> 1) + 4) & 7)) * 16);
#pragma unroll
        for (int i = 0; i < 9; ++i) {
          if (i + 1 < 9) ldc(i + 1, cst[(i + 1) & 1]);
          GAP();
          const f4 (&k)[4] = cst[i & 1];
          const f2 sh[4] = {{k[0][0], k[0][1]}, {k[0][2], k[0][3]}, {k[1][0], k[1][1]}, {k[1][2], k[1][3]}}, cc[4] = {{k[2][0], k[2][1]}, {k[2][2], k[2][3]}, {k[3][0], k[3][1]}, {k[3][2], k[3][3]}};
          f2 t[2][4]; u4 o[2];
#pragma unroll
          for (int ps = 0; ps < 2; ++ps)
#pragma unroll
            for (int e = 0; e < 4; ++e) t[ps][e] = y[ps][i][e] * cc[e];
#pragma unroll
          for (int ps = 0; ps < 2; ++ps)
#pragma unroll
            for (int e = 0; e < 4; ++e) t[ps][e] = rb2(t[ps][e]);
#pragma unroll
          for (int ps = 0; ps < 2; ++ps)
#pragma unroll
            for (int e = 0; e < 4; ++e) t[ps][e] = t[ps][e] + sh[e];
#pragma unroll
          for (int ps = 0; ps < 2; ++ps)
#pragma unroll
            for (int e = 0; e < 4; ++e) o[ps][e] = pk(t[ps][e]);
          *(LAS u4*)(size_t)(ad0 + i * 128) = o[0];
          *(LAS u4*)(size_t)(ad1 + i * 128) = o[1];
          GAP();
        }
      }
    }
    {
      const int blk = (lane >> 4) & 1, q = (lane & 15) >> 2, pp = lane & 3;
#pragma unroll
      for (int dt = 0; dt < 4; ++dt) s.wb[dt] = s.ring + (8 * hh + q) * 256 + 64 * (dt ^ q) + 32 * blk + 16 * (pp & 1) + 8 * (pp >> 1);
#pragma unroll
      for (int i = 0; i < 4; ++i) s.hb[i] = ldsb + r32 * ROWB + (((2 * i + hh) ^ ((r32 >> 1) & 7)) * 16);
    }
    asm volatile("s_waitcnt vmcnt(9) lgkmcnt(0)" ::: "memory");
    BAR();
    STAMP(1);
    PST(7);
#pragma unroll
    for (int f = 0; f < 8; ++f) { s.wlo[0][f] = ldT(s.wb[f & 3], 4096 * (f >> 2)); s.whi[0][f] = ldT(s.wb[f & 3], 4096 * (f >> 2) + 1024); }
#pragma unroll
    for (int i = 0; i < 4; ++i) s.hf[0][i] = ldA(s.hb[i >> 1], (i & 1) * 32 * ROWB);
    asm volatile("s_waitcnt lgkmcnt(0)" ::: "memory");
    issueq(0, gq + 2 * KS_Q);
    auto no = [&]() __attribute__((always_inline)) {};
    auto pfkv = [&](int h) __attribute__((always_inline)) {
      if (lane < 16) touch((const char*)(w < 2 ? p.kc : p.vc) + ((size_t)(2 * j + (w & 1)) * SK + (SK - WIN)) * DH * 2, (32 * c + 16 * h + lane) * 128, __builtin_amdgcn_readfirstlane(ldsb + LDS_DUMMY));
    };
    qstep<0, true, true, true, 9>(s, Qt, voffQ, gq + 3 * KS_Q, 0, no);
    qstep<1, false, true, true, 8>(s, Qt, voffQ, gq + 4 * KS_Q, 128, [&]() __attribute__((always_inline)) { pfkv(0); });
    qstep<0, false, true, true, 9>(s, Qt, voffQ, gq + 5 * KS_Q, 128, no);
    qstep<1, false, true, true, 9>(s, Qt, voffQ, gq + 6 * KS_Q, 256, [&]() __attribute__((always_inline)) { pfkv(1); });
    qstep<0, false, true, true, 9>(s, Qt, voffQ, gq + 7 * KS_Q, 256, no);
    qstep<1, false, true, true, 9>(s, Qt, voffQ, gq + 8 * KS_Q, 384, no);
    for (int n = 6; n < 14; n += 2) {
      qstep<0, false, true, true, 8>(s, Qt, voffQ, gq + (size_t)(n + 3) * KS_Q, 128 * ((n + 1) >> 1), no);
      qstep<1, false, true, true, 8>(s, Qt, voffQ, gq + (size_t)(n + 4) * KS_Q, 128 * ((n + 2) >> 1), no);
    }
    qstep<0, false, true, true, 8>(s, Qt, voffQ, gq + (size_t)17 * KS_Q, 128 * 7, no);
    qstep<1, false, true, false, 8>(s, Qt, voffQ, gq, 128 * 8, no);
    qstep<0, false, true, false, 0>(s, Qt, voffQ, gq, 128 * 8, no);
    qstep<1, false, false, false, 0>(s, Qt, voffQ, gq, 0, no);
    STAMP(2);
    PST(8);
    {
      const int kvl = w >> 1, half = w & 1;
      const unsigned kr = ldsb + kvl * 65536, vr = kr + 32768;
#pragma unroll
      for (int i = 0; i < 2; ++i) a.kb_[i] = kr + 2048 * (r32 >> 3) + 64 * (r32 & 7) + 16 * ((2 * i + hh) ^ ((r32 >> 2) & 3));
      const int blk = (lane >> 4) & 1, q = (lane & 15) >> 2, pp = lane & 3;
#pragma unroll
      for (int t = 0; t < 2; ++t) a.vb_[t] = vr + 64 * (4 * hh + q) + 16 * ((2 * blk + (pp >> 1)) ^ ((2 * t + hh) & 3)) + 8 * (pp & 1);
      const int st = lane >> 5, r7 = (lane >> 2) & 7, pos = lane & 3;
#pragma unroll
      for (int i = 0; i < 8; ++i) {
        const int rg = 4 * half + (i >> 1);
        a.voff[i] = rg * 2048 + r7 * 256 + (i & 1) * 128 + st * 64 + 16 * (pos ^ ((2 * (rg & 1) + (r7 >> 2)) & 3)) - (i - 4) * 1024 + 4096;
      }
      a.m0k = __builtin_amdgcn_readfirstlane(kr + 8192 * half + 4096); a.m0v = __builtin_amdgcn_readfirstlane(vr + 8192 * half + 4096);
      const size_t kvoff = ((size_t)(2 * j + kvl) * SK + (SK - WIN)) * DH * 2;
      a.gk = (const char*)p.kc + kvoff - 4096; a.gv = (const char*)p.vc + kvoff - 4096;
      a.pend = false; a.one = p.one;
    }
    asm volatile("s_waitcnt lgkmcnt(0)" ::: "memory");
    BAR();
    setm0(a.m0k); asm volatile("s_nop 0");
    dma<-4096>(a.voff[0], a.gk); dma<-3072>(a.voff[1], a.gk); dma<-2048>(a.voff[2], a.gk); dma<-1024>(a.voff[3], a.gk);
    dma<0>(a.voff[4], a.gk); dma<1024>(a.voff[5], a.gk); dma<2048>(a.voff[6], a.gk); dma<3072>(a.voff[7], a.gk);
    asm volatile("s_nop 7" ::: "memory");
#pragma unroll
    for (int dt = 0; dt < 4; ++dt)
#pragma unroll
      for (int qb = 0; qb < 2; ++qb) {
        asm volatile("" : "+v"(Qt[dt][qb]));
#pragma unroll
        for (int s2 = 0; s2 < 2; ++s2) {
          u4 w4;
#pragma unroll
          for (int e = 0; e < 4; ++e) w4[e] = pk((f2){Qt[dt][qb][8 * s2 + 2 * e], Qt[dt][qb][8 * s2 + 2 * e + 1]} * QSCALE);
          a.q[qb][2 * dt + s2] = __builtin_bit_cast(b8, w4);
          asm volatile("" : "+a"(a.q[qb][2 * dt + s2]));
        }
      }
  }
  STAMP(3);
  {
    auto tile_bar = [&]() __attribute__((always_inline)) { asm volatile("s_waitcnt vmcnt(0) lgkmcnt(0)" ::: "memory"); BAR(); };
    const char* pfp = lane >= 32 ? p.ws + WS_X + (size_t)c * 64 * SLICE + (size_t)j * SLICE + (18 * w + lane - 32) * 128
                    : w < 2 ? (const char*)(w == 0 ? p.wg : p.wu) + (size_t)576 * j * 2 + (size_t)(18 * c + lane / 9 - 3) * (DF * 2) + (lane % 9) * 128
                            : (const char*)p.wo + (size_t)512 * j * ROWB + (16 * c) * ROWB + (12 * (w - 2) + lane - 24) * 128;
    const int pfs = lane >= 32 ? 8 * SLICE : w < 2 ? 3 * DF * 2 : 24 * 128, pfn = w < 2 ? 27 : 12;
    auto pft = [&](int t) __attribute__((always_inline)) {
      const bool on = lane < pfn && t >= 1 && t <= 6;
      touchp(on ? pfp : (const char*)p.wo, __builtin_amdgcn_readfirstlane(ldsb + LDS_DUMMY));
      pfp += pfs;
    };
    const __amdgpu_buffer_rsrc_t rXw = __builtin_amdgcn_make_buffer_rsrc(p.ws + WS_X + (size_t)c * 64 * SLICE, 0, 64 * SLICE, 0x00020000);
    int pwq = w * 18;
    auto pw = [&](int) __attribute__((always_inline)) {
    };
    auto nopw = [&](int) __attribute__((always_inline)) {};
    auto tile_bar1 = [&]() __attribute__((always_inline)) { asm volatile("s_waitcnt vmcnt(1) lgkmcnt(0)" ::: "memory"); BAR(); };
    LAS unsigned* flag = (LAS unsigned*)(size_t)(ldsb + LDS_X1);
    if (tid == 0) *flag = 0u;
    auto k0 = [&]() __attribute__((always_inline)) {
      setm0(a.m0k); asm volatile("s_nop 0");
      dma<-4096>(a.voff[0], a.gk); dma<-3072>(a.voff[1], a.gk); dma<-2048>(a.voff[2], a.gk); dma<-1024>(a.voff[3], a.gk);
      dma<0>(a.voff[4], a.gk); dma<1024>(a.voff[5], a.gk); dma<2048>(a.voff[6], a.gk); dma<3072>(a.voff[7], a.gk);
    };
    tile_bar(); fastA<0, true, true, true>(a, 0, [&]() __attribute__((always_inline)) { pft(0); }, pw); asm volatile("s_nop 7\n\ts_nop 3" ::: "memory"); GAP(); fastB<0, 2>(a);
    tile_bar1(); fastA<1, false, true, true>(a, 1, [&]() __attribute__((always_inline)) { pft(1); }, pw); fastB<1, 1>(a);
    for (int t = 2; t < 6; t += 2) {
      tile_bar1(); fastA<0, false, true, true>(a, t, [&]() __attribute__((always_inline)) { pft(t); }, pw); fastB<0, 0>(a);
      tile_bar1(); fastA<1, false, true, true>(a, t + 1, [&]() __attribute__((always_inline)) { pft(t + 1); }, pw); fastB<1, 0>(a);
    }
    tile_bar1(); fastA<0, false, true, true>(a, 6, [&]() __attribute__((always_inline)) { pft(6); }, nopw); fastB<0, 0>(a);
    tile_bar1(); fastA<1, false, false, true>(a, 7, [&]() __attribute__((always_inline)) { pft(7); }, nopw); fastB<1, 0>(a);
    tile_bar1();
    fastB<1, 3>(a);
    {
      const bool bad = !(a.l[0] < 0x1p40f) || !(a.l[1] < 0x1p40f);
      if (__any((int)bad) && lane == 0) *flag = 1u;
      asm volatile("s_waitcnt lgkmcnt(0)" ::: "memory"); BAR();
      unsigned redo = __builtin_amdgcn_readfirstlane(*(volatile LAS unsigned*)flag); asm volatile("" : "+s"(redo));
      if (__builtin_expect(redo != 0u, 0)) {
        auto nopf = [&]() __attribute__((always_inline)) {};
        a.pend = false;
        auto upt = [&](const char* q) __attribute__((always_inline)) { const u64 v = (u64)q; return (const char*)((u64)(unsigned)__builtin_amdgcn_readfirstlane((unsigned)(v >> 32)) << 32 | (u64)(unsigned)__builtin_amdgcn_readfirstlane((unsigned)v)); };
        a.gk = upt(a.gk); a.gv = upt(a.gv); a.m0k = __builtin_amdgcn_readfirstlane(a.m0k); a.m0v = __builtin_amdgcn_readfirstlane(a.m0v);
        k0();
        tile_bar(); phaseA<0, true, true, true>(a, 0, nopf); asm volatile("s_nop 7\n\ts_nop 3" ::: "memory"); GAP(); phaseB<0, true, false>(a);
        tile_bar(); phaseA<1, false, true, true>(a, 1, nopf); phaseB<1, false, true>(a);
        for (int t = 2; t < 6; t += 2) {
          tile_bar(); phaseA<0, false, true, true>(a, t, nopf); phaseB<0, false, false>(a);
          tile_bar(); phaseA<1, false, true, true>(a, t + 1, nopf); phaseB<1, false, false>(a);
        }
        tile_bar(); phaseA<0, false, true, true>(a, 6, nopf); phaseB<0, false, false>(a);
        tile_bar(); phaseA<1, false, false, true>(a, 7, nopf); phaseB<1, false, false>(a);
        { auto f1 = [&](auto ic) __attribute__((always_inline)) { sm_exp<1, 1, decltype(ic)::value>(a); }; UNROLL(32, f1);
          auto f2_ = [&](auto ic) __attribute__((always_inline)) { sm_pack<1, decltype(ic)::value>(a); }; UNROLL(32, f2_);
          auto f3 = [&](auto ic) __attribute__((always_inline)) { sm_add<1, 1, decltype(ic)::value>(a); }; UNROLL(32, f3); }
#pragma unroll
        for (int qb = 0; qb < 2; ++qb) a.l[qb] += a.rs0[qb] + a.rs1[qb];
        tile_bar();
        const unsigned vslot = 16384;
        b4 vlo[16], vhi[16];
#pragma unroll
        for (int I = 0; I < 16; ++I) { vlo[I] = ldT(a.vb_[0], vslot + 2048 * (2 * (I >> 2)) + 512 * (I & 3)); vhi[I] = ldT(a.vb_[1], vslot + 2048 * (2 * (I >> 2) + 1) + 512 * (I & 3)); }
        asm volatile("s_nop 1" ::: "memory"); GAP();
#pragma unroll
        for (int G = 0; G < 32; ++G) { const int I = G >> 1; mfma32<true, false>(a.O[I & 3][G & 1], cat(vlo[I], vhi[I]), a.P[G & 1][I >> 2]); }
        asm volatile("s_waitcnt vmcnt(0) lgkmcnt(0)" ::: "memory"); BAR();
      }
    }
  }
  STAMP(4);
  int tid2 = threadIdx.x; asm volatile("" : "+v"(tid2));
  {
  const int tid = tid2, lane = tid2 & 63, hh = lane >> 5, r32 = lane & 31;
  GS s;
  unsigned voffB[9];
#pragma unroll
  for (int i = 0; i < 9; ++i) { const int idx = i * 64 + lane, imm = i < 8 ? (i - 4) * 1024 : 0; voffB[i] = (idx / 18) * ROWB + (idx % 18) * 16 - imm + 4096; }
  s.ring = __builtin_amdgcn_readfirstlane(ldsb + ABYTES + w * 2 * SLOT);
  const int g4 = lane >> 4, cl = lane & 15;
  s.wbase = s.ring + (4 * g4 + ((lane & 15) >> 2)) * 288 + 8 * (lane & 3);
  const unsigned sw = (cl >> 1) & 7;
  const unsigned ab_e = ldsb + cl * ROWB + ((g4 ^ sw) * 16), ab_o = ldsb + cl * ROWB + (((4 + g4) ^ sw) * 16);
  const __amdgpu_buffer_rsrc_t rX = __builtin_amdgcn_make_buffer_rsrc(p.ws + WS_X + (size_t)c * 64 * SLICE, 0, 64 * SLICE, 0x00020000);
  unsigned voffA[9];
#pragma unroll
  for (int i = 0; i < 9; ++i) { const int idx = i * 64 + lane, imm = i < 8 ? (i - 4) * 1024 : 0; voffA[i] = (idx / 18) * (DF * 2) + (idx % 18) * 16 - imm + 4096; }
  const char* gG = (const char*)p.wg + (size_t)(576 * j + 144 * w) * 2 - 4096;
  const char* gU = (const char*)p.wu + (size_t)(576 * j + 144 * w) * 2 - 4096;
  const char* gD = (const char*)p.wd + (size_t)(576 * j) * ROWB + 288 * w - 4096;
  f4 aG[9][4];
  {
    const char* gO = (const char*)p.wo + (size_t)(512 * j) * ROWB + 288 * w - 4096;
    issue_all(s, 0, voffB, gO); issue_all(s, 1, voffB, gO + (size_t)32 * ROWB);
    asm volatile("s_nop 7" ::: "memory");
#pragma unroll
    for (int qb = 0; qb < 2; ++qb) {
      const float il = __builtin_amdgcn_rcpf(swap32_add(a.l[qb]));
      const int row = 32 * qb + r32; const unsigned swr = (r32 >> 1) & 7;
#pragma unroll
      for (int db = 0; db < 4; ++db) {
        asm volatile("" : "+a"(a.O[db][qb]));
        const f16v o = a.O[db][qb];
#pragma unroll
        for (int u = 0; u < 2; ++u) {
          const u4 wv = {pk((f2){o[4 * u], o[4 * u + 1]} * il), pk((f2){o[4 * u + 2], o[4 * u + 3]} * il), pk((f2){o[4 * u + 8], o[4 * u + 9]} * il), pk((f2){o[4 * u + 10], o[4 * u + 11]} * il)};
          *(LAS u4*)(size_t)(ldsb + row * ROWB + (((4 * (4 * w + db) + 2 * u + hh) ^ swr) * 16)) = wv;
        }
      }
    }
    asm volatile("s_waitcnt vmcnt(9) lgkmcnt(0)" ::: "memory");
    BAR();
    STAMP(5);
    read_w(s, 0); read_a(s, 0, ab_e);
    asm volatile("s_waitcnt lgkmcnt(0)" ::: "memory");
    issue_all(s, 0, voffB, gO + (size_t)2 * 32 * ROWB);
    const Epi e1 = {rX, (unsigned)(j * SLICE), 8u * SLICE, ldsb + LDS_X1, j, w, lane};
    gemm_rows<16, 2, 0>(s, aG, voffB, gO, ab_e, ab_o, voffA, gG, gU, e1, [&]() __attribute__((always_inline)) {});
    STAMP(6);
  }
  {
    int zlr[3], zcc[3];
    const int t3 = tid < 192 ? tid : tid - 64;
    u4 xr[3], gv[3];
#pragma unroll
    for (int i = 0; i < 3; ++i) {
      zdecode(t3 + 192 * i, zlr[i], zcc[i]);
      xr[i] = *(const u4*)(p.x + (size_t)(c * 64 + j * 8 + zlr[i]) * D + zcc[i] * 8);
      gv[i] = *(LAS u4*)(size_t)(ldsb + LDS_MOD + (2 * D + zcc[i] * 8) * 2);
    }
    const int lrow = 2 * w + hh, grow = j * 8 + lrow;
    u2 cbv[3][2], sbv[3][2];
#pragma unroll
    for (int i = 0; i < 3; ++i) {
      const int pos = (i < 2 || r32 < 8) ? r32 + 32 * i : r32;
      const int q = pos ^ ((grow >> 1) & 7), k0 = 32 * (q >> 2) + 4 * (q & 3);
#pragma unroll
      for (int h = 0; h < 2; ++h) { cbv[i][h] = *(LAS u2*)(size_t)(ldsb + LDS_MOD + (4 * D + k0 + 16 * h) * 2); sbv[i][h] = *(LAS u2*)(size_t)(ldsb + LDS_MOD + (3 * D + k0 + 16 * h) * 2); }
    }
    asm volatile("s_waitcnt lgkmcnt(0)" ::: "memory");
    publish(flags + (0 * 256 + blockIdx.x) * FLAG_STRIDE, tag);
    STAMP(7);
    await8(flags + (0 * 256 + c * 8) * FLAG_STRIDE, tag, tmo, w, lane);
    STAMP(8);
    {
      u4 v[3][8];
#pragma unroll
      for (int i = 0; i < 3; ++i) {
#pragma unroll
        for (int jj = 0; jj < 7; ++jj) v[i][jj] = __builtin_amdgcn_raw_buffer_load_b128(rX, (j * 8 + jj + (jj >= j)) * SLICE + (t3 + 192 * i) * 16, 0, AUX_X);
      }
#pragma unroll
      for (int i = 0; i < 3; ++i) v[i][7] = *(LAS u4*)(size_t)(ldsb + LDS_X1 + (t3 + 192 * i) * 16);
#pragma unroll
      for (int i = 0; i < 3; ++i) {
        const u4 x1 = gated_residual(v[i], xr[i], gv[i]);
        if (tid < 192) {
          *(LAS u4*)(size_t)(ldsb + LDS_X1 + (tid + 192 * i) * 16) = x1;
          LAS f4* dst = (LAS f4*)(size_t)(ldsb + (zlr[i] * D + zcc[i] * 8) * 4);
          const f2 a0 = bf2(x1[0]), a1 = bf2(x1[1]), a2 = bf2(x1[2]), a3 = bf2(x1[3]);
          dst[0] = (f4){a0[0], a0[1], a1[0], a1[1]}; dst[1] = (f4){a2[0], a2[1], a3[0], a3[1]};
        }
      }
    }
    asm volatile("s_waitcnt lgkmcnt(0)" ::: "memory");
    BAR();
    STAMP(9);
    {
      f4 v[3][2];
      f2 sum = {0.f, 0.f};
#pragma unroll
      for (int i = 0; i < 3; ++i) {
        const bool on = (i < 2 || r32 < 8);
        const int pos = on ? r32 + 32 * i : r32;
        const int q = pos ^ ((grow >> 1) & 7), k0 = 32 * (q >> 2) + 4 * (q & 3);
#pragma unroll
        for (int h = 0; h < 2; ++h) {
          v[i][h] = *(LAS f4*)(size_t)(ldsb + (lrow * D + k0 + 16 * h) * 4);
          if (on) sum += (f2){v[i][h][0] + v[i][h][1], v[i][h][2] + v[i][h][3]};
        }
      }
      float sm = sum[0] + sum[1];
      sm = sum32(sm);
      const float mean = sm * (1.f / D);
      float q2 = 0.f;
#pragma unroll
      for (int i = 0; i < 3; ++i)
        if (i < 2 || r32 < 8) {
#pragma unroll
          for (int h = 0; h < 2; ++h)
#pragma unroll
            for (int e = 0; e < 4; ++e) { const float dl = v[i][h][e] - mean; q2 += dl * dl; }
        }
      q2 = sum32(q2);
      const float mr = rbf(mean), rs = __builtin_amdgcn_rcpf(rbf(__builtin_amdgcn_sqrtf(rbf(rbf(q2 * (1.f / D)) + 1e-5f))));
      const __amdgpu_buffer_rsrc_t rH = __builtin_amdgcn_make_buffer_rsrc(p.ws + WS_H2 + (size_t)c * ABYTES, 0, ABYTES, 0x00020000);
      u4 oh[3];
#pragma unroll
      for (int i = 0; i < 3; ++i) {
        u4& o = oh[i];
#pragma unroll
        for (int h = 0; h < 2; ++h)
#pragma unroll
          for (int e = 0; e < 2; ++e) {
            unsigned t = pk((f2){v[i][h][2 * e], v[i][h][2 * e + 1]} - mr);
            t = pk(bf2(t) * rs);
            t = pk(bf2(t) * bf2(pk(bf2(cbv[i][h][e]) + 1.f)));
            o[2 * h + e] = pk(bf2(t) + bf2(sbv[i][h][e]));
          }
        if (i < 2 || r32 < 8) __builtin_amdgcn_raw_buffer_store_b128(o, rH, grow * ROWB + (r32 + 32 * i) * 16, 0, 16);
      }
      publish(flags + (1 * 256 + blockIdx.x) * FLAG_STRIDE, tag);
      STAMP(10);
      if (w != 0) {
        const unsigned dummy = __builtin_amdgcn_readfirstlane(ldsb + LDS_DUMMY);
        int li = tid - 64; li = li < 162 ? li : li - 94;
        const int r = 18 * c + li / 9, l = li % 9;
        touch((const char*)p.wg + (size_t)576 * j * 2, r * (DF * 2) + l * 128, dummy);
        touch((const char*)p.wu + (size_t)576 * j * 2, r * (DF * 2) + l * 128, dummy);
      }
      await8(flags + (1 * 256 + c * 8) * FLAG_STRIDE, tag, tmo, w, lane);
      STAMP(11);
      u4 hv[16];
#pragma unroll
      for (int i = 0; i < 16; ++i) { const int y = i * 256 + tid < 4032 ? i * 256 + tid : tid; hv[i] = __builtin_amdgcn_raw_buffer_load_b128(rH, (y + (y >= j * 576 ? 576 : 0)) * 16, 0, AUX_H); }
#pragma unroll
      for (int i = 0; i < 3; ++i) if (i < 2 || r32 < 8) *(LAS u4*)(size_t)(ldsb + grow * ROWB + (r32 + 32 * i) * 16) = oh[i];
#pragma unroll
      for (int i = 0; i < 16; ++i) { const int y = i * 256 + tid; if (i < 15 || y < 4032) *(LAS u4*)(size_t)(ldsb + (y + (y >= j * 576 ? 576 : 0)) * 16) = hv[i]; }
    }
    asm volatile("s_waitcnt vmcnt(0) lgkmcnt(0)" ::: "memory");
    BAR();
  }
  STAMP(12);
  {
    constexpr size_t KS_GU = (size_t)32 * DF * 2, KS_D = (size_t)32 * ROWB;
    read_w(s, 0); read_a(s, 0, ab_e);
    asm volatile("s_waitcnt lgkmcnt(0)" ::: "memory");
    issue_all(s, 0, voffA, gG + KS_GU);
    f4 aU[9][4];
    auto no = [&]() __attribute__((always_inline)) {};
    auto pfd = [&](int h) __attribute__((always_inline)) {
      int li = 64 * h + 16 * w + lane; li = li < 162 ? li : 161;
      if (lane < 16) touch((const char*)p.wd + (size_t)576 * j * ROWB, (18 * c) * ROWB + li * 128, __builtin_amdgcn_readfirstlane(ldsb + LDS_DUMMY));
    };
    auto kpair = [&](auto zc, auto lastc, auto vmc, int k, auto&& pf) __attribute__((always_inline)) {
      constexpr bool Z = decltype(zc)::value, LAST = decltype(lastc)::value; constexpr int VM = decltype(vmc)::value;
      const char* u1 = gU + (size_t)(k + 1) * KS_GU; const char* g2 = gG + (size_t)(k + 2) * KS_GU;
      step<0, 0, Z, true, false, true, 9, 0>(s, aG, voffA, u1, 0, no);
      if constexpr (!LAST) {
        step<1, 0, Z, true, true, true, 9, NVU>(s, aU, voffA, g2, ab_o + 128 * (k >> 1), pf);
        step<0, 1, false, true, false, true, VM, 0>(s, aG, voffA, u1 + KS_GU, 0, no);
        step<1, 1, false, true, true, true, VM, NVU>(s, aU, voffA, g2 + KS_GU, ab_e + 128 * ((k + 2) >> 1), no);
      } else {
        step<1, 0, Z, true, true, true, 9, NVU>(s, aU, voffB, gD, ab_o + 128 * (k >> 1), no);
        step<0, 1, false, true, false, true, 9, 0>(s, aG, voffB, gD + KS_D, 0, no);
        step<1, 1, false, true, false, true, 9, NVU>(s, aU, voffB, gD + 2 * KS_D, 0, no);
      }
    };
    kpair(std::true_type{}, std::false_type{}, IC<9>{}, 0, no);
    for (int k = 2; k < 8; k += 2) kpair(std::false_type{}, std::false_type{}, IC<9>{}, k, no);
    for (int k = 8; k < 14; k += 2) kpair(std::false_type{}, std::false_type{}, IC<10>{}, k, [&]() __attribute__((always_inline)) { pfd((k - 8) >> 1); });
    kpair(std::false_type{}, std::false_type{}, IC<9>{}, 14, no);
    kpair(std::false_type{}, std::true_type{}, IC<9>{}, 16, no);
    STAMP(13);
    settle<0>(aG); settle<NVU>(aU);
    u2 act[9][4];
#pragma unroll
    for (int t = 0; t < 9; ++t)
#pragma unroll
      for (int m = 0; m < 4; ++m)
#pragma unroll
        for (int e = 0; e < 2; ++e) {
          const f2 x = {aG[t][m][2 * e], aG[t][m][2 * e + 1]}, u = {aU[t][m][2 * e], aU[t][m][2 * e + 1]};
          const f2 z = x * (x * x * -0.10294324f + -2.3022082f);
          const f2 dn = (f2){__builtin_amdgcn_exp2f(z[0]), __builtin_amdgcn_exp2f(z[1])} + 1.f;
          act[t][m][e] = pk((x * u) * (f2){__builtin_amdgcn_rcpf(dn[0]), __builtin_amdgcn_rcpf(dn[1])});
        }
    asm volatile("s_waitcnt lgkmcnt(0)" ::: "memory");
    BAR();
#pragma unroll
    for (int t = 0; t < 9; ++t) {
      const int tt = 9 * w + t, ks = tt >> 1, ah = tt & 1;
      const unsigned ad = ldsb + cl * ROWB + (((4 * ks + g4) ^ sw) * 16) + 8 * ah;
#pragma unroll
      for (int m = 0; m < 4; ++m) *(LAS u2*)(size_t)(ad + m * 16 * ROWB) = act[t][m];
    }
    asm volatile("s_waitcnt lgkmcnt(0)" ::: "memory");
    BAR();
    STAMP(14);
    read_a(s, 0, ab_e);
    const Epi e2 = {rX, (unsigned)(j * 8 * SLICE), (unsigned)SLICE, s.ring - w * 2304, j, w, lane};
    gemm_rows<18, 0, 0>(s, aG, voffB, gD, ab_e, ab_o, voffB, gD, gD, e2, [&]() __attribute__((always_inline)) {});
    STAMP(15);
  }
  {
    int tid3 = threadIdx.x; asm volatile("" : "+v"(tid3));
    const int tid = tid3, lane = tid3 & 63;
    asm volatile("s_waitcnt lgkmcnt(0)" ::: "memory");
    publish(flags + (2 * 256 + blockIdx.x) * FLAG_STRIDE, tag);
    STAMP(16);
    const int t3 = tid < 192 ? tid : tid - 64;
    int zlr[3], zcc[3];
    u4 gv[3], x1[3];
#pragma unroll
    for (int i = 0; i < 3; ++i) {
      zdecode(t3 + 192 * i, zlr[i], zcc[i]);
      gv[i] = *(LAS u4*)(size_t)(ldsb + LDS_MOD + (5 * D + zcc[i] * 8) * 2);
      x1[i] = *(LAS u4*)(size_t)(ldsb + LDS_X1 + (t3 + 192 * i) * 16);
    }
    if (blockIdx.x == 0 && w == 1) {
      for (unsigned spins = 0; spins <= SPIN_CAP; ++spins) {
        bool ok = true;
#pragma unroll
        for (int i = 0; i < 4; ++i) ok &= __hip_atomic_load((gu64*)(flags + (lane + 64 * i) * FLAG_STRIDE), __ATOMIC_RELAXED, __HIP_MEMORY_SCOPE_AGENT) == tag;
        if (__all(ok)) break;
        __builtin_amdgcn_s_sleep(4);
      }
      if (lane == 0) __hip_atomic_store((gu64*)(p.ws + WS_EPOCH), tag, __ATOMIC_RELAXED, __HIP_MEMORY_SCOPE_AGENT);
    }
    await8(flags + (2 * 256 + c * 8) * FLAG_STRIDE, tag, tmo, w, lane);
    STAMP(17);
    const __amdgpu_buffer_rsrc_t rO = __builtin_amdgcn_make_buffer_rsrc(p.out + (size_t)(c * 64 + j * 8) * D, 0, SLICE, 0x00020000); (void)rO;
    u4 v[3][8];
#pragma unroll
    for (int i = 0; i < 3; ++i)
#pragma unroll
      for (int jj = 0; jj < 7; ++jj) v[i][jj] = __builtin_amdgcn_raw_buffer_load_b128(rX, ((jj + (jj >= j)) * 8 + j) * SLICE + (t3 + 192 * i) * 16, 0, AUX_X);
#pragma unroll
    for (int i = 0; i < 3; ++i) { const int z = t3 + 192 * i, zw = z / 144; v[i][7] = *(LAS u4*)(size_t)(ldsb + ABYTES + zw * 2 * SLOT + (z - zw * 144) * 16); }
#pragma unroll
    for (int i = 0; i < 3; ++i) {
      const u4 o = gated_residual(v[i], x1[i], gv[i]);
      if (tid < 192) *(u4*)(p.out + (size_t)(c * 64 + j * 8 + zlr[i]) * D + zcc[i] * 8) = o;
    }
  }
  STAMP(18);
  asm volatile("s_waitcnt lgkmcnt(0)" ::: "memory");
  STAMP_FLUSH();
  }
}

extern "C" void kernel_launch(void* const* d_in, const int* in_sizes, int n_in, void* d_out, int out_size, void* d_ws, size_t ws_size, hipStream_t stream) {
  Params p;
  p.x = (const u16*)d_in[0]; p.kc = (const u16*)d_in[1]; p.vc = (const u16*)d_in[2]; p.wq = (const u16*)d_in[3]; p.wo = (const u16*)d_in[4];
  p.wg = (const u16*)d_in[6]; p.wu = (const u16*)d_in[7]; p.wd = (const u16*)d_in[8];
  p.c = (const u16*)d_in[9]; p.wmod = (const u16*)d_in[10];
  p.out = (u16*)d_out; p.ws = (char*)d_ws;
 p.one = 1.0f; p.gap = 0u;
  (void)hipFuncSetAttribute((const void*)k_main, hipFuncAttributeMaxDynamicSharedMemorySize, LDS_BYTES);
  k_main<<<256, 256, LDS_BYTES, stream>>>(p);
}
```

```cpp
#define TLBWARM
#include <hip/hip_runtime.h>
#include <stdint.h>
#include <utility>
#include <type_traits>

typedef unsigned short u16;
typedef unsigned long long u64;
typedef __bf16 b2 __attribute__((ext_vector_type(2)));
typedef __bf16 b4 __attribute__((ext_vector_type(4)));
typedef __bf16 b8 __attribute__((ext_vector_type(8)));
typedef float f2 __attribute__((ext_vector_type(2)));
typedef float f4 __attribute__((ext_vector_type(4)));
typedef float f16v __attribute__((ext_vector_type(16)));
typedef unsigned u2 __attribute__((ext_vector_type(2)));
typedef unsigned u4 __attribute__((ext_vector_type(4)));
typedef __attribute__((address_space(1))) unsigned gu32;
typedef __attribute__((address_space(1))) unsigned long long gu64;
#define LAS __attribute__((address_space(3)))
#define DI __device__ __forceinline__
#define GAP() __builtin_amdgcn_sched_barrier(0)
#define LGKM0() __builtin_amdgcn_s_waitcnt(0xC07F)
#define BAR() do { asm volatile("" ::: "memory"); __builtin_amdgcn_s_barrier(); asm volatile("" ::: "memory"); } while (0)
template <int V> using IC = std::integral_constant<int, V>;
#define AUX_X 18
#define AUX_H 18
#ifndef NVU
#define NVU 8
#endif
#define PFD_K 14
#define UNROLL(N, F) [&]<int... I_>(std::integer_sequence<int, I_...>) __attribute__((always_inline)) { (F(IC<I_>{}), ...); }(std::make_integer_sequence<int, N>{})

constexpr int S = 2048, D = 576, DH = 128, SK = 2048, WIN = 512, DF = 4608;
constexpr int ROWB = D * 2;
constexpr int ABYTES = 64 * ROWB;
constexpr int SLICE = 8 * ROWB;
constexpr int SLOT = 9216;
constexpr int QSLOT = 8192;
constexpr int LDS_DUMMY = ABYTES + 8 * SLOT;
constexpr int LDS_X1 = LDS_DUMMY + 256;
constexpr int LDS_MOD = LDS_X1 + SLICE;
constexpr int LDS_BYTES = LDS_MOD + 6 * D * 2;
static_assert(LDS_BYTES <= 163840, "LDS");
constexpr size_t WS_EPOCH = 0;
constexpr size_t WS_TMO = 256;
constexpr size_t WS_MODG = 4096;
constexpr size_t WS_MODP = 131072;
constexpr size_t WS_FLAG = 32768;
constexpr int FLAG_STRIDE = 8;
constexpr size_t WS_X = 1048576;
constexpr size_t WS_H2 = WS_X + (size_t)32 * 64 * SLICE;
constexpr size_t WS_DBG = 200u << 20;
#define STAMP(i)
#define STAMP_FLUSH()
#define PST(i)

DI float bf2f(u16 v) { return __uint_as_float(((unsigned)v) << 16); }
DI float bflo(unsigned w) { return __uint_as_float(w << 16); }
DI float bfhi(unsigned w) { return __uint_as_float(w & 0xffff0000u); }
DI f2 bf2(unsigned w) { return (f2){bflo(w), bfhi(w)}; }
DI unsigned pk(float lo, float hi) { f2 v = {lo, hi}; b2 b = __builtin_convertvector(v, b2); return __builtin_bit_cast(unsigned, b); }
DI unsigned pk(f2 v) { b2 b = __builtin_convertvector(v, b2); return __builtin_bit_cast(unsigned, b); }
DI float rbf(float f) { return bflo(pk(f, f)); }
DI f2 rb2(f2 v) { return (f2){__uint_as_float(pk(0.f, v[0])), __uint_as_float(pk(0.f, v[1]))}; }
template <int CTRL> DI float dpp(float v) { return __uint_as_float(__builtin_amdgcn_update_dpp(0u, __float_as_uint(v), CTRL, 0xf, 0xf, false)); }
DI float sum8(float v) { v += dpp<0xB1>(v); v += dpp<0x4E>(v); v += dpp<0x141>(v); return v; }
DI float swap16_add(float x) { auto r = __builtin_amdgcn_permlane16_swap(__float_as_uint(x), __float_as_uint(x), false, false); return __uint_as_float(r[0]) + __uint_as_float(r[1]); }
DI float swap32_add(float x) { auto r = __builtin_amdgcn_permlane32_swap(__float_as_uint(x), __float_as_uint(x), false, false); return __uint_as_float(r[0]) + __uint_as_float(r[1]); }
DI float sum32(float v) { v = sum8(v); v += dpp<0x140>(v); return swap16_add(v); }
DI u16 f2bf(float f) { return (u16)(pk(f, f) & 0xffffu); }
DI float fmaxx(float a, float b) { return __builtin_elementwise_maximum(a, b); }
DI float fmax3(float a, float b, float c) { return __builtin_elementwise_maximum(__builtin_elementwise_maximum(a, b), c); }

DI void zdecode(int z, int& lr, int& cc) {
  const int w = z / 144, y = z - w * 144;
  if (y < 128) { const int tp = y >> 5; lr = (y >> 2) & 7; cc = 18 * w + 4 * tp + (y & 3); }
  else { const int y2 = y - 128; lr = y2 >> 1; cc = 18 * w + 16 + (y2 & 1); }
}

template <int IMM> DI void dma(unsigned voff, const char* g) { asm volatile("global_load_lds_dwordx4 %0, %1 offset:%2" :: "v"(voff), "s"(g), "i"(IMM) : "memory"); }
DI void setm0(unsigned a) { asm volatile("s_mov_b32 m0, %0" :: "s"(a) : "memory"); }
DI void touch(const char* p, unsigned voff, unsigned ldsdummy) {
  asm volatile("s_mov_b32 m0, %0\n\ts_nop 0\n\tglobal_load_lds_dword %1, %2" :: "s"(ldsdummy), "v"(voff), "s"(p) : "memory");
}
DI void touchp(const char* a, unsigned ldsdummy) {
  asm volatile("s_mov_b32 m0, %0\n\ts_nop 0\n\tglobal_load_lds_dword %1, off" :: "s"(ldsdummy), "v"(a) : "memory");
}
DI b4 ldT(unsigned base, int off) { return __builtin_amdgcn_ds_read_tr16_b64_v4bf16((LAS b4*)(size_t)(base + off)); }
DI b8 ldA(unsigned base, int off) { return *(LAS b8*)(size_t)(base + off); }
DI b8 cat(const b4& lo, const b4& hi) { return __builtin_shufflevector(lo, hi, 0, 1, 2, 3, 4, 5, 6, 7); }
template <bool AG, bool ZERO> DI void mfma16(f4& c, const b8& a, const b8& b) {
  if constexpr (AG) {
    if constexpr (ZERO) asm volatile("v_mfma_f32_16x16x32_bf16 %0, %1, %2, 0" : "=a"(c) : "v"(a), "v"(b));
    else asm volatile("v_mfma_f32_16x16x32_bf16 %0, %1, %2, %0" : "+a"(c) : "v"(a), "v"(b));
  } else {
    if constexpr (ZERO) asm volatile("v_mfma_f32_16x16x32_bf16 %0, %1, %2, 0" : "=&v"(c) : "v"(a), "v"(b));
    else asm volatile("v_mfma_f32_16x16x32_bf16 %0, %1, %2, %0" : "+v"(c) : "v"(a), "v"(b));
  }
}
template <bool AG, bool ZERO> DI void mfma32(f16v& c, const b8& a, const b8& b) {
  if constexpr (AG) {
    if constexpr (ZERO) asm volatile("v_mfma_f32_32x32x16_bf16 %0, %1, %2, 0" : "=a"(c) : "v"(a), "v"(b));
    else asm volatile("v_mfma_f32_32x32x16_bf16 %0, %1, %2, %0" : "+a"(c) : "v"(a), "v"(b));
  } else {
    if constexpr (ZERO) asm volatile("v_mfma_f32_32x32x16_bf16 %0, %1, %2, 0" : "=&v"(c) : "v"(a), "v"(b));
    else asm volatile("v_mfma_f32_32x32x16_bf16 %0, %1, %2, %0" : "+v"(c) : "v"(a), "v"(b));
  }
}
template <int NVG> DI void settle(f4 (&acc)[9][4]) {
  asm volatile("s_nop 7\n\ts_nop 7" ::: "memory");
#pragma unroll
  for (int t = 0; t < 9; ++t)
#pragma unroll
    for (int m = 0; m < 4; ++m) { if (t * 4 + m < 36 - NVG) asm volatile("" : "+a"(acc[t][m])); else asm volatile("" : "+v"(acc[t][m])); }
}

struct GS {
  b4 wlo[2][9], whi[2][9];
  b8 af[2][4];
  unsigned wbase, ring;
};
DI void issue_all(const GS& s, int slot, const unsigned (&voff)[9], const char* gp) {
  const unsigned sl = s.ring + slot * SLOT;
  setm0(sl + 4096); asm volatile("s_nop 0");
  dma<-4096>(voff[0], gp); dma<-3072>(voff[1], gp); dma<-2048>(voff[2], gp); dma<-1024>(voff[3], gp);
  dma<0>(voff[4], gp); dma<1024>(voff[5], gp); dma<2048>(voff[6], gp); dma<3072>(voff[7], gp);
  setm0(sl + 8192); asm volatile("s_nop 0"); dma<0>(voff[8], gp);
}
template <int PAR, int AP, bool ZERO, bool RDW, bool RDA, bool DMA, int VM, int NVG, typename PF>
DI void step(GS& s, f4 (&acc)[9][4], const unsigned (&voff)[9], const char* gp, unsigned ab, PF&& pf) {
  const unsigned sl = s.ring + (PAR ^ 1) * SLOT;
  const unsigned wb = s.wbase + (PAR ^ 1) * SLOT;
  auto mm = [&](auto ic) __attribute__((always_inline)) {
    constexpr int I = decltype(ic)::value, t = I >> 2, m = I & 3;
    mfma16<(I < 36 - NVG), ZERO>(acc[t][m], cat(s.wlo[PAR][t], s.whi[PAR][t]), s.af[AP][m]);
  };
  auto rd = [&](auto ic) __attribute__((always_inline)) {
    constexpr int I = decltype(ic)::value;
    if constexpr (I < 4) { if constexpr (RDA) s.af[AP ^ 1][I] = ldA(ab, I * 16 * ROWB); }
    else if constexpr (RDW) {
      if constexpr (((I - 4) & 1) == 0) s.wlo[PAR ^ 1][(I - 4) >> 1] = ldT(wb, 32 * ((I - 4) >> 1));
      else s.whi[PAR ^ 1][(I - 4) >> 1] = ldT(wb, 32 * ((I - 4) >> 1) + 4608);
    }
  };
  auto gap = [&](auto gc) __attribute__((always_inline)) {
    constexpr int G = decltype(gc)::value;
    mm(IC<2 * G>{});
    if constexpr (G == 0 && RDW) asm volatile("s_waitcnt vmcnt(%0)" :: "i"(VM) : "memory");
    if constexpr (G <= 10) rd(IC<2 * G>{});
    if constexpr (DMA) {
      if constexpr (G == 12) { LGKM0(); setm0(sl + 4096); }
      if constexpr (G >= 13 && G <= 16) dma<(2 * (G - 13) - 4) * 1024>(voff[2 * (G - 13)], gp);
      if constexpr (G == 17) dma<0>(voff[8], gp);
    }
    GAP();
    mm(IC<2 * G + 1>{});
    if constexpr (G <= 10) rd(IC<2 * G + 1>{});
    if constexpr (DMA) {
      if constexpr (G >= 13 && G <= 16) dma<(2 * (G - 13) + 1 - 4) * 1024>(voff[2 * (G - 13) + 1], gp);
      if constexpr (G == 16) setm0(sl + 8192);
    }
    if constexpr (G == 17) pf();
    GAP();
  };
  UNROLL(18, gap);
}
DI void read_w(GS& s, int par) {
#pragma unroll
  for (int t = 0; t < 9; ++t) { s.wlo[par][t] = ldT(s.wbase + par * SLOT, 32 * t); s.whi[par][t] = ldT(s.wbase + par * SLOT, 32 * t + 4608); }
}
DI void read_a(GS& s, int set, unsigned ab) {
#pragma unroll
  for (int m = 0; m < 4; ++m) s.af[set][m] = ldA(ab, m * 16 * ROWB);
}
struct Epi { __amdgpu_buffer_rsrc_t rs; unsigned base, dstride, ownlds; int own, w, lane; };
template <int U> DI void epi_unit(f4 (&acc)[9][4], const Epi& e) {
  const int G = e.lane >> 4, c = e.lane & 15, lr = c & 7, dh = c >> 3;
  constexpr int ta = U < 16 ? 2 * (U >> 2) : 8, tb = U < 16 ? ta + 1 : 8, ma = U < 16 ? (U & 3) : 2 * (U - 16), mb = U < 16 ? ma : ma + 1;
  asm volatile("" : "+a"(acc[ta][ma]), "+a"(acc[tb][mb]));
  const f4 a = acc[ta][ma], b = acc[tb][mb];
  auto r0 = __builtin_amdgcn_permlane16_swap(pk(a[0], a[1]), pk(b[0], b[1]), false, false);
  auto r1 = __builtin_amdgcn_permlane16_swap(pk(a[2], a[3]), pk(b[2], b[3]), false, false);
  const u4 v = {r0[0], r1[0], r0[1], r1[1]};
  const int d = U < 16 ? 2 * ma + dh : 2 * (ma + (G & 1)) + dh;
  const unsigned off = U < 16 ? e.w * 2304 + (U >> 2) * 512 + lr * 64 + (2 * (G & 1) + (G >> 1)) * 16 : e.w * 2304 + 2048 + lr * 32 + (G >> 1) * 16;
  __builtin_amdgcn_raw_buffer_store_b128(v, e.rs, d == e.own ? 0x7ffffff0u : e.base + d * e.dstride + off, 0, 16);
  if ((U < 16 ? ma : ma >> 1) == (U < 16 ? e.own >> 1 : e.own >> 2)) { if (d == e.own) *(LAS u4*)(size_t)(e.ownlds + off) = v; }
}
template <bool NEXT, int VM> DI void final2(GS& s, f4 (&acc)[9][4], const unsigned (&nvoff)[9], const char* nx1, unsigned ab, const Epi& e) {
  const unsigned sl = s.ring + SLOT, wb = s.wbase + SLOT;
  asm volatile("s_waitcnt vmcnt(%0)" :: "i"(VM) : "memory");
#pragma unroll
  for (int m = 0; m < 4; ++m) s.af[1][m] = ldA(ab, m * 16 * ROWB);
  auto rdw = [&](auto ic) __attribute__((always_inline)) { constexpr int t = decltype(ic)::value; s.wlo[1][t] = ldT(wb, 32 * t); s.whi[1][t] = ldT(wb, 32 * t + 4608); };
  rdw(IC<0>{}); rdw(IC<1>{});
  GAP();
  auto grp = [&](auto gc) __attribute__((always_inline)) {
    constexpr int G = decltype(gc)::value, t = G >> 1, st = G & 1;
    auto mm = [&](auto mc) __attribute__((always_inline)) { constexpr int m = decltype(mc)::value; mfma16<true, false>(acc[t][m], cat(s.wlo[st][t], s.whi[st][t]), s.af[st][m]); };
    mm(IC<0>{});
    if constexpr (st == 0 && t + 2 < 9) rdw(IC<t + 2>{});
    if constexpr (NEXT && G == 15) { LGKM0(); setm0(sl + 4096); }
    GAP();
    constexpr int U = G >= 5 ? G - 5 : -1;
    mm(IC<1>{});
    if constexpr (U >= 0 && U < 16 && (U >> 2) * 4 + 5 <= G) epi_unit<(U >= 0 && U < 16) ? U : 0>(acc, e);
    GAP();
    mm(IC<2>{});
    if constexpr (NEXT && G == 16) { dma<-4096>(nvoff[0], nx1); dma<-3072>(nvoff[1], nx1); dma<-2048>(nvoff[2], nx1); dma<-1024>(nvoff[3], nx1); }
    if constexpr (NEXT && G == 17) { dma<0>(nvoff[4], nx1); dma<1024>(nvoff[5], nx1); dma<2048>(nvoff[6], nx1); dma<3072>(nvoff[7], nx1); setm0(sl + 8192); }
    GAP();
    mm(IC<3>{});
    GAP();
  };
  UNROLL(18, grp);
  if constexpr (NEXT) dma<0>(nvoff[8], nx1);
  asm volatile("s_nop 7" ::: "memory");
  epi_unit<13>(acc, e); epi_unit<14>(acc, e); epi_unit<15>(acc, e); epi_unit<16>(acc, e); epi_unit<17>(acc, e);
}
template <int NS, int NEXT, int NPF, typename PF> DI void gemm_rows(GS& s, f4 (&acc)[9][4], const unsigned (&voff)[9], const char* gW, unsigned ab_e, unsigned ab_o,
                                              const unsigned (&nvoff)[9], const char* nx0, const char* nx1, const Epi& e, PF&& pf) {
  constexpr size_t KS = (size_t)32 * ROWB;
  auto no = [&]() __attribute__((always_inline)) {};
  step<0, 0, true, true, true, true, 9, 0>(s, acc, voff, gW + 3 * KS, ab_o, no);
  step<1, 1, false, true, true, true, 9, 0>(s, acc, voff, gW + 4 * KS, ab_e + 128, pf);
  step<0, 0, false, true, true, true, 9 + NPF, 0>(s, acc, voff, gW + 5 * KS, ab_o + 128, no);
  step<1, 1, false, true, true, true, 9 + NPF, 0>(s, acc, voff, gW + 6 * KS, ab_e + 256, no);
  for (int k = 4; k < NS - 4; k += 2) {
    step<0, 0, false, true, true, true, 9, 0>(s, acc, voff, gW + (size_t)(k + 3) * KS, ab_o + 128 * (k >> 1), no);
    step<1, 1, false, true, true, true, 9, 0>(s, acc, voff, gW + (size_t)(k + 4) * KS, ab_e + 128 * ((k + 2) >> 1), no);
  }
  step<0, 0, false, true, true, true, 9, 0>(s, acc, voff, gW + (size_t)(NS - 1) * KS, ab_o + 128 * ((NS - 4) >> 1), no);
  step<1, 1, false, true, true, NEXT != 0, 9, 0>(s, acc, nvoff, nx0, ab_e + 128 * ((NS - 2) >> 1), no);
  final2<NEXT != 0, NEXT != 0 ? 9 : 0>(s, acc, nvoff, nx1, ab_o + 128 * ((NS - 2) >> 1), e);
}

constexpr unsigned SPIN_CAP = 1u << 17;
DI void publish(u64* flag, u64 tag) {
  asm volatile("s_waitcnt vmcnt(0)" ::: "memory");
  BAR();
  if (threadIdx.x == 0) __hip_atomic_store((gu64*)flag, tag, __ATOMIC_RELAXED, __HIP_MEMORY_SCOPE_AGENT);
}
DI void await8(u64* flags8, u64 tag, unsigned* tmo, int w, int lane) {
  if (w == 0) {
    for (unsigned spins = 0;; ++spins) {
      const u64 v = __hip_atomic_load((gu64*)(flags8 + (lane & 7) * FLAG_STRIDE), __ATOMIC_RELAXED, __HIP_MEMORY_SCOPE_AGENT);
      if (__all(v == tag)) break;
      if (spins > SPIN_CAP) { if (lane == 0) __hip_atomic_store((gu32*)tmo, 1u, __ATOMIC_RELAXED, __HIP_MEMORY_SCOPE_AGENT); break; }
      __builtin_amdgcn_s_sleep(1);
    }
  }
  BAR();
}
template <int P0, int NP, bool F32, int NT> DI void gather_mod(char* ws, u64 tag, unsigned ldsb, unsigned* tmo, int tid) {
  constexpr int N = (NP + NT - 1) / NT;
  const __amdgpu_buffer_rsrc_t rM = __builtin_amdgcn_make_buffer_rsrc(ws + WS_MODG, 0, 6 * D * 8, 0x00020000);
  const u64 gtag = tag << 16;
  const unsigned thi = (unsigned)(gtag >> 32), tlo = (unsigned)gtag & 0xffff0000u;
  u4 gr[N];
  for (unsigned spins = 0;; ++spins) {
    bool ok = true;
#pragma unroll
    for (int i = 0; i < N; ++i) gr[i] = __builtin_amdgcn_raw_buffer_load_b128(rM, (P0 + (tid + NT * i < NP ? tid + NT * i : (tid & 63))) * 16, 0, 16);
#pragma unroll
    for (int i = 0; i < N; ++i) ok &= gr[i][1] == thi && gr[i][3] == thi && (gr[i][0] & 0xffff0000u) == tlo && (gr[i][2] & 0xffff0000u) == tlo;
    if (__all(ok)) break;
    if (spins > SPIN_CAP / 8) { if ((tid & 63) == 0) __hip_atomic_store((gu32*)tmo, 2u, __ATOMIC_RELAXED, __HIP_MEMORY_SCOPE_AGENT); break; }
    __builtin_amdgcn_s_sleep(8);
  }
#pragma unroll
  for (int i = 0; i < N; ++i)
    if (tid + NT * i < NP) {
      const int idx = P0 + tid + NT * i;
      if (F32 && idx < D) {
        f2 v = {bflo(gr[i][0]), bflo(gr[i][2])};
        if (idx >= D / 2) v = rb2(v + 1.f);
        *(LAS f2*)(size_t)(ldsb + LDS_X1 + 1024 + idx * 8) = v;
      } else *(LAS unsigned*)(size_t)(ldsb + LDS_MOD + idx * 4) = (gr[i][0] & 0xffffu) | (gr[i][2] << 16);
    }
}
DI u4 gated_residual(const u4 (&v)[8], const u4& res, const u4& g) {
  u4 o;
#pragma unroll
  for (int e = 0; e < 4; ++e) {
    f2 s = bf2(v[0][e]);
#pragma unroll
    for (int j = 1; j < 8; ++j) s += bf2(v[j][e]);
    const unsigned t = pk(bf2(pk(s)) * bf2(g[e]));
    o[e] = pk(bf2(res[e]) + bf2(t));
  }
  return o;
}

constexpr float QSCALE = 0.08838834764831845f * 1.4426950408889634f;
constexpr float THR = 40.0f;
struct QS {
  b4 wlo[2][8], whi[2][8];
  b8 hf[2][4];
  unsigned wb[4];
  unsigned hb[4];
  unsigned ring;
};
template <int PAR, bool ZERO, bool RD, bool DMA, int VM, typename PF>
DI void qstep(QS& s, f16v (&acc)[4][2], const unsigned (&voff)[8], const char* gp, unsigned hoff, PF&& pf) {
  const unsigned sl = s.ring + (PAR ^ 1) * QSLOT;
  auto rd = [&](auto ic) __attribute__((always_inline)) {
    constexpr int I = decltype(ic)::value;
    if constexpr (RD) {
      if constexpr (I < 4) {
        s.hf[PAR ^ 1][I] = ldA(s.hb[2 * (PAR ^ 1) + (I >> 1)] + hoff, (I & 1) * 32 * ROWB);
      } else {
        constexpr int J = I - 4, f = J >> 1, t = J & 1, ks = f >> 2, dt = f & 3;
        const b4 v = ldT(s.wb[dt], (PAR ^ 1) * QSLOT + 4096 * ks + 1024 * t);
        if constexpr (t == 0) s.wlo[PAR ^ 1][f] = v; else s.whi[PAR ^ 1][f] = v;
      }
    }
  };
  auto gap = [&](auto gc) __attribute__((always_inline)) {
    constexpr int G = decltype(gc)::value, ks = G >> 3, dt = (G >> 1) & 3, qb = G & 1, f = ks * 4 + dt;
    mfma32<false, ZERO && ks == 0>(acc[dt][qb], cat(s.wlo[PAR][f], s.whi[PAR][f]), s.hf[PAR][ks * 2 + qb]);
    if constexpr (G == 0 && RD) asm volatile("s_waitcnt vmcnt(%0)" :: "i"(VM) : "memory");
    if constexpr (G <= 6) { rd(IC<3 * G>{}); rd(IC<3 * G + 1>{}); if constexpr (3 * G + 2 < 20) rd(IC<3 * G + 2>{}); }
    if constexpr (DMA) {
      if constexpr (G == 9) { LGKM0(); setm0(sl + 4096); }
      if constexpr (G >= 10 && G <= 13) { dma<(2 * (G - 10) - 4) * 1024>(voff[2 * (G - 10)], gp); dma<(2 * (G - 10) + 1 - 4) * 1024>(voff[2 * (G - 10) + 1], gp); }
    }
    if constexpr (G == 15) pf();
    GAP();
  };
  UNROLL(16, gap);
}

struct AT {
  f16v S[2][2][2];
  f16v negm[2];
  f16v O[4][2];
  b8 q[2][8];
  b8 P[2][4];
  float m[2], l[2], rs0[2], rs1[2], fct[2], pm0[2], pm1[2];
  float one;
  unsigned kb_[2], vb_[2];
  unsigned voff[8];
  unsigned m0k, m0v;
  const char *gk, *gv;
  bool pend;
};
template <int MODE> DI void qk(f16v& s, const b8& k, const b8& q, const f16v& negm) {
  if constexpr (MODE == 0) asm volatile("v_mfma_f32_32x32x16_bf16 %0, %1, %2, 0" : "=&v"(s) : "v"(k), "a"(q));
  else if constexpr (MODE == 1) asm volatile("v_mfma_f32_32x32x16_bf16 %0, %1, %2, %3" : "=&v"(s) : "v"(k), "a"(q), "v"(negm));
  else asm volatile("v_mfma_f32_32x32x16_bf16 %0, %1, %2, %0" : "+v"(s) : "v"(k), "a"(q));
}
template <int XP, int KB, int E> DI void sm_exp(AT& a) { constexpr int qb = E >> 4, r = E & 15; a.S[XP][KB][qb][r] = __builtin_amdgcn_exp2f(a.S[XP][KB][qb][r]); }
template <int XP, int KB, int E> DI void sm_add(AT& a) {
  constexpr int qb = E >> 4, r = E & 15;
  if constexpr (r & 1) a.rs1[qb] = __builtin_fmaf(a.S[XP][KB][qb][r], a.one, a.rs1[qb]); else a.rs0[qb] += a.S[XP][KB][qb][r];
}
template <int XP, int C> DI void sm_pack(AT& a) {
  constexpr int kb = C >> 4, qb = (C >> 3) & 1, s = (C >> 2) & 1, p = C & 3;
  u4 w = __builtin_bit_cast(u4, a.P[qb][kb * 2 + s]);
  w[p] = pk(a.S[XP][kb][qb][8 * s + 2 * p], a.S[XP][kb][qb][8 * s + 2 * p + 1]);
  a.P[qb][kb * 2 + s] = __builtin_bit_cast(b8, w);
  if constexpr (p == 3) asm volatile("" : "+v"(a.P[qb][kb * 2 + s]));
}
template <int X, bool FIRST, bool DK, bool DV, typename PF> DI void phaseA(AT& a, int t, PF&& pf) {
  constexpr int XP = X ^ 1;
  const unsigned kslot = X * 16384;
  b8 kf[16];
  auto kread = [&](auto ic) __attribute__((always_inline)) { constexpr int I = decltype(ic)::value, ks = I >> 1, kb = I & 1; kf[I] = ldA(a.kb_[ks & 1], kslot + 8192 * kb + 512 * (ks >> 1)); };
  kread(IC<0>{}); kread(IC<1>{}); kread(IC<2>{}); kread(IC<3>{});
  const char* gk = a.gk + (size_t)(t + 1) * 16384; const char* gv = a.gv + (size_t)t * 16384;
  auto gap = [&](auto gc) __attribute__((always_inline)) {
    constexpr int G = decltype(gc)::value, ks = G >> 2, kb = (G >> 1) & 1, qb = G & 1, I = G >> 1;
    qk<(ks == 0) ? (FIRST ? 0 : 1) : 2>(a.S[X][kb][qb], kf[I], a.q[qb][ks], a.negm[qb]);
    if constexpr ((G & 1) == 0 && I + 4 < 16) kread(IC<I + 4>{});
    if constexpr (!FIRST && G == 8) asm volatile("" :: "v"(a.negm[0]), "v"(a.negm[1]));
    if constexpr (DK) { if constexpr (G == 0) setm0(a.m0k + (X ^ 1) * 16384); if constexpr ((G & 1) && G < 16) dma<((G >> 1) - 4) * 1024>(a.voff[G >> 1], gk); }
    if constexpr (DV) { if constexpr (G == 16) setm0(a.m0v + X * 16384); if constexpr ((G & 1) && G > 16) dma<(((G - 16) >> 1) - 4) * 1024>(a.voff[(G - 16) >> 1], gv); }
    if constexpr (!FIRST) {
      if constexpr (G < 16) { sm_exp<XP, 1, 2 * G>(a); sm_exp<XP, 1, 2 * G + 1>(a); sm_pack<XP, G>(a); }
      else { sm_pack<XP, G>(a); sm_add<XP, 1, 2 * (G - 16)>(a); sm_add<XP, 1, 2 * (G - 16) + 1>(a); }
    }
    GAP();
  };
  UNROLL(32, gap);
  pf();
  if constexpr (!FIRST) {
#pragma unroll
    for (int qb = 0; qb < 2; ++qb) { a.l[qb] += a.rs0[qb] + a.rs1[qb]; asm volatile("" : "+v"(a.l[qb])); }
  }
}
DI float swap32_max(float x) { auto r = __builtin_amdgcn_permlane32_swap(__float_as_uint(x), __float_as_uint(x), false, false); return fmaxx(__uint_as_float(r[0]), __uint_as_float(r[1])); }
template <int X, bool NOPV, bool ZERO> DI void phaseB(AT& a) {
  const unsigned vslot = (X ^ 1) * 16384;
  b4 vlo[16], vhi[16];
  auto vread = [&](auto ic) __attribute__((always_inline)) {
    constexpr int I = decltype(ic)::value, k4 = I >> 2, db = I & 3;
    vlo[I] = ldT(a.vb_[0], vslot + 2048 * (2 * k4) + 512 * db);
    vhi[I] = ldT(a.vb_[1], vslot + 2048 * (2 * k4 + 1) + 512 * db);
  };
  if constexpr (!NOPV) { vread(IC<0>{}); vread(IC<1>{}); vread(IC<2>{}); }
  float rmax[2];
  auto pv = [&](auto gc) __attribute__((always_inline)) {
    constexpr int G = decltype(gc)::value, I = G >> 1, k4 = I >> 2, db = I & 3, qb = G & 1;
    if constexpr (!NOPV) {
      mfma32<true, ZERO && k4 == 0>(a.O[db][qb], cat(vlo[I], vhi[I]), a.P[qb][k4]);
      if constexpr ((G & 1) == 0 && I + 3 < 16) vread(IC<I + 3>{});
    }
  };
  auto gap1 = [&](auto gc) __attribute__((always_inline)) {
    constexpr int G = decltype(gc)::value;
    pv(gc);
    if constexpr (G >= 4 && G < 12) {
      constexpr int i = G - 4, q = i >> 2, r = 4 * (i & 3);
      if constexpr ((i & 3) == 0) { a.pm0[q] = fmax3(a.S[X][0][q][0], a.S[X][0][q][1], a.S[X][1][q][0]); a.pm1[q] = fmax3(a.S[X][0][q][2], a.S[X][0][q][3], a.S[X][1][q][1]);
                                    a.pm0[q] = fmax3(a.pm0[q], a.S[X][1][q][2], a.S[X][1][q][3]); }
      else { a.pm0[q] = fmax3(a.pm0[q], a.S[X][0][q][r], a.S[X][0][q][r + 1]); a.pm1[q] = fmax3(a.pm1[q], a.S[X][0][q][r + 2], a.S[X][0][q][r + 3]);
             a.pm0[q] = fmax3(a.pm0[q], a.S[X][1][q][r], a.S[X][1][q][r + 1]); a.pm1[q] = fmax3(a.pm1[q], a.S[X][1][q][r + 2], a.S[X][1][q][r + 3]); }
    }
    if constexpr (G == 12 || G == 13) { constexpr int q = G - 12; rmax[q] = swap32_max(fmaxx(a.pm0[q], a.pm1[q])); }
    GAP();
  };
  UNROLL(14, gap1);
  if constexpr (NOPV) {
#pragma unroll
    for (int q = 0; q < 2; ++q) {
      a.m[q] = rmax[q]; a.l[q] = 0.f; a.fct[q] = 1.f;
#pragma unroll
      for (int kb = 0; kb < 2; ++kb)
#pragma unroll
        for (int e = 0; e < 16; ++e) a.S[X][kb][q][e] -= rmax[q];
#pragma unroll
      for (int e = 0; e < 16; ++e) a.negm[q][e] = -rmax[q];
      asm volatile("" : "+v"(a.negm[q]));
    }
  } else {
    const bool grew = (rmax[0] > THR) | (rmax[1] > THR);
    if (__builtin_expect(__any((int)grew), 0)) {
#pragma unroll
      for (int q = 0; q < 2; ++q) {
        const float dl = fmaxx(rmax[q], 0.f); a.m[q] += dl; a.fct[q] = __builtin_amdgcn_exp2f(-dl);
#pragma unroll
        for (int kb = 0; kb < 2; ++kb)
#pragma unroll
          for (int e = 0; e < 16; ++e) a.S[X][kb][q][e] -= dl;
#pragma unroll
        for (int e = 0; e < 16; ++e) a.negm[q][e] = -a.m[q];
        asm volatile("" : "+v"(a.negm[q]));
      }
      a.pend = true;
    }
  }
  a.rs0[0] = a.rs0[1] = a.rs1[0] = a.rs1[1] = 0.f;
  GAP();
  auto gap2 = [&](auto gc) __attribute__((always_inline)) {
    constexpr int G = decltype(gc)::value + 14;
    pv(IC<G>{});
    if constexpr (G >= 15) {
      constexpr int e0 = 2 * (G - 15);
      if constexpr (e0 < 32) { sm_exp<X, 0, e0>(a); sm_exp<X, 0, e0 + 1>(a); }
      if constexpr (G >= 16) { sm_add<X, 0, e0 - 2>(a); sm_add<X, 0, e0 - 1>(a); }
    }
    GAP();
  };
  UNROLL(18, gap2);
  asm volatile("" : "+v"(a.S[X][0][0]), "+v"(a.S[X][0][1]), "+v"(a.rs0[0]), "+v"(a.rs0[1]), "+v"(a.rs1[0]), "+v"(a.rs1[1]));
  if constexpr (!NOPV) {
    if (__builtin_expect(a.pend, 0)) {
      asm volatile("s_nop 7\n\ts_nop 7" ::: "memory");
#pragma unroll
      for (int db = 0; db < 4; ++db)
#pragma unroll
        for (int q = 0; q < 2; ++q) {
          asm volatile("" : "+a"(a.O[db][q]));
          f16v o = a.O[db][q];
#pragma unroll
          for (int e = 0; e < 16; ++e) o[e] *= a.fct[q];
          a.O[db][q] = o;
          asm volatile("" : "+a"(a.O[db][q]));
        }
      a.l[0] *= a.fct[0]; a.l[1] *= a.fct[1]; a.pend = false;
      asm volatile("s_nop 1" ::: "memory");
    }
  }
}

template <int X, bool FIRST, bool DK, bool DV, typename PF, typename PW> DI void fastA(AT& a, int t, PF&& pf, PW&& pw) {
  constexpr int XP = X ^ 1;
  const unsigned kslot = X * 16384;
  b8 kf[16];
  auto kread = [&](auto ic) __attribute__((always_inline)) { constexpr int I = decltype(ic)::value, ks = I >> 1, kb = I & 1; kf[I] = ldA(a.kb_[ks & 1], kslot + 8192 * kb + 512 * (ks >> 1)); };
  kread(IC<0>{}); kread(IC<1>{}); kread(IC<2>{}); kread(IC<3>{});
  const char* gk = a.gk + (size_t)(t + 1) * 16384; const char* gv = a.gv + (size_t)t * 16384;
  auto gap = [&](auto gc) __attribute__((always_inline)) {
    constexpr int G = decltype(gc)::value, ks = G >> 2, kb = (G >> 1) & 1, qb = G & 1, I = G >> 1;
    qk<(ks == 0) ? (FIRST ? 0 : 1) : 2>(a.S[X][kb][qb], kf[I], a.q[qb][ks], a.negm[qb]);
    if constexpr ((G & 1) == 0 && I + 4 < 16) kread(IC<I + 4>{});
    if constexpr (!FIRST && G == 8) asm volatile("" :: "v"(a.negm[0]), "v"(a.negm[1]));
    if constexpr (DK) { if constexpr (G == 0) setm0(a.m0k + (X ^ 1) * 16384); if constexpr ((G & 1) && G < 16) dma<((G >> 1) - 4) * 1024>(a.voff[G >> 1], gk); }
    if constexpr (DV) { if constexpr (G == 16) setm0(a.m0v + X * 16384); if constexpr ((G & 1) && G > 16) dma<(((G - 16) >> 1) - 4) * 1024>(a.voff[(G - 16) >> 1], gv); }
    if constexpr (G == 2 || G == 4 || G == 6) pw((G - 2) / 2);
    if constexpr (!FIRST) {
      sm_exp<XP, 1, G>(a);
      if constexpr (G >= 1) sm_add<XP, 1, G - 1>(a);
      if constexpr (G >= 3 && (G & 1)) sm_pack<XP, 16 + (G - 3) / 2>(a);
    }
    GAP();
  };
  UNROLL(32, gap);
  pf();
  if constexpr (!FIRST) {
    sm_add<XP, 1, 31>(a); sm_pack<XP, 31>(a);
#pragma unroll
    for (int qb = 0; qb < 2; ++qb) { a.l[qb] += a.rs0[qb] + a.rs1[qb]; asm volatile("" : "+v"(a.l[qb])); }
  }
}
constexpr int fb_e(int G) { return G < 2 ? 0 : G - 2 + (G > 5) + (G > 9); }
constexpr int dr_e(int n) { return n < 8 ? n : n < 16 ? n + 8 : n < 24 ? n - 8 : n; }
template <int X, int MODE> DI void fastB(AT& a) {
  constexpr bool ZERO = MODE == 1, DRAIN = MODE == 3;
  if constexpr (MODE == 2) {
#pragma unroll
    for (int q = 0; q < 2; ++q) {
      float p0 = fmax3(a.S[X][0][q][0], a.S[X][1][q][0], a.S[X][0][q][2]), p1 = fmax3(a.S[X][0][q][1], a.S[X][1][q][1], a.S[X][0][q][3]);
      p0 = fmaxx(p0, a.S[X][1][q][2]); p1 = fmaxx(p1, a.S[X][1][q][3]);
#pragma unroll
      for (int r = 4; r < 16; r += 2) { p0 = fmax3(p0, a.S[X][0][q][r], a.S[X][1][q][r]); p1 = fmax3(p1, a.S[X][0][q][r + 1], a.S[X][1][q][r + 1]); }
      const float mx = swap32_max(fmaxx(p0, p1));
      f2 m2 = {mx, mx}; asm volatile("" : "+v"(m2));
      a.l[q] = 0.f;
#pragma unroll
      for (int kb = 0; kb < 2; ++kb)
#pragma unroll
        for (int e = 0; e < 16; e += 2) { const f2 v = (f2){a.S[X][kb][q][e], a.S[X][kb][q][e + 1]} - m2; a.S[X][kb][q][e] = v[0]; a.S[X][kb][q][e + 1] = v[1]; }
#pragma unroll
      for (int e = 0; e < 16; ++e) a.negm[q][e] = -mx;
      asm volatile("" : "+v"(a.negm[q]));
    }
    a.rs0[0] = a.rs0[1] = a.rs1[0] = a.rs1[1] = 0.f;
    { auto f = [&](auto ic) __attribute__((always_inline)) { sm_exp<X, 0, decltype(ic)::value>(a); }; UNROLL(32, f); }
    { auto f = [&](auto ic) __attribute__((always_inline)) { sm_add<X, 0, decltype(ic)::value>(a); }; UNROLL(32, f); }
    { auto f = [&](auto ic) __attribute__((always_inline)) { sm_pack<X, decltype(ic)::value>(a); }; UNROLL(16, f); }
  } else {
    const unsigned vslot = (DRAIN ? X : X ^ 1) * 16384;
    b4 vlo[16], vhi[16];
    auto vread = [&](auto ic) __attribute__((always_inline)) {
      constexpr int I = decltype(ic)::value, k4 = I >> 2, db = I & 3;
      vlo[I] = ldT(a.vb_[0], vslot + 2048 * (2 * k4) + 512 * db);
      vhi[I] = ldT(a.vb_[1], vslot + 2048 * (2 * k4 + 1) + 512 * db);
    };
    vread(IC<0>{}); vread(IC<1>{}); vread(IC<2>{});
    if constexpr (!DRAIN) a.rs0[0] = a.rs0[1] = a.rs1[0] = a.rs1[1] = 0.f;
    auto gap = [&](auto gc) __attribute__((always_inline)) {
      constexpr int G = decltype(gc)::value, I = G >> 1, k4 = I >> 2, db = I & 3, qb = G & 1;
      mfma32<true, ZERO && k4 == 0>(a.O[db][qb], cat(vlo[I], vhi[I]), a.P[qb][k4]);
      if constexpr ((G & 1) == 0 && I + 3 < 16) vread(IC<I + 3>{});
      if constexpr (DRAIN) {
        if constexpr (G < 16) { sm_exp<X, 1, dr_e(2 * G)>(a); sm_exp<X, 1, dr_e(2 * G) + 1>(a); }
        if constexpr (G >= 2 && G < 18) sm_pack<X, 16 + dr_e(2 * (G - 2)) / 2>(a);
        if constexpr (G >= 16) { sm_add<X, 1, dr_e(2 * (G - 16))>(a); sm_add<X, 1, dr_e(2 * (G - 16)) + 1>(a); }
      } else {
        if constexpr (fb_e(G) < fb_e(G + 1)) sm_exp<X, 0, fb_e(G)>(a);
        if constexpr (fb_e(G) + 1 < fb_e(G + 1)) sm_exp<X, 0, fb_e(G) + 1>(a);
        if constexpr (G >= 3 && fb_e(G - 1) < fb_e(G)) sm_add<X, 0, G >= 3 ? fb_e(G - 1) : 0>(a);
        if constexpr (G >= 3 && fb_e(G - 1) + 1 < fb_e(G)) sm_add<X, 0, G >= 3 ? fb_e(G - 1) + 1 : 0>(a);
        if constexpr (G >= 16 && G <= 29) sm_pack<X, G >= 16 ? G - 16 : 0>(a);
        if constexpr (G == 31) sm_pack<X, 14>(a);
      }
      GAP();
    };
    UNROLL(32, gap);
    if constexpr (!DRAIN) {
      sm_add<X, 0, 31>(a);
      static_assert(fb_e(32) == 32 && fb_e(28) >= 28 && fb_e(30) >= 30, "schedule");
      sm_pack<X, 15>(a);
      asm volatile("" : "+v"(a.S[X][0][0]), "+v"(a.S[X][0][1]), "+v"(a.rs0[0]), "+v"(a.rs0[1]), "+v"(a.rs1[0]), "+v"(a.rs1[1]));
    } else {
#pragma unroll
      for (int qb = 0; qb < 2; ++qb) a.l[qb] += a.rs0[qb] + a.rs1[qb];
    }
  }
}

struct Params { const u16 *x, *c, *wmod, *wq, *kc, *vc, *wo, *wg, *wu, *wd; u16* out; char* ws; float one; unsigned gap; };

__global__ __launch_bounds__(256, 1) void k_main(Params p) {
  extern __shared__ __attribute__((aligned(16))) char lds[];
  const int tid = threadIdx.x, w = __builtin_amdgcn_readfirstlane(tid >> 6), lane = tid & 63;
  const int j = blockIdx.x & 7, c = blockIdx.x >> 3;
  const unsigned ldsb = (unsigned)(size_t)lds;
  const int hh = lane >> 5, r32 = lane & 31;
  u64* const flags = (u64*)(p.ws + WS_FLAG);
  unsigned* const tmo = (unsigned*)(p.ws + WS_TMO);
  STAMP(0);
  PST(0);
  QS qs;
  unsigned voffQ[8];
#pragma unroll
  for (int i = 0; i < 8; ++i) voffQ[i] = (4 * i + (lane >> 4)) * 8192 + 16 * ((lane & 15) ^ ((lane >> 4) << 2)) - (i - 4) * 1024 + 4096;
  const char* gq = (const char*)p.wq + (size_t)(4 * j + w) * 256 - 4096;
  qs.ring = __builtin_amdgcn_readfirstlane(ldsb + ABYTES + w * 2 * QSLOT);
  constexpr size_t KS_Q = (size_t)32 * 8192;
  auto issueq = [&](int slot, const char* gp) __attribute__((always_inline)) {
    setm0(qs.ring + slot * QSLOT + 4096); asm volatile("s_nop 0");
    dma<-4096>(voffQ[0], gp); dma<-3072>(voffQ[1], gp); dma<-2048>(voffQ[2], gp); dma<-1024>(voffQ[3], gp);
    dma<0>(voffQ[4], gp); dma<1024>(voffQ[5], gp); dma<2048>(voffQ[6], gp); dma<3072>(voffQ[7], gp);
  };
  {
    const unsigned dummy = __builtin_amdgcn_readfirstlane(ldsb + LDS_DUMMY);
    const char* a_;
    if (w == 0) a_ = (const char*)p.wq + (size_t)lane * 73728 + j * 1024;
    else if (w == 1) a_ = (const char*)p.wg + (size_t)lane * 82944 + j * 1152;
    else if (w == 2) a_ = (const char*)p.wu + (size_t)lane * 82944 + j * 1152;
    else {
      const int g8 = lane >> 3, l8 = lane & 7;
      a_ = g8 == 0 ? (const char*)p.wo + (size_t)512 * j * ROWB + l8 * 65536
         : g8 == 1 ? (const char*)p.wd + (size_t)576 * j * ROWB + l8 * 65536
         : g8 == 2 ? (const char*)p.kc + ((size_t)(2 * j + (l8 >> 2)) * SK + (SK - WIN)) * DH * 2 + (l8 & 3) * 32768
         : g8 == 3 ? (const char*)p.vc + ((size_t)(2 * j + (l8 >> 2)) * SK + (SK - WIN)) * DH * 2 + (l8 & 3) * 32768
         : g8 == 4 ? p.ws + WS_X + (size_t)c * 64 * SLICE + (size_t)j * SLICE + l8 * 8 * SLICE
         : g8 == 5 ? p.ws + WS_X + (size_t)c * 64 * SLICE + (size_t)j * 8 * SLICE + (size_t)j * SLICE + l8 * 0
         : g8 == 6 ? p.ws + WS_H2 + (size_t)c * ABYTES + (size_t)j * SLICE
                   : (const char*)p.out + (size_t)(c * 64 + j * 8) * ROWB;
    }
    touchp(a_, dummy);
  }
  const int mg = blockIdx.x % 54, ms = blockIdx.x / 54;
  u4 wm[5]; u16 cval[5];
#pragma unroll
  for (int i = 0; i < 5; ++i) {
    const int r = 144 * (ms & 3) + (i < 4 || tid < 128 ? (tid >> 3) + 32 * i : 0);
    wm[i] = *(const u4*)(p.wmod + (size_t)r * (6 * D) + mg * 64 + (tid & 7) * 8); cval[i] = p.c[r];
  }
  const u64 e0 = __hip_atomic_load((gu64*)(p.ws + WS_EPOCH), __ATOMIC_RELAXED, __HIP_MEMORY_SCOPE_AGENT);
  AT a;
  u64 tag;
  {
    f16v Qt[4][2];
    QS& s = qs;
    {
      const int rr = lane >> 3, sub = lane & 7;
      tag = ((u64)(unsigned)__builtin_amdgcn_readfirstlane((unsigned)(e0 >> 32)) << 32 | (u64)(unsigned)__builtin_amdgcn_readfirstlane((unsigned)e0)) + 1;
      const u64 gtag = tag << 16;
      PST(9);
      const __amdgpu_buffer_rsrc_t rP = __builtin_amdgcn_make_buffer_rsrc(p.ws + WS_MODP + (size_t)mg * 8192, 0, 8192, 0x00020000);
      if (blockIdx.x < 216) {
        float acc[8] = {};
#pragma unroll
        for (int i = 0; i < 5; ++i) {
          const float xc = bf2f(cval[i]);
          float sc = rbf(xc * __builtin_amdgcn_rcpf(1.f + __expf(-xc)));
          if (i == 4 && tid >= 128) sc = 0.f;
#pragma unroll
          for (int e = 0; e < 4; ++e) { acc[2 * e] += sc * bflo(wm[i][e]); acc[2 * e + 1] += sc * bfhi(wm[i][e]); }
        }
#pragma unroll
        for (int e = 0; e < 8; ++e) { float t = acc[e]; t += dpp<0x128>(t); acc[e] = swap32_add(swap16_add(t)); }
        const int k = (lane >> 3) & 3;
        const float v0 = k == 0 ? acc[0] : k == 1 ? acc[2] : k == 2 ? acc[4] : acc[6], v1 = k == 0 ? acc[1] : k == 1 ? acc[3] : k == 2 ? acc[5] : acc[7];
        if (lane < 32) __builtin_amdgcn_raw_buffer_store_b128((u4){__float_as_uint(v0), __float_as_uint(v1), (unsigned)tag, (unsigned)(tag >> 32)}, rP, (4 * ms + w) * 512 + (4 * (lane & 7) + k) * 16, 0, 16);
      }
      PST(10);
      if (blockIdx.x < 54 && w == 0) {
        const int pr = lane & 31, hf = lane >> 5;
        u4 gr[8];
        for (unsigned spins = 0;; ++spins) {
          bool ok = true;
#pragma unroll
          for (int k = 0; k < 8; ++k) gr[k] = __builtin_amdgcn_raw_buffer_load_b128(rP, (8 * hf + k) * 512 + pr * 16, 0, 16);
#pragma unroll
          for (int k = 0; k < 8; ++k) ok &= gr[k][2] == (unsigned)tag && gr[k][3] == (unsigned)(tag >> 32);
          if (__all(ok)) break;
          if (spins > SPIN_CAP) { if (lane == 0) __hip_atomic_store((gu32*)tmo, 3u, __ATOMIC_RELAXED, __HIP_MEMORY_SCOPE_AGENT); break; }
          __builtin_amdgcn_s_sleep(1);
        }
        f2 v = {0.f, 0.f};
#pragma unroll
        for (int k = 0; k < 8; ++k) v += (f2){__uint_as_float(gr[k][0]), __uint_as_float(gr[k][1])};
        v[0] = swap32_add(v[0]); v[1] = swap32_add(v[1]);
        const u64 g0 = gtag | f2bf(v[0]), g1 = gtag | f2bf(v[1]);
        const __amdgpu_buffer_rsrc_t rG = __builtin_amdgcn_make_buffer_rsrc(p.ws + WS_MODG, 0, 6 * D * 8, 0x00020000);
        if (lane < 32) __builtin_amdgcn_raw_buffer_store_b128((u4){(unsigned)g0, (unsigned)(g0 >> 32), (unsigned)g1, (unsigned)(g1 >> 32)}, rG, (mg * 32 + pr) * 16, 0, 16);
      }
      PST(11);
      u4 xv[2][9];
      GAP();
#pragma unroll
      for (int ps = 0; ps < 2; ++ps)
#pragma unroll
        for (int i = 0; i < 9; ++i) xv[ps][i] = *(const u4*)(p.x + (size_t)(c * 64 + 16 * w + 8 * ps + rr) * D + (sub + 8 * i) * 8);
      GAP();
      PST(1);
      STAMP(20);
      PST(2);
      f2 y[2][9][4];
      {
#pragma unroll
        for (int ps = 0; ps < 2; ++ps) {
          f2 a1 = {0.f, 0.f}, a2 = {0.f, 0.f};
#pragma unroll
          for (int i = 0; i < 9; ++i)
#pragma unroll
            for (int e = 0; e < 4; ++e) { y[ps][i][e] = bf2(xv[ps][i][e]); a1 += y[ps][i][e]; a2 = __builtin_elementwise_fma(y[ps][i][e], y[ps][i][e], a2); }
          float s1 = a1[0] + a1[1], s2 = a2[0] + a2[1];
          s1 = sum8(s1); s2 = sum8(s2);
          if (ps == 0) PST(3);
          const float mean = s1 * (1.f / D), var = __builtin_fmaxf(__builtin_fmaf(-mean, mean, s2 * (1.f / D)), 0.f);
          const float mr = rbf(mean), rs = __builtin_amdgcn_rcpf(rbf(__builtin_amdgcn_sqrtf(rbf(rbf(var) + 1e-5f))));
          f2 mr2 = {mr, mr}, rs2 = {rs, rs}; asm volatile("" : "+v"(mr2), "+v"(rs2));
#pragma unroll
          for (int i = 0; i < 9; ++i) {
            f2 t[4];
#pragma unroll
            for (int e = 0; e < 4; ++e) t[e] = y[ps][i][e] - mr2;
#pragma unroll
            for (int e = 0; e < 4; ++e) t[e] = rb2(t[e]);
#pragma unroll
            for (int e = 0; e < 4; ++e) t[e] = t[e] * rs2;
#pragma unroll
            for (int e = 0; e < 4; ++e) y[ps][i][e] = rb2(t[e]);
            asm volatile("" : "+v"(y[ps][i][0]), "+v"(y[ps][i][1]), "+v"(y[ps][i][2]), "+v"(y[ps][i][3]));
          }
        }
      }
      STAMP(21);
      PST(4);
      gather_mod<0, 1728, true, 256>(p.ws, tag, ldsb, tmo, tid);
      STAMP(22);
      PST(5);
      issueq(0, gq); issueq(1, gq + KS_Q);
      {
        const int li = tid < 144 ? tid : tid - 112;
        touch((const char*)p.wq + (size_t)512 * j * 2, (18 * c + (li >> 3)) * 8192 + (li & 7) * 128, __builtin_amdgcn_readfirstlane(ldsb + LDS_DUMMY));
      }
      asm volatile("s_waitcnt lgkmcnt(0)" ::: "memory");
      BAR();
      STAMP(23);
      PST(6);
      {
        const unsigned tb = ldsb + LDS_X1 + 1024 + sub * 32;
        f4 cst[2][4];
        auto ldc = [&](int i, f4 (&d)[4]) { d[0] = *(LAS f4*)(size_t)(tb + i * 256); d[1] = *(LAS f4*)(size_t)(tb + i * 256 + 16); d[2] = *(LAS f4*)(size_t)(tb + i * 256 + D * 4); d[3] = *(LAS f4*)(size_t)(tb + i * 256 + D * 4 + 16); };
        ldc(0, cst[0]);
        const unsigned ad0 = ldsb + (16 * w + rr) * ROWB + ((sub ^ ((rr >> 1) & 7)) * 16);
        const unsigned ad1 = ldsb + (16 * w + 8 + rr) * ROWB + ((sub ^ (((rr >> 1) + 4) & 7)) * 16);
#pragma unroll
        for (int i = 0; i < 9; ++i) {
          if (i + 1 < 9) ldc(i + 1, cst[(i + 1) & 1]);
          GAP();
          const f4 (&k)[4] = cst[i & 1];
          const f2 sh[4] = {{k[0][0], k[0][1]}, {k[0][2], k[0][3]}, {k[1][0], k[1][1]}, {k[1][2], k[1][3]}}, cc[4] = {{k[2][0], k[2][1]}, {k[2][2], k[2][3]}, {k[3][0], k[3][1]}, {k[3][2], k[3][3]}};
          f2 t[2][4]; u4 o[2];
#pragma unroll
          for (int ps = 0; ps < 2; ++ps)
#pragma unroll
            for (int e = 0; e < 4; ++e) t[ps][e] = y[ps][i][e] * cc[e];
#pragma unroll
          for (int ps = 0; ps < 2; ++ps)
#pragma unroll
            for (int e = 0; e < 4; ++e) t[ps][e] = rb2(t[ps][e]);
#pragma unroll
          for (int ps = 0; ps < 2; ++ps)
#pragma unroll
            for (int e = 0; e < 4; ++e) t[ps][e] = t[ps][e] + sh[e];
#pragma unroll
          for (int ps = 0; ps < 2; ++ps)
#pragma unroll
            for (int e = 0; e < 4; ++e) o[ps][e] = pk(t[ps][e]);
          *(LAS u4*)(size_t)(ad0 + i * 128) = o[0];
          *(LAS u4*)(size_t)(ad1 + i * 128) = o[1];
          GAP();
        }
      }
    }
    {
      const int blk = (lane >> 4) & 1, q = (lane & 15) >> 2, pp = lane & 3;
#pragma unroll
      for (int dt = 0; dt < 4; ++dt) s.wb[dt] = s.ring + (8 * hh + q) * 256 + 64 * (dt ^ q) + 32 * blk + 16 * (pp & 1) + 8 * (pp >> 1);
#pragma unroll
      for (int i = 0; i < 4; ++i) s.hb[i] = ldsb + r32 * ROWB + (((2 * i + hh) ^ ((r32 >> 1) & 7)) * 16);
    }
    asm volatile("s_waitcnt vmcnt(9) lgkmcnt(0)" ::: "memory");
    BAR();
    STAMP(1);
    PST(7);
#pragma unroll
    for (int f = 0; f < 8; ++f) { s.wlo[0][f] = ldT(s.wb[f & 3], 4096 * (f >> 2)); s.whi[0][f] = ldT(s.wb[f & 3], 4096 * (f >> 2) + 1024); }
#pragma unroll
    for (int i = 0; i < 4; ++i) s.hf[0][i] = ldA(s.hb[i >> 1], (i & 1) * 32 * ROWB);
    asm volatile("s_waitcnt lgkmcnt(0)" ::: "memory");
    issueq(0, gq + 2 * KS_Q);
    auto no = [&]() __attribute__((always_inline)) {};
    auto pfkv = [&](int h) __attribute__((always_inline)) {
      if (lane < 16) touch((const char*)(w < 2 ? p.kc : p.vc) + ((size_t)(2 * j + (w & 1)) * SK + (SK - WIN)) * DH * 2, (32 * c + 16 * h + lane) * 128, __builtin_amdgcn_readfirstlane(ldsb + LDS_DUMMY));
    };
    qstep<0, true, true, true, 9>(s, Qt, voffQ, gq + 3 * KS_Q, 0, no);
    qstep<1, false, true, true, 8>(s, Qt, voffQ, gq + 4 * KS_Q, 128, [&]() __attribute__((always_inline)) { pfkv(0); });
    qstep<0, false, true, true, 9>(s, Qt, voffQ, gq + 5 * KS_Q, 128, no);
    qstep<1, false, true, true, 9>(s, Qt, voffQ, gq + 6 * KS_Q, 256, [&]() __attribute__((always_inline)) { pfkv(1); });
    qstep<0, false, true, true, 9>(s, Qt, voffQ, gq + 7 * KS_Q, 256, no);
    qstep<1, false, true, true, 9>(s, Qt, voffQ, gq + 8 * KS_Q, 384, no);
    for (int n = 6; n < 14; n += 2) {
      qstep<0, false, true, true, 8>(s, Qt, voffQ, gq + (size_t)(n + 3) * KS_Q, 128 * ((n + 1) >> 1), no);
      qstep<1, false, true, true, 8>(s, Qt, voffQ, gq + (size_t)(n + 4) * KS_Q, 128 * ((n + 2) >> 1), no);
    }
    qstep<0, false, true, true, 8>(s, Qt, voffQ, gq + (size_t)17 * KS_Q, 128 * 7, no);
    qstep<1, false, true, false, 8>(s, Qt, voffQ, gq, 128 * 8, no);
    qstep<0, false, true, false, 0>(s, Qt, voffQ, gq, 128 * 8, no);
    qstep<1, false, false, false, 0>(s, Qt, voffQ, gq, 0, no);
    STAMP(2);
    PST(8);
    {
      const int kvl = w >> 1, half = w & 1;
      const unsigned kr = ldsb + kvl * 65536, vr = kr + 32768;
#pragma unroll
      for (int i = 0; i < 2; ++i) a.kb_[i] = kr + 2048 * (r32 >> 3) + 64 * (r32 & 7) + 16 * ((2 * i + hh) ^ ((r32 >> 2) & 3));
      const int blk = (lane >> 4) & 1, q = (lane & 15) >> 2, pp = lane & 3;
#pragma unroll
      for (int t = 0; t < 2; ++t) a.vb_[t] = vr + 64 * (4 * hh + q) + 16 * ((2 * blk + (pp >> 1)) ^ ((2 * t + hh) & 3)) + 8 * (pp & 1);
      const int st = lane >> 5, r7 = (lane >> 2) & 7, pos = lane & 3;
#pragma unroll
      for (int i = 0; i < 8; ++i) {
        const int rg = 4 * half + (i >> 1);
        a.voff[i] = rg * 2048 + r7 * 256 + (i & 1) * 128 + st * 64 + 16 * (pos ^ ((2 * (rg & 1) + (r7 >> 2)) & 3)) - (i - 4) * 1024 + 4096;
      }
      a.m0k = __builtin_amdgcn_readfirstlane(kr + 8192 * half + 4096); a.m0v = __builtin_amdgcn_readfirstlane(vr + 8192 * half + 4096);
      const size_t kvoff = ((size_t)(2 * j + kvl) * SK + (SK - WIN)) * DH * 2;
      a.gk = (const char*)p.kc + kvoff - 4096; a.gv = (const char*)p.vc + kvoff - 4096;
      a.pend = false; a.one = p.one;
    }
    asm volatile("s_waitcnt lgkmcnt(0)" ::: "memory");
    BAR();
    setm0(a.m0k); asm volatile("s_nop 0");
    dma<-4096>(a.voff[0], a.gk); dma<-3072>(a.voff[1], a.gk); dma<-2048>(a.voff[2], a.gk); dma<-1024>(a.voff[3], a.gk);
    dma<0>(a.voff[4], a.gk); dma<1024>(a.voff[5], a.gk); dma<2048>(a.voff[6], a.gk); dma<3072>(a.voff[7], a.gk);
    asm volatile("s_nop 7" ::: "memory");
#pragma unroll
    for (int dt = 0; dt < 4; ++dt)
#pragma unroll
      for (int qb = 0; qb < 2; ++qb) {
        asm volatile("" : "+v"(Qt[dt][qb]));
#pragma unroll
        for (int s2 = 0; s2 < 2; ++s2) {
          u4 w4;
#pragma unroll
          for (int e = 0; e < 4; ++e) w4[e] = pk((f2){Qt[dt][qb][8 * s2 + 2 * e], Qt[dt][qb][8 * s2 + 2 * e + 1]} * QSCALE);
          a.q[qb][2 * dt + s2] = __builtin_bit_cast(b8, w4);
          asm volatile("" : "+a"(a.q[qb][2 * dt + s2]));
        }
      }
  }
  STAMP(3);
  {
    auto tile_bar = [&]() __attribute__((always_inline)) { asm volatile("s_waitcnt vmcnt(0) lgkmcnt(0)" ::: "memory"); BAR(); };
    const char* pfp = lane >= 32 ? p.ws + WS_X + (size_t)c * 64 * SLICE + (size_t)j * SLICE + (18 * w + lane - 32) * 128
                    : w < 2 ? (const char*)(w == 0 ? p.wg : p.wu) + (size_t)576 * j * 2 + (size_t)(18 * c + lane / 9 - 3) * (DF * 2) + (lane % 9) * 128
                            : (const char*)p.wo + (size_t)512 * j * ROWB + (16 * c) * ROWB + (12 * (w - 2) + lane - 24) * 128;
    const int pfs = lane >= 32 ? 8 * SLICE : w < 2 ? 3 * DF * 2 : 24 * 128, pfn = w < 2 ? 27 : 12;
    auto pft = [&](int t) __attribute__((always_inline)) {
      const bool on = lane < pfn && t >= 1 && t <= 6;
      touchp(on ? pfp : (const char*)p.wo, __builtin_amdgcn_readfirstlane(ldsb + LDS_DUMMY));
      pfp += pfs;
    };
    const __amdgpu_buffer_rsrc_t rXw = __builtin_amdgcn_make_buffer_rsrc(p.ws + WS_X + (size_t)c * 64 * SLICE, 0, 64 * SLICE, 0x00020000);
    int pwq = w * 18;
    auto pw = [&](int) __attribute__((always_inline)) {
    };
    auto nopw = [&](int) __attribute__((always_inline)) {};
    auto tile_bar1 = [&]() __attribute__((always_inline)) { asm volatile("s_waitcnt vmcnt(1) lgkmcnt(0)" ::: "memory"); BAR(); };
    LAS unsigned* flag = (LAS unsigned*)(size_t)(ldsb + LDS_X1);
    if (tid == 0) *flag = 0u;
    auto k0 = [&]() __attribute__((always_inline)) {
      setm0(a.m0k); asm volatile("s_nop 0");
      dma<-4096>(a.voff[0], a.gk); dma<-3072>(a.voff[1], a.gk); dma<-2048>(a.voff[2], a.gk); dma<-1024>(a.voff[3], a.gk);
      dma<0>(a.voff[4], a.gk); dma<1024>(a.voff[5], a.gk); dma<2048>(a.voff[6], a.gk); dma<3072>(a.voff[7], a.gk);
    };
    tile_bar(); fastA<0, true, true, true>(a, 0, [&]() __attribute__((always_inline)) { pft(0); }, pw); asm volatile("s_nop 7\n\ts_nop 3" ::: "memory"); GAP(); fastB<0, 2>(a);
    tile_bar1(); fastA<1, false, true, true>(a, 1, [&]() __attribute__((always_inline)) { pft(1); }, pw); fastB<1, 1>(a);
    for (int t = 2; t < 6; t += 2) {
      tile_bar1(); fastA<0, false, true, true>(a, t, [&]() __attribute__((always_inline)) { pft(t); }, pw); fastB<0, 0>(a);
      tile_bar1(); fastA<1, false, true, true>(a, t + 1, [&]() __attribute__((always_inline)) { pft(t + 1); }, pw); fastB<1, 0>(a);
    }
    tile_bar1(); fastA<0, false, true, true>(a, 6, [&]() __attribute__((always_inline)) { pft(6); }, nopw); fastB<0, 0>(a);
    tile_bar1(); fastA<1, false, false, true>(a, 7, [&]() __attribute__((always_inline)) { pft(7); }, nopw); fastB<1, 0>(a);
    tile_bar1();
    fastB<1, 3>(a);
    {
      const bool bad = !(a.l[0] < 0x1p40f) || !(a.l[1] < 0x1p40f);
      if (__any((int)bad) && lane == 0) *flag = 1u;
      asm volatile("s_waitcnt lgkmcnt(0)" ::: "memory"); BAR();
      unsigned redo = __builtin_amdgcn_readfirstlane(*(volatile LAS unsigned*)flag); asm volatile("" : "+s"(redo));
      if (__builtin_expect(redo != 0u, 0)) {
        auto nopf = [&]() __attribute__((always_inline)) {};
        a.pend = false;
        auto upt = [&](const char* q) __attribute__((always_inline)) { const u64 v = (u64)q; return (const char*)((u64)(unsigned)__builtin_amdgcn_readfirstlane((unsigned)(v >> 32)) << 32 | (u64)(unsigned)__builtin_amdgcn_readfirstlane((unsigned)v)); };
        a.gk = upt(a.gk); a.gv = upt(a.gv); a.m0k = __builtin_amdgcn_readfirstlane(a.m0k); a.m0v = __builtin_amdgcn_readfirstlane(a.m0v);
        k0();
        tile_bar(); phaseA<0, true, true, true>(a, 0, nopf); asm volatile("s_nop 7\n\ts_nop 3" ::: "memory"); GAP(); phaseB<0, true, false>(a);
        tile_bar(); phaseA<1, false, true, true>(a, 1, nopf); phaseB<1, false, true>(a);
        for (int t = 2; t < 6; t += 2) {
          tile_bar(); phaseA<0, false, true, true>(a, t, nopf); phaseB<0, false, false>(a);
          tile_bar(); phaseA<1, false, true, true>(a, t + 1, nopf); phaseB<1, false, false>(a);
        }
        tile_bar(); phaseA<0, false, true, true>(a, 6, nopf); phaseB<0, false, false>(a);
        tile_bar(); phaseA<1, false, false, true>(a, 7, nopf); phaseB<1, false, false>(a);
        { auto f1 = [&](auto ic) __attribute__((always_inline)) { sm_exp<1, 1, decltype(ic)::value>(a); }; UNROLL(32, f1);
          auto f2_ = [&](auto ic) __attribute__((always_inline)) { sm_pack<1, decltype(ic)::value>(a); }; UNROLL(32, f2_);
          auto f3 = [&](auto ic) __attribute__((always_inline)) { sm_add<1, 1, decltype(ic)::value>(a); }; UNROLL(32, f3); }
#pragma unroll
        for (int qb = 0; qb < 2; ++qb) a.l[qb] += a.rs0[qb] + a.rs1[qb];
        tile_bar();
        const unsigned vslot = 16384;
        b4 vlo[16], vhi[16];
#pragma unroll
        for (int I = 0; I < 16; ++I) { vlo[I] = ldT(a.vb_[0], vslot + 2048 * (2 * (I >> 2)) + 512 * (I & 3)); vhi[I] = ldT(a.vb_[1], vslot + 2048 * (2 * (I >> 2) + 1) + 512 * (I & 3)); }
        asm volatile("s_nop 1" ::: "memory"); GAP();
#pragma unroll
        for (int G = 0; G < 32; ++G) { const int I = G >> 1; mfma32<true, false>(a.O[I & 3][G & 1], cat(vlo[I], vhi[I]), a.P[G & 1][I >> 2]); }
        asm volatile("s_waitcnt vmcnt(0) lgkmcnt(0)" ::: "memory"); BAR();
      }
    }
  }
  STAMP(4);
  int tid2 = threadIdx.x; asm volatile("" : "+v"(tid2));
  {
  const int tid = tid2, lane = tid2 & 63, hh = lane >> 5, r32 = lane & 31;
  GS s;
  unsigned voffB[9];
#pragma unroll
  for (int i = 0; i < 9; ++i) { const int idx = i * 64 + lane, imm = i < 8 ? (i - 4) * 1024 : 0; voffB[i] = (idx / 18) * ROWB + (idx % 18) * 16 - imm + 4096; }
  s.ring = __builtin_amdgcn_readfirstlane(ldsb + ABYTES + w * 2 * SLOT);
  const int g4 = lane >> 4, cl = lane & 15;
  s.wbase = s.ring + (4 * g4 + ((lane & 15) >> 2)) * 288 + 8 * (lane & 3);
  const unsigned sw = (cl >> 1) & 7;
  const unsigned ab_e = ldsb + cl * ROWB + ((g4 ^ sw) * 16), ab_o = ldsb + cl * ROWB + (((4 + g4) ^ sw) * 16);
  const __amdgpu_buffer_rsrc_t rX = __builtin_amdgcn_make_buffer_rsrc(p.ws + WS_X + (size_t)c * 64 * SLICE, 0, 64 * SLICE, 0x00020000);
  unsigned voffA[9];
#pragma unroll
  for (int i = 0; i < 9; ++i) { const int idx = i * 64 + lane, imm = i < 8 ? (i - 4) * 1024 : 0; voffA[i] = (idx / 18) * (DF * 2) + (idx % 18) * 16 - imm + 4096; }
  const char* gG = (const char*)p.wg + (size_t)(576 * j + 144 * w) * 2 - 4096;
  const char* gU = (const char*)p.wu + (size_t)(576 * j + 144 * w) * 2 - 4096;
  const char* gD = (const char*)p.wd + (size_t)(576 * j) * ROWB + 288 * w - 4096;
  f4 aG[9][4];
  {
    const char* gO = (const char*)p.wo + (size_t)(512 * j) * ROWB + 288 * w - 4096;
    issue_all(s, 0, voffB, gO); issue_all(s, 1, voffB, gO + (size_t)32 * ROWB);
    asm volatile("s_nop 7" ::: "memory");
#pragma unroll
    for (int qb = 0; qb < 2; ++qb) {
      const float il = __builtin_amdgcn_rcpf(swap32_add(a.l[qb]));
      const int row = 32 * qb + r32; const unsigned swr = (r32 >> 1) & 7;
#pragma unroll
      for (int db = 0; db < 4; ++db) {
        asm volatile("" : "+a"(a.O[db][qb]));
        const f16v o = a.O[db][qb];
#pragma unroll
        for (int u = 0; u < 2; ++u) {
          const u4 wv = {pk((f2){o[4 * u], o[4 * u + 1]} * il), pk((f2){o[4 * u + 2], o[4 * u + 3]} * il), pk((f2){o[4 * u + 8], o[4 * u + 9]} * il), pk((f2){o[4 * u + 10], o[4 * u + 11]} * il)};
          *(LAS u4*)(size_t)(ldsb + row * ROWB + (((4 * (4 * w + db) + 2 * u + hh) ^ swr) * 16)) = wv;
        }
      }
    }
    asm volatile("s_waitcnt vmcnt(9) lgkmcnt(0)" ::: "memory");
    BAR();
    STAMP(5);
    read_w(s, 0); read_a(s, 0, ab_e);
    asm volatile("s_waitcnt lgkmcnt(0)" ::: "memory");
    issue_all(s, 0, voffB, gO + (size_t)2 * 32 * ROWB);
    const Epi e1 = {rX, (unsigned)(j * SLICE), 8u * SLICE, ldsb + LDS_X1, j, w, lane};
    gemm_rows<16, 2, 0>(s, aG, voffB, gO, ab_e, ab_o, voffA, gG, gU, e1, [&]() __attribute__((always_inline)) {});
    STAMP(6);
  }
  {
    int zlr[3], zcc[3];
    const int t3 = tid < 192 ? tid : tid - 64;
    u4 xr[3], gv[3];
#pragma unroll
    for (int i = 0; i < 3; ++i) {
      zdecode(t3 + 192 * i, zlr[i], zcc[i]);
      xr[i] = *(const u4*)(p.x + (size_t)(c * 64 + j * 8 + zlr[i]) * D + zcc[i] * 8);
      gv[i] = *(LAS u4*)(size_t)(ldsb + LDS_MOD + (2 * D + zcc[i] * 8) * 2);
    }
    const int lrow = 2 * w + hh, grow = j * 8 + lrow;
    u2 cbv[3][2], sbv[3][2];
#pragma unroll
    for (int i = 0; i < 3; ++i) {
      const int pos = (i < 2 || r32 < 8) ? r32 + 32 * i : r32;
      const int q = pos ^ ((grow >> 1) & 7), k0 = 32 * (q >> 2) + 4 * (q & 3);
#pragma unroll
      for (int h = 0; h < 2; ++h) { cbv[i][h] = *(LAS u2*)(size_t)(ldsb + LDS_MOD + (4 * D + k0 + 16 * h) * 2); sbv[i][h] = *(LAS u2*)(size_t)(ldsb + LDS_MOD + (3 * D + k0 + 16 * h) * 2); }
    }
    asm volatile("s_waitcnt lgkmcnt(0)" ::: "memory");
    publish(flags + (0 * 256 + blockIdx.x) * FLAG_STRIDE, tag);
    STAMP(7);
    await8(flags + (0 * 256 + c * 8) * FLAG_STRIDE, tag, tmo, w, lane);
    STAMP(8);
    {
      u4 v[3][8];
#pragma unroll
      for (int i = 0; i < 3; ++i) {
#pragma unroll
        for (int jj = 0; jj < 7; ++jj) v[i][jj] = __builtin_amdgcn_raw_buffer_load_b128(rX, (j * 8 + jj + (jj >= j)) * SLICE + (t3 + 192 * i) * 16, 0, AUX_X);
      }
#pragma unroll
      for (int i = 0; i < 3; ++i) v[i][7] = *(LAS u4*)(size_t)(ldsb + LDS_X1 + (t3 + 192 * i) * 16);
#pragma unroll
      for (int i = 0; i < 3; ++i) {
        const u4 x1 = gated_residual(v[i], xr[i], gv[i]);
        if (tid < 192) {
          *(LAS u4*)(size_t)(ldsb + LDS_X1 + (tid + 192 * i) * 16) = x1;
          LAS f4* dst = (LAS f4*)(size_t)(ldsb + (zlr[i] * D + zcc[i] * 8) * 4);
          const f2 a0 = bf2(x1[0]), a1 = bf2(x1[1]), a2 = bf2(x1[2]), a3 = bf2(x1[3]);
          dst[0] = (f4){a0[0], a0[1], a1[0], a1[1]}; dst[1] = (f4){a2[0], a2[1], a3[0], a3[1]};
        }
      }
    }
    asm volatile("s_waitcnt lgkmcnt(0)" ::: "memory");
    BAR();
    STAMP(9);
    {
      f4 v[3][2];
      f2 sum = {0.f, 0.f};
#pragma unroll
      for (int i = 0; i < 3; ++i) {
        const bool on = (i < 2 || r32 < 8);
        const int pos = on ? r32 + 32 * i : r32;
        const int q = pos ^ ((grow >> 1) & 7), k0 = 32 * (q >> 2) + 4 * (q & 3);
#pragma unroll
        for (int h = 0; h < 2; ++h) {
          v[i][h] = *(LAS f4*)(size_t)(ldsb + (lrow * D + k0 + 16 * h) * 4);
          if (on) sum += (f2){v[i][h][0] + v[i][h][1], v[i][h][2] + v[i][h][3]};
        }
      }
      float sm = sum[0] + sum[1];
      sm = sum32(sm);
      const float mean = sm * (1.f / D);
      float q2 = 0.f;
#pragma unroll
      for (int i = 0; i < 3; ++i)
        if (i < 2 || r32 < 8) {
#pragma unroll
          for (int h = 0; h < 2; ++h)
#pragma unroll
            for (int e = 0; e < 4; ++e) { const float dl = v[i][h][e] - mean; q2 += dl * dl; }
        }
      q2 = sum32(q2);
      const float mr = rbf(mean), rs = __builtin_amdgcn_rcpf(rbf(__builtin_amdgcn_sqrtf(rbf(rbf(q2 * (1.f / D)) + 1e-5f))));
      const __amdgpu_buffer_rsrc_t rH = __builtin_amdgcn_make_buffer_rsrc(p.ws + WS_H2 + (size_t)c * ABYTES, 0, ABYTES, 0x00020000);
      u4 oh[3];
#pragma unroll
      for (int i = 0; i < 3; ++i) {
        u4& o = oh[i];
#pragma unroll
        for (int h = 0; h < 2; ++h)
#pragma unroll
          for (int e = 0; e < 2; ++e) {
            unsigned t = pk((f2){v[i][h][2 * e], v[i][h][2 * e + 1]} - mr);
            t = pk(bf2(t) * rs);
            t = pk(bf2(t) * bf2(pk(bf2(cbv[i][h][e]) + 1.f)));
            o[2 * h + e] = pk(bf2(t) + bf2(sbv[i][h][e]));
          }
        if (i < 2 || r32 < 8) __builtin_amdgcn_raw_buffer_store_b128(o, rH, grow * ROWB + (r32 + 32 * i) * 16, 0, 16);
      }
      publish(flags + (1 * 256 + blockIdx.x) * FLAG_STRIDE, tag);
      STAMP(10);
      if (w != 0) {
        const unsigned dummy = __builtin_amdgcn_readfirstlane(ldsb + LDS_DUMMY);
        int li = tid - 64; li = li < 162 ? li : li - 94;
        const int r = 18 * c + li / 9, l = li % 9;
        touch((const char*)p.wg + (size_t)576 * j * 2, r * (DF * 2) + l * 128, dummy);
        touch((const char*)p.wu + (size_t)576 * j * 2, r * (DF * 2) + l * 128, dummy);
      }
      await8(flags + (1 * 256 + c * 8) * FLAG_STRIDE, tag, tmo, w, lane);
      STAMP(11);
      u4 hv[16];
#pragma unroll
      for (int i = 0; i < 16; ++i) { const int y = i * 256 + tid < 4032 ? i * 256 + tid : tid; hv[i] = __builtin_amdgcn_raw_buffer_load_b128(rH, (y + (y >= j * 576 ? 576 : 0)) * 16, 0, AUX_H); }
#pragma unroll
      for (int i = 0; i < 3; ++i) if (i < 2 || r32 < 8) *(LAS u4*)(size_t)(ldsb + grow * ROWB + (r32 + 32 * i) * 16) = oh[i];
#pragma unroll
      for (int i = 0; i < 16; ++i) { const int y = i * 256 + tid; if (i < 15 || y < 4032) *(LAS u4*)(size_t)(ldsb + (y + (y >= j * 576 ? 576 : 0)) * 16) = hv[i]; }
    }
    asm volatile("s_waitcnt vmcnt(0) lgkmcnt(0)" ::: "memory");
    BAR();
  }
  STAMP(12);
  {
    constexpr size_t KS_GU = (size_t)32 * DF * 2, KS_D = (size_t)32 * ROWB;
    read_w(s, 0); read_a(s, 0, ab_e);
    asm volatile("s_waitcnt lgkmcnt(0)" ::: "memory");
    issue_all(s, 0, voffA, gG + KS_GU);
    f4 aU[9][4];
    auto no = [&]() __attribute__((always_inline)) {};
    auto pfd = [&](int h) __attribute__((always_inline)) {
      int li = 64 * h + 16 * w + lane; li = li < 162 ? li : 161;
      if (lane < 16) touch((const char*)p.wd + (size_t)576 * j * ROWB, (18 * c) * ROWB + li * 128, __builtin_amdgcn_readfirstlane(ldsb + LDS_DUMMY));
    };
    auto kpair = [&](auto zc, auto lastc, auto vmc, int k, auto&& pf) __attribute__((always_inline)) {
      constexpr bool Z = decltype(zc)::value, LAST = decltype(lastc)::value; constexpr int VM = decltype(vmc)::value;
      const char* u1 = gU + (size_t)(k + 1) * KS_GU; const char* g2 = gG + (size_t)(k + 2) * KS_GU;
      step<0, 0, Z, true, false, true, 9, 0>(s, aG, voffA, u1, 0, no);
      if constexpr (!LAST) {
        step<1, 0, Z, true, true, true, 9, NVU>(s, aU, voffA, g2, ab_o + 128 * (k >> 1), pf);
        step<0, 1, false, true, false, true, VM, 0>(s, aG, voffA, u1 + KS_GU, 0, no);
        step<1, 1, false, true, true, true, VM, NVU>(s, aU, voffA, g2 + KS_GU, ab_e + 128 * ((k + 2) >> 1), no);
      } else {
        step<1, 0, Z, true, true, true, 9, NVU>(s, aU, voffB, gD, ab_o + 128 * (k >> 1), no);
        step<0, 1, false, true, false, true, 9, 0>(s, aG, voffB, gD + KS_D, 0, no);
        step<1, 1, false, true, false, true, 9, NVU>(s, aU, voffB, gD + 2 * KS_D, 0, no);
      }
    };
    kpair(std::true_type{}, std::false_type{}, IC<9>{}, 0, no);
    for (int k = 2; k < 8; k += 2) kpair(std::false_type{}, std::false_type{}, IC<9>{}, k, no);
    for (int k = 8; k < 14; k += 2) kpair(std::false_type{}, std::false_type{}, IC<10>{}, k, [&]() __attribute__((always_inline)) { pfd((k - 8) >> 1); });
    kpair(std::false_type{}, std::false_type{}, IC<9>{}, 14, no);
    kpair(std::false_type{}, std::true_type{}, IC<9>{}, 16, no);
    STAMP(13);
    settle<0>(aG); settle<NVU>(aU);
    u2 act[9][4];
#pragma unroll
    for (int t = 0; t < 9; ++t)
#pragma unroll
      for (int m = 0; m < 4; ++m)
#pragma unroll
        for (int e = 0; e < 2; ++e) {
          const f2 x = {aG[t][m][2 * e], aG[t][m][2 * e + 1]}, u = {aU[t][m][2 * e], aU[t][m][2 * e + 1]};
          const f2 z = x * (x * x * -0.10294324f + -2.3022082f);
          const f2 dn = (f2){__builtin_amdgcn_exp2f(z[0]), __builtin_amdgcn_exp2f(z[1])} + 1.f;
          act[t][m][e] = pk((x * u) * (f2){__builtin_amdgcn_rcpf(dn[0]), __builtin_amdgcn_rcpf(dn[1])});
        }
    asm volatile("s_waitcnt lgkmcnt(0)" ::: "memory");
    BAR();
#pragma unroll
    for (int t = 0; t < 9; ++t) {
      const int tt = 9 * w + t, ks = tt >> 1, ah = tt & 1;
      const unsigned ad = ldsb + cl * ROWB + (((4 * ks + g4) ^ sw) * 16) + 8 * ah;
#pragma unroll
      for (int m = 0; m < 4; ++m) *(LAS u2*)(size_t)(ad + m * 16 * ROWB) = act[t][m];
    }
    asm volatile("s_waitcnt lgkmcnt(0)" ::: "memory");
    BAR();
    STAMP(14);
    read_a(s, 0, ab_e);
    const Epi e2 = {rX, (unsigned)(j * 8 * SLICE), (unsigned)SLICE, s.ring - w * 2304, j, w, lane};
    gemm_rows<18, 0, 0>(s, aG, voffB, gD, ab_e, ab_o, voffB, gD, gD, e2, [&]() __attribute__((always_inline)) {});
    STAMP(15);
  }
  {
    int tid3 = threadIdx.x; asm volatile("" : "+v"(tid3));
    const int tid = tid3, lane = tid3 & 63;
    asm volatile("s_waitcnt lgkmcnt(0)" ::: "memory");
    publish(flags + (2 * 256 + blockIdx.x) * FLAG_STRIDE, tag);
    STAMP(16);
    const int t3 = tid < 192 ? tid : tid - 64;
    int zlr[3], zcc[3];
    u4 gv[3], x1[3];
#pragma unroll
    for (int i = 0; i < 3; ++i) {
      zdecode(t3 + 192 * i, zlr[i], zcc[i]);
      gv[i] = *(LAS u4*)(size_t)(ldsb + LDS_MOD + (5 * D + zcc[i] * 8) * 2);
      x1[i] = *(LAS u4*)(size_t)(ldsb + LDS_X1 + (t3 + 192 * i) * 16);
    }
    if (blockIdx.x == 0 && w == 1) {
      for (unsigned spins = 0; spins <= SPIN_CAP; ++spins) {
        bool ok = true;
#pragma unroll
        for (int i = 0; i < 4; ++i) ok &= __hip_atomic_load((gu64*)(flags + (lane + 64 * i) * FLAG_STRIDE), __ATOMIC_RELAXED, __HIP_MEMORY_SCOPE_AGENT) == tag;
        if (__all(ok)) break;
        __builtin_amdgcn_s_sleep(4);
      }
      if (lane == 0) __hip_atomic_store((gu64*)(p.ws + WS_EPOCH), tag, __ATOMIC_RELAXED, __HIP_MEMORY_SCOPE_AGENT);
    }
    await8(flags + (2 * 256 + c * 8) * FLAG_STRIDE, tag, tmo, w, lane);
    STAMP(17);
    const __amdgpu_buffer_rsrc_t rO = __builtin_amdgcn_make_buffer_rsrc(p.out + (size_t)(c * 64 + j * 8) * D, 0, SLICE, 0x00020000); (void)rO;
    u4 v[3][8];
#pragma unroll
    for (int i = 0; i < 3; ++i)
#pragma unroll
      for (int jj = 0; jj < 7; ++jj) v[i][jj] = __builtin_amdgcn_raw_buffer_load_b128(rX, ((jj + (jj >= j)) * 8 + j) * SLICE + (t3 + 192 * i) * 16, 0, AUX_X);
#pragma unroll
    for (int i = 0; i < 3; ++i) { const int z = t3 + 192 * i, zw = z / 144; v[i][7] = *(LAS u4*)(size_t)(ldsb + ABYTES + zw * 2 * SLOT + (z - zw * 144) * 16); }
#pragma unroll
    for (int i = 0; i < 3; ++i) {
      const u4 o = gated_residual(v[i], x1[i], gv[i]);
      if (tid < 192) *(u4*)(p.out + (size_t)(c * 64 + j * 8 + zlr[i]) * D + zcc[i] * 8) = o;
    }
  }
  STAMP(18);
  asm volatile("s_waitcnt lgkmcnt(0)" ::: "memory");
  STAMP_FLUSH();
  }
}

extern "C" void kernel_launch(void* const* d_in, const int* in_sizes, int n_in, void* d_out, int out_size, void* d_ws, size_t ws_size, hipStream_t stream) {
  Params p;
  p.x = (const u16*)d_in[0]; p.kc = (const u16*)d_in[1]; p.vc = (const u16*)d_in[2]; p.wq = (const u16*)d_in[3]; p.wo = (const u16*)d_in[4];
  p.wg = (const u16*)d_in[6]; p.wu = (const u16*)d_in[7]; p.wd = (const u16*)d_in[8];
  p.c = (const u16*)d_in[9]; p.wmod = (const u16*)d_in[10];
  p.out = (u16*)d_out; p.ws = (char*)d_ws;
 p.one = 1.0f; p.gap = 0u;
  (void)hipFuncSetAttribute((const void*)k_main, hipFuncAttributeMaxDynamicSharedMemorySize, LDS_BYTES);
  k_main<<<256, 256, LDS_BYTES, stream>>>(p);
}
```

```cpp
#include <hip/hip_runtime.h>
#include <stdint.h>
#include <utility>
#include <type_traits>

typedef unsigned short u16;
typedef unsigned long long u64;
typedef __bf16 b2 __attribute__((ext_vector_type(2)));
typedef __bf16 b4 __attribute__((ext_vector_type(4)));
typedef __bf16 b8 __attribute__((ext_vector_type(8)));
typedef float f2 __attribute__((ext_vector_type(2)));
typedef float f4 __attribute__((ext_vector_type(4)));
typedef float f16v __attribute__((ext_vector_type(16)));
typedef unsigned u2 __attribute__((ext_vector_type(2)));
typedef unsigned u4 __attribute__((ext_vector_type(4)));
typedef __attribute__((address_space(1))) unsigned gu32;
typedef __attribute__((address_space(1))) unsigned long long gu64;
#define LAS __attribute__((address_space(3)))
#define DI __device__ __forceinline__
#define GAP() __builtin_amdgcn_sched_barrier(0)
#define LGKM0() __builtin_amdgcn_s_waitcnt(0xC07F)
#define BAR() do { asm volatile("" ::: "memory"); __builtin_amdgcn_s_barrier(); asm volatile("" ::: "memory"); } while (0)
template <int V> using IC = std::integral_constant<int, V>;
#define AUX_X 18
#define AUX_H 18
#ifndef NVU
#define NVU 8
#endif
#define PFD_K 14
#define UNROLL(N, F) [&]<int... I_>(std::integer_sequence<int, I_...>) __attribute__((always_inline)) { (F(IC<I_>{}), ...); }(std::make_integer_sequence<int, N>{})

constexpr int S = 2048, D = 576, DH = 128, SK = 2048, WIN = 512, DF = 4608;
constexpr int ROWB = D * 2;
constexpr int ABYTES = 64 * ROWB;
constexpr int SLICE = 8 * ROWB;
constexpr int SLOT = 9216;
constexpr int QSLOT = 8192;
constexpr int LDS_DUMMY = ABYTES + 8 * SLOT;
constexpr int LDS_X1 = LDS_DUMMY + 256;
constexpr int LDS_MOD = LDS_X1 + SLICE;
constexpr int LDS_BYTES = LDS_MOD + 6 * D * 2;
static_assert(LDS_BYTES <= 163840, "LDS");
constexpr size_t WS_EPOCH = 0;
constexpr size_t WS_TMO = 256;
constexpr size_t WS_MODP = 131072;
constexpr size_t WS_FLAG = 32768;
constexpr int FLAG_STRIDE = 8;
constexpr size_t WS_X = 1048576;
constexpr size_t WS_H2 = WS_X + (size_t)32 * 64 * SLICE;
constexpr size_t WS_DBG = 200u << 20;
#define STAMP(i)
#define STAMP_FLUSH()
#define PST(i)

DI float bf2f(u16 v) { return __uint_as_float(((unsigned)v) << 16); }
DI float bflo(unsigned w) { return __uint_as_float(w << 16); }
DI float bfhi(unsigned w) { return __uint_as_float(w & 0xffff0000u); }
DI f2 bf2(unsigned w) { return (f2){bflo(w), bfhi(w)}; }
DI unsigned pk(float lo, float hi) { f2 v = {lo, hi}; b2 b = __builtin_convertvector(v, b2); return __builtin_bit_cast(unsigned, b); }
DI unsigned pk(f2 v) { b2 b = __builtin_convertvector(v, b2); return __builtin_bit_cast(unsigned, b); }
DI float rbf(float f) { return bflo(pk(f, f)); }
DI f2 rb2(f2 v) { return (f2){__uint_as_float(pk(0.f, v[0])), __uint_as_float(pk(0.f, v[1]))}; }
template <int CTRL> DI float dpp(float v) { return __uint_as_float(__builtin_amdgcn_update_dpp(0u, __float_as_uint(v), CTRL, 0xf, 0xf, false)); }
DI float sum8(float v) { v += dpp<0xB1>(v); v += dpp<0x4E>(v); v += dpp<0x141>(v); return v; }
DI float swap16_add(float x) { auto r = __builtin_amdgcn_permlane16_swap(__float_as_uint(x), __float_as_uint(x), false, false); return __uint_as_float(r[0]) + __uint_as_float(r[1]); }
DI float swap32_add(float x) { auto r = __builtin_amdgcn_permlane32_swap(__float_as_uint(x), __float_as_uint(x), false, false); return __uint_as_float(r[0]) + __uint_as_float(r[1]); }
DI float sum32(float v) { v = sum8(v); v += dpp<0x140>(v); return swap16_add(v); }
DI u16 f2bf(float f) { return (u16)(pk(f, f) & 0xffffu); }
DI float fmaxx(float a, float b) { return __builtin_elementwise_maximum(a, b); }
DI float fmax3(float a, float b, float c) { return __builtin_elementwise_maximum(__builtin_elementwise_maximum(a, b), c); }

DI void zdecode(int z, int& lr, int& cc) {
  const int w = z / 144, y = z - w * 144;
  if (y < 128) { const int tp = y >> 5; lr = (y >> 2) & 7; cc = 18 * w + 4 * tp + (y & 3); }
  else { const int y2 = y - 128; lr = y2 >> 1; cc = 18 * w + 16 + (y2 & 1); }
}

template <int IMM> DI void dma(unsigned voff, const char* g) { asm volatile("global_load_lds_dwordx4 %0, %1 offset:%2" :: "v"(voff), "s"(g), "i"(IMM) : "memory"); }
DI void setm0(unsigned a) { asm volatile("s_mov_b32 m0, %0" :: "s"(a) : "memory"); }
DI void touch(const char* p, unsigned voff, unsigned ldsdummy) {
  asm volatile("s_mov_b32 m0, %0\n\ts_nop 0\n\tglobal_load_lds_dword %1, %2" :: "s"(ldsdummy), "v"(voff), "s"(p) : "memory");
}
DI void touchp(const char* a, unsigned ldsdummy) {
  asm volatile("s_mov_b32 m0, %0\n\ts_nop 0\n\tglobal_load_lds_dword %1, off" :: "s"(ldsdummy), "v"(a) : "memory");
}
DI b4 ldT(unsigned base, int off) { return __builtin_amdgcn_ds_read_tr16_b64_v4bf16((LAS b4*)(size_t)(base + off)); }
DI b8 ldA(unsigned base, int off) { return *(LAS b8*)(size_t)(base + off); }
DI b8 cat(const b4& lo, const b4& hi) { return __builtin_shufflevector(lo, hi, 0, 1, 2, 3, 4, 5, 6, 7); }
template <bool AG, bool ZERO> DI void mfma16(f4& c, const b8& a, const b8& b) {
  if constexpr (AG) {
    if constexpr (ZERO) asm volatile("v_mfma_f32_16x16x32_bf16 %0, %1, %2, 0" : "=a"(c) : "v"(a), "v"(b));
    else asm volatile("v_mfma_f32_16x16x32_bf16 %0, %1, %2, %0" : "+a"(c) : "v"(a), "v"(b));
  } else {
    if constexpr (ZERO) asm volatile("v_mfma_f32_16x16x32_bf16 %0, %1, %2, 0" : "=&v"(c) : "v"(a), "v"(b));
    else asm volatile("v_mfma_f32_16x16x32_bf16 %0, %1, %2, %0" : "+v"(c) : "v"(a), "v"(b));
  }
}
template <bool AG, bool ZERO> DI void mfma32(f16v& c, const b8& a, const b8& b) {
  if constexpr (AG) {
    if constexpr (ZERO) asm volatile("v_mfma_f32_32x32x16_bf16 %0, %1, %2, 0" : "=a"(c) : "v"(a), "v"(b));
    else asm volatile("v_mfma_f32_32x32x16_bf16 %0, %1, %2, %0" : "+a"(c) : "v"(a), "v"(b));
  } else {
    if constexpr (ZERO) asm volatile("v_mfma_f32_32x32x16_bf16 %0, %1, %2, 0" : "=&v"(c) : "v"(a), "v"(b));
    else asm volatile("v_mfma_f32_32x32x16_bf16 %0, %1, %2, %0" : "+v"(c) : "v"(a), "v"(b));
  }
}
template <int NVG> DI void settle(f4 (&acc)[9][4]) {
  asm volatile("s_nop 7\n\ts_nop 7" ::: "memory");
#pragma unroll
  for (int t = 0; t < 9; ++t)
#pragma unroll
    for (int m = 0; m < 4; ++m) { if (t * 4 + m < 36 - NVG) asm volatile("" : "+a"(acc[t][m])); else asm volatile("" : "+v"(acc[t][m])); }
}

struct GS {
  b4 wlo[2][9], whi[2][9];
  b8 af[2][4];
  unsigned wbase, ring;
};
DI void issue_all(const GS& s, int slot, const unsigned (&voff)[9], const char* gp) {
  const unsigned sl = s.ring + slot * SLOT;
  setm0(sl + 4096); asm volatile("s_nop 0");
  dma<-4096>(voff[0], gp); dma<-3072>(voff[1], gp); dma<-2048>(voff[2], gp); dma<-1024>(voff[3], gp);
  dma<0>(voff[4], gp); dma<1024>(voff[5], gp); dma<2048>(voff[6], gp); dma<3072>(voff[7], gp);
  setm0(sl + 8192); asm volatile("s_nop 0"); dma<0>(voff[8], gp);
}
template <int PAR, int AP, bool ZERO, bool RDW, bool RDA, bool DMA, int VM, int NVG, typename PF>
DI void step(GS& s, f4 (&acc)[9][4], const unsigned (&voff)[9], const char* gp, unsigned ab, PF&& pf) {
  const unsigned sl = s.ring + (PAR ^ 1) * SLOT;
  const unsigned wb = s.wbase + (PAR ^ 1) * SLOT;
  auto mm = [&](auto ic) __attribute__((always_inline)) {
    constexpr int I = decltype(ic)::value, t = I >> 2, m = I & 3;
    mfma16<(I < 36 - NVG), ZERO>(acc[t][m], cat(s.wlo[PAR][t], s.whi[PAR][t]), s.af[AP][m]);
  };
  auto rd = [&](auto ic) __attribute__((always_inline)) {
    constexpr int I = decltype(ic)::value;
    if constexpr (I < 4) { if constexpr (RDA) s.af[AP ^ 1][I] = ldA(ab, I * 16 * ROWB); }
    else if constexpr (RDW) {
      if constexpr (((I - 4) & 1) == 0) s.wlo[PAR ^ 1][(I - 4) >> 1] = ldT(wb, 32 * ((I - 4) >> 1));
      else s.whi[PAR ^ 1][(I - 4) >> 1] = ldT(wb, 32 * ((I - 4) >> 1) + 4608);
    }
  };
  auto gap = [&](auto gc) __attribute__((always_inline)) {
    constexpr int G = decltype(gc)::value;
    mm(IC<2 * G>{});
    if constexpr (G == 0 && RDW) asm volatile("s_waitcnt vmcnt(%0)" :: "i"(VM) : "memory");
    if constexpr (G <= 10) rd(IC<2 * G>{});
    if constexpr (DMA) {
      if constexpr (G == 12) { LGKM0(); setm0(sl + 4096); }
      if constexpr (G >= 13 && G <= 16) dma<(2 * (G - 13) - 4) * 1024>(voff[2 * (G - 13)], gp);
      if constexpr (G == 17) dma<0>(voff[8], gp);
    }
    GAP();
    mm(IC<2 * G + 1>{});
    if constexpr (G <= 10) rd(IC<2 * G + 1>{});
    if constexpr (DMA) {
      if constexpr (G >= 13 && G <= 16) dma<(2 * (G - 13) + 1 - 4) * 1024>(voff[2 * (G - 13) + 1], gp);
      if constexpr (G == 16) setm0(sl + 8192);
    }
    if constexpr (G == 17) pf();
    GAP();
  };
  UNROLL(18, gap);
}
DI void read_w(GS& s, int par) {
#pragma unroll
  for (int t = 0; t < 9; ++t) { s.wlo[par][t] = ldT(s.wbase + par * SLOT, 32 * t); s.whi[par][t] = ldT(s.wbase + par * SLOT, 32 * t + 4608); }
}
DI void read_a(GS& s, int set, unsigned ab) {
#pragma unroll
  for (int m = 0; m < 4; ++m) s.af[set][m] = ldA(ab, m * 16 * ROWB);
}
struct Epi { __amdgpu_buffer_rsrc_t rs; unsigned base, dstride, ownlds; int own, w, lane; };
template <int U> DI void epi_unit(f4 (&acc)[9][4], const Epi& e) {
  const int G = e.lane >> 4, c = e.lane & 15, lr = c & 7, dh = c >> 3;
  constexpr int ta = U < 16 ? 2 * (U >> 2) : 8, tb = U < 16 ? ta + 1 : 8, ma = U < 16 ? (U & 3) : 2 * (U - 16), mb = U < 16 ? ma : ma + 1;
  asm volatile("" : "+a"(acc[ta][ma]), "+a"(acc[tb][mb]));
  const f4 a = acc[ta][ma], b = acc[tb][mb];
  auto r0 = __builtin_amdgcn_permlane16_swap(pk(a[0], a[1]), pk(b[0], b[1]), false, false);
  auto r1 = __builtin_amdgcn_permlane16_swap(pk(a[2], a[3]), pk(b[2], b[3]), false, false);
  const u4 v = {r0[0], r1[0], r0[1], r1[1]};
  const int d = U < 16 ? 2 * ma + dh : 2 * (ma + (G & 1)) + dh;
  const unsigned off = U < 16 ? e.w * 2304 + (U >> 2) * 512 + lr * 64 + (2 * (G & 1) + (G >> 1)) * 16 : e.w * 2304 + 2048 + lr * 32 + (G >> 1) * 16;
  __builtin_amdgcn_raw_buffer_store_b128(v, e.rs, d == e.own ? 0x7ffffff0u : e.base + d * e.dstride + off, 0, 16);
  if ((U < 16 ? ma : ma >> 1) == (U < 16 ? e.own >> 1 : e.own >> 2)) { if (d == e.own) *(LAS u4*)(size_t)(e.ownlds + off) = v; }
}
template <bool NEXT, int VM> DI void final2(GS& s, f4 (&acc)[9][4], const unsigned (&nvoff)[9], const char* nx1, unsigned ab, const Epi& e) {
  const unsigned sl = s.ring + SLOT, wb = s.wbase + SLOT;
  asm volatile("s_waitcnt vmcnt(%0)" :: "i"(VM) : "memory");
#pragma unroll
  for (int m = 0; m < 4; ++m) s.af[1][m] = ldA(ab, m * 16 * ROWB);
  auto rdw = [&](auto ic) __attribute__((always_inline)) { constexpr int t = decltype(ic)::value; s.wlo[1][t] = ldT(wb, 32 * t); s.whi[1][t] = ldT(wb, 32 * t + 4608); };
  rdw(IC<0>{}); rdw(IC<1>{});
  GAP();
  auto grp = [&](auto gc) __attribute__((always_inline)) {
    constexpr int G = decltype(gc)::value, t = G >> 1, st = G & 1;
    auto mm = [&](auto mc) __attribute__((always_inline)) { constexpr int m = decltype(mc)::value; mfma16<true, false>(acc[t][m], cat(s.wlo[st][t], s.whi[st][t]), s.af[st][m]); };
    mm(IC<0>{});
    if constexpr (st == 0 && t + 2 < 9) rdw(IC<t + 2>{});
    if constexpr (NEXT && G == 15) { LGKM0(); setm0(sl + 4096); }
    GAP();
    constexpr int U = G >= 5 ? G - 5 : -1;
    mm(IC<1>{});
    if constexpr (U >= 0 && U < 16 && (U >> 2) * 4 + 5 <= G) epi_unit<(U >= 0 && U < 16) ? U : 0>(acc, e);
    GAP();
    mm(IC<2>{});
    if constexpr (NEXT && G == 16) { dma<-4096>(nvoff[0], nx1); dma<-3072>(nvoff[1], nx1); dma<-2048>(nvoff[2], nx1); dma<-1024>(nvoff[3], nx1); }
    if constexpr (NEXT && G == 17) { dma<0>(nvoff[4], nx1); dma<1024>(nvoff[5], nx1); dma<2048>(nvoff[6], nx1); dma<3072>(nvoff[7], nx1); setm0(sl + 8192); }
    GAP();
    mm(IC<3>{});
    GAP();
  };
  UNROLL(18, grp);
  if constexpr (NEXT) dma<0>(nvoff[8], nx1);
  asm volatile("s_nop 7" ::: "memory");
  epi_unit<13>(acc, e); epi_unit<14>(acc, e); epi_unit<15>(acc, e); epi_unit<16>(acc, e); epi_unit<17>(acc, e);
}
template <int NS, int NEXT, int NPF, typename PF> DI void gemm_rows(GS& s, f4 (&acc)[9][4], const unsigned (&voff)[9], const char* gW, unsigned ab_e, unsigned ab_o,
                                              const unsigned (&nvoff)[9], const char* nx0, const char* nx1, const Epi& e, PF&& pf) {
  constexpr size_t KS = (size_t)32 * ROWB;
  auto no = [&]() __attribute__((always_inline)) {};
  step<0, 0, true, true, true, true, 9, 0>(s, acc, voff, gW + 3 * KS, ab_o, no);
  step<1, 1, false, true, true, true, 9, 0>(s, acc, voff, gW + 4 * KS, ab_e + 128, pf);
  step<0, 0, false, true, true, true, 9 + NPF, 0>(s, acc, voff, gW + 5 * KS, ab_o + 128, no);
  step<1, 1, false, true, true, true, 9 + NPF, 0>(s, acc, voff, gW + 6 * KS, ab_e + 256, no);
  for (int k = 4; k < NS - 4; k += 2) {
    step<0, 0, false, true, true, true, 9, 0>(s, acc, voff, gW + (size_t)(k + 3) * KS, ab_o + 128 * (k >> 1), no);
    step<1, 1, false, true, true, true, 9, 0>(s, acc, voff, gW + (size_t)(k + 4) * KS, ab_e + 128 * ((k + 2) >> 1), no);
  }
  step<0, 0, false, true, true, true, 9, 0>(s, acc, voff, gW + (size_t)(NS - 1) * KS, ab_o + 128 * ((NS - 4) >> 1), no);
  step<1, 1, false, true, true, NEXT != 0, 9, 0>(s, acc, nvoff, nx0, ab_e + 128 * ((NS - 2) >> 1), no);
  final2<NEXT != 0, NEXT != 0 ? 9 : 0>(s, acc, nvoff, nx1, ab_o + 128 * ((NS - 2) >> 1), e);
}

constexpr unsigned SPIN_CAP = 1u << 17;
DI void publish(u64* flag, u64 tag) {
  asm volatile("s_waitcnt vmcnt(0)" ::: "memory");
  BAR();
  if (threadIdx.x == 0) __hip_atomic_store((gu64*)flag, tag, __ATOMIC_RELAXED, __HIP_MEMORY_SCOPE_AGENT);
}
DI void await8(u64* flags8, u64 tag, unsigned* tmo, int w, int lane) {
  if (w == 0) {
    for (unsigned spins = 0;; ++spins) {
      const u64 v = __hip_atomic_load((gu64*)(flags8 + (lane & 7) * FLAG_STRIDE), __ATOMIC_RELAXED, __HIP_MEMORY_SCOPE_AGENT);
      if (__all(v == tag)) break;
      if (spins > SPIN_CAP) { if (lane == 0) __hip_atomic_store((gu32*)tmo, 1u, __ATOMIC_RELAXED, __HIP_MEMORY_SCOPE_AGENT); break; }
      __builtin_amdgcn_s_sleep(1);
    }
  }
  BAR();
}
template <int NP, int NT> struct ModGather {
  static constexpr int N = (NP + NT - 1) / NT;
  u4 gr[N][2];
  __amdgpu_buffer_rsrc_t rM;
  DI void load(int tid) {
#pragma unroll
    for (int i = 0; i < N; ++i) {
      const int idx = tid + NT * i < NP ? tid + NT * i : (tid & 63), off = (idx >> 5) * 1024 + (idx & 31) * 16;
      gr[i][0] = __builtin_amdgcn_raw_buffer_load_b128(rM, off, 0, 16); gr[i][1] = __builtin_amdgcn_raw_buffer_load_b128(rM, off + 512, 0, 16);
    }
  }
  DI void issue(char* ws, int tid) { rM = __builtin_amdgcn_make_buffer_rsrc(ws + WS_MODP, 0, 54 * 1024, 0x00020000); load(tid); }
  DI void finish(u64 tag, unsigned ldsb, unsigned* tmo, int tid) {
    const unsigned thi = (unsigned)(tag >> 32), tlo = (unsigned)tag;
    for (unsigned spins = 0;; ++spins) {
      bool ok = true;
#pragma unroll
      for (int i = 0; i < N; ++i) ok &= gr[i][0][2] == tlo && gr[i][0][3] == thi && gr[i][1][2] == tlo && gr[i][1][3] == thi;
      if (__all(ok)) break;
      if (spins > SPIN_CAP / 8) { if ((tid & 63) == 0) __hip_atomic_store((gu32*)tmo, 2u, __ATOMIC_RELAXED, __HIP_MEMORY_SCOPE_AGENT); break; }
      __builtin_amdgcn_s_sleep(4);
      load(tid);
    }
#pragma unroll
    for (int i = 0; i < N; ++i)
      if (tid + NT * i < NP) {
        const int idx = tid + NT * i;
        const f2 m = (f2){__uint_as_float(gr[i][0][0]), __uint_as_float(gr[i][0][1])} + (f2){__uint_as_float(gr[i][1][0]), __uint_as_float(gr[i][1][1])};
        if (idx < D) {
          f2 v = rb2(m);
          if (idx >= D / 2) v = rb2(v + 1.f);
          *(LAS f2*)(size_t)(ldsb + LDS_X1 + 1024 + idx * 8) = v;
        } else *(LAS unsigned*)(size_t)(ldsb + LDS_MOD + idx * 4) = pk(m);
      }
  }
};
DI u4 gated_residual(const u4 (&v)[8], const u4& res, const u4& g) {
  u4 o;
#pragma unroll
  for (int e = 0; e < 4; ++e) {
    f2 s = bf2(v[0][e]);
#pragma unroll
    for (int j = 1; j < 8; ++j) s += bf2(v[j][e]);
    const unsigned t = pk(bf2(pk(s)) * bf2(g[e]));
    o[e] = pk(bf2(res[e]) + bf2(t));
  }
  return o;
}

constexpr float QSCALE = 0.08838834764831845f * 1.4426950408889634f;
constexpr float THR = 40.0f;
struct QS {
  b4 wlo[2][8], whi[2][8];
  b8 hf[2][4];
  unsigned wb[4];
  unsigned hb[4];
  unsigned ring;
};
template <int PAR, bool ZERO, bool RD, bool DMA, int VM, typename PF>
DI void qstep(QS& s, f16v (&acc)[4][2], const unsigned (&voff)[8], const char* gp, unsigned hoff, PF&& pf) {
  const unsigned sl = s.ring + (PAR ^ 1) * QSLOT;
  auto rd = [&](auto ic) __attribute__((always_inline)) {
    constexpr int I = decltype(ic)::value;
    if constexpr (RD) {
      if constexpr (I < 4) {
        s.hf[PAR ^ 1][I] = ldA(s.hb[2 * (PAR ^ 1) + (I >> 1)] + hoff, (I & 1) * 32 * ROWB);
      } else {
        constexpr int J = I - 4, f = J >> 1, t = J & 1, ks = f >> 2, dt = f & 3;
        const b4 v = ldT(s.wb[dt], (PAR ^ 1) * QSLOT + 4096 * ks + 1024 * t);
        if constexpr (t == 0) s.wlo[PAR ^ 1][f] = v; else s.whi[PAR ^ 1][f] = v;
      }
    }
  };
  auto gap = [&](auto gc) __attribute__((always_inline)) {
    constexpr int G = decltype(gc)::value, ks = G >> 3, dt = (G >> 1) & 3, qb = G & 1, f = ks * 4 + dt;
    mfma32<false, ZERO && ks == 0>(acc[dt][qb], cat(s.wlo[PAR][f], s.whi[PAR][f]), s.hf[PAR][ks * 2 + qb]);
    if constexpr (G == 0 && RD) asm volatile("s_waitcnt vmcnt(%0)" :: "i"(VM) : "memory");
    if constexpr (G <= 6) { rd(IC<3 * G>{}); rd(IC<3 * G + 1>{}); if constexpr (3 * G + 2 < 20) rd(IC<3 * G + 2>{}); }
    if constexpr (DMA) {
      if constexpr (G == 9) { LGKM0(); setm0(sl + 4096); }
      if constexpr (G >= 10 && G <= 13) { dma<(2 * (G - 10) - 4) * 1024>(voff[2 * (G - 10)], gp); dma<(2 * (G - 10) + 1 - 4) * 1024>(voff[2 * (G - 10) + 1], gp); }
    }
    if constexpr (G == 15) pf();
    GAP();
  };
  UNROLL(16, gap);
}

struct AT {
  f16v S[2][2][2];
  f16v negm[2];
  f16v O[4][2];
  b8 q[2][8];
  b8 P[2][4];
  float m[2], l[2], rs0[2], rs1[2], fct[2], pm0[2], pm1[2];
  float one;
  unsigned kb_[2], vb_[2];
  unsigned voff[8];
  unsigned m0k, m0v;
  const char *gk, *gv;
  bool pend;
};
template <int MODE> DI void qk(f16v& s, const b8& k, const b8& q, const f16v& negm) {
  if constexpr (MODE == 0) asm volatile("v_mfma_f32_32x32x16_bf16 %0, %1, %2, 0" : "=&v"(s) : "v"(k), "a"(q));
  else if constexpr (MODE == 1) asm volatile("v_mfma_f32_32x32x16_bf16 %0, %1, %2, %3" : "=&v"(s) : "v"(k), "a"(q), "v"(negm));
  else asm volatile("v_mfma_f32_32x32x16_bf16 %0, %1, %2, %0" : "+v"(s) : "v"(k), "a"(q));
}
template <int XP, int KB, int E> DI void sm_exp(AT& a) { constexpr int qb = E >> 4, r = E & 15; a.S[XP][KB][qb][r] = __builtin_amdgcn_exp2f(a.S[XP][KB][qb][r]); }
template <int XP, int KB, int E> DI void sm_add(AT& a) {
  constexpr int qb = E >> 4, r = E & 15;
  if constexpr (r & 1) a.rs1[qb] = __builtin_fmaf(a.S[XP][KB][qb][r], a.one, a.rs1[qb]); else a.rs0[qb] += a.S[XP][KB][qb][r];
}
template <int XP, int C> DI void sm_pack(AT& a) {
  constexpr int kb = C >> 4, qb = (C >> 3) & 1, s = (C >> 2) & 1, p = C & 3;
  u4 w = __builtin_bit_cast(u4, a.P[qb][kb * 2 + s]);
  w[p] = pk(a.S[XP][kb][qb][8 * s + 2 * p], a.S[XP][kb][qb][8 * s + 2 * p + 1]);
  a.P[qb][kb * 2 + s] = __builtin_bit_cast(b8, w);
  if constexpr (p == 3) asm volatile("" : "+v"(a.P[qb][kb * 2 + s]));
}
template <int X, bool FIRST, bool DK, bool DV, typename PF> DI void phaseA(AT& a, int t, PF&& pf) {
  constexpr int XP = X ^ 1;
  const unsigned kslot = X * 16384;
  b8 kf[16];
  auto kread = [&](auto ic) __attribute__((always_inline)) { constexpr int I = decltype(ic)::value, ks = I >> 1, kb = I & 1; kf[I] = ldA(a.kb_[ks & 1], kslot + 8192 * kb + 512 * (ks >> 1)); };
  kread(IC<0>{}); kread(IC<1>{}); kread(IC<2>{}); kread(IC<3>{});
  const char* gk = a.gk + (size_t)(t + 1) * 16384; const char* gv = a.gv + (size_t)t * 16384;
  auto gap = [&](auto gc) __attribute__((always_inline)) {
    constexpr int G = decltype(gc)::value, ks = G >> 2, kb = (G >> 1) & 1, qb = G & 1, I = G >> 1;
    qk<(ks == 0) ? (FIRST ? 0 : 1) : 2>(a.S[X][kb][qb], kf[I], a.q[qb][ks], a.negm[qb]);
    if constexpr ((G & 1) == 0 && I + 4 < 16) kread(IC<I + 4>{});
    if constexpr (!FIRST && G == 8) asm volatile("" :: "v"(a.negm[0]), "v"(a.negm[1]));
    if constexpr (DK) { if constexpr (G == 0) setm0(a.m0k + (X ^ 1) * 16384); if constexpr ((G & 1) && G < 16) dma<((G >> 1) - 4) * 1024>(a.voff[G >> 1], gk); }
    if constexpr (DV) { if constexpr (G == 16) setm0(a.m0v + X * 16384); if constexpr ((G & 1) && G > 16) dma<(((G - 16) >> 1) - 4) * 1024>(a.voff[(G - 16) >> 1], gv); }
    if constexpr (!FIRST) {
      if constexpr (G < 16) { sm_exp<XP, 1, 2 * G>(a); sm_exp<XP, 1, 2 * G + 1>(a); sm_pack<XP, G>(a); }
      else { sm_pack<XP, G>(a); sm_add<XP, 1, 2 * (G - 16)>(a); sm_add<XP, 1, 2 * (G - 16) + 1>(a); }
    }
    GAP();
  };
  UNROLL(32, gap);
  pf();
  if constexpr (!FIRST) {
#pragma unroll
    for (int qb = 0; qb < 2; ++qb) { a.l[qb] += a.rs0[qb] + a.rs1[qb]; asm volatile("" : "+v"(a.l[qb])); }
  }
}
DI float swap32_max(float x) { auto r = __builtin_amdgcn_permlane32_swap(__float_as_uint(x), __float_as_uint(x), false, false); return fmaxx(__uint_as_float(r[0]), __uint_as_float(r[1])); }
template <int X, bool NOPV, bool ZERO> DI void phaseB(AT& a) {
  const unsigned vslot = (X ^ 1) * 16384;
  b4 vlo[16], vhi[16];
  auto vread = [&](auto ic) __attribute__((always_inline)) {
    constexpr int I = decltype(ic)::value, k4 = I >> 2, db = I & 3;
    vlo[I] = ldT(a.vb_[0], vslot + 2048 * (2 * k4) + 512 * db);
    vhi[I] = ldT(a.vb_[1], vslot + 2048 * (2 * k4 + 1) + 512 * db);
  };
  if constexpr (!NOPV) { vread(IC<0>{}); vread(IC<1>{}); vread(IC<2>{}); }
  float rmax[2];
  auto pv = [&](auto gc) __attribute__((always_inline)) {
    constexpr int G = decltype(gc)::value, I = G >> 1, k4 = I >> 2, db = I & 3, qb = G & 1;
    if constexpr (!NOPV) {
      mfma32<true, ZERO && k4 == 0>(a.O[db][qb], cat(vlo[I], vhi[I]), a.P[qb][k4]);
      if constexpr ((G & 1) == 0 && I + 3 < 16) vread(IC<I + 3>{});
    }
  };
  auto gap1 = [&](auto gc) __attribute__((always_inline)) {
    constexpr int G = decltype(gc)::value;
    pv(gc);
    if constexpr (G >= 4 && G < 12) {
      constexpr int i = G - 4, q = i >> 2, r = 4 * (i & 3);
      if constexpr ((i & 3) == 0) { a.pm0[q] = fmax3(a.S[X][0][q][0], a.S[X][0][q][1], a.S[X][1][q][0]); a.pm1[q] = fmax3(a.S[X][0][q][2], a.S[X][0][q][3], a.S[X][1][q][1]);
                                    a.pm0[q] = fmax3(a.pm0[q], a.S[X][1][q][2], a.S[X][1][q][3]); }
      else { a.pm0[q] = fmax3(a.pm0[q], a.S[X][0][q][r], a.S[X][0][q][r + 1]); a.pm1[q] = fmax3(a.pm1[q], a.S[X][0][q][r + 2], a.S[X][0][q][r + 3]);
             a.pm0[q] = fmax3(a.pm0[q], a.S[X][1][q][r], a.S[X][1][q][r + 1]); a.pm1[q] = fmax3(a.pm1[q], a.S[X][1][q][r + 2], a.S[X][1][q][r + 3]); }
    }
    if constexpr (G == 12 || G == 13) { constexpr int q = G - 12; rmax[q] = swap32_max(fmaxx(a.pm0[q], a.pm1[q])); }
    GAP();
  };
  UNROLL(14, gap1);
  if constexpr (NOPV) {
#pragma unroll
    for (int q = 0; q < 2; ++q) {
      a.m[q] = rmax[q]; a.l[q] = 0.f; a.fct[q] = 1.f;
#pragma unroll
      for (int kb = 0; kb < 2; ++kb)
#pragma unroll
        for (int e = 0; e < 16; ++e) a.S[X][kb][q][e] -= rmax[q];
#pragma unroll
      for (int e = 0; e < 16; ++e) a.negm[q][e] = -rmax[q];
      asm volatile("" : "+v"(a.negm[q]));
    }
  } else {
    const bool grew = (rmax[0] > THR) | (rmax[1] > THR);
    if (__builtin_expect(__any((int)grew), 0)) {
#pragma unroll
      for (int q = 0; q < 2; ++q) {
        const float dl = fmaxx(rmax[q], 0.f); a.m[q] += dl; a.fct[q] = __builtin_amdgcn_exp2f(-dl);
#pragma unroll
        for (int kb = 0; kb < 2; ++kb)
#pragma unroll
          for (int e = 0; e < 16; ++e) a.S[X][kb][q][e] -= dl;
#pragma unroll
        for (int e = 0; e < 16; ++e) a.negm[q][e] = -a.m[q];
        asm volatile("" : "+v"(a.negm[q]));
      }
      a.pend = true;
    }
  }
  a.rs0[0] = a.rs0[1] = a.rs1[0] = a.rs1[1] = 0.f;
  GAP();
  auto gap2 = [&](auto gc) __attribute__((always_inline)) {
    constexpr int G = decltype(gc)::value + 14;
    pv(IC<G>{});
    if constexpr (G >= 15) {
      constexpr int e0 = 2 * (G - 15);
      if constexpr (e0 < 32) { sm_exp<X, 0, e0>(a); sm_exp<X, 0, e0 + 1>(a); }
      if constexpr (G >= 16) { sm_add<X, 0, e0 - 2>(a); sm_add<X, 0, e0 - 1>(a); }
    }
    GAP();
  };
  UNROLL(18, gap2);
  asm volatile("" : "+v"(a.S[X][0][0]), "+v"(a.S[X][0][1]), "+v"(a.rs0[0]), "+v"(a.rs0[1]), "+v"(a.rs1[0]), "+v"(a.rs1[1]));
  if constexpr (!NOPV) {
    if (__builtin_expect(a.pend, 0)) {
      asm volatile("s_nop 7\n\ts_nop 7" ::: "memory");
#pragma unroll
      for (int db = 0; db < 4; ++db)
#pragma unroll
        for (int q = 0; q < 2; ++q) {
          asm volatile("" : "+a"(a.O[db][q]));
          f16v o = a.O[db][q];
#pragma unroll
          for (int e = 0; e < 16; ++e) o[e] *= a.fct[q];
          a.O[db][q] = o;
          asm volatile("" : "+a"(a.O[db][q]));
        }
      a.l[0] *= a.fct[0]; a.l[1] *= a.fct[1]; a.pend = false;
      asm volatile("s_nop 1" ::: "memory");
    }
  }
}

template <int X, bool FIRST, bool DK, bool DV, typename PF, typename PW> DI void fastA(AT& a, int t, PF&& pf, PW&& pw) {
  constexpr int XP = X ^ 1;
  const unsigned kslot = X * 16384;
  b8 kf[16];
  auto kread = [&](auto ic) __attribute__((always_inline)) { constexpr int I = decltype(ic)::value, ks = I >> 1, kb = I & 1; kf[I] = ldA(a.kb_[ks & 1], kslot + 8192 * kb + 512 * (ks >> 1)); };
  kread(IC<0>{}); kread(IC<1>{}); kread(IC<2>{}); kread(IC<3>{});
  const char* gk = a.gk + (size_t)(t + 1) * 16384; const char* gv = a.gv + (size_t)t * 16384;
  auto gap = [&](auto gc) __attribute__((always_inline)) {
    constexpr int G = decltype(gc)::value, ks = G >> 2, kb = (G >> 1) & 1, qb = G & 1, I = G >> 1;
    qk<(ks == 0) ? (FIRST ? 0 : 1) : 2>(a.S[X][kb][qb], kf[I], a.q[qb][ks], a.negm[qb]);
    if constexpr ((G & 1) == 0 && I + 4 < 16) kread(IC<I + 4>{});
    if constexpr (!FIRST && G == 8) asm volatile("" :: "v"(a.negm[0]), "v"(a.negm[1]));
    if constexpr (DK) { if constexpr (G == 0) setm0(a.m0k + (X ^ 1) * 16384); if constexpr ((G & 1) && G < 16) dma<((G >> 1) - 4) * 1024>(a.voff[G >> 1], gk); }
    if constexpr (DV) { if constexpr (G == 16) setm0(a.m0v + X * 16384); if constexpr ((G & 1) && G > 16) dma<(((G - 16) >> 1) - 4) * 1024>(a.voff[(G - 16) >> 1], gv); }
    if constexpr (G == 2 || G == 4 || G == 6) pw((G - 2) / 2);
    if constexpr (!FIRST) {
      sm_exp<XP, 1, G>(a);
      if constexpr (G >= 1) sm_add<XP, 1, G - 1>(a);
      if constexpr (G >= 3 && (G & 1)) sm_pack<XP, 16 + (G - 3) / 2>(a);
    }
    GAP();
  };
  UNROLL(32, gap);
  pf();
  if constexpr (!FIRST) {
    sm_add<XP, 1, 31>(a); sm_pack<XP, 31>(a);
#pragma unroll
    for (int qb = 0; qb < 2; ++qb) { a.l[qb] += a.rs0[qb] + a.rs1[qb]; asm volatile("" : "+v"(a.l[qb])); }
  }
}
constexpr int fb_e(int G) { return G < 2 ? 0 : G - 2 + (G > 5) + (G > 9); }
constexpr int dr_e(int n) { return n < 8 ? n : n < 16 ? n + 8 : n < 24 ? n - 8 : n; }
template <int X, int MODE> DI void fastB(AT& a) {
  constexpr bool ZERO = MODE == 1, DRAIN = MODE == 3;
  if constexpr (MODE == 2) {
#pragma unroll
    for (int q = 0; q < 2; ++q) {
      float p0 = fmax3(a.S[X][0][q][0], a.S[X][1][q][0], a.S[X][0][q][2]), p1 = fmax3(a.S[X][0][q][1], a.S[X][1][q][1], a.S[X][0][q][3]);
      p0 = fmaxx(p0, a.S[X][1][q][2]); p1 = fmaxx(p1, a.S[X][1][q][3]);
#pragma unroll
      for (int r = 4; r < 16; r += 2) { p0 = fmax3(p0, a.S[X][0][q][r], a.S[X][1][q][r]); p1 = fmax3(p1, a.S[X][0][q][r + 1], a.S[X][1][q][r + 1]); }
      const float mx = swap32_max(fmaxx(p0, p1));
      f2 m2 = {mx, mx}; asm volatile("" : "+v"(m2));
      a.l[q] = 0.f;
#pragma unroll
      for (int kb = 0; kb < 2; ++kb)
#pragma unroll
        for (int e = 0; e < 16; e += 2) { const f2 v = (f2){a.S[X][kb][q][e], a.S[X][kb][q][e + 1]} - m2; a.S[X][kb][q][e] = v[0]; a.S[X][kb][q][e + 1] = v[1]; }
#pragma unroll
      for (int e = 0; e < 16; ++e) a.negm[q][e] = -mx;
      asm volatile("" : "+v"(a.negm[q]));
    }
    a.rs0[0] = a.rs0[1] = a.rs1[0] = a.rs1[1] = 0.f;
    { auto f = [&](auto ic) __attribute__((always_inline)) { sm_exp<X, 0, decltype(ic)::value>(a); }; UNROLL(32, f); }
    { auto f = [&](auto ic) __attribute__((always_inline)) { sm_add<X, 0, decltype(ic)::value>(a); }; UNROLL(32, f); }
    { auto f = [&](auto ic) __attribute__((always_inline)) { sm_pack<X, decltype(ic)::value>(a); }; UNROLL(16, f); }
  } else {
    const unsigned vslot = (DRAIN ? X : X ^ 1) * 16384;
    b4 vlo[16], vhi[16];
    auto vread = [&](auto ic) __attribute__((always_inline)) {
      constexpr int I = decltype(ic)::value, k4 = I >> 2, db = I & 3;
      vlo[I] = ldT(a.vb_[0], vslot + 2048 * (2 * k4) + 512 * db);
      vhi[I] = ldT(a.vb_[1], vslot + 2048 * (2 * k4 + 1) + 512 * db);
    };
    vread(IC<0>{}); vread(IC<1>{}); vread(IC<2>{});
    if constexpr (!DRAIN) a.rs0[0] = a.rs0[1] = a.rs1[0] = a.rs1[1] = 0.f;
    auto gap = [&](auto gc) __attribute__((always_inline)) {
      constexpr int G = decltype(gc)::value, I = G >> 1, k4 = I >> 2, db = I & 3, qb = G & 1;
      mfma32<true, ZERO && k4 == 0>(a.O[db][qb], cat(vlo[I], vhi[I]), a.P[qb][k4]);
      if constexpr ((G & 1) == 0 && I + 3 < 16) vread(IC<I + 3>{});
      if constexpr (DRAIN) {
        if constexpr (G < 16) { sm_exp<X, 1, dr_e(2 * G)>(a); sm_exp<X, 1, dr_e(2 * G) + 1>(a); }
        if constexpr (G >= 2 && G < 18) sm_pack<X, 16 + dr_e(2 * (G - 2)) / 2>(a);
        if constexpr (G >= 16) { sm_add<X, 1, dr_e(2 * (G - 16))>(a); sm_add<X, 1, dr_e(2 * (G - 16)) + 1>(a); }
      } else {
        if constexpr (fb_e(G) < fb_e(G + 1)) sm_exp<X, 0, fb_e(G)>(a);
        if constexpr (fb_e(G) + 1 < fb_e(G + 1)) sm_exp<X, 0, fb_e(G) + 1>(a);
        if constexpr (G >= 3 && fb_e(G - 1) < fb_e(G)) sm_add<X, 0, G >= 3 ? fb_e(G - 1) : 0>(a);
        if constexpr (G >= 3 && fb_e(G - 1) + 1 < fb_e(G)) sm_add<X, 0, G >= 3 ? fb_e(G - 1) + 1 : 0>(a);
        if constexpr (G >= 16 && G <= 29) sm_pack<X, G >= 16 ? G - 16 : 0>(a);
        if constexpr (G == 31) sm_pack<X, 14>(a);
      }
      GAP();
    };
    UNROLL(32, gap);
    if constexpr (!DRAIN) {
      sm_add<X, 0, 31>(a);
      static_assert(fb_e(32) == 32 && fb_e(28) >= 28 && fb_e(30) >= 30, "schedule");
      sm_pack<X, 15>(a);
      asm volatile("" : "+v"(a.S[X][0][0]), "+v"(a.S[X][0][1]), "+v"(a.rs0[0]), "+v"(a.rs0[1]), "+v"(a.rs1[0]), "+v"(a.rs1[1]));
    } else {
#pragma unroll
      for (int qb = 0; qb < 2; ++qb) a.l[qb] += a.rs0[qb] + a.rs1[qb];
    }
  }
}

struct Params { const u16 *x, *c, *wmod, *wq, *kc, *vc, *wo, *wg, *wu, *wd; u16* out; char* ws; float one; unsigned gap; };

__global__ __launch_bounds__(256, 1) void k_main(Params p) {
  extern __shared__ __attribute__((aligned(16))) char lds[];
  const int tid = threadIdx.x, w = __builtin_amdgcn_readfirstlane(tid >> 6), lane = tid & 63;
  const int j = blockIdx.x & 7, c = blockIdx.x >> 3;
  const unsigned ldsb = (unsigned)(size_t)lds;
  const int hh = lane >> 5, r32 = lane & 31;
  u64* const flags = (u64*)(p.ws + WS_FLAG);
  unsigned* const tmo = (unsigned*)(p.ws + WS_TMO);
  STAMP(0);
  PST(0);
  QS qs;
  unsigned voffQ[8];
#pragma unroll
  for (int i = 0; i < 8; ++i) voffQ[i] = (4 * i + (lane >> 4)) * 8192 + 16 * ((lane & 15) ^ ((lane >> 4) << 2)) - (i - 4) * 1024 + 4096;
  const char* gq = (const char*)p.wq + (size_t)(4 * j + w) * 256 - 4096;
  qs.ring = __builtin_amdgcn_readfirstlane(ldsb + ABYTES + w * 2 * QSLOT);
  constexpr size_t KS_Q = (size_t)32 * 8192;
  auto issueq = [&](int slot, const char* gp) __attribute__((always_inline)) {
    setm0(qs.ring + slot * QSLOT + 4096); asm volatile("s_nop 0");
    dma<-4096>(voffQ[0], gp); dma<-3072>(voffQ[1], gp); dma<-2048>(voffQ[2], gp); dma<-1024>(voffQ[3], gp);
    dma<0>(voffQ[4], gp); dma<1024>(voffQ[5], gp); dma<2048>(voffQ[6], gp); dma<3072>(voffQ[7], gp);
  };
  const int mg = blockIdx.x % 54, ms = (blockIdx.x / 54) & 1;
  u4 wm[9]; u16 cval[9];
#pragma unroll
  for (int i = 0; i < 9; ++i) {
    const int r = 288 * ms + (tid >> 3) + 32 * i;
    wm[i] = *(const u4*)(p.wmod + (size_t)r * (6 * D) + mg * 64 + (tid & 7) * 8); cval[i] = p.c[r];
  }
  const u64 e0 = __hip_atomic_load((gu64*)(p.ws + WS_EPOCH), __ATOMIC_RELAXED, __HIP_MEMORY_SCOPE_AGENT);
  AT a;
  u64 tag;
  {
    f16v Qt[4][2];
    QS& s = qs;
    {
      const int rr = lane >> 3, sub = lane & 7;
      tag = ((u64)(unsigned)__builtin_amdgcn_readfirstlane((unsigned)(e0 >> 32)) << 32 | (u64)(unsigned)__builtin_amdgcn_readfirstlane((unsigned)e0)) + 1;
      PST(9);
      if (blockIdx.x < 108) {
        float acc[8] = {};
#pragma unroll
        for (int i = 0; i < 9; ++i) {
          const float xc = bf2f(cval[i]);
          const float sc = rbf(xc * __builtin_amdgcn_rcpf(1.f + __expf(-xc)));
#pragma unroll
          for (int e = 0; e < 4; ++e) { acc[2 * e] += sc * bflo(wm[i][e]); acc[2 * e + 1] += sc * bfhi(wm[i][e]); }
        }
        LAS float* red = (LAS float*)(size_t)(ldsb + LDS_X1);
#pragma unroll
        for (int e = 0; e < 8; ++e) { float t = acc[e]; t += dpp<0x128>(t); t = swap32_add(swap16_add(t)); if (lane < 8) red[w * 64 + lane * 8 + e] = t; }
        asm volatile("s_waitcnt lgkmcnt(0)" ::: "memory");
        BAR();
        if (tid < 32) {
          f2 v = {0.f, 0.f};
#pragma unroll
          for (int k = 0; k < 4; ++k) v += *(LAS f2*)(red + k * 64 + 2 * tid);
          const __amdgpu_buffer_rsrc_t rP = __builtin_amdgcn_make_buffer_rsrc(p.ws + WS_MODP, 0, 54 * 1024, 0x00020000);
          __builtin_amdgcn_raw_buffer_store_b128((u4){__float_as_uint(v[0]), __float_as_uint(v[1]), (unsigned)tag, (unsigned)(tag >> 32)}, rP, mg * 1024 + ms * 512 + tid * 16, 0, 16);
        }
      }
      PST(10);
      PST(11);
      u4 xv[2][9];
      GAP();
#pragma unroll
      for (int ps = 0; ps < 2; ++ps)
#pragma unroll
        for (int i = 0; i < 9; ++i) xv[ps][i] = *(const u4*)(p.x + (size_t)(c * 64 + 16 * w + 8 * ps + rr) * D + (sub + 8 * i) * 8);
      GAP();
      PST(1);
      STAMP(20);
      PST(2);
      f2 y[2][9][4];
      ModGather<1728, 256> mgat;
      {
#pragma unroll
        for (int ps = 0; ps < 2; ++ps) {
          f2 a1 = {0.f, 0.f}, a2 = {0.f, 0.f};
#pragma unroll
          for (int i = 0; i < 9; ++i)
#pragma unroll
            for (int e = 0; e < 4; ++e) { y[ps][i][e] = bf2(xv[ps][i][e]); a1 += y[ps][i][e]; a2 = __builtin_elementwise_fma(y[ps][i][e], y[ps][i][e], a2); }
          float s1 = a1[0] + a1[1], s2 = a2[0] + a2[1];
          s1 = sum8(s1); s2 = sum8(s2);
          if (ps == 0) PST(3);
          const float mean = s1 * (1.f / D), var = __builtin_fmaxf(__builtin_fmaf(-mean, mean, s2 * (1.f / D)), 0.f);
          const float mr = rbf(mean), rs = __builtin_amdgcn_rcpf(rbf(__builtin_amdgcn_sqrtf(rbf(rbf(var) + 1e-5f))));
          f2 mr2 = {mr, mr}, rs2 = {rs, rs}; asm volatile("" : "+v"(mr2), "+v"(rs2));
          if (ps == 1) { GAP(); mgat.issue(p.ws, tid); GAP(); }
#pragma unroll
          for (int i = 0; i < 9; ++i) {
            f2 t[4];
#pragma unroll
            for (int e = 0; e < 4; ++e) t[e] = y[ps][i][e] - mr2;
#pragma unroll
            for (int e = 0; e < 4; ++e) t[e] = rb2(t[e]);
#pragma unroll
            for (int e = 0; e < 4; ++e) t[e] = t[e] * rs2;
#pragma unroll
            for (int e = 0; e < 4; ++e) y[ps][i][e] = rb2(t[e]);
            asm volatile("" : "+v"(y[ps][i][0]), "+v"(y[ps][i][1]), "+v"(y[ps][i][2]), "+v"(y[ps][i][3]));
          }
        }
      }
      STAMP(21);
      PST(4);
      mgat.finish(tag, ldsb, tmo, tid);
      STAMP(22);
      PST(5);
      issueq(0, gq); issueq(1, gq + KS_Q);
      {
        const int li = tid < 144 ? tid : tid - 112;
        touch((const char*)p.wq + (size_t)512 * j * 2, (18 * c + (li >> 3)) * 8192 + (li & 7) * 128, __builtin_amdgcn_readfirstlane(ldsb + LDS_DUMMY));
      }
      asm volatile("s_waitcnt lgkmcnt(0)" ::: "memory");
      BAR();
      STAMP(23);
      PST(6);
      {
        const unsigned tb = ldsb + LDS_X1 + 1024 + sub * 32;
        f4 cst[2][4];
        auto ldc = [&](int i, f4 (&d)[4]) { d[0] = *(LAS f4*)(size_t)(tb + i * 256); d[1] = *(LAS f4*)(size_t)(tb + i * 256 + 16); d[2] = *(LAS f4*)(size_t)(tb + i * 256 + D * 4); d[3] = *(LAS f4*)(size_t)(tb + i * 256 + D * 4 + 16); };
        ldc(0, cst[0]);
        const unsigned ad0 = ldsb + (16 * w + rr) * ROWB + ((sub ^ ((rr >> 1) & 7)) * 16);
        const unsigned ad1 = ldsb + (16 * w + 8 + rr) * ROWB + ((sub ^ (((rr >> 1) + 4) & 7)) * 16);
#pragma unroll
        for (int i = 0; i < 9; ++i) {
          if (i + 1 < 9) ldc(i + 1, cst[(i + 1) & 1]);
          GAP();
          const f4 (&k)[4] = cst[i & 1];
          const f2 sh[4] = {{k[0][0], k[0][1]}, {k[0][2], k[0][3]}, {k[1][0], k[1][1]}, {k[1][2], k[1][3]}}, cc[4] = {{k[2][0], k[2][1]}, {k[2][2], k[2][3]}, {k[3][0], k[3][1]}, {k[3][2], k[3][3]}};
          f2 t[2][4]; u4 o[2];
#pragma unroll
          for (int ps = 0; ps < 2; ++ps)
#pragma unroll
            for (int e = 0; e < 4; ++e) t[ps][e] = y[ps][i][e] * cc[e];
#pragma unroll
          for (int ps = 0; ps < 2; ++ps)
#pragma unroll
            for (int e = 0; e < 4; ++e) t[ps][e] = rb2(t[ps][e]);
#pragma unroll
          for (int ps = 0; ps < 2; ++ps)
#pragma unroll
            for (int e = 0; e < 4; ++e) t[ps][e] = t[ps][e] + sh[e];
#pragma unroll
          for (int ps = 0; ps < 2; ++ps)
#pragma unroll
            for (int e = 0; e < 4; ++e) o[ps][e] = pk(t[ps][e]);
          *(LAS u4*)(size_t)(ad0 + i * 128) = o[0];
          *(LAS u4*)(size_t)(ad1 + i * 128) = o[1];
          GAP();
        }
      }
    }
    {
      const int blk = (lane >> 4) & 1, q = (lane & 15) >> 2, pp = lane & 3;
#pragma unroll
      for (int dt = 0; dt < 4; ++dt) s.wb[dt] = s.ring + (8 * hh + q) * 256 + 64 * (dt ^ q) + 32 * blk + 16 * (pp & 1) + 8 * (pp >> 1);
#pragma unroll
      for (int i = 0; i < 4; ++i) s.hb[i] = ldsb + r32 * ROWB + (((2 * i + hh) ^ ((r32 >> 1) & 7)) * 16);
    }
    asm volatile("s_waitcnt vmcnt(9) lgkmcnt(0)" ::: "memory");
    BAR();
    STAMP(1);
    PST(7);
#pragma unroll
    for (int f = 0; f < 8; ++f) { s.wlo[0][f] = ldT(s.wb[f & 3], 4096 * (f >> 2)); s.whi[0][f] = ldT(s.wb[f & 3], 4096 * (f >> 2) + 1024); }
#pragma unroll
    for (int i = 0; i < 4; ++i) s.hf[0][i] = ldA(s.hb[i >> 1], (i & 1) * 32 * ROWB);
    asm volatile("s_waitcnt lgkmcnt(0)" ::: "memory");
    issueq(0, gq + 2 * KS_Q);
    auto no = [&]() __attribute__((always_inline)) {};
    auto pfkv = [&](int h) __attribute__((always_inline)) {
      if (lane < 16) touch((const char*)(w < 2 ? p.kc : p.vc) + ((size_t)(2 * j + (w & 1)) * SK + (SK - WIN)) * DH * 2, (32 * c + 16 * h + lane) * 128, __builtin_amdgcn_readfirstlane(ldsb + LDS_DUMMY));
    };
    qstep<0, true, true, true, 9>(s, Qt, voffQ, gq + 3 * KS_Q, 0, no);
    qstep<1, false, true, true, 8>(s, Qt, voffQ, gq + 4 * KS_Q, 128, [&]() __attribute__((always_inline)) { pfkv(0); });
    qstep<0, false, true, true, 9>(s, Qt, voffQ, gq + 5 * KS_Q, 128, no);
    qstep<1, false, true, true, 9>(s, Qt, voffQ, gq + 6 * KS_Q, 256, [&]() __attribute__((always_inline)) { pfkv(1); });
    qstep<0, false, true, true, 9>(s, Qt, voffQ, gq + 7 * KS_Q, 256, no);
    qstep<1, false, true, true, 9>(s, Qt, voffQ, gq + 8 * KS_Q, 384, no);
    for (int n = 6; n < 14; n += 2) {
      qstep<0, false, true, true, 8>(s, Qt, voffQ, gq + (size_t)(n + 3) * KS_Q, 128 * ((n + 1) >> 1), no);
      qstep<1, false, true, true, 8>(s, Qt, voffQ, gq + (size_t)(n + 4) * KS_Q, 128 * ((n + 2) >> 1), no);
    }
    qstep<0, false, true, true, 8>(s, Qt, voffQ, gq + (size_t)17 * KS_Q, 128 * 7, no);
    qstep<1, false, true, false, 8>(s, Qt, voffQ, gq, 128 * 8, no);
    qstep<0, false, true, false, 0>(s, Qt, voffQ, gq, 128 * 8, no);
    qstep<1, false, false, false, 0>(s, Qt, voffQ, gq, 0, no);
    STAMP(2);
    PST(8);
    {
      const int kvl = w >> 1, half = w & 1;
      const unsigned kr = ldsb + kvl * 65536, vr = kr + 32768;
#pragma unroll
      for (int i = 0; i < 2; ++i) a.kb_[i] = kr + 2048 * (r32 >> 3) + 64 * (r32 & 7) + 16 * ((2 * i + hh) ^ ((r32 >> 2) & 3));
      const int blk = (lane >> 4) & 1, q = (lane & 15) >> 2, pp = lane & 3;
#pragma unroll
      for (int t = 0; t < 2; ++t) a.vb_[t] = vr + 64 * (4 * hh + q) + 16 * ((2 * blk + (pp >> 1)) ^ ((2 * t + hh) & 3)) + 8 * (pp & 1);
      const int st = lane >> 5, r7 = (lane >> 2) & 7, pos = lane & 3;
#pragma unroll
      for (int i = 0; i < 8; ++i) {
        const int rg = 4 * half + (i >> 1);
        a.voff[i] = rg * 2048 + r7 * 256 + (i & 1) * 128 + st * 64 + 16 * (pos ^ ((2 * (rg & 1) + (r7 >> 2)) & 3)) - (i - 4) * 1024 + 4096;
      }
      a.m0k = __builtin_amdgcn_readfirstlane(kr + 8192 * half + 4096); a.m0v = __builtin_amdgcn_readfirstlane(vr + 8192 * half + 4096);
      const size_t kvoff = ((size_t)(2 * j + kvl) * SK + (SK - WIN)) * DH * 2;
      a.gk = (const char*)p.kc + kvoff - 4096; a.gv = (const char*)p.vc + kvoff - 4096;
      a.pend = false; a.one = p.one;
    }
    asm volatile("s_waitcnt lgkmcnt(0)" ::: "memory");
    BAR();
    setm0(a.m0k); asm volatile("s_nop 0");
    dma<-4096>(a.voff[0], a.gk); dma<-3072>(a.voff[1], a.gk); dma<-2048>(a.voff[2], a.gk); dma<-1024>(a.voff[3], a.gk);
    dma<0>(a.voff[4], a.gk); dma<1024>(a.voff[5], a.gk); dma<2048>(a.voff[6], a.gk); dma<3072>(a.voff[7], a.gk);
    asm volatile("s_nop 7" ::: "memory");
#pragma unroll
    for (int dt = 0; dt < 4; ++dt)
#pragma unroll
      for (int qb = 0; qb < 2; ++qb) {
        asm volatile("" : "+v"(Qt[dt][qb]));
#pragma unroll
        for (int s2 = 0; s2 < 2; ++s2) {
          u4 w4;
#pragma unroll
          for (int e = 0; e < 4; ++e) w4[e] = pk((f2){Qt[dt][qb][8 * s2 + 2 * e], Qt[dt][qb][8 * s2 + 2 * e + 1]} * QSCALE);
          a.q[qb][2 * dt + s2] = __builtin_bit_cast(b8, w4);
          asm volatile("" : "+a"(a.q[qb][2 * dt + s2]));
        }
      }
  }
  STAMP(3);
  {
    auto tile_bar = [&]() __attribute__((always_inline)) { asm volatile("s_waitcnt vmcnt(0) lgkmcnt(0)" ::: "memory"); BAR(); };
    const char* pfp = lane >= 32 ? p.ws + WS_X + (size_t)c * 64 * SLICE + (size_t)j * SLICE + (18 * w + lane - 32) * 128
                    : w < 2 ? (const char*)(w == 0 ? p.wg : p.wu) + (size_t)576 * j * 2 + (size_t)(18 * c + lane / 9 - 3) * (DF * 2) + (lane % 9) * 128
                            : (const char*)p.wo + (size_t)512 * j * ROWB + (16 * c) * ROWB + (12 * (w - 2) + lane - 24) * 128;
    const int pfs = lane >= 32 ? 8 * SLICE : w < 2 ? 3 * DF * 2 : 24 * 128, pfn = w < 2 ? 27 : 12;
    auto pft = [&](int t) __attribute__((always_inline)) {
      const bool on = lane < pfn && t >= 1 && t <= 6;
      touchp(on ? pfp : (const char*)p.wo, __builtin_amdgcn_readfirstlane(ldsb + LDS_DUMMY));
      pfp += pfs;
    };
    const __amdgpu_buffer_rsrc_t rXw = __builtin_amdgcn_make_buffer_rsrc(p.ws + WS_X + (size_t)c * 64 * SLICE, 0, 64 * SLICE, 0x00020000);
    int pwq = w * 18;
    auto pw = [&](int) __attribute__((always_inline)) {
    };
    auto nopw = [&](int) __attribute__((always_inline)) {};
    auto tile_bar1 = [&]() __attribute__((always_inline)) { asm volatile("s_waitcnt vmcnt(1) lgkmcnt(0)" ::: "memory"); BAR(); };
    LAS unsigned* flag = (LAS unsigned*)(size_t)(ldsb + LDS_X1);
    if (tid == 0) *flag = 0u;
    auto k0 = [&]() __attribute__((always_inline)) {
      setm0(a.m0k); asm volatile("s_nop 0");
      dma<-4096>(a.voff[0], a.gk); dma<-3072>(a.voff[1], a.gk); dma<-2048>(a.voff[2], a.gk); dma<-1024>(a.voff[3], a.gk);
      dma<0>(a.voff[4], a.gk); dma<1024>(a.voff[5], a.gk); dma<2048>(a.voff[6], a.gk); dma<3072>(a.voff[7], a.gk);
    };
    tile_bar(); fastA<0, true, true, true>(a, 0, [&]() __attribute__((always_inline)) { pft(0); }, pw); asm volatile("s_nop 7\n\ts_nop 3" ::: "memory"); GAP(); fastB<0, 2>(a);
    tile_bar1(); fastA<1, false, true, true>(a, 1, [&]() __attribute__((always_inline)) { pft(1); }, pw); fastB<1, 1>(a);
    for (int t = 2; t < 6; t += 2) {
      tile_bar1(); fastA<0, false, true, true>(a, t, [&]() __attribute__((always_inline)) { pft(t); }, pw); fastB<0, 0>(a);
      tile_bar1(); fastA<1, false, true, true>(a, t + 1, [&]() __attribute__((always_inline)) { pft(t + 1); }, pw); fastB<1, 0>(a);
    }
    tile_bar1(); fastA<0, false, true, true>(a, 6, [&]() __attribute__((always_inline)) { pft(6); }, nopw); fastB<0, 0>(a);
    tile_bar1(); fastA<1, false, false, true>(a, 7, [&]() __attribute__((always_inline)) { pft(7); }, nopw); fastB<1, 0>(a);
    tile_bar1();
    fastB<1, 3>(a);
    {
      const bool bad = !(a.l[0] < 0x1p40f) || !(a.l[1] < 0x1p40f);
      if (__any((int)bad) && lane == 0) *flag = 1u;
      asm volatile("s_waitcnt lgkmcnt(0)" ::: "memory"); BAR();
      unsigned redo = __builtin_amdgcn_readfirstlane(*(volatile LAS unsigned*)flag); asm volatile("" : "+s"(redo));
      if (__builtin_expect(redo != 0u, 0)) {
        auto nopf = [&]() __attribute__((always_inline)) {};
        a.pend = false;
        auto upt = [&](const char* q) __attribute__((always_inline)) { const u64 v = (u64)q; return (const char*)((u64)(unsigned)__builtin_amdgcn_readfirstlane((unsigned)(v >> 32)) << 32 | (u64)(unsigned)__builtin_amdgcn_readfirstlane((unsigned)v)); };
        a.gk = upt(a.gk); a.gv = upt(a.gv); a.m0k = __builtin_amdgcn_readfirstlane(a.m0k); a.m0v = __builtin_amdgcn_readfirstlane(a.m0v);
        k0();
        tile_bar(); phaseA<0, true, true, true>(a, 0, nopf); asm volatile("s_nop 7\n\ts_nop 3" ::: "memory"); GAP(); phaseB<0, true, false>(a);
        tile_bar(); phaseA<1, false, true, true>(a, 1, nopf); phaseB<1, false, true>(a);
        for (int t = 2; t < 6; t += 2) {
          tile_bar(); phaseA<0, false, true, true>(a, t, nopf); phaseB<0, false, false>(a);
          tile_bar(); phaseA<1, false, true, true>(a, t + 1, nopf); phaseB<1, false, false>(a);
        }
        tile_bar(); phaseA<0, false, true, true>(a, 6, nopf); phaseB<0, false, false>(a);
        tile_bar(); phaseA<1, false, false, true>(a, 7, nopf); phaseB<1, false, false>(a);
        { auto f1 = [&](auto ic) __attribute__((always_inline)) { sm_exp<1, 1, decltype(ic)::value>(a); }; UNROLL(32, f1);
          auto f2_ = [&](auto ic) __attribute__((always_inline)) { sm_pack<1, decltype(ic)::value>(a); }; UNROLL(32, f2_);
          auto f3 = [&](auto ic) __attribute__((always_inline)) { sm_add<1, 1, decltype(ic)::value>(a); }; UNROLL(32, f3); }
#pragma unroll
        for (int qb = 0; qb < 2; ++qb) a.l[qb] += a.rs0[qb] + a.rs1[qb];
        tile_bar();
        const unsigned vslot = 16384;
        b4 vlo[16], vhi[16];
#pragma unroll
        for (int I = 0; I < 16; ++I) { vlo[I] = ldT(a.vb_[0], vslot + 2048 * (2 * (I >> 2)) + 512 * (I & 3)); vhi[I] = ldT(a.vb_[1], vslot + 2048 * (2 * (I >> 2) + 1) + 512 * (I & 3)); }
        asm volatile("s_nop 1" ::: "memory"); GAP();
#pragma unroll
        for (int G = 0; G < 32; ++G) { const int I = G >> 1; mfma32<true, false>(a.O[I & 3][G & 1], cat(vlo[I], vhi[I]), a.P[G & 1][I >> 2]); }
        asm volatile("s_waitcnt vmcnt(0) lgkmcnt(0)" ::: "memory"); BAR();
      }
    }
  }
  STAMP(4);
  int tid2 = threadIdx.x; asm volatile("" : "+v"(tid2));
  {
  const int tid = tid2, lane = tid2 & 63, hh = lane >> 5, r32 = lane & 31;
  GS s;
  unsigned voffB[9];
#pragma unroll
  for (int i = 0; i < 9; ++i) { const int idx = i * 64 + lane, imm = i < 8 ? (i - 4) * 1024 : 0; voffB[i] = (idx / 18) * ROWB + (idx % 18) * 16 - imm + 4096; }
  s.ring = __builtin_amdgcn_readfirstlane(ldsb + ABYTES + w * 2 * SLOT);
  const int g4 = lane >> 4, cl = lane & 15;
  s.wbase = s.ring + (4 * g4 + ((lane & 15) >> 2)) * 288 + 8 * (lane & 3);
  const unsigned sw = (cl >> 1) & 7;
  const unsigned ab_e = ldsb + cl * ROWB + ((g4 ^ sw) * 16), ab_o = ldsb + cl * ROWB + (((4 + g4) ^ sw) * 16);
  const __amdgpu_buffer_rsrc_t rX = __builtin_amdgcn_make_buffer_rsrc(p.ws + WS_X + (size_t)c * 64 * SLICE, 0, 64 * SLICE, 0x00020000);
  unsigned voffA[9];
#pragma unroll
  for (int i = 0; i < 9; ++i) { const int idx = i * 64 + lane, imm = i < 8 ? (i - 4) * 1024 : 0; voffA[i] = (idx / 18) * (DF * 2) + (idx % 18) * 16 - imm + 4096; }
  const char* gG = (const char*)p.wg + (size_t)(576 * j + 144 * w) * 2 - 4096;
  const char* gU = (const char*)p.wu + (size_t)(576 * j + 144 * w) * 2 - 4096;
  const char* gD = (const char*)p.wd + (size_t)(576 * j) * ROWB + 288 * w - 4096;
  f4 aG[9][4];
  {
    const char* gO = (const char*)p.wo + (size_t)(512 * j) * ROWB + 288 * w - 4096;
    issue_all(s, 0, voffB, gO); issue_all(s, 1, voffB, gO + (size_t)32 * ROWB);
    asm volatile("s_nop 7" ::: "memory");
#pragma unroll
    for (int qb = 0; qb < 2; ++qb) {
      const float il = __builtin_amdgcn_rcpf(swap32_add(a.l[qb]));
      const int row = 32 * qb + r32; const unsigned swr = (r32 >> 1) & 7;
#pragma unroll
      for (int db = 0; db < 4; ++db) {
        asm volatile("" : "+a"(a.O[db][qb]));
        const f16v o = a.O[db][qb];
#pragma unroll
        for (int u = 0; u < 2; ++u) {
          const u4 wv = {pk((f2){o[4 * u], o[4 * u + 1]} * il), pk((f2){o[4 * u + 2], o[4 * u + 3]} * il), pk((f2){o[4 * u + 8], o[4 * u + 9]} * il), pk((f2){o[4 * u + 10], o[4 * u + 11]} * il)};
          *(LAS u4*)(size_t)(ldsb + row * ROWB + (((4 * (4 * w + db) + 2 * u + hh) ^ swr) * 16)) = wv;
        }
      }
    }
    asm volatile("s_waitcnt vmcnt(9) lgkmcnt(0)" ::: "memory");
    BAR();
    STAMP(5);
    read_w(s, 0); read_a(s, 0, ab_e);
    asm volatile("s_waitcnt lgkmcnt(0)" ::: "memory");
    issue_all(s, 0, voffB, gO + (size_t)2 * 32 * ROWB);
    const Epi e1 = {rX, (unsigned)(j * SLICE), 8u * SLICE, ldsb + LDS_X1, j, w, lane};
    gemm_rows<16, 2, 0>(s, aG, voffB, gO, ab_e, ab_o, voffA, gG, gU, e1, [&]() __attribute__((always_inline)) {});
    STAMP(6);
  }
  {
    int zlr[3], zcc[3];
    const int t3 = tid < 192 ? tid : tid - 64;
    u4 xr[3], gv[3];
#pragma unroll
    for (int i = 0; i < 3; ++i) {
      zdecode(t3 + 192 * i, zlr[i], zcc[i]);
      xr[i] = *(const u4*)(p.x + (size_t)(c * 64 + j * 8 + zlr[i]) * D + zcc[i] * 8);
      gv[i] = *(LAS u4*)(size_t)(ldsb + LDS_MOD + (2 * D + zcc[i] * 8) * 2);
    }
    const int lrow = 2 * w + hh, grow = j * 8 + lrow;
    u2 cbv[3][2], sbv[3][2];
#pragma unroll
    for (int i = 0; i < 3; ++i) {
      const int pos = (i < 2 || r32 < 8) ? r32 + 32 * i : r32;
      const int q = pos ^ ((grow >> 1) & 7), k0 = 32 * (q >> 2) + 4 * (q & 3);
#pragma unroll
      for (int h = 0; h < 2; ++h) { cbv[i][h] = *(LAS u2*)(size_t)(ldsb + LDS_MOD + (4 * D + k0 + 16 * h) * 2); sbv[i][h] = *(LAS u2*)(size_t)(ldsb + LDS_MOD + (3 * D + k0 + 16 * h) * 2); }
    }
    asm volatile("s_waitcnt lgkmcnt(0)" ::: "memory");
    publish(flags + (0 * 256 + blockIdx.x) * FLAG_STRIDE, tag);
    STAMP(7);
    await8(flags + (0 * 256 + c * 8) * FLAG_STRIDE, tag, tmo, w, lane);
    STAMP(8);
    {
      u4 v[3][8];
#pragma unroll
      for (int i = 0; i < 3; ++i) {
#pragma unroll
        for (int jj = 0; jj < 7; ++jj) v[i][jj] = __builtin_amdgcn_raw_buffer_load_b128(rX, (j * 8 + jj + (jj >= j)) * SLICE + (t3 + 192 * i) * 16, 0, AUX_X);
      }
#pragma unroll
      for (int i = 0; i < 3; ++i) v[i][7] = *(LAS u4*)(size_t)(ldsb + LDS_X1 + (t3 + 192 * i) * 16);
#pragma unroll
      for (int i = 0; i < 3; ++i) {
        const u4 x1 = gated_residual(v[i], xr[i], gv[i]);
        if (tid < 192) {
          *(LAS u4*)(size_t)(ldsb + LDS_X1 + (tid + 192 * i) * 16) = x1;
          LAS f4* dst = (LAS f4*)(size_t)(ldsb + (zlr[i] * D + zcc[i] * 8) * 4);
          const f2 a0 = bf2(x1[0]), a1 = bf2(x1[1]), a2 = bf2(x1[2]), a3 = bf2(x1[3]);
          dst[0] = (f4){a0[0], a0[1], a1[0], a1[1]}; dst[1] = (f4){a2[0], a2[1], a3[0], a3[1]};
        }
      }
    }
    asm volatile("s_waitcnt lgkmcnt(0)" ::: "memory");
    BAR();
    STAMP(9);
    {
      f4 v[3][2];
      f2 sum = {0.f, 0.f};
#pragma unroll
      for (int i = 0; i < 3; ++i) {
        const bool on = (i < 2 || r32 < 8);
        const int pos = on ? r32 + 32 * i : r32;
        const int q = pos ^ ((grow >> 1) & 7), k0 = 32 * (q >> 2) + 4 * (q & 3);
#pragma unroll
        for (int h = 0; h < 2; ++h) {
          v[i][h] = *(LAS f4*)(size_t)(ldsb + (lrow * D + k0 + 16 * h) * 4);
          if (on) sum += (f2){v[i][h][0] + v[i][h][1], v[i][h][2] + v[i][h][3]};
        }
      }
      float sm = sum[0] + sum[1];
      sm = sum32(sm);
      const float mean = sm * (1.f / D);
      float q2 = 0.f;
#pragma unroll
      for (int i = 0; i < 3; ++i)
        if (i < 2 || r32 < 8) {
#pragma unroll
          for (int h = 0; h < 2; ++h)
#pragma unroll
            for (int e = 0; e < 4; ++e) { const float dl = v[i][h][e] - mean; q2 += dl * dl; }
        }
      q2 = sum32(q2);
      const float mr = rbf(mean), rs = __builtin_amdgcn_rcpf(rbf(__builtin_amdgcn_sqrtf(rbf(rbf(q2 * (1.f / D)) + 1e-5f))));
      const __amdgpu_buffer_rsrc_t rH = __builtin_amdgcn_make_buffer_rsrc(p.ws + WS_H2 + (size_t)c * ABYTES, 0, ABYTES, 0x00020000);
      u4 oh[3];
#pragma unroll
      for (int i = 0; i < 3; ++i) {
        u4& o = oh[i];
#pragma unroll
        for (int h = 0; h < 2; ++h)
#pragma unroll
          for (int e = 0; e < 2; ++e) {
            unsigned t = pk((f2){v[i][h][2 * e], v[i][h][2 * e + 1]} - mr);
            t = pk(bf2(t) * rs);
            t = pk(bf2(t) * bf2(pk(bf2(cbv[i][h][e]) + 1.f)));
            o[2 * h + e] = pk(bf2(t) + bf2(sbv[i][h][e]));
          }
        if (i < 2 || r32 < 8) __builtin_amdgcn_raw_buffer_store_b128(o, rH, grow * ROWB + (r32 + 32 * i) * 16, 0, 16);
      }
      publish(flags + (1 * 256 + blockIdx.x) * FLAG_STRIDE, tag);
      STAMP(10);
      if (w != 0) {
        const unsigned dummy = __builtin_amdgcn_readfirstlane(ldsb + LDS_DUMMY);
        int li = tid - 64; li = li < 162 ? li : li - 94;
        const int r = 18 * c + li / 9, l = li % 9;
        touch((const char*)p.wg + (size_t)576 * j * 2, r * (DF * 2) + l * 128, dummy);
        touch((const char*)p.wu + (size_t)576 * j * 2, r * (DF * 2) + l * 128, dummy);
      }
      await8(flags + (1 * 256 + c * 8) * FLAG_STRIDE, tag, tmo, w, lane);
      STAMP(11);
      u4 hv[16];
#pragma unroll
      for (int i = 0; i < 16; ++i) { const int y = i * 256 + tid < 4032 ? i * 256 + tid : tid; hv[i] = __builtin_amdgcn_raw_buffer_load_b128(rH, (y + (y >= j * 576 ? 576 : 0)) * 16, 0, AUX_H); }
#pragma unroll
      for (int i = 0; i < 3; ++i) if (i < 2 || r32 < 8) *(LAS u4*)(size_t)(ldsb + grow * ROWB + (r32 + 32 * i) * 16) = oh[i];
#pragma unroll
      for (int i = 0; i < 16; ++i) { const int y = i * 256 + tid; if (i < 15 || y < 4032) *(LAS u4*)(size_t)(ldsb + (y + (y >= j * 576 ? 576 : 0)) * 16) = hv[i]; }
    }
    asm volatile("s_waitcnt vmcnt(0) lgkmcnt(0)" ::: "memory");
    BAR();
  }
  STAMP(12);
  {
    constexpr size_t KS_GU = (size_t)32 * DF * 2, KS_D = (size_t)32 * ROWB;
    read_w(s, 0); read_a(s, 0, ab_e);
    asm volatile("s_waitcnt lgkmcnt(0)" ::: "memory");
    issue_all(s, 0, voffA, gG + KS_GU);
    f4 aU[9][4];
    auto no = [&]() __attribute__((always_inline)) {};
    auto pfd = [&](int h) __attribute__((always_inline)) {
      int li = 64 * h + 16 * w + lane; li = li < 162 ? li : 161;
      if (lane < 16) touch((const char*)p.wd + (size_t)576 * j * ROWB, (18 * c) * ROWB + li * 128, __builtin_amdgcn_readfirstlane(ldsb + LDS_DUMMY));
    };
    auto kpair = [&](auto zc, auto lastc, auto vmc, int k, auto&& pf) __attribute__((always_inline)) {
      constexpr bool Z = decltype(zc)::value, LAST = decltype(lastc)::value; constexpr int VM = decltype(vmc)::value;
      const char* u1 = gU + (size_t)(k + 1) * KS_GU; const char* g2 = gG + (size_t)(k + 2) * KS_GU;
      step<0, 0, Z, true, false, true, 9, 0>(s, aG, voffA, u1, 0, no);
      if constexpr (!LAST) {
        step<1, 0, Z, true, true, true, 9, NVU>(s, aU, voffA, g2, ab_o + 128 * (k >> 1), pf);
        step<0, 1, false, true, false, true, VM, 0>(s, aG, voffA, u1 + KS_GU, 0, no);
        step<1, 1, false, true, true, true, VM, NVU>(s, aU, voffA, g2 + KS_GU, ab_e + 128 * ((k + 2) >> 1), no);
      } else {
        step<1, 0, Z, true, true, true, 9, NVU>(s, aU, voffB, gD, ab_o + 128 * (k >> 1), no);
        step<0, 1, false, true, false, true, 9, 0>(s, aG, voffB, gD + KS_D, 0, no);
        step<1, 1, false, true, false, true, 9, NVU>(s, aU, voffB, gD + 2 * KS_D, 0, no);
      }
    };
    kpair(std::true_type{}, std::false_type{}, IC<9>{}, 0, no);
    for (int k = 2; k < 8; k += 2) kpair(std::false_type{}, std::false_type{}, IC<9>{}, k, no);
    for (int k = 8; k < 14; k += 2) kpair(std::false_type{}, std::false_type{}, IC<10>{}, k, [&]() __attribute__((always_inline)) { pfd((k - 8) >> 1); });
    kpair(std::false_type{}, std::false_type{}, IC<9>{}, 14, no);
    kpair(std::false_type{}, std::true_type{}, IC<9>{}, 16, no);
    STAMP(13);
    settle<0>(aG); settle<NVU>(aU);
    u2 act[9][4];
#pragma unroll
    for (int t = 0; t < 9; ++t)
#pragma unroll
      for (int m = 0; m < 4; ++m)
#pragma unroll
        for (int e = 0; e < 2; ++e) {
          const f2 x = {aG[t][m][2 * e], aG[t][m][2 * e + 1]}, u = {aU[t][m][2 * e], aU[t][m][2 * e + 1]};
          const f2 z = x * (x * x * -0.10294324f + -2.3022082f);
          const f2 dn = (f2){__builtin_amdgcn_exp2f(z[0]), __builtin_amdgcn_exp2f(z[1])} + 1.f;
          act[t][m][e] = pk((x * u) * (f2){__builtin_amdgcn_rcpf(dn[0]), __builtin_amdgcn_rcpf(dn[1])});
        }
    asm volatile("s_waitcnt lgkmcnt(0)" ::: "memory");
    BAR();
#pragma unroll
    for (int t = 0; t < 9; ++t) {
      const int tt = 9 * w + t, ks = tt >> 1, ah = tt & 1;
      const unsigned ad = ldsb + cl * ROWB + (((4 * ks + g4) ^ sw) * 16) + 8 * ah;
#pragma unroll
      for (int m = 0; m < 4; ++m) *(LAS u2*)(size_t)(ad + m * 16 * ROWB) = act[t][m];
    }
    asm volatile("s_waitcnt lgkmcnt(0)" ::: "memory");
    BAR();
    STAMP(14);
    read_a(s, 0, ab_e);
    const Epi e2 = {rX, (unsigned)(j * 8 * SLICE), (unsigned)SLICE, s.ring - w * 2304, j, w, lane};
    gemm_rows<18, 0, 0>(s, aG, voffB, gD, ab_e, ab_o, voffB, gD, gD, e2, [&]() __attribute__((always_inline)) {});
    STAMP(15);
  }
  {
    int tid3 = threadIdx.x; asm volatile("" : "+v"(tid3));
    const int tid = tid3, lane = tid3 & 63;
    asm volatile("s_waitcnt lgkmcnt(0)" ::: "memory");
    publish(flags + (2 * 256 + blockIdx.x) * FLAG_STRIDE, tag);
    STAMP(16);
    const int t3 = tid < 192 ? tid : tid - 64;
    int zlr[3], zcc[3];
    u4 gv[3], x1[3];
#pragma unroll
    for (int i = 0; i < 3; ++i) {
      zdecode(t3 + 192 * i, zlr[i], zcc[i]);
      gv[i] = *(LAS u4*)(size_t)(ldsb + LDS_MOD + (5 * D + zcc[i] * 8) * 2);
      x1[i] = *(LAS u4*)(size_t)(ldsb + LDS_X1 + (t3 + 192 * i) * 16);
    }
    if (blockIdx.x == 0 && w == 1) {
      for (unsigned spins = 0; spins <= SPIN_CAP; ++spins) {
        bool ok = true;
#pragma unroll
        for (int i = 0; i < 4; ++i) ok &= __hip_atomic_load((gu64*)(flags + (lane + 64 * i) * FLAG_STRIDE), __ATOMIC_RELAXED, __HIP_MEMORY_SCOPE_AGENT) == tag;
        if (__all(ok)) break;
        __builtin_amdgcn_s_sleep(4);
      }
      if (lane == 0) __hip_atomic_store((gu64*)(p.ws + WS_EPOCH), tag, __ATOMIC_RELAXED, __HIP_MEMORY_SCOPE_AGENT);
    }
    await8(flags + (2 * 256 + c * 8) * FLAG_STRIDE, tag, tmo, w, lane);
    STAMP(17);
    const __amdgpu_buffer_rsrc_t rO = __builtin_amdgcn_make_buffer_rsrc(p.out + (size_t)(c * 64 + j * 8) * D, 0, SLICE, 0x00020000); (void)rO;
    u4 v[3][8];
#pragma unroll
    for (int i = 0; i < 3; ++i)
#pragma unroll
      for (int jj = 0; jj < 7; ++jj) v[i][jj] = __builtin_amdgcn_raw_buffer_load_b128(rX, ((jj + (jj >= j)) * 8 + j) * SLICE + (t3 + 192 * i) * 16, 0, AUX_X);
#pragma unroll
    for (int i = 0; i < 3; ++i) { const int z = t3 + 192 * i, zw = z / 144; v[i][7] = *(LAS u4*)(size_t)(ldsb + ABYTES + zw * 2 * SLOT + (z - zw * 144) * 16); }
#pragma unroll
    for (int i = 0; i < 3; ++i) {
      const u4 o = gated_residual(v[i], x1[i], gv[i]);
      if (tid < 192) *(u4*)(p.out + (size_t)(c * 64 + j * 8 + zlr[i]) * D + zcc[i] * 8) = o;
    }
  }
  STAMP(18);
  asm volatile("s_waitcnt lgkmcnt(0)" ::: "memory");
  STAMP_FLUSH();
  }
}

extern "C" void kernel_launch(void* const* d_in, const int* in_sizes, int n_in, void* d_out, int out_size, void* d_ws, size_t ws_size, hipStream_t stream) {
  Params p;
  p.x = (const u16*)d_in[0]; p.kc = (const u16*)d_in[1]; p.vc = (const u16*)d_in[2]; p.wq = (const u16*)d_in[3]; p.wo = (const u16*)d_in[4];
  p.wg = (const u16*)d_in[6]; p.wu = (const u16*)d_in[7]; p.wd = (const u16*)d_in[8];
  p.c = (const u16*)d_in[9]; p.wmod = (const u16*)d_in[10];
  p.out = (u16*)d_out; p.ws = (char*)d_ws;
 p.one = 1.0f; p.gap = 0u;
  (void)hipFuncSetAttribute((const void*)k_main, hipFuncAttributeMaxDynamicSharedMemorySize, LDS_BYTES);
  k_main<<<256, 256, LDS_BYTES, stream>>>(p);
}
```

```cpp
#define STAG_N 2
#define STAG_D 500
#include <hip/hip_runtime.h>
#include <stdint.h>
#include <utility>
#include <type_traits>

typedef unsigned short u16;
typedef unsigned long long u64;
typedef __bf16 b2 __attribute__((ext_vector_type(2)));
typedef __bf16 b4 __attribute__((ext_vector_type(4)));
typedef __bf16 b8 __attribute__((ext_vector_type(8)));
typedef float f2 __attribute__((ext_vector_type(2)));
typedef float f4 __attribute__((ext_vector_type(4)));
typedef float f16v __attribute__((ext_vector_type(16)));
typedef unsigned u2 __attribute__((ext_vector_type(2)));
typedef unsigned u4 __attribute__((ext_vector_type(4)));
typedef __attribute__((address_space(1))) unsigned gu32;
typedef __attribute__((address_space(1))) unsigned long long gu64;
#define LAS __attribute__((address_space(3)))
#define DI __device__ __forceinline__
#define GAP() __builtin_amdgcn_sched_barrier(0)
#define LGKM0() __builtin_amdgcn_s_waitcnt(0xC07F)
#define BAR() do { asm volatile("" ::: "memory"); __builtin_amdgcn_s_barrier(); asm volatile("" ::: "memory"); } while (0)
template <int V> using IC = std::integral_constant<int, V>;
#define AUX_X 18
#define AUX_H 18
#ifndef NVU
#define NVU 8
#endif
#define PFD_K 14
#define UNROLL(N, F) [&]<int... I_>(std::integer_sequence<int, I_...>) __attribute__((always_inline)) { (F(IC<I_>{}), ...); }(std::make_integer_sequence<int, N>{})

constexpr int S = 2048, D = 576, DH = 128, SK = 2048, WIN = 512, DF = 4608;
constexpr int ROWB = D * 2;
constexpr int ABYTES = 64 * ROWB;
constexpr int SLICE = 8 * ROWB;
constexpr int SLOT = 9216;
constexpr int QSLOT = 8192;
constexpr int LDS_DUMMY = ABYTES + 8 * SLOT;
constexpr int LDS_X1 = LDS_DUMMY + 256;
constexpr int LDS_MOD = LDS_X1 + SLICE;
constexpr int LDS_BYTES = LDS_MOD + 6 * D * 2;
static_assert(LDS_BYTES <= 163840, "LDS");
constexpr size_t WS_EPOCH = 0;
constexpr size_t WS_TMO = 256;
constexpr size_t WS_MODP = 131072;
constexpr size_t WS_FLAG = 32768;
constexpr int FLAG_STRIDE = 8;
constexpr size_t WS_X = 1048576;
constexpr size_t WS_H2 = WS_X + (size_t)32 * 64 * SLICE;
constexpr size_t WS_DBG = 200u << 20;
#define STAMP(i)
#define STAMP_FLUSH()
#define PST(i)

DI float bf2f(u16 v) { return __uint_as_float(((unsigned)v) << 16); }
DI float bflo(unsigned w) { return __uint_as_float(w << 16); }
DI float bfhi(unsigned w) { return __uint_as_float(w & 0xffff0000u); }
DI f2 bf2(unsigned w) { return (f2){bflo(w), bfhi(w)}; }
DI unsigned pk(float lo, float hi) { f2 v = {lo, hi}; b2 b = __builtin_convertvector(v, b2); return __builtin_bit_cast(unsigned, b); }
DI unsigned pk(f2 v) { b2 b = __builtin_convertvector(v, b2); return __builtin_bit_cast(unsigned, b); }
DI float rbf(float f) { return bflo(pk(f, f)); }
DI f2 rb2(f2 v) { return (f2){__uint_as_float(pk(0.f, v[0])), __uint_as_float(pk(0.f, v[1]))}; }
template <int CTRL> DI float dpp(float v) { return __uint_as_float(__builtin_amdgcn_update_dpp(0u, __float_as_uint(v), CTRL, 0xf, 0xf, false)); }
DI float sum8(float v) { v += dpp<0xB1>(v); v += dpp<0x4E>(v); v += dpp<0x141>(v); return v; }
DI float swap16_add(float x) { auto r = __builtin_amdgcn_permlane16_swap(__float_as_uint(x), __float_as_uint(x), false, false); return __uint_as_float(r[0]) + __uint_as_float(r[1]); }
DI float swap32_add(float x) { auto r = __builtin_amdgcn_permlane32_swap(__float_as_uint(x), __float_as_uint(x), false, false); return __uint_as_float(r[0]) + __uint_as_float(r[1]); }
DI float sum32(float v) { v = sum8(v); v += dpp<0x140>(v); return swap16_add(v); }
DI u16 f2bf(float f) { return (u16)(pk(f, f) & 0xffffu); }
DI float fmaxx(float a, float b) { return __builtin_elementwise_maximum(a, b); }
DI float fmax3(float a, float b, float c) { return __builtin_elementwise_maximum(__builtin_elementwise_maximum(a, b), c); }

DI void zdecode(int z, int& lr, int& cc) {
  const int w = z / 144, y = z - w * 144;
  if (y < 128) { const int tp = y >> 5; lr = (y >> 2) & 7; cc = 18 * w + 4 * tp + (y & 3); }
  else { const int y2 = y - 128; lr = y2 >> 1; cc = 18 * w + 16 + (y2 & 1); }
}

template <int IMM> DI void dma(unsigned voff, const char* g) { asm volatile("global_load_lds_dwordx4 %0, %1 offset:%2" :: "v"(voff), "s"(g), "i"(IMM) : "memory"); }
DI void setm0(unsigned a) { asm volatile("s_mov_b32 m0, %0" :: "s"(a) : "memory"); }
DI void touch(const char* p, unsigned voff, unsigned ldsdummy) {
  asm volatile("s_mov_b32 m0, %0\n\ts_nop 0\n\tglobal_load_lds_dword %1, %2" :: "s"(ldsdummy), "v"(voff), "s"(p) : "memory");
}
DI void touchp(const char* a, unsigned ldsdummy) {
  asm volatile("s_mov_b32 m0, %0\n\ts_nop 0\n\tglobal_load_lds_dword %1, off" :: "s"(ldsdummy), "v"(a) : "memory");
}
DI b4 ldT(unsigned base, int off) { return __builtin_amdgcn_ds_read_tr16_b64_v4bf16((LAS b4*)(size_t)(base + off)); }
DI b8 ldA(unsigned base, int off) { return *(LAS b8*)(size_t)(base + off); }
DI b8 cat(const b4& lo, const b4& hi) { return __builtin_shufflevector(lo, hi, 0, 1, 2, 3, 4, 5, 6, 7); }
template <bool AG, bool ZERO> DI void mfma16(f4& c, const b8& a, const b8& b) {
  if constexpr (AG) {
    if constexpr (ZERO) asm volatile("v_mfma_f32_16x16x32_bf16 %0, %1, %2, 0" : "=a"(c) : "v"(a), "v"(b));
    else asm volatile("v_mfma_f32_16x16x32_bf16 %0, %1, %2, %0" : "+a"(c) : "v"(a), "v"(b));
  } else {
    if constexpr (ZERO) asm volatile("v_mfma_f32_16x16x32_bf16 %0, %1, %2, 0" : "=&v"(c) : "v"(a), "v"(b));
    else asm volatile("v_mfma_f32_16x16x32_bf16 %0, %1, %2, %0" : "+v"(c) : "v"(a), "v"(b));
  }
}
template <bool AG, bool ZERO> DI void mfma32(f16v& c, const b8& a, const b8& b) {
  if constexpr (AG) {
    if constexpr (ZERO) asm volatile("v_mfma_f32_32x32x16_bf16 %0, %1, %2, 0" : "=a"(c) : "v"(a), "v"(b));
    else asm volatile("v_mfma_f32_32x32x16_bf16 %0, %1, %2, %0" : "+a"(c) : "v"(a), "v"(b));
  } else {
    if constexpr (ZERO) asm volatile("v_mfma_f32_32x32x16_bf16 %0, %1, %2, 0" : "=&v"(c) : "v"(a), "v"(b));
    else asm volatile("v_mfma_f32_32x32x16_bf16 %0, %1, %2, %0" : "+v"(c) : "v"(a), "v"(b));
  }
}
template <int NVG> DI void settle(f4 (&acc)[9][4]) {
  asm volatile("s_nop 7\n\ts_nop 7" ::: "memory");
#pragma unroll
  for (int t = 0; t < 9; ++t)
#pragma unroll
    for (int m = 0; m < 4; ++m) { if (t * 4 + m < 36 - NVG) asm volatile("" : "+a"(acc[t][m])); else asm volatile("" : "+v"(acc[t][m])); }
}

struct GS {
  b4 wlo[2][9], whi[2][9];
  b8 af[2][4];
  unsigned wbase, ring;
};
DI void issue_all(const GS& s, int slot, const unsigned (&voff)[9], const char* gp) {
  const unsigned sl = s.ring + slot * SLOT;
  setm0(sl + 4096); asm volatile("s_nop 0");
  dma<-4096>(voff[0], gp); dma<-3072>(voff[1], gp); dma<-2048>(voff[2], gp); dma<-1024>(voff[3], gp);
  dma<0>(voff[4], gp); dma<1024>(voff[5], gp); dma<2048>(voff[6], gp); dma<3072>(voff[7], gp);
  setm0(sl + 8192); asm volatile("s_nop 0"); dma<0>(voff[8], gp);
}
template <int PAR, int AP, bool ZERO, bool RDW, bool RDA, bool DMA, int VM, int NVG, typename PF>
DI void step(GS& s, f4 (&acc)[9][4], const unsigned (&voff)[9], const char* gp, unsigned ab, PF&& pf) {
  const unsigned sl = s.ring + (PAR ^ 1) * SLOT;
  const unsigned wb = s.wbase + (PAR ^ 1) * SLOT;
  auto mm = [&](auto ic) __attribute__((always_inline)) {
    constexpr int I = decltype(ic)::value, t = I >> 2, m = I & 3;
    mfma16<(I < 36 - NVG), ZERO>(acc[t][m], cat(s.wlo[PAR][t], s.whi[PAR][t]), s.af[AP][m]);
  };
  auto rd = [&](auto ic) __attribute__((always_inline)) {
    constexpr int I = decltype(ic)::value;
    if constexpr (I < 4) { if constexpr (RDA) s.af[AP ^ 1][I] = ldA(ab, I * 16 * ROWB); }
    else if constexpr (RDW) {
      if constexpr (((I - 4) & 1) == 0) s.wlo[PAR ^ 1][(I - 4) >> 1] = ldT(wb, 32 * ((I - 4) >> 1));
      else s.whi[PAR ^ 1][(I - 4) >> 1] = ldT(wb, 32 * ((I - 4) >> 1) + 4608);
    }
  };
  auto gap = [&](auto gc) __attribute__((always_inline)) {
    constexpr int G = decltype(gc)::value;
    mm(IC<2 * G>{});
    if constexpr (G == 0 && RDW) asm volatile("s_waitcnt vmcnt(%0)" :: "i"(VM) : "memory");
    if constexpr (G <= 10) rd(IC<2 * G>{});
    if constexpr (DMA) {
      if constexpr (G == 12) { LGKM0(); setm0(sl + 4096); }
      if constexpr (G >= 13 && G <= 16) dma<(2 * (G - 13) - 4) * 1024>(voff[2 * (G - 13)], gp);
      if constexpr (G == 17) dma<0>(voff[8], gp);
    }
    GAP();
    mm(IC<2 * G + 1>{});
    if constexpr (G <= 10) rd(IC<2 * G + 1>{});
    if constexpr (DMA) {
      if constexpr (G >= 13 && G <= 16) dma<(2 * (G - 13) + 1 - 4) * 1024>(voff[2 * (G - 13) + 1], gp);
      if constexpr (G == 16) setm0(sl + 8192);
    }
    if constexpr (G == 17) pf();
    GAP();
  };
  UNROLL(18, gap);
}
DI void read_w(GS& s, int par) {
#pragma unroll
  for (int t = 0; t < 9; ++t) { s.wlo[par][t] = ldT(s.wbase + par * SLOT, 32 * t); s.whi[par][t] = ldT(s.wbase + par * SLOT, 32 * t + 4608); }
}
DI void read_a(GS& s, int set, unsigned ab) {
#pragma unroll
  for (int m = 0; m < 4; ++m) s.af[set][m] = ldA(ab, m * 16 * ROWB);
}
struct Epi { __amdgpu_buffer_rsrc_t rs; unsigned base, dstride, ownlds; int own, w, lane; };
template <int U> DI void epi_unit(f4 (&acc)[9][4], const Epi& e) {
  const int G = e.lane >> 4, c = e.lane & 15, lr = c & 7, dh = c >> 3;
  constexpr int ta = U < 16 ? 2 * (U >> 2) : 8, tb = U < 16 ? ta + 1 : 8, ma = U < 16 ? (U & 3) : 2 * (U - 16), mb = U < 16 ? ma : ma + 1;
  asm volatile("" : "+a"(acc[ta][ma]), "+a"(acc[tb][mb]));
  const f4 a = acc[ta][ma], b = acc[tb][mb];
  auto r0 = __builtin_amdgcn_permlane16_swap(pk(a[0], a[1]), pk(b[0], b[1]), false, false);
  auto r1 = __builtin_amdgcn_permlane16_swap(pk(a[2], a[3]), pk(b[2], b[3]), false, false);
  const u4 v = {r0[0], r1[0], r0[1], r1[1]};
  const int d = U < 16 ? 2 * ma + dh : 2 * (ma + (G & 1)) + dh;
  const unsigned off = U < 16 ? e.w * 2304 + (U >> 2) * 512 + lr * 64 + (2 * (G & 1) + (G >> 1)) * 16 : e.w * 2304 + 2048 + lr * 32 + (G >> 1) * 16;
  __builtin_amdgcn_raw_buffer_store_b128(v, e.rs, d == e.own ? 0x7ffffff0u : e.base + d * e.dstride + off, 0, 16);
  if ((U < 16 ? ma : ma >> 1) == (U < 16 ? e.own >> 1 : e.own >> 2)) { if (d == e.own) *(LAS u4*)(size_t)(e.ownlds + off) = v; }
}
template <bool NEXT, int VM> DI void final2(GS& s, f4 (&acc)[9][4], const unsigned (&nvoff)[9], const char* nx1, unsigned ab, const Epi& e) {
  const unsigned sl = s.ring + SLOT, wb = s.wbase + SLOT;
  asm volatile("s_waitcnt vmcnt(%0)" :: "i"(VM) : "memory");
#pragma unroll
  for (int m = 0; m < 4; ++m) s.af[1][m] = ldA(ab, m * 16 * ROWB);
  auto rdw = [&](auto ic) __attribute__((always_inline)) { constexpr int t = decltype(ic)::value; s.wlo[1][t] = ldT(wb, 32 * t); s.whi[1][t] = ldT(wb, 32 * t + 4608); };
  rdw(IC<0>{}); rdw(IC<1>{});
  GAP();
  auto grp = [&](auto gc) __attribute__((always_inline)) {
    constexpr int G = decltype(gc)::value, t = G >> 1, st = G & 1;
    auto mm = [&](auto mc) __attribute__((always_inline)) { constexpr int m = decltype(mc)::value; mfma16<true, false>(acc[t][m], cat(s.wlo[st][t], s.whi[st][t]), s.af[st][m]); };
    mm(IC<0>{});
    if constexpr (st == 0 && t + 2 < 9) rdw(IC<t + 2>{});
    if constexpr (NEXT && G == 15) { LGKM0(); setm0(sl + 4096); }
    GAP();
    constexpr int U = G >= 5 ? G - 5 : -1;
    mm(IC<1>{});
    if constexpr (U >= 0 && U < 16 && (U >> 2) * 4 + 5 <= G) epi_unit<(U >= 0 && U < 16) ? U : 0>(acc, e);
    GAP();
    mm(IC<2>{});
    if constexpr (NEXT && G == 16) { dma<-4096>(nvoff[0], nx1); dma<-3072>(nvoff[1], nx1); dma<-2048>(nvoff[2], nx1); dma<-1024>(nvoff[3], nx1); }
    if constexpr (NEXT && G == 17) { dma<0>(nvoff[4], nx1); dma<1024>(nvoff[5], nx1); dma<2048>(nvoff[6], nx1); dma<3072>(nvoff[7], nx1); setm0(sl + 8192); }
    GAP();
    mm(IC<3>{});
    GAP();
  };
  UNROLL(18, grp);
  if constexpr (NEXT) dma<0>(nvoff[8], nx1);
  asm volatile("s_nop 7" ::: "memory");
  epi_unit<13>(acc, e); epi_unit<14>(acc, e); epi_unit<15>(acc, e); epi_unit<16>(acc, e); epi_unit<17>(acc, e);
}
template <int NS, int NEXT, int NPF, typename PF> DI void gemm_rows(GS& s, f4 (&acc)[9][4], const unsigned (&voff)[9], const char* gW, unsigned ab_e, unsigned ab_o,
                                              const unsigned (&nvoff)[9], const char* nx0, const char* nx1, const Epi& e, PF&& pf) {
  constexpr size_t KS = (size_t)32 * ROWB;
  auto no = [&]() __attribute__((always_inline)) {};
  step<0, 0, true, true, true, true, 9, 0>(s, acc, voff, gW + 3 * KS, ab_o, no);
  step<1, 1, false, true, true, true, 9, 0>(s, acc, voff, gW + 4 * KS, ab_e + 128, pf);
  step<0, 0, false, true, true, true, 9 + NPF, 0>(s, acc, voff, gW + 5 * KS, ab_o + 128, no);
  step<1, 1, false, true, true, true, 9 + NPF, 0>(s, acc, voff, gW + 6 * KS, ab_e + 256, no);
  for (int k = 4; k < NS - 4; k += 2) {
    step<0, 0, false, true, true, true, 9, 0>(s, acc, voff, gW + (size_t)(k + 3) * KS, ab_o + 128 * (k >> 1), no);
    step<1, 1, false, true, true, true, 9, 0>(s, acc, voff, gW + (size_t)(k + 4) * KS, ab_e + 128 * ((k + 2) >> 1), no);
  }
  step<0, 0, false, true, true, true, 9, 0>(s, acc, voff, gW + (size_t)(NS - 1) * KS, ab_o + 128 * ((NS - 4) >> 1), no);
  step<1, 1, false, true, true, NEXT != 0, 9, 0>(s, acc, nvoff, nx0, ab_e + 128 * ((NS - 2) >> 1), no);
  final2<NEXT != 0, NEXT != 0 ? 9 : 0>(s, acc, nvoff, nx1, ab_o + 128 * ((NS - 2) >> 1), e);
}

constexpr unsigned SPIN_CAP = 1u << 17;
DI void publish(u64* flag, u64 tag) {
  asm volatile("s_waitcnt vmcnt(0)" ::: "memory");
  BAR();
  if (threadIdx.x == 0) __hip_atomic_store((gu64*)flag, tag, __ATOMIC_RELAXED, __HIP_MEMORY_SCOPE_AGENT);
}
DI void await8(u64* flags8, u64 tag, unsigned* tmo, int w, int lane) {
  if (w == 0) {
    for (unsigned spins = 0;; ++spins) {
      const u64 v = __hip_atomic_load((gu64*)(flags8 + (lane & 7) * FLAG_STRIDE), __ATOMIC_RELAXED, __HIP_MEMORY_SCOPE_AGENT);
      if (__all(v == tag)) break;
      if (spins > SPIN_CAP) { if (lane == 0) __hip_atomic_store((gu32*)tmo, 1u, __ATOMIC_RELAXED, __HIP_MEMORY_SCOPE_AGENT); break; }
      __builtin_amdgcn_s_sleep(1);
    }
  }
  BAR();
}
template <int NP, int NT> struct ModGather {
  static constexpr int N = (NP + NT - 1) / NT;
  u4 gr[N][2];
  __amdgpu_buffer_rsrc_t rM;
  DI void load(int tid) {
#pragma unroll
    for (int i = 0; i < N; ++i) {
      const int idx = tid + NT * i < NP ? tid + NT * i : (tid & 63), off = (idx >> 5) * 1024 + (idx & 31) * 16;
      gr[i][0] = __builtin_amdgcn_raw_buffer_load_b128(rM, off, 0, 16); gr[i][1] = __builtin_amdgcn_raw_buffer_load_b128(rM, off + 512, 0, 16);
    }
  }
  DI void issue(char* ws, int tid) { rM = __builtin_amdgcn_make_buffer_rsrc(ws + WS_MODP, 0, 54 * 1024, 0x00020000); load(tid); }
  DI void finish(u64 tag, unsigned ldsb, unsigned* tmo, int tid) {
    const unsigned thi = (unsigned)(tag >> 32), tlo = (unsigned)tag;
    for (unsigned spins = 0;; ++spins) {
      bool ok = true;
#pragma unroll
      for (int i = 0; i < N; ++i) ok &= gr[i][0][2] == tlo && gr[i][0][3] == thi && gr[i][1][2] == tlo && gr[i][1][3] == thi;
      if (__all(ok)) break;
      if (spins > SPIN_CAP / 8) { if ((tid & 63) == 0) __hip_atomic_store((gu32*)tmo, 2u, __ATOMIC_RELAXED, __HIP_MEMORY_SCOPE_AGENT); break; }
      __builtin_amdgcn_s_sleep(4);
      load(tid);
    }
#pragma unroll
    for (int i = 0; i < N; ++i)
      if (tid + NT * i < NP) {
        const int idx = tid + NT * i;
        const f2 m = (f2){__uint_as_float(gr[i][0][0]), __uint_as_float(gr[i][0][1])} + (f2){__uint_as_float(gr[i][1][0]), __uint_as_float(gr[i][1][1])};
        if (idx < D) {
          f2 v = rb2(m);
          if (idx >= D / 2) v = rb2(v + 1.f);
          *(LAS f2*)(size_t)(ldsb + LDS_X1 + 1024 + idx * 8) = v;
        } else *(LAS unsigned*)(size_t)(ldsb + LDS_MOD + idx * 4) = pk(m);
      }
  }
};
DI u4 gated_residual(const u4 (&v)[8], const u4& res, const u4& g) {
  u4 o;
#pragma unroll
  for (int e = 0; e < 4; ++e) {
    f2 s = bf2(v[0][e]);
#pragma unroll
    for (int j = 1; j < 8; ++j) s += bf2(v[j][e]);
    const unsigned t = pk(bf2(pk(s)) * bf2(g[e]));
    o[e] = pk(bf2(res[e]) + bf2(t));
  }
  return o;
}

constexpr float QSCALE = 0.08838834764831845f * 1.4426950408889634f;
constexpr float THR = 40.0f;
struct QS {
  b4 wlo[2][8], whi[2][8];
  b8 hf[2][4];
  unsigned wb[4];
  unsigned hb[4];
  unsigned ring;
};
template <int PAR, bool ZERO, bool RD, bool DMA, int VM, typename PF>
DI void qstep(QS& s, f16v (&acc)[4][2], const unsigned (&voff)[8], const char* gp, unsigned hoff, PF&& pf) {
  const unsigned sl = s.ring + (PAR ^ 1) * QSLOT;
  auto rd = [&](auto ic) __attribute__((always_inline)) {
    constexpr int I = decltype(ic)::value;
    if constexpr (RD) {
      if constexpr (I < 4) {
        s.hf[PAR ^ 1][I] = ldA(s.hb[2 * (PAR ^ 1) + (I >> 1)] + hoff, (I & 1) * 32 * ROWB);
      } else {
        constexpr int J = I - 4, f = J >> 1, t = J & 1, ks = f >> 2, dt = f & 3;
        const b4 v = ldT(s.wb[dt], (PAR ^ 1) * QSLOT + 4096 * ks + 1024 * t);
        if constexpr (t == 0) s.wlo[PAR ^ 1][f] = v; else s.whi[PAR ^ 1][f] = v;
      }
    }
  };
  auto gap = [&](auto gc) __attribute__((always_inline)) {
    constexpr int G = decltype(gc)::value, ks = G >> 3, dt = (G >> 1) & 3, qb = G & 1, f = ks * 4 + dt;
    mfma32<false, ZERO && ks == 0>(acc[dt][qb], cat(s.wlo[PAR][f], s.whi[PAR][f]), s.hf[PAR][ks * 2 + qb]);
    if constexpr (G == 0 && RD) asm volatile("s_waitcnt vmcnt(%0)" :: "i"(VM) : "memory");
    if constexpr (G <= 6) { rd(IC<3 * G>{}); rd(IC<3 * G + 1>{}); if constexpr (3 * G + 2 < 20) rd(IC<3 * G + 2>{}); }
    if constexpr (DMA) {
      if constexpr (G == 9) { LGKM0(); setm0(sl + 4096); }
      if constexpr (G >= 10 && G <= 13) { dma<(2 * (G - 10) - 4) * 1024>(voff[2 * (G - 10)], gp); dma<(2 * (G - 10) + 1 - 4) * 1024>(voff[2 * (G - 10) + 1], gp); }
    }
    if constexpr (G == 15) pf();
    GAP();
  };
  UNROLL(16, gap);
}

struct AT {
  f16v S[2][2][2];
  f16v negm[2];
  f16v O[4][2];
  b8 q[2][8];
  b8 P[2][4];
  float m[2], l[2], rs0[2], rs1[2], fct[2], pm0[2], pm1[2];
  float one;
  unsigned kb_[2], vb_[2];
  unsigned voff[8];
  unsigned m0k, m0v;
  const char *gk, *gv;
  bool pend;
};
template <int MODE> DI void qk(f16v& s, const b8& k, const b8& q, const f16v& negm) {
  if constexpr (MODE == 0) asm volatile("v_mfma_f32_32x32x16_bf16 %0, %1, %2, 0" : "=&v"(s) : "v"(k), "a"(q));
  else if constexpr (MODE == 1) asm volatile("v_mfma_f32_32x32x16_bf16 %0, %1, %2, %3" : "=&v"(s) : "v"(k), "a"(q), "v"(negm));
  else asm volatile("v_mfma_f32_32x32x16_bf16 %0, %1, %2, %0" : "+v"(s) : "v"(k), "a"(q));
}
template <int XP, int KB, int E> DI void sm_exp(AT& a) { constexpr int qb = E >> 4, r = E & 15; a.S[XP][KB][qb][r] = __builtin_amdgcn_exp2f(a.S[XP][KB][qb][r]); }
template <int XP, int KB, int E> DI void sm_add(AT& a) {
  constexpr int qb = E >> 4, r = E & 15;
  if constexpr (r & 1) a.rs1[qb] = __builtin_fmaf(a.S[XP][KB][qb][r], a.one, a.rs1[qb]); else a.rs0[qb] += a.S[XP][KB][qb][r];
}
template <int XP, int C> DI void sm_pack(AT& a) {
  constexpr int kb = C >> 4, qb = (C >> 3) & 1, s = (C >> 2) & 1, p = C & 3;
  u4 w = __builtin_bit_cast(u4, a.P[qb][kb * 2 + s]);
  w[p] = pk(a.S[XP][kb][qb][8 * s + 2 * p], a.S[XP][kb][qb][8 * s + 2 * p + 1]);
  a.P[qb][kb * 2 + s] = __builtin_bit_cast(b8, w);
  if constexpr (p == 3) asm volatile("" : "+v"(a.P[qb][kb * 2 + s]));
}
template <int X, bool FIRST, bool DK, bool DV, typename PF> DI void phaseA(AT& a, int t, PF&& pf) {
  constexpr int XP = X ^ 1;
  const unsigned kslot = X * 16384;
  b8 kf[16];
  auto kread = [&](auto ic) __attribute__((always_inline)) { constexpr int I = decltype(ic)::value, ks = I >> 1, kb = I & 1; kf[I] = ldA(a.kb_[ks & 1], kslot + 8192 * kb + 512 * (ks >> 1)); };
  kread(IC<0>{}); kread(IC<1>{}); kread(IC<2>{}); kread(IC<3>{});
  const char* gk = a.gk + (size_t)(t + 1) * 16384; const char* gv = a.gv + (size_t)t * 16384;
  auto gap = [&](auto gc) __attribute__((always_inline)) {
    constexpr int G = decltype(gc)::value, ks = G >> 2, kb = (G >> 1) & 1, qb = G & 1, I = G >> 1;
    qk<(ks == 0) ? (FIRST ? 0 : 1) : 2>(a.S[X][kb][qb], kf[I], a.q[qb][ks], a.negm[qb]);
    if constexpr ((G & 1) == 0 && I + 4 < 16) kread(IC<I + 4>{});
    if constexpr (!FIRST && G == 8) asm volatile("" :: "v"(a.negm[0]), "v"(a.negm[1]));
    if constexpr (DK) { if constexpr (G == 0) setm0(a.m0k + (X ^ 1) * 16384); if constexpr ((G & 1) && G < 16) dma<((G >> 1) - 4) * 1024>(a.voff[G >> 1], gk); }
    if constexpr (DV) { if constexpr (G == 16) setm0(a.m0v + X * 16384); if constexpr ((G & 1) && G > 16) dma<(((G - 16) >> 1) - 4) * 1024>(a.voff[(G - 16) >> 1], gv); }
    if constexpr (!FIRST) {
      if constexpr (G < 16) { sm_exp<XP, 1, 2 * G>(a); sm_exp<XP, 1, 2 * G + 1>(a); sm_pack<XP, G>(a); }
      else { sm_pack<XP, G>(a); sm_add<XP, 1, 2 * (G - 16)>(a); sm_add<XP, 1, 2 * (G - 16) + 1>(a); }
    }
    GAP();
  };
  UNROLL(32, gap);
  pf();
  if constexpr (!FIRST) {
#pragma unroll
    for (int qb = 0; qb < 2; ++qb) { a.l[qb] += a.rs0[qb] + a.rs1[qb]; asm volatile("" : "+v"(a.l[qb])); }
  }
}
DI float swap32_max(float x) { auto r = __builtin_amdgcn_permlane32_swap(__float_as_uint(x), __float_as_uint(x), false, false); return fmaxx(__uint_as_float(r[0]), __uint_as_float(r[1])); }
template <int X, bool NOPV, bool ZERO> DI void phaseB(AT& a) {
  const unsigned vslot = (X ^ 1) * 16384;
  b4 vlo[16], vhi[16];
  auto vread = [&](auto ic) __attribute__((always_inline)) {
    constexpr int I = decltype(ic)::value, k4 = I >> 2, db = I & 3;
    vlo[I] = ldT(a.vb_[0], vslot + 2048 * (2 * k4) + 512 * db);
    vhi[I] = ldT(a.vb_[1], vslot + 2048 * (2 * k4 + 1) + 512 * db);
  };
  if constexpr (!NOPV) { vread(IC<0>{}); vread(IC<1>{}); vread(IC<2>{}); }
  float rmax[2];
  auto pv = [&](auto gc) __attribute__((always_inline)) {
    constexpr int G = decltype(gc)::value, I = G >> 1, k4 = I >> 2, db = I & 3, qb = G & 1;
    if constexpr (!NOPV) {
      mfma32<true, ZERO && k4 == 0>(a.O[db][qb], cat(vlo[I], vhi[I]), a.P[qb][k4]);
      if constexpr ((G & 1) == 0 && I + 3 < 16) vread(IC<I + 3>{});
    }
  };
  auto gap1 = [&](auto gc) __attribute__((always_inline)) {
    constexpr int G = decltype(gc)::value;
    pv(gc);
    if constexpr (G >= 4 && G < 12) {
      constexpr int i = G - 4, q = i >> 2, r = 4 * (i & 3);
      if constexpr ((i & 3) == 0) { a.pm0[q] = fmax3(a.S[X][0][q][0], a.S[X][0][q][1], a.S[X][1][q][0]); a.pm1[q] = fmax3(a.S[X][0][q][2], a.S[X][0][q][3], a.S[X][1][q][1]);
                                    a.pm0[q] = fmax3(a.pm0[q], a.S[X][1][q][2], a.S[X][1][q][3]); }
      else { a.pm0[q] = fmax3(a.pm0[q], a.S[X][0][q][r], a.S[X][0][q][r + 1]); a.pm1[q] = fmax3(a.pm1[q], a.S[X][0][q][r + 2], a.S[X][0][q][r + 3]);
             a.pm0[q] = fmax3(a.pm0[q], a.S[X][1][q][r], a.S[X][1][q][r + 1]); a.pm1[q] = fmax3(a.pm1[q], a.S[X][1][q][r + 2], a.S[X][1][q][r + 3]); }
    }
    if constexpr (G == 12 || G == 13) { constexpr int q = G - 12; rmax[q] = swap32_max(fmaxx(a.pm0[q], a.pm1[q])); }
    GAP();
  };
  UNROLL(14, gap1);
  if constexpr (NOPV) {
#pragma unroll
    for (int q = 0; q < 2; ++q) {
      a.m[q] = rmax[q]; a.l[q] = 0.f; a.fct[q] = 1.f;
#pragma unroll
      for (int kb = 0; kb < 2; ++kb)
#pragma unroll
        for (int e = 0; e < 16; ++e) a.S[X][kb][q][e] -= rmax[q];
#pragma unroll
      for (int e = 0; e < 16; ++e) a.negm[q][e] = -rmax[q];
      asm volatile("" : "+v"(a.negm[q]));
    }
  } else {
    const bool grew = (rmax[0] > THR) | (rmax[1] > THR);
    if (__builtin_expect(__any((int)grew), 0)) {
#pragma unroll
      for (int q = 0; q < 2; ++q) {
        const float dl = fmaxx(rmax[q], 0.f); a.m[q] += dl; a.fct[q] = __builtin_amdgcn_exp2f(-dl);
#pragma unroll
        for (int kb = 0; kb < 2; ++kb)
#pragma unroll
          for (int e = 0; e < 16; ++e) a.S[X][kb][q][e] -= dl;
#pragma unroll
        for (int e = 0; e < 16; ++e) a.negm[q][e] = -a.m[q];
        asm volatile("" : "+v"(a.negm[q]));
      }
      a.pend = true;
    }
  }
  a.rs0[0] = a.rs0[1] = a.rs1[0] = a.rs1[1] = 0.f;
  GAP();
  auto gap2 = [&](auto gc) __attribute__((always_inline)) {
    constexpr int G = decltype(gc)::value + 14;
    pv(IC<G>{});
    if constexpr (G >= 15) {
      constexpr int e0 = 2 * (G - 15);
      if constexpr (e0 < 32) { sm_exp<X, 0, e0>(a); sm_exp<X, 0, e0 + 1>(a); }
      if constexpr (G >= 16) { sm_add<X, 0, e0 - 2>(a); sm_add<X, 0, e0 - 1>(a); }
    }
    GAP();
  };
  UNROLL(18, gap2);
  asm volatile("" : "+v"(a.S[X][0][0]), "+v"(a.S[X][0][1]), "+v"(a.rs0[0]), "+v"(a.rs0[1]), "+v"(a.rs1[0]), "+v"(a.rs1[1]));
  if constexpr (!NOPV) {
    if (__builtin_expect(a.pend, 0)) {
      asm volatile("s_nop 7\n\ts_nop 7" ::: "memory");
#pragma unroll
      for (int db = 0; db < 4; ++db)
#pragma unroll
        for (int q = 0; q < 2; ++q) {
          asm volatile("" : "+a"(a.O[db][q]));
          f16v o = a.O[db][q];
#pragma unroll
          for (int e = 0; e < 16; ++e) o[e] *= a.fct[q];
          a.O[db][q] = o;
          asm volatile("" : "+a"(a.O[db][q]));
        }
      a.l[0] *= a.fct[0]; a.l[1] *= a.fct[1]; a.pend = false;
      asm volatile("s_nop 1" ::: "memory");
    }
  }
}

template <int X, bool FIRST, bool DK, bool DV, typename PF> DI void fastA(AT& a, int t, PF&& pf) {
  constexpr int XP = X ^ 1;
  const unsigned kslot = X * 16384;
  b8 kf[16];
  auto kread = [&](auto ic) __attribute__((always_inline)) { constexpr int I = decltype(ic)::value, ks = I >> 1, kb = I & 1; kf[I] = ldA(a.kb_[ks & 1], kslot + 8192 * kb + 512 * (ks >> 1)); };
  kread(IC<0>{}); kread(IC<1>{}); kread(IC<2>{}); kread(IC<3>{});
  const char* gk = a.gk + (size_t)(t + 1) * 16384; const char* gv = a.gv + (size_t)t * 16384;
  auto gap = [&](auto gc) __attribute__((always_inline)) {
    constexpr int G = decltype(gc)::value, ks = G >> 2, kb = (G >> 1) & 1, qb = G & 1, I = G >> 1;
    qk<(ks == 0) ? (FIRST ? 0 : 1) : 2>(a.S[X][kb][qb], kf[I], a.q[qb][ks], a.negm[qb]);
    if constexpr ((G & 1) == 0 && I + 4 < 16) kread(IC<I + 4>{});
    if constexpr (!FIRST && G == 8) asm volatile("" :: "v"(a.negm[0]), "v"(a.negm[1]));
    if constexpr (DK) { if constexpr (G == 0) setm0(a.m0k + (X ^ 1) * 16384); if constexpr ((G & 1) && G < 16) dma<((G >> 1) - 4) * 1024>(a.voff[G >> 1], gk); }
    if constexpr (DV) { if constexpr (G == 16) setm0(a.m0v + X * 16384); if constexpr ((G & 1) && G > 16) dma<(((G - 16) >> 1) - 4) * 1024>(a.voff[(G - 16) >> 1], gv); }
    if constexpr (!FIRST) {
      sm_exp<XP, 1, G>(a);
      if constexpr (G >= 1) sm_add<XP, 1, G - 1>(a);
      if constexpr (G >= 3 && (G & 1)) sm_pack<XP, 16 + (G - 3) / 2>(a);
    }
    GAP();
  };
  UNROLL(32, gap);
  pf();
  if constexpr (!FIRST) {
    sm_add<XP, 1, 31>(a); sm_pack<XP, 31>(a);
#pragma unroll
    for (int qb = 0; qb < 2; ++qb) { a.l[qb] += a.rs0[qb] + a.rs1[qb]; asm volatile("" : "+v"(a.l[qb])); }
  }
}
constexpr int fb_e(int G) { return G < 2 ? 0 : G - 2 + (G > 5) + (G > 9); }
constexpr int dr_e(int n) { return n < 8 ? n : n < 16 ? n + 8 : n < 24 ? n - 8 : n; }
template <int X, int MODE> DI void fastB(AT& a) {
  constexpr bool ZERO = MODE == 1, DRAIN = MODE == 3;
  if constexpr (MODE == 2) {
#pragma unroll
    for (int q = 0; q < 2; ++q) {
      float p0 = fmax3(a.S[X][0][q][0], a.S[X][1][q][0], a.S[X][0][q][2]), p1 = fmax3(a.S[X][0][q][1], a.S[X][1][q][1], a.S[X][0][q][3]);
      p0 = fmaxx(p0, a.S[X][1][q][2]); p1 = fmaxx(p1, a.S[X][1][q][3]);
#pragma unroll
      for (int r = 4; r < 16; r += 2) { p0 = fmax3(p0, a.S[X][0][q][r], a.S[X][1][q][r]); p1 = fmax3(p1, a.S[X][0][q][r + 1], a.S[X][1][q][r + 1]); }
      const float mx = swap32_max(fmaxx(p0, p1));
      f2 m2 = {mx, mx}; asm volatile("" : "+v"(m2));
      a.l[q] = 0.f;
#pragma unroll
      for (int kb = 0; kb < 2; ++kb)
#pragma unroll
        for (int e = 0; e < 16; e += 2) { const f2 v = (f2){a.S[X][kb][q][e], a.S[X][kb][q][e + 1]} - m2; a.S[X][kb][q][e] = v[0]; a.S[X][kb][q][e + 1] = v[1]; }
#pragma unroll
      for (int e = 0; e < 16; ++e) a.negm[q][e] = -mx;
      asm volatile("" : "+v"(a.negm[q]));
    }
    a.rs0[0] = a.rs0[1] = a.rs1[0] = a.rs1[1] = 0.f;
    { auto f = [&](auto ic) __attribute__((always_inline)) { sm_exp<X, 0, decltype(ic)::value>(a); }; UNROLL(32, f); }
    { auto f = [&](auto ic) __attribute__((always_inline)) { sm_add<X, 0, decltype(ic)::value>(a); }; UNROLL(32, f); }
    { auto f = [&](auto ic) __attribute__((always_inline)) { sm_pack<X, decltype(ic)::value>(a); }; UNROLL(16, f); }
  } else {
    const unsigned vslot = (DRAIN ? X : X ^ 1) * 16384;
    b4 vlo[16], vhi[16];
    auto vread = [&](auto ic) __attribute__((always_inline)) {
      constexpr int I = decltype(ic)::value, k4 = I >> 2, db = I & 3;
      vlo[I] = ldT(a.vb_[0], vslot + 2048 * (2 * k4) + 512 * db);
      vhi[I] = ldT(a.vb_[1], vslot + 2048 * (2 * k4 + 1) + 512 * db);
    };
    vread(IC<0>{}); vread(IC<1>{}); vread(IC<2>{});
    if constexpr (!DRAIN) a.rs0[0] = a.rs0[1] = a.rs1[0] = a.rs1[1] = 0.f;
    auto gap = [&](auto gc) __attribute__((always_inline)) {
      constexpr int G = decltype(gc)::value, I = G >> 1, k4 = I >> 2, db = I & 3, qb = G & 1;
      mfma32<true, ZERO && k4 == 0>(a.O[db][qb], cat(vlo[I], vhi[I]), a.P[qb][k4]);
      if constexpr ((G & 1) == 0 && I + 3 < 16) vread(IC<I + 3>{});
      if constexpr (DRAIN) {
        if constexpr (G < 16) { sm_exp<X, 1, dr_e(2 * G)>(a); sm_exp<X, 1, dr_e(2 * G) + 1>(a); }
        if constexpr (G >= 2 && G < 18) sm_pack<X, 16 + dr_e(2 * (G - 2)) / 2>(a);
        if constexpr (G >= 16) { sm_add<X, 1, dr_e(2 * (G - 16))>(a); sm_add<X, 1, dr_e(2 * (G - 16)) + 1>(a); }
      } else {
        if constexpr (fb_e(G) < fb_e(G + 1)) sm_exp<X, 0, fb_e(G)>(a);
        if constexpr (fb_e(G) + 1 < fb_e(G + 1)) sm_exp<X, 0, fb_e(G) + 1>(a);
        if constexpr (G >= 3 && fb_e(G - 1) < fb_e(G)) sm_add<X, 0, G >= 3 ? fb_e(G - 1) : 0>(a);
        if constexpr (G >= 3 && fb_e(G - 1) + 1 < fb_e(G)) sm_add<X, 0, G >= 3 ? fb_e(G - 1) + 1 : 0>(a);
        if constexpr (G >= 16 && G <= 29) sm_pack<X, G >= 16 ? G - 16 : 0>(a);
        if constexpr (G == 31) sm_pack<X, 14>(a);
      }
      GAP();
    };
    UNROLL(32, gap);
    if constexpr (!DRAIN) {
      sm_add<X, 0, 31>(a);
      static_assert(fb_e(32) == 32 && fb_e(28) >= 28 && fb_e(30) >= 30, "schedule");
      sm_pack<X, 15>(a);
      asm volatile("" : "+v"(a.S[X][0][0]), "+v"(a.S[X][0][1]), "+v"(a.rs0[0]), "+v"(a.rs0[1]), "+v"(a.rs1[0]), "+v"(a.rs1[1]));
    } else {
#pragma unroll
      for (int qb = 0; qb < 2; ++qb) a.l[qb] += a.rs0[qb] + a.rs1[qb];
    }
  }
}

struct Params { const u16 *x, *c, *wmod, *wq, *kc, *vc, *wo, *wg, *wu, *wd; u16* out; char* ws; float one; unsigned gap; };

__global__ __launch_bounds__(256, 1) void k_main(Params p) {
  extern __shared__ __attribute__((aligned(16))) char lds[];
  const int tid = threadIdx.x, w = __builtin_amdgcn_readfirstlane(tid >> 6), lane = tid & 63;
  const int j = blockIdx.x & 7, c = blockIdx.x >> 3;
  const unsigned ldsb = (unsigned)(size_t)lds;
  const int hh = lane >> 5, r32 = lane & 31;
  u64* const flags = (u64*)(p.ws + WS_FLAG);
  unsigned* const tmo = (unsigned*)(p.ws + WS_TMO);
  STAMP(0);
  PST(0);
  QS qs;
  unsigned voffQ[8];
#pragma unroll
  for (int i = 0; i < 8; ++i) voffQ[i] = (4 * i + (lane >> 4)) * 8192 + 16 * ((lane & 15) ^ ((lane >> 4) << 2)) - (i - 4) * 1024 + 4096;
  const char* gq = (const char*)p.wq + (size_t)(4 * j + w) * 256 - 4096;
  qs.ring = __builtin_amdgcn_readfirstlane(ldsb + ABYTES + w * 2 * QSLOT);
  constexpr size_t KS_Q = (size_t)32 * 8192;
  auto issueq = [&](int slot, const char* gp) __attribute__((always_inline)) {
    setm0(qs.ring + slot * QSLOT + 4096); asm volatile("s_nop 0");
    dma<-4096>(voffQ[0], gp); dma<-3072>(voffQ[1], gp); dma<-2048>(voffQ[2], gp); dma<-1024>(voffQ[3], gp);
    dma<0>(voffQ[4], gp); dma<1024>(voffQ[5], gp); dma<2048>(voffQ[6], gp); dma<3072>(voffQ[7], gp);
  };
  const int mg = blockIdx.x % 54, ms = (blockIdx.x / 54) & 1;
  u4 wm[9]; u16 cval[9];
#pragma unroll
  for (int i = 0; i < 9; ++i) {
    const int r = 288 * ms + (tid >> 3) + 32 * i;
    wm[i] = *(const u4*)(p.wmod + (size_t)r * (6 * D) + mg * 64 + (tid & 7) * 8); cval[i] = p.c[r];
  }
  const u64 e0 = __hip_atomic_load((gu64*)(p.ws + WS_EPOCH), __ATOMIC_RELAXED, __HIP_MEMORY_SCOPE_AGENT);
  AT a;
  u64 tag;
  {
    f16v Qt[4][2];
    QS& s = qs;
    {
      const int rr = lane >> 3, sub = lane & 7;
      tag = ((u64)(unsigned)__builtin_amdgcn_readfirstlane((unsigned)(e0 >> 32)) << 32 | (u64)(unsigned)__builtin_amdgcn_readfirstlane((unsigned)e0)) + 1;
      PST(9);
      if (blockIdx.x < 108) {
        float acc[8] = {};
#pragma unroll
        for (int i = 0; i < 9; ++i) {
          const float xc = bf2f(cval[i]);
          const float sc = rbf(xc * __builtin_amdgcn_rcpf(1.f + __expf(-xc)));
#pragma unroll
          for (int e = 0; e < 4; ++e) { acc[2 * e] += sc * bflo(wm[i][e]); acc[2 * e + 1] += sc * bfhi(wm[i][e]); }
        }
        LAS float* red = (LAS float*)(size_t)(ldsb + LDS_X1);
#pragma unroll
        for (int e = 0; e < 8; ++e) { float t = acc[e]; t += dpp<0x128>(t); t = swap32_add(swap16_add(t)); if (lane < 8) red[w * 64 + lane * 8 + e] = t; }
        asm volatile("s_waitcnt lgkmcnt(0)" ::: "memory");
        BAR();
        if (tid < 32) {
          f2 v = {0.f, 0.f};
#pragma unroll
          for (int k = 0; k < 4; ++k) v += *(LAS f2*)(red + k * 64 + 2 * tid);
          const __amdgpu_buffer_rsrc_t rP = __builtin_amdgcn_make_buffer_rsrc(p.ws + WS_MODP, 0, 54 * 1024, 0x00020000);
          __builtin_amdgcn_raw_buffer_store_b128((u4){__float_as_uint(v[0]), __float_as_uint(v[1]), (unsigned)tag, (unsigned)(tag >> 32)}, rP, mg * 1024 + ms * 512 + tid * 16, 0, 16);
        }
      }
      PST(10);
      PST(11);
      {
        const u64 t0_ = __builtin_amdgcn_s_memrealtime(); const u64 dl_ = (u64)((c * STAG_N) >> 5) * STAG_D;
        while (__builtin_amdgcn_s_memrealtime() - t0_ < dl_) __builtin_amdgcn_s_sleep(2);
      }
      u4 xv[2][9];
      GAP();
#pragma unroll
      for (int ps = 0; ps < 2; ++ps)
#pragma unroll
        for (int i = 0; i < 9; ++i) xv[ps][i] = *(const u4*)(p.x + (size_t)(c * 64 + 16 * w + 8 * ps + rr) * D + (sub + 8 * i) * 8);
      GAP();
      PST(1);
      STAMP(20);
      PST(2);
      f2 y[2][9][4];
      ModGather<1728, 256> mgat;
      {
#pragma unroll
        for (int ps = 0; ps < 2; ++ps) {
          f2 a1 = {0.f, 0.f}, a2 = {0.f, 0.f};
#pragma unroll
          for (int i = 0; i < 9; ++i)
#pragma unroll
            for (int e = 0; e < 4; ++e) { y[ps][i][e] = bf2(xv[ps][i][e]); a1 += y[ps][i][e]; a2 = __builtin_elementwise_fma(y[ps][i][e], y[ps][i][e], a2); }
          float s1 = a1[0] + a1[1], s2 = a2[0] + a2[1];
          s1 = sum8(s1); s2 = sum8(s2);
          const float mean = s1 * (1.f / D), var = __builtin_fmaxf(__builtin_fmaf(-mean, mean, s2 * (1.f / D)), 0.f);
          const float mr = rbf(mean), rs = __builtin_amdgcn_rcpf(rbf(__builtin_amdgcn_sqrtf(rbf(rbf(var) + 1e-5f))));
          f2 mr2 = {mr, mr}, rs2 = {rs, rs}; asm volatile("" : "+v"(mr2), "+v"(rs2));
          if (ps == 1) { GAP(); mgat.issue(p.ws, tid); GAP(); }
#pragma unroll
          for (int i = 0; i < 9; ++i) {
            f2 t[4];
#pragma unroll
            for (int e = 0; e < 4; ++e) t[e] = y[ps][i][e] - mr2;
#pragma unroll
            for (int e = 0; e < 4; ++e) t[e] = rb2(t[e]);
#pragma unroll
            for (int e = 0; e < 4; ++e) t[e] = t[e] * rs2;
#pragma unroll
            for (int e = 0; e < 4; ++e) y[ps][i][e] = rb2(t[e]);
            asm volatile("" : "+v"(y[ps][i][0]), "+v"(y[ps][i][1]), "+v"(y[ps][i][2]), "+v"(y[ps][i][3]));
          }
        }
      }
      STAMP(21);
      PST(4);
      mgat.finish(tag, ldsb, tmo, tid);
      STAMP(22);
      PST(5);
      issueq(0, gq); issueq(1, gq + KS_Q);
      {
        const int li = tid < 144 ? tid : tid - 112;
        touch((const char*)p.wq + (size_t)512 * j * 2, (18 * c + (li >> 3)) * 8192 + (li & 7) * 128, __builtin_amdgcn_readfirstlane(ldsb + LDS_DUMMY));
      }
      asm volatile("s_waitcnt lgkmcnt(0)" ::: "memory");
      BAR();
      STAMP(23);
      PST(6);
      {
        const unsigned tb = ldsb + LDS_X1 + 1024 + sub * 32;
        f4 cst[2][4];
        auto ldc = [&](int i, f4 (&d)[4]) { d[0] = *(LAS f4*)(size_t)(tb + i * 256); d[1] = *(LAS f4*)(size_t)(tb + i * 256 + 16); d[2] = *(LAS f4*)(size_t)(tb + i * 256 + D * 4); d[3] = *(LAS f4*)(size_t)(tb + i * 256 + D * 4 + 16); };
        ldc(0, cst[0]);
        const unsigned ad0 = ldsb + (16 * w + rr) * ROWB + ((sub ^ ((rr >> 1) & 7)) * 16);
        const unsigned ad1 = ldsb + (16 * w + 8 + rr) * ROWB + ((sub ^ (((rr >> 1) + 4) & 7)) * 16);
#pragma unroll
        for (int i = 0; i < 9; ++i) {
          if (i + 1 < 9) ldc(i + 1, cst[(i + 1) & 1]);
          GAP();
          const f4 (&k)[4] = cst[i & 1];
          const f2 sh[4] = {{k[0][0], k[0][1]}, {k[0][2], k[0][3]}, {k[1][0], k[1][1]}, {k[1][2], k[1][3]}}, cc[4] = {{k[2][0], k[2][1]}, {k[2][2], k[2][3]}, {k[3][0], k[3][1]}, {k[3][2], k[3][3]}};
          f2 t[2][4]; u4 o[2];
#pragma unroll
          for (int ps = 0; ps < 2; ++ps)
#pragma unroll
            for (int e = 0; e < 4; ++e) t[ps][e] = y[ps][i][e] * cc[e];
#pragma unroll
          for (int ps = 0; ps < 2; ++ps)
#pragma unroll
            for (int e = 0; e < 4; ++e) t[ps][e] = rb2(t[ps][e]);
#pragma unroll
          for (int ps = 0; ps < 2; ++ps)
#pragma unroll
            for (int e = 0; e < 4; ++e) t[ps][e] = t[ps][e] + sh[e];
#pragma unroll
          for (int ps = 0; ps < 2; ++ps)
#pragma unroll
            for (int e = 0; e < 4; ++e) o[ps][e] = pk(t[ps][e]);
          *(LAS u4*)(size_t)(ad0 + i * 128) = o[0];
          *(LAS u4*)(size_t)(ad1 + i * 128) = o[1];
          GAP();
        }
      }
    }
    {
      const int blk = (lane >> 4) & 1, q = (lane & 15) >> 2, pp = lane & 3;
#pragma unroll
      for (int dt = 0; dt < 4; ++dt) s.wb[dt] = s.ring + (8 * hh + q) * 256 + 64 * (dt ^ q) + 32 * blk + 16 * (pp & 1) + 8 * (pp >> 1);
#pragma unroll
      for (int i = 0; i < 4; ++i) s.hb[i] = ldsb + r32 * ROWB + (((2 * i + hh) ^ ((r32 >> 1) & 7)) * 16);
    }
    asm volatile("s_waitcnt vmcnt(9) lgkmcnt(0)" ::: "memory");
    BAR();
    STAMP(1);
    PST(7);
#pragma unroll
    for (int f = 0; f < 8; ++f) { s.wlo[0][f] = ldT(s.wb[f & 3], 4096 * (f >> 2)); s.whi[0][f] = ldT(s.wb[f & 3], 4096 * (f >> 2) + 1024); }
#pragma unroll
    for (int i = 0; i < 4; ++i) s.hf[0][i] = ldA(s.hb[i >> 1], (i & 1) * 32 * ROWB);
    asm volatile("s_waitcnt lgkmcnt(0)" ::: "memory");
    issueq(0, gq + 2 * KS_Q);
    auto no = [&]() __attribute__((always_inline)) {};
    auto pfkv = [&](int h) __attribute__((always_inline)) {
      if (lane < 16) touch((const char*)(w < 2 ? p.kc : p.vc) + ((size_t)(2 * j + (w & 1)) * SK + (SK - WIN)) * DH * 2, (32 * c + 16 * h + lane) * 128, __builtin_amdgcn_readfirstlane(ldsb + LDS_DUMMY));
    };
    qstep<0, true, true, true, 9>(s, Qt, voffQ, gq + 3 * KS_Q, 0, no);
    qstep<1, false, true, true, 8>(s, Qt, voffQ, gq + 4 * KS_Q, 128, [&]() __attribute__((always_inline)) { pfkv(0); });
    qstep<0, false, true, true, 9>(s, Qt, voffQ, gq + 5 * KS_Q, 128, no);
    qstep<1, false, true, true, 9>(s, Qt, voffQ, gq + 6 * KS_Q, 256, [&]() __attribute__((always_inline)) { pfkv(1); });
    qstep<0, false, true, true, 9>(s, Qt, voffQ, gq + 7 * KS_Q, 256, no);
    qstep<1, false, true, true, 9>(s, Qt, voffQ, gq + 8 * KS_Q, 384, no);
    for (int n = 6; n < 14; n += 2) {
      qstep<0, false, true, true, 8>(s, Qt, voffQ, gq + (size_t)(n + 3) * KS_Q, 128 * ((n + 1) >> 1), no);
      qstep<1, false, true, true, 8>(s, Qt, voffQ, gq + (size_t)(n + 4) * KS_Q, 128 * ((n + 2) >> 1), no);
    }
    qstep<0, false, true, true, 8>(s, Qt, voffQ, gq + (size_t)17 * KS_Q, 128 * 7, no);
    qstep<1, false, true, false, 8>(s, Qt, voffQ, gq, 128 * 8, no);
    qstep<0, false, true, false, 0>(s, Qt, voffQ, gq, 128 * 8, no);
    qstep<1, false, false, false, 0>(s, Qt, voffQ, gq, 0, no);
    STAMP(2);
    PST(8);
    {
      const int kvl = w >> 1, half = w & 1;
      const unsigned kr = ldsb + kvl * 65536, vr = kr + 32768;
#pragma unroll
      for (int i = 0; i < 2; ++i) a.kb_[i] = kr + 2048 * (r32 >> 3) + 64 * (r32 & 7) + 16 * ((2 * i + hh) ^ ((r32 >> 2) & 3));
      const int blk = (lane >> 4) & 1, q = (lane & 15) >> 2, pp = lane & 3;
#pragma unroll
      for (int t = 0; t < 2; ++t) a.vb_[t] = vr + 64 * (4 * hh + q) + 16 * ((2 * blk + (pp >> 1)) ^ ((2 * t + hh) & 3)) + 8 * (pp & 1);
      const int st = lane >> 5, r7 = (lane >> 2) & 7, pos = lane & 3;
#pragma unroll
      for (int i = 0; i < 8; ++i) {
        const int rg = 4 * half + (i >> 1);
        a.voff[i] = rg * 2048 + r7 * 256 + (i & 1) * 128 + st * 64 + 16 * (pos ^ ((2 * (rg & 1) + (r7 >> 2)) & 3)) - (i - 4) * 1024 + 4096;
      }
      a.m0k = __builtin_amdgcn_readfirstlane(kr + 8192 * half + 4096); a.m0v = __builtin_amdgcn_readfirstlane(vr + 8192 * half + 4096);
      const size_t kvoff = ((size_t)(2 * j + kvl) * SK + (SK - WIN)) * DH * 2;
      a.gk = (const char*)p.kc + kvoff - 4096; a.gv = (const char*)p.vc + kvoff - 4096;
      a.pend = false; a.one = p.one;
    }
    asm volatile("s_waitcnt lgkmcnt(0)" ::: "memory");
    BAR();
    setm0(a.m0k); asm volatile("s_nop 0");
    dma<-4096>(a.voff[0], a.gk); dma<-3072>(a.voff[1], a.gk); dma<-2048>(a.voff[2], a.gk); dma<-1024>(a.voff[3], a.gk);
    dma<0>(a.voff[4], a.gk); dma<1024>(a.voff[5], a.gk); dma<2048>(a.voff[6], a.gk); dma<3072>(a.voff[7], a.gk);
    asm volatile("s_nop 7" ::: "memory");
#pragma unroll
    for (int dt = 0; dt < 4; ++dt)
#pragma unroll
      for (int qb = 0; qb < 2; ++qb) {
        asm volatile("" : "+v"(Qt[dt][qb]));
#pragma unroll
        for (int s2 = 0; s2 < 2; ++s2) {
          u4 w4;
#pragma unroll
          for (int e = 0; e < 4; ++e) w4[e] = pk((f2){Qt[dt][qb][8 * s2 + 2 * e], Qt[dt][qb][8 * s2 + 2 * e + 1]} * QSCALE);
          a.q[qb][2 * dt + s2] = __builtin_bit_cast(b8, w4);
          asm volatile("" : "+a"(a.q[qb][2 * dt + s2]));
        }
      }
  }
  STAMP(3);
  {
    auto tile_bar = [&]() __attribute__((always_inline)) { asm volatile("s_waitcnt vmcnt(0) lgkmcnt(0)" ::: "memory"); BAR(); };
    const char* pfp = lane >= 32 ? p.ws + WS_X + (size_t)c * 64 * SLICE + (size_t)j * SLICE + (18 * w + lane - 32) * 128
                    : w < 2 ? (const char*)(w == 0 ? p.wg : p.wu) + (size_t)576 * j * 2 + (size_t)(18 * c + lane / 9 - 3) * (DF * 2) + (lane % 9) * 128
                            : (const char*)p.wo + (size_t)512 * j * ROWB + (16 * c) * ROWB + (12 * (w - 2) + lane - 24) * 128;
    const int pfs = lane >= 32 ? 8 * SLICE : w < 2 ? 3 * DF * 2 : 24 * 128, pfn = w < 2 ? 27 : 12;
    auto pft = [&](int t) __attribute__((always_inline)) {
      const bool on = lane < pfn && t >= 1 && t <= 6;
      touchp(on ? pfp : (const char*)p.wo, __builtin_amdgcn_readfirstlane(ldsb + LDS_DUMMY));
      pfp += pfs;
    };
    auto tile_bar1 = [&]() __attribute__((always_inline)) { asm volatile("s_waitcnt vmcnt(1) lgkmcnt(0)" ::: "memory"); BAR(); };
    LAS unsigned* flag = (LAS unsigned*)(size_t)(ldsb + LDS_X1);
    if (tid == 0) *flag = 0u;
    auto k0 = [&]() __attribute__((always_inline)) {
      setm0(a.m0k); asm volatile("s_nop 0");
      dma<-4096>(a.voff[0], a.gk); dma<-3072>(a.voff[1], a.gk); dma<-2048>(a.voff[2], a.gk); dma<-1024>(a.voff[3], a.gk);
      dma<0>(a.voff[4], a.gk); dma<1024>(a.voff[5], a.gk); dma<2048>(a.voff[6], a.gk); dma<3072>(a.voff[7], a.gk);
    };
    tile_bar(); fastA<0, true, true, true>(a, 0, [&]() __attribute__((always_inline)) { pft(0); }); asm volatile("s_nop 7\n\ts_nop 3" ::: "memory"); GAP(); fastB<0, 2>(a);
    tile_bar1(); fastA<1, false, true, true>(a, 1, [&]() __attribute__((always_inline)) { pft(1); }); fastB<1, 1>(a);
    for (int t = 2; t < 6; t += 2) {
      tile_bar1(); fastA<0, false, true, true>(a, t, [&]() __attribute__((always_inline)) { pft(t); }); fastB<0, 0>(a);
      tile_bar1(); fastA<1, false, true, true>(a, t + 1, [&]() __attribute__((always_inline)) { pft(t + 1); }); fastB<1, 0>(a);
    }
    tile_bar1(); fastA<0, false, true, true>(a, 6, [&]() __attribute__((always_inline)) { pft(6); }); fastB<0, 0>(a);
    tile_bar1(); fastA<1, false, false, true>(a, 7, [&]() __attribute__((always_inline)) { pft(7); }); fastB<1, 0>(a);
    tile_bar1();
    fastB<1, 3>(a);
    {
      const bool bad = !(a.l[0] < 0x1p40f) || !(a.l[1] < 0x1p40f);
      if (__any((int)bad) && lane == 0) *flag = 1u;
      asm volatile("s_waitcnt lgkmcnt(0)" ::: "memory"); BAR();
      unsigned redo = __builtin_amdgcn_readfirstlane(*(volatile LAS unsigned*)flag); asm volatile("" : "+s"(redo));
      if (__builtin_expect(redo != 0u, 0)) {
        auto nopf = [&]() __attribute__((always_inline)) {};
        a.pend = false;
        auto upt = [&](const char* q) __attribute__((always_inline)) { const u64 v = (u64)q; return (const char*)((u64)(unsigned)__builtin_amdgcn_readfirstlane((unsigned)(v >> 32)) << 32 | (u64)(unsigned)__builtin_amdgcn_readfirstlane((unsigned)v)); };
        a.gk = upt(a.gk); a.gv = upt(a.gv); a.m0k = __builtin_amdgcn_readfirstlane(a.m0k); a.m0v = __builtin_amdgcn_readfirstlane(a.m0v);
        k0();
        tile_bar(); phaseA<0, true, true, true>(a, 0, nopf); asm volatile("s_nop 7\n\ts_nop 3" ::: "memory"); GAP(); phaseB<0, true, false>(a);
        tile_bar(); phaseA<1, false, true, true>(a, 1, nopf); phaseB<1, false, true>(a);
        for (int t = 2; t < 6; t += 2) {
          tile_bar(); phaseA<0, false, true, true>(a, t, nopf); phaseB<0, false, false>(a);
          tile_bar(); phaseA<1, false, true, true>(a, t + 1, nopf); phaseB<1, false, false>(a);
        }
        tile_bar(); phaseA<0, false, true, true>(a, 6, nopf); phaseB<0, false, false>(a);
        tile_bar(); phaseA<1, false, false, true>(a, 7, nopf); phaseB<1, false, false>(a);
        { auto f1 = [&](auto ic) __attribute__((always_inline)) { sm_exp<1, 1, decltype(ic)::value>(a); }; UNROLL(32, f1);
          auto f2_ = [&](auto ic) __attribute__((always_inline)) { sm_pack<1, decltype(ic)::value>(a); }; UNROLL(32, f2_);
          auto f3 = [&](auto ic) __attribute__((always_inline)) { sm_add<1, 1, decltype(ic)::value>(a); }; UNROLL(32, f3); }
#pragma unroll
        for (int qb = 0; qb < 2; ++qb) a.l[qb] += a.rs0[qb] + a.rs1[qb];
        tile_bar();
        const unsigned vslot = 16384;
        b4 vlo[16], vhi[16];
#pragma unroll
        for (int I = 0; I < 16; ++I) { vlo[I] = ldT(a.vb_[0], vslot + 2048 * (2 * (I >> 2)) + 512 * (I & 3)); vhi[I] = ldT(a.vb_[1], vslot + 2048 * (2 * (I >> 2) + 1) + 512 * (I & 3)); }
        asm volatile("s_nop 1" ::: "memory"); GAP();
#pragma unroll
        for (int G = 0; G < 32; ++G) { const int I = G >> 1; mfma32<true, false>(a.O[I & 3][G & 1], cat(vlo[I], vhi[I]), a.P[G & 1][I >> 2]); }
        asm volatile("s_waitcnt vmcnt(0) lgkmcnt(0)" ::: "memory"); BAR();
      }
    }
  }
  STAMP(4);
  int tid2 = threadIdx.x; asm volatile("" : "+v"(tid2));
  {
  const int tid = tid2, lane = tid2 & 63, hh = lane >> 5, r32 = lane & 31;
  GS s;
  unsigned voffB[9];
#pragma unroll
  for (int i = 0; i < 9; ++i) { const int idx = i * 64 + lane, imm = i < 8 ? (i - 4) * 1024 : 0; voffB[i] = (idx / 18) * ROWB + (idx % 18) * 16 - imm + 4096; }
  s.ring = __builtin_amdgcn_readfirstlane(ldsb + ABYTES + w * 2 * SLOT);
  const int g4 = lane >> 4, cl = lane & 15;
  s.wbase = s.ring + (4 * g4 + ((lane & 15) >> 2)) * 288 + 8 * (lane & 3);
  const unsigned sw = (cl >> 1) & 7;
  const unsigned ab_e = ldsb + cl * ROWB + ((g4 ^ sw) * 16), ab_o = ldsb + cl * ROWB + (((4 + g4) ^ sw) * 16);
  const __amdgpu_buffer_rsrc_t rX = __builtin_amdgcn_make_buffer_rsrc(p.ws + WS_X + (size_t)c * 64 * SLICE, 0, 64 * SLICE, 0x00020000);
  unsigned voffA[9];
#pragma unroll
  for (int i = 0; i < 9; ++i) { const int idx = i * 64 + lane, imm = i < 8 ? (i - 4) * 1024 : 0; voffA[i] = (idx / 18) * (DF * 2) + (idx % 18) * 16 - imm + 4096; }
  const char* gG = (const char*)p.wg + (size_t)(576 * j + 144 * w) * 2 - 4096;
  const char* gU = (const char*)p.wu + (size_t)(576 * j + 144 * w) * 2 - 4096;
  const char* gD = (const char*)p.wd + (size_t)(576 * j) * ROWB + 288 * w - 4096;
  f4 aG[9][4];
  {
    const char* gO = (const char*)p.wo + (size_t)(512 * j) * ROWB + 288 * w - 4096;
    issue_all(s, 0, voffB, gO); issue_all(s, 1, voffB, gO + (size_t)32 * ROWB);
    asm volatile("s_nop 7" ::: "memory");
#pragma unroll
    for (int qb = 0; qb < 2; ++qb) {
      const float il = __builtin_amdgcn_rcpf(swap32_add(a.l[qb]));
      const int row = 32 * qb + r32; const unsigned swr = (r32 >> 1) & 7;
#pragma unroll
      for (int db = 0; db < 4; ++db) {
        asm volatile("" : "+a"(a.O[db][qb]));
        const f16v o = a.O[db][qb];
#pragma unroll
        for (int u = 0; u < 2; ++u) {
          const u4 wv = {pk((f2){o[4 * u], o[4 * u + 1]} * il), pk((f2){o[4 * u + 2], o[4 * u + 3]} * il), pk((f2){o[4 * u + 8], o[4 * u + 9]} * il), pk((f2){o[4 * u + 10], o[4 * u + 11]} * il)};
          *(LAS u4*)(size_t)(ldsb + row * ROWB + (((4 * (4 * w + db) + 2 * u + hh) ^ swr) * 16)) = wv;
        }
      }
    }
    asm volatile("s_waitcnt vmcnt(9) lgkmcnt(0)" ::: "memory");
    BAR();
    STAMP(5);
    read_w(s, 0); read_a(s, 0, ab_e);
    asm volatile("s_waitcnt lgkmcnt(0)" ::: "memory");
    issue_all(s, 0, voffB, gO + (size_t)2 * 32 * ROWB);
    const Epi e1 = {rX, (unsigned)(j * SLICE), 8u * SLICE, ldsb + LDS_X1, j, w, lane};
    gemm_rows<16, 2, 0>(s, aG, voffB, gO, ab_e, ab_o, voffA, gG, gU, e1, [&]() __attribute__((always_inline)) {});
    STAMP(6);
  }
  {
    int zlr[3], zcc[3];
    const int t3 = tid < 192 ? tid : tid - 64;
    u4 xr[3], gv[3];
#pragma unroll
    for (int i = 0; i < 3; ++i) {
      zdecode(t3 + 192 * i, zlr[i], zcc[i]);
      xr[i] = *(const u4*)(p.x + (size_t)(c * 64 + j * 8 + zlr[i]) * D + zcc[i] * 8);
      gv[i] = *(LAS u4*)(size_t)(ldsb + LDS_MOD + (2 * D + zcc[i] * 8) * 2);
    }
    const int lrow = 2 * w + hh, grow = j * 8 + lrow;
    u2 cbv[3][2], sbv[3][2];
#pragma unroll
    for (int i = 0; i < 3; ++i) {
      const int pos = (i < 2 || r32 < 8) ? r32 + 32 * i : r32;
      const int q = pos ^ ((grow >> 1) & 7), k0 = 32 * (q >> 2) + 4 * (q & 3);
#pragma unroll
      for (int h = 0; h < 2; ++h) { cbv[i][h] = *(LAS u2*)(size_t)(ldsb + LDS_MOD + (4 * D + k0 + 16 * h) * 2); sbv[i][h] = *(LAS u2*)(size_t)(ldsb + LDS_MOD + (3 * D + k0 + 16 * h) * 2); }
    }
    asm volatile("s_waitcnt lgkmcnt(0)" ::: "memory");
    publish(flags + (0 * 256 + blockIdx.x) * FLAG_STRIDE, tag);
    STAMP(7);
    await8(flags + (0 * 256 + c * 8) * FLAG_STRIDE, tag, tmo, w, lane);
    STAMP(8);
    {
      u4 v[3][8];
#pragma unroll
      for (int i = 0; i < 3; ++i) {
#pragma unroll
        for (int jj = 0; jj < 7; ++jj) v[i][jj] = __builtin_amdgcn_raw_buffer_load_b128(rX, (j * 8 + jj + (jj >= j)) * SLICE + (t3 + 192 * i) * 16, 0, AUX_X);
      }
#pragma unroll
      for (int i = 0; i < 3; ++i) v[i][7] = *(LAS u4*)(size_t)(ldsb + LDS_X1 + (t3 + 192 * i) * 16);
#pragma unroll
      for (int i = 0; i < 3; ++i) {
        const u4 x1 = gated_residual(v[i], xr[i], gv[i]);
        if (tid < 192) {
          *(LAS u4*)(size_t)(ldsb + LDS_X1 + (tid + 192 * i) * 16) = x1;
          LAS f4* dst = (LAS f4*)(size_t)(ldsb + (zlr[i] * D + zcc[i] * 8) * 4);
          const f2 a0 = bf2(x1[0]), a1 = bf2(x1[1]), a2 = bf2(x1[2]), a3 = bf2(x1[3]);
          dst[0] = (f4){a0[0], a0[1], a1[0], a1[1]}; dst[1] = (f4){a2[0], a2[1], a3[0], a3[1]};
        }
      }
    }
    asm volatile("s_waitcnt lgkmcnt(0)" ::: "memory");
    BAR();
    STAMP(9);
    {
      f4 v[3][2];
      f2 sum = {0.f, 0.f};
#pragma unroll
      for (int i = 0; i < 3; ++i) {
        const bool on = (i < 2 || r32 < 8);
        const int pos = on ? r32 + 32 * i : r32;
        const int q = pos ^ ((grow >> 1) & 7), k0 = 32 * (q >> 2) + 4 * (q & 3);
#pragma unroll
        for (int h = 0; h < 2; ++h) {
          v[i][h] = *(LAS f4*)(size_t)(ldsb + (lrow * D + k0 + 16 * h) * 4);
          if (on) sum += (f2){v[i][h][0] + v[i][h][1], v[i][h][2] + v[i][h][3]};
        }
      }
      float sm = sum[0] + sum[1];
      sm = sum32(sm);
      const float mean = sm * (1.f / D);
      float q2 = 0.f;
#pragma unroll
      for (int i = 0; i < 3; ++i)
        if (i < 2 || r32 < 8) {
#pragma unroll
          for (int h = 0; h < 2; ++h)
#pragma unroll
            for (int e = 0; e < 4; ++e) { const float dl = v[i][h][e] - mean; q2 += dl * dl; }
        }
      q2 = sum32(q2);
      const float mr = rbf(mean), rs = __builtin_amdgcn_rcpf(rbf(__builtin_amdgcn_sqrtf(rbf(rbf(q2 * (1.f / D)) + 1e-5f))));
      const __amdgpu_buffer_rsrc_t rH = __builtin_amdgcn_make_buffer_rsrc(p.ws + WS_H2 + (size_t)c * ABYTES, 0, ABYTES, 0x00020000);
      u4 oh[3];
#pragma unroll
      for (int i = 0; i < 3; ++i) {
        u4& o = oh[i];
#pragma unroll
        for (int h = 0; h < 2; ++h)
#pragma unroll
          for (int e = 0; e < 2; ++e) {
            unsigned t = pk((f2){v[i][h][2 * e], v[i][h][2 * e + 1]} - mr);
            t = pk(bf2(t) * rs);
            t = pk(bf2(t) * bf2(pk(bf2(cbv[i][h][e]) + 1.f)));
            o[2 * h + e] = pk(bf2(t) + bf2(sbv[i][h][e]));
          }
        if (i < 2 || r32 < 8) __builtin_amdgcn_raw_buffer_store_b128(o, rH, grow * ROWB + (r32 + 32 * i) * 16, 0, 16);
      }
      publish(flags + (1 * 256 + blockIdx.x) * FLAG_STRIDE, tag);
      STAMP(10);
      if (w != 0) {
        const unsigned dummy = __builtin_amdgcn_readfirstlane(ldsb + LDS_DUMMY);
        int li = tid - 64; li = li < 162 ? li : li - 94;
        const int r = 18 * c + li / 9, l = li % 9;
        touch((const char*)p.wg + (size_t)576 * j * 2, r * (DF * 2) + l * 128, dummy);
        touch((const char*)p.wu + (size_t)576 * j * 2, r * (DF * 2) + l * 128, dummy);
      }
      await8(flags + (1 * 256 + c * 8) * FLAG_STRIDE, tag, tmo, w, lane);
      STAMP(11);
      u4 hv[16];
#pragma unroll
      for (int i = 0; i < 16; ++i) { const int y = i * 256 + tid < 4032 ? i * 256 + tid : tid; hv[i] = __builtin_amdgcn_raw_buffer_load_b128(rH, (y + (y >= j * 576 ? 576 : 0)) * 16, 0, AUX_H); }
#pragma unroll
      for (int i = 0; i < 3; ++i) if (i < 2 || r32 < 8) *(LAS u4*)(size_t)(ldsb + grow * ROWB + (r32 + 32 * i) * 16) = oh[i];
#pragma unroll
      for (int i = 0; i < 16; ++i) { const int y = i * 256 + tid; if (i < 15 || y < 4032) *(LAS u4*)(size_t)(ldsb + (y + (y >= j * 576 ? 576 : 0)) * 16) = hv[i]; }
    }
    asm volatile("s_waitcnt vmcnt(0) lgkmcnt(0)" ::: "memory");
    BAR();
  }
  STAMP(12);
  {
    constexpr size_t KS_GU = (size_t)32 * DF * 2, KS_D = (size_t)32 * ROWB;
    read_w(s, 0); read_a(s, 0, ab_e);
    asm volatile("s_waitcnt lgkmcnt(0)" ::: "memory");
    issue_all(s, 0, voffA, gG + KS_GU);
    f4 aU[9][4];
    auto no = [&]() __attribute__((always_inline)) {};
    auto pfd = [&](int h) __attribute__((always_inline)) {
      int li = 64 * h + 16 * w + lane; li = li < 162 ? li : 161;
      if (lane < 16) touch((const char*)p.wd + (size_t)576 * j * ROWB, (18 * c) * ROWB + li * 128, __builtin_amdgcn_readfirstlane(ldsb + LDS_DUMMY));
    };
    auto kpair = [&](auto zc, auto lastc, auto vmc, int k, auto&& pf) __attribute__((always_inline)) {
      constexpr bool Z = decltype(zc)::value, LAST = decltype(lastc)::value; constexpr int VM = decltype(vmc)::value;
      const char* u1 = gU + (size_t)(k + 1) * KS_GU; const char* g2 = gG + (size_t)(k + 2) * KS_GU;
      step<0, 0, Z, true, false, true, 9, 0>(s, aG, voffA, u1, 0, no);
      if constexpr (!LAST) {
        step<1, 0, Z, true, true, true, 9, NVU>(s, aU, voffA, g2, ab_o + 128 * (k >> 1), pf);
        step<0, 1, false, true, false, true, VM, 0>(s, aG, voffA, u1 + KS_GU, 0, no);
        step<1, 1, false, true, true, true, VM, NVU>(s, aU, voffA, g2 + KS_GU, ab_e + 128 * ((k + 2) >> 1), no);
      } else {
        step<1, 0, Z, true, true, true, 9, NVU>(s, aU, voffB, gD, ab_o + 128 * (k >> 1), no);
        step<0, 1, false, true, false, true, 9, 0>(s, aG, voffB, gD + KS_D, 0, no);
        step<1, 1, false, true, false, true, 9, NVU>(s, aU, voffB, gD + 2 * KS_D, 0, no);
      }
    };
    kpair(std::true_type{}, std::false_type{}, IC<9>{}, 0, no);
    for (int k = 2; k < 8; k += 2) kpair(std::false_type{}, std::false_type{}, IC<9>{}, k, no);
    for (int k = 8; k < 14; k += 2) kpair(std::false_type{}, std::false_type{}, IC<10>{}, k, [&]() __attribute__((always_inline)) { pfd((k - 8) >> 1); });
    kpair(std::false_type{}, std::false_type{}, IC<9>{}, 14, no);
    kpair(std::false_type{}, std::true_type{}, IC<9>{}, 16, no);
    STAMP(13);
    settle<0>(aG); settle<NVU>(aU);
    u2 act[9][4];
#pragma unroll
    for (int t = 0; t < 9; ++t)
#pragma unroll
      for (int m = 0; m < 4; ++m)
#pragma unroll
        for (int e = 0; e < 2; ++e) {
          const f2 x = {aG[t][m][2 * e], aG[t][m][2 * e + 1]}, u = {aU[t][m][2 * e], aU[t][m][2 * e + 1]};
          const f2 z = x * (x * x * -0.10294324f + -2.3022082f);
          const f2 dn = (f2){__builtin_amdgcn_exp2f(z[0]), __builtin_amdgcn_exp2f(z[1])} + 1.f;
          act[t][m][e] = pk((x * u) * (f2){__builtin_amdgcn_rcpf(dn[0]), __builtin_amdgcn_rcpf(dn[1])});
        }
    asm volatile("s_waitcnt lgkmcnt(0)" ::: "memory");
    BAR();
#pragma unroll
    for (int t = 0; t < 9; ++t) {
      const int tt = 9 * w + t, ks = tt >> 1, ah = tt & 1;
      const unsigned ad = ldsb + cl * ROWB + (((4 * ks + g4) ^ sw) * 16) + 8 * ah;
#pragma unroll
      for (int m = 0; m < 4; ++m) *(LAS u2*)(size_t)(ad + m * 16 * ROWB) = act[t][m];
    }
    asm volatile("s_waitcnt lgkmcnt(0)" ::: "memory");
    BAR();
    STAMP(14);
    read_a(s, 0, ab_e);
    const Epi e2 = {rX, (unsigned)(j * 8 * SLICE), (unsigned)SLICE, s.ring - w * 2304, j, w, lane};
    gemm_rows<18, 0, 0>(s, aG, voffB, gD, ab_e, ab_o, voffB, gD, gD, e2, [&]() __attribute__((always_inline)) {});
    STAMP(15);
  }
  {
    int tid3 = threadIdx.x; asm volatile("" : "+v"(tid3));
    const int tid = tid3, lane = tid3 & 63;
    asm volatile("s_waitcnt lgkmcnt(0)" ::: "memory");
    publish(flags + (2 * 256 + blockIdx.x) * FLAG_STRIDE, tag);
    STAMP(16);
    const int t3 = tid < 192 ? tid : tid - 64;
    int zlr[3], zcc[3];
    u4 gv[3], x1[3];
#pragma unroll
    for (int i = 0; i < 3; ++i) {
      zdecode(t3 + 192 * i, zlr[i], zcc[i]);
      gv[i] = *(LAS u4*)(size_t)(ldsb + LDS_MOD + (5 * D + zcc[i] * 8) * 2);
      x1[i] = *(LAS u4*)(size_t)(ldsb + LDS_X1 + (t3 + 192 * i) * 16);
    }
    if (blockIdx.x == 0 && w == 1) {
      for (unsigned spins = 0; spins <= SPIN_CAP; ++spins) {
        bool ok = true;
#pragma unroll
        for (int i = 0; i < 4; ++i) ok &= __hip_atomic_load((gu64*)(flags + (lane + 64 * i) * FLAG_STRIDE), __ATOMIC_RELAXED, __HIP_MEMORY_SCOPE_AGENT) == tag;
        if (__all(ok)) break;
        __builtin_amdgcn_s_sleep(4);
      }
      if (lane == 0) __hip_atomic_store((gu64*)(p.ws + WS_EPOCH), tag, __ATOMIC_RELAXED, __HIP_MEMORY_SCOPE_AGENT);
    }
    await8(flags + (2 * 256 + c * 8) * FLAG_STRIDE, tag, tmo, w, lane);
    STAMP(17);
    const __amdgpu_buffer_rsrc_t rO = __builtin_amdgcn_make_buffer_rsrc(p.out + (size_t)(c * 64 + j * 8) * D, 0, SLICE, 0x00020000); (void)rO;
    u4 v[3][8];
#pragma unroll
    for (int i = 0; i < 3; ++i)
#pragma unroll
      for (int jj = 0; jj < 7; ++jj) v[i][jj] = __builtin_amdgcn_raw_buffer_load_b128(rX, ((jj + (jj >= j)) * 8 + j) * SLICE + (t3 + 192 * i) * 16, 0, AUX_X);
#pragma unroll
    for (int i = 0; i < 3; ++i) { const int z = t3 + 192 * i, zw = z / 144; v[i][7] = *(LAS u4*)(size_t)(ldsb + ABYTES + zw * 2 * SLOT + (z - zw * 144) * 16); }
#pragma unroll
    for (int i = 0; i < 3; ++i) {
      const u4 o = gated_residual(v[i], x1[i], gv[i]);
      if (tid < 192) *(u4*)(p.out + (size_t)(c * 64 + j * 8 + zlr[i]) * D + zcc[i] * 8) = o;
    }
  }
  STAMP(18);
  asm volatile("s_waitcnt lgkmcnt(0)" ::: "memory");
  STAMP_FLUSH();
  }
}

extern "C" void kernel_launch(void* const* d_in, const int* in_sizes, int n_in, void* d_out, int out_size, void* d_ws, size_t ws_size, hipStream_t stream) {
  Params p;
  p.x = (const u16*)d_in[0]; p.kc = (const u16*)d_in[1]; p.vc = (const u16*)d_in[2]; p.wq = (const u16*)d_in[3]; p.wo = (const u16*)d_in[4];
  p.wg = (const u16*)d_in[6]; p.wu = (const u16*)d_in[7]; p.wd = (const u16*)d_in[8];
  p.c = (const u16*)d_in[9]; p.wmod = (const u16*)d_in[10];
  p.out = (u16*)d_out; p.ws = (char*)d_ws;
 p.one = 1.0f; p.gap = 0u;
  (void)hipFuncSetAttribute((const void*)k_main, hipFuncAttributeMaxDynamicSharedMemorySize, LDS_BYTES);
  k_main<<<256, 256, LDS_BYTES, stream>>>(p);
}
```

```cpp
#include <hip/hip_runtime.h>
#include <stdint.h>
#include <utility>
#include <type_traits>

typedef unsigned short u16;
typedef unsigned long long u64;
typedef __bf16 b2 __attribute__((ext_vector_type(2)));
typedef __bf16 b4 __attribute__((ext_vector_type(4)));
typedef __bf16 b8 __attribute__((ext_vector_type(8)));
typedef float f2 __attribute__((ext_vector_type(2)));
typedef float f4 __attribute__((ext_vector_type(4)));
typedef float f16v __attribute__((ext_vector_type(16)));
typedef unsigned u2 __attribute__((ext_vector_type(2)));
typedef unsigned u4 __attribute__((ext_vector_type(4)));
typedef __attribute__((address_space(1))) unsigned gu32;
typedef __attribute__((address_space(1))) unsigned long long gu64;
#define LAS __attribute__((address_space(3)))
#define DI __device__ __forceinline__
#define GAP() __builtin_amdgcn_sched_barrier(0)
#define LGKM0() __builtin_amdgcn_s_waitcnt(0xC07F)
#define BAR() do { asm volatile("" ::: "memory"); __builtin_amdgcn_s_barrier(); asm volatile("" ::: "memory"); } while (0)
template <int V> using IC = std::integral_constant<int, V>;
#define AUX_X 16
#define AUX_H 16
#define STAG_D 500
#define PF_D 150
#ifndef NVU
#define NVU 8
#endif
#define PFD_K 14
#define UNROLL(N, F) [&]<int... I_>(std::integer_sequence<int, I_...>) __attribute__((always_inline)) { (F(IC<I_>{}), ...); }(std::make_integer_sequence<int, N>{})

constexpr int S = 2048, D = 576, DH = 128, SK = 2048, WIN = 512, DF = 4608;
constexpr int ROWB = D * 2;
constexpr int ABYTES = 64 * ROWB;
constexpr int SLICE = 8 * ROWB;
constexpr int SLOT = 9216;
constexpr int QSLOT = 8192;
constexpr int LDS_DUMMY = ABYTES + 8 * SLOT;
constexpr int LDS_X1 = LDS_DUMMY + 256;
constexpr int LDS_MOD = LDS_X1 + SLICE;
constexpr int LDS_BYTES = LDS_MOD + 6 * D * 2;
static_assert(LDS_BYTES <= 163840, "LDS");
constexpr size_t WS_EPOCH = 0;
constexpr size_t WS_TMO = 256;
constexpr size_t WS_MODP = 131072;
constexpr size_t WS_FLAG = 32768;
constexpr int FLAG_STRIDE = 8;
constexpr size_t WS_X = 1048576;
constexpr size_t WS_H2 = WS_X + (size_t)32 * 64 * SLICE;
constexpr size_t WS_DBG = 200u << 20;
#define STAMP(i)
#define STAMP_FLUSH()
#define PST(i)

DI float bf2f(u16 v) { return __uint_as_float(((unsigned)v) << 16); }
DI float bflo(unsigned w) { return __uint_as_float(w << 16); }
DI float bfhi(unsigned w) { return __uint_as_float(w & 0xffff0000u); }
DI f2 bf2(unsigned w) { return (f2){bflo(w), bfhi(w)}; }
DI unsigned pk(float lo, float hi) { f2 v = {lo, hi}; b2 b = __builtin_convertvector(v, b2); return __builtin_bit_cast(unsigned, b); }
DI unsigned pk(f2 v) { b2 b = __builtin_convertvector(v, b2); return __builtin_bit_cast(unsigned, b); }
DI float rbf(float f) { return bflo(pk(f, f)); }
DI f2 rb2(f2 v) { return (f2){__uint_as_float(pk(0.f, v[0])), __uint_as_float(pk(0.f, v[1]))}; }
template <int CTRL> DI float dpp(float v) { return __uint_as_float(__builtin_amdgcn_update_dpp(0u, __float_as_uint(v), CTRL, 0xf, 0xf, false)); }
DI float sum8(float v) { v += dpp<0xB1>(v); v += dpp<0x4E>(v); v += dpp<0x141>(v); return v; }
DI float swap16_add(float x) { auto r = __builtin_amdgcn_permlane16_swap(__float_as_uint(x), __float_as_uint(x), false, false); return __uint_as_float(r[0]) + __uint_as_float(r[1]); }
DI float swap32_add(float x) { auto r = __builtin_amdgcn_permlane32_swap(__float_as_uint(x), __float_as_uint(x), false, false); return __uint_as_float(r[0]) + __uint_as_float(r[1]); }
DI float sum32(float v) { v = sum8(v); v += dpp<0x140>(v); return swap16_add(v); }
DI u16 f2bf(float f) { return (u16)(pk(f, f) & 0xffffu); }
DI float fmaxx(float a, float b) { return __builtin_elementwise_maximum(a, b); }
DI float fmax3(float a, float b, float c) { return __builtin_elementwise_maximum(__builtin_elementwise_maximum(a, b), c); }

DI void zdecode(int z, int& lr, int& cc) {
  const int w = z / 144, y = z - w * 144;
  if (y < 128) { const int tp = y >> 5; lr = (y >> 2) & 7; cc = 18 * w + 4 * tp + (y & 3); }
  else { const int y2 = y - 128; lr = y2 >> 1; cc = 18 * w + 16 + (y2 & 1); }
}

template <int IMM> DI void dma(unsigned voff, const char* g) { asm volatile("global_load_lds_dwordx4 %0, %1 offset:%2" :: "v"(voff), "s"(g), "i"(IMM) : "memory"); }
DI void setm0(unsigned a) { asm volatile("s_mov_b32 m0, %0" :: "s"(a) : "memory"); }
DI void touch(const char* p, unsigned voff, unsigned ldsdummy) {
  asm volatile("s_mov_b32 m0, %0\n\ts_nop 0\n\tglobal_load_lds_dword %1, %2" :: "s"(ldsdummy), "v"(voff), "s"(p) : "memory");
}
DI void touchp(const char* a, unsigned ldsdummy) {
  asm volatile("s_mov_b32 m0, %0\n\ts_nop 0\n\tglobal_load_lds_dword %1, off" :: "s"(ldsdummy), "v"(a) : "memory");
}
DI b4 ldT(unsigned base, int off) { return __builtin_amdgcn_ds_read_tr16_b64_v4bf16((LAS b4*)(size_t)(base + off)); }
DI b8 ldA(unsigned base, int off) { return *(LAS b8*)(size_t)(base + off); }
DI b8 cat(const b4& lo, const b4& hi) { return __builtin_shufflevector(lo, hi, 0, 1, 2, 3, 4, 5, 6, 7); }
template <bool AG, bool ZERO> DI void mfma16(f4& c, const b8& a, const b8& b) {
  if constexpr (AG) {
    if constexpr (ZERO) asm volatile("v_mfma_f32_16x16x32_bf16 %0, %1, %2, 0" : "=a"(c) : "v"(a), "v"(b));
    else asm volatile("v_mfma_f32_16x16x32_bf16 %0, %1, %2, %0" : "+a"(c) : "v"(a), "v"(b));
  } else {
    if constexpr (ZERO) asm volatile("v_mfma_f32_16x16x32_bf16 %0, %1, %2, 0" : "=&v"(c) : "v"(a), "v"(b));
    else asm volatile("v_mfma_f32_16x16x32_bf16 %0, %1, %2, %0" : "+v"(c) : "v"(a), "v"(b));
  }
}
template <bool AG, bool ZERO> DI void mfma32(f16v& c, const b8& a, const b8& b) {
  if constexpr (AG) {
    if constexpr (ZERO) asm volatile("v_mfma_f32_32x32x16_bf16 %0, %1, %2, 0" : "=a"(c) : "v"(a), "v"(b));
    else asm volatile("v_mfma_f32_32x32x16_bf16 %0, %1, %2, %0" : "+a"(c) : "v"(a), "v"(b));
  } else {
    if constexpr (ZERO) asm volatile("v_mfma_f32_32x32x16_bf16 %0, %1, %2, 0" : "=&v"(c) : "v"(a), "v"(b));
    else asm volatile("v_mfma_f32_32x32x16_bf16 %0, %1, %2, %0" : "+v"(c) : "v"(a), "v"(b));
  }
}
template <int NVG> DI void settle(f4 (&acc)[9][4]) {
  asm volatile("s_nop 7\n\ts_nop 7" ::: "memory");
#pragma unroll
  for (int t = 0; t < 9; ++t)
#pragma unroll
    for (int m = 0; m < 4; ++m) { if (t * 4 + m < 36 - NVG) asm volatile("" : "+a"(acc[t][m])); else asm volatile("" : "+v"(acc[t][m])); }
}

struct GS {
  b4 wlo[2][9], whi[2][9];
  b8 af[2][4];
  unsigned wbase, ring;
};
DI void issue_all(const GS& s, int slot, const unsigned (&voff)[9], const char* gp) {
  const unsigned sl = s.ring + slot * SLOT;
  setm0(sl + 4096); asm volatile("s_nop 0");
  dma<-4096>(voff[0], gp); dma<-3072>(voff[1], gp); dma<-2048>(voff[2], gp); dma<-1024>(voff[3], gp);
  dma<0>(voff[4], gp); dma<1024>(voff[5], gp); dma<2048>(voff[6], gp); dma<3072>(voff[7], gp);
  setm0(sl + 8192); asm volatile("s_nop 0"); dma<0>(voff[8], gp);
}
template <int PAR, int AP, bool ZERO, bool RDW, bool RDA, bool DMA, int VM, int NVG, typename PF>
DI void step(GS& s, f4 (&acc)[9][4], const unsigned (&voff)[9], const char* gp, unsigned ab, PF&& pf) {
  const unsigned sl = s.ring + (PAR ^ 1) * SLOT;
  const unsigned wb = s.wbase + (PAR ^ 1) * SLOT;
  auto mm = [&](auto ic) __attribute__((always_inline)) {
    constexpr int I = decltype(ic)::value, t = I >> 2, m = I & 3;
    mfma16<(I < 36 - NVG), ZERO>(acc[t][m], cat(s.wlo[PAR][t], s.whi[PAR][t]), s.af[AP][m]);
  };
  auto rd = [&](auto ic) __attribute__((always_inline)) {
    constexpr int I = decltype(ic)::value;
    if constexpr (I < 4) { if constexpr (RDA) s.af[AP ^ 1][I] = ldA(ab, I * 16 * ROWB); }
    else if constexpr (RDW) {
      if constexpr (((I - 4) & 1) == 0) s.wlo[PAR ^ 1][(I - 4) >> 1] = ldT(wb, 32 * ((I - 4) >> 1));
      else s.whi[PAR ^ 1][(I - 4) >> 1] = ldT(wb, 32 * ((I - 4) >> 1) + 4608);
    }
  };
  auto gap = [&](auto gc) __attribute__((always_inline)) {
    constexpr int G = decltype(gc)::value;
    mm(IC<2 * G>{});
    if constexpr (G == 0 && RDW) asm volatile("s_waitcnt vmcnt(%0)" :: "i"(VM) : "memory");
    if constexpr (G <= 10) rd(IC<2 * G>{});
    if constexpr (DMA) {
      if constexpr (G == 12) { LGKM0(); setm0(sl + 4096); }
      if constexpr (G >= 13 && G <= 16) dma<(2 * (G - 13) - 4) * 1024>(voff[2 * (G - 13)], gp);
      if constexpr (G == 17) dma<0>(voff[8], gp);
    }
    GAP();
    mm(IC<2 * G + 1>{});
    if constexpr (G <= 10) rd(IC<2 * G + 1>{});
    if constexpr (DMA) {
      if constexpr (G >= 13 && G <= 16) dma<(2 * (G - 13) + 1 - 4) * 1024>(voff[2 * (G - 13) + 1], gp);
      if constexpr (G == 16) setm0(sl + 8192);
    }
    if constexpr (G == 17) pf();
    GAP();
  };
  UNROLL(18, gap);
}
DI void read_w(GS& s, int par) {
#pragma unroll
  for (int t = 0; t < 9; ++t) { s.wlo[par][t] = ldT(s.wbase + par * SLOT, 32 * t); s.whi[par][t] = ldT(s.wbase + par * SLOT, 32 * t + 4608); }
}
DI void read_a(GS& s, int set, unsigned ab) {
#pragma unroll
  for (int m = 0; m < 4; ++m) s.af[set][m] = ldA(ab, m * 16 * ROWB);
}
struct Epi { __amdgpu_buffer_rsrc_t rs; unsigned base, dstride, ownlds; int own, w, lane; };
template <int U> DI void epi_unit(f4 (&acc)[9][4], const Epi& e) {
  const int G = e.lane >> 4, c = e.lane & 15, lr = c & 7, dh = c >> 3;
  constexpr int ta = U < 16 ? 2 * (U >> 2) : 8, tb = U < 16 ? ta + 1 : 8, ma = U < 16 ? (U & 3) : 2 * (U - 16), mb = U < 16 ? ma : ma + 1;
  asm volatile("" : "+a"(acc[ta][ma]), "+a"(acc[tb][mb]));
  const f4 a = acc[ta][ma], b = acc[tb][mb];
  auto r0 = __builtin_amdgcn_permlane16_swap(pk(a[0], a[1]), pk(b[0], b[1]), false, false);
  auto r1 = __builtin_amdgcn_permlane16_swap(pk(a[2], a[3]), pk(b[2], b[3]), false, false);
  const u4 v = {r0[0], r1[0], r0[1], r1[1]};
  const int d = U < 16 ? 2 * ma + dh : 2 * (ma + (G & 1)) + dh;
  const unsigned off = U < 16 ? e.w * 2304 + (U >> 2) * 512 + lr * 64 + (2 * (G & 1) + (G >> 1)) * 16 : e.w * 2304 + 2048 + lr * 32 + (G >> 1) * 16;
  __builtin_amdgcn_raw_buffer_store_b128(v, e.rs, d == e.own ? 0x7ffffff0u : e.base + d * e.dstride + off, 0, 16);
  if ((U < 16 ? ma : ma >> 1) == (U < 16 ? e.own >> 1 : e.own >> 2)) { if (d == e.own) *(LAS u4*)(size_t)(e.ownlds + off) = v; }
}
template <bool NEXT, int VM> DI void final2(GS& s, f4 (&acc)[9][4], const unsigned (&nvoff)[9], const char* nx1, unsigned ab, const Epi& e) {
  const unsigned sl = s.ring + SLOT, wb = s.wbase + SLOT;
  asm volatile("s_waitcnt vmcnt(%0)" :: "i"(VM) : "memory");
#pragma unroll
  for (int m = 0; m < 4; ++m) s.af[1][m] = ldA(ab, m * 16 * ROWB);
  auto rdw = [&](auto ic) __attribute__((always_inline)) { constexpr int t = decltype(ic)::value; s.wlo[1][t] = ldT(wb, 32 * t); s.whi[1][t] = ldT(wb, 32 * t + 4608); };
  rdw(IC<0>{}); rdw(IC<1>{});
  GAP();
  auto grp = [&](auto gc) __attribute__((always_inline)) {
    constexpr int G = decltype(gc)::value, t = G >> 1, st = G & 1;
    auto mm = [&](auto mc) __attribute__((always_inline)) { constexpr int m = decltype(mc)::value; mfma16<true, false>(acc[t][m], cat(s.wlo[st][t], s.whi[st][t]), s.af[st][m]); };
    mm(IC<0>{});
    if constexpr (st == 0 && t + 2 < 9) rdw(IC<t + 2>{});
    if constexpr (NEXT && G == 15) { LGKM0(); setm0(sl + 4096); }
    GAP();
    constexpr int U = G >= 5 ? G - 5 : -1;
    mm(IC<1>{});
    if constexpr (U >= 0 && U < 16 && (U >> 2) * 4 + 5 <= G) epi_unit<(U >= 0 && U < 16) ? U : 0>(acc, e);
    GAP();
    mm(IC<2>{});
    if constexpr (NEXT && G == 16) { dma<-4096>(nvoff[0], nx1); dma<-3072>(nvoff[1], nx1); dma<-2048>(nvoff[2], nx1); dma<-1024>(nvoff[3], nx1); }
    if constexpr (NEXT && G == 17) { dma<0>(nvoff[4], nx1); dma<1024>(nvoff[5], nx1); dma<2048>(nvoff[6], nx1); dma<3072>(nvoff[7], nx1); setm0(sl + 8192); }
    GAP();
    mm(IC<3>{});
    GAP();
  };
  UNROLL(18, grp);
  if constexpr (NEXT) dma<0>(nvoff[8], nx1);
  asm volatile("s_nop 7" ::: "memory");
  epi_unit<13>(acc, e); epi_unit<14>(acc, e); epi_unit<15>(acc, e); epi_unit<16>(acc, e); epi_unit<17>(acc, e);
}
template <int NS, int NEXT, int NPF, typename PF> DI void gemm_rows(GS& s, f4 (&acc)[9][4], const unsigned (&voff)[9], const char* gW, unsigned ab_e, unsigned ab_o,
                                              const unsigned (&nvoff)[9], const char* nx0, const char* nx1, const Epi& e, PF&& pf) {
  constexpr size_t KS = (size_t)32 * ROWB;
  auto no = [&]() __attribute__((always_inline)) {};
  step<0, 0, true, true, true, true, 9, 0>(s, acc, voff, gW + 3 * KS, ab_o, no);
  step<1, 1, false, true, true, true, 9, 0>(s, acc, voff, gW + 4 * KS, ab_e + 128, pf);
  step<0, 0, false, true, true, true, 9 + NPF, 0>(s, acc, voff, gW + 5 * KS, ab_o + 128, no);
  step<1, 1, false, true, true, true, 9 + NPF, 0>(s, acc, voff, gW + 6 * KS, ab_e + 256, no);
  for (int k = 4; k < NS - 4; k += 2) {
    step<0, 0, false, true, true, true, 9, 0>(s, acc, voff, gW + (size_t)(k + 3) * KS, ab_o + 128 * (k >> 1), no);
    step<1, 1, false, true, true, true, 9, 0>(s, acc, voff, gW + (size_t)(k + 4) * KS, ab_e + 128 * ((k + 2) >> 1), no);
  }
  step<0, 0, false, true, true, true, 9, 0>(s, acc, voff, gW + (size_t)(NS - 1) * KS, ab_o + 128 * ((NS - 4) >> 1), no);
  step<1, 1, false, true, true, NEXT != 0, 9, 0>(s, acc, nvoff, nx0, ab_e + 128 * ((NS - 2) >> 1), no);
  final2<NEXT != 0, NEXT != 0 ? 9 : 0>(s, acc, nvoff, nx1, ab_o + 128 * ((NS - 2) >> 1), e);
}

constexpr unsigned SPIN_CAP = 1u << 17;
DI void publish(u64* flag, u64 tag) {
  asm volatile("s_waitcnt vmcnt(0)" ::: "memory");
  BAR();
  if (threadIdx.x == 0) __hip_atomic_store((gu64*)flag, tag, __ATOMIC_RELAXED, __HIP_MEMORY_SCOPE_AGENT);
}
DI void await8(u64* flags8, u64 tag, unsigned* tmo, int w, int lane) {
  if (w == 0) {
    for (unsigned spins = 0;; ++spins) {
      const u64 v = __hip_atomic_load((gu64*)(flags8 + (lane & 7) * FLAG_STRIDE), __ATOMIC_RELAXED, __HIP_MEMORY_SCOPE_AGENT);
      if (__all(v == tag)) break;
      if (spins > SPIN_CAP) { if (lane == 0) __hip_atomic_store((gu32*)tmo, 1u, __ATOMIC_RELAXED, __HIP_MEMORY_SCOPE_AGENT); break; }
      __builtin_amdgcn_s_sleep(1);
    }
  }
  BAR();
}
template <int NP, int NT> struct ModGather {
  static constexpr int N = (NP + NT - 1) / NT;
  u4 gr[N][2];
  __amdgpu_buffer_rsrc_t rM;
  DI void load(int tid) {
#pragma unroll
    for (int i = 0; i < N; ++i) {
      const int idx = tid + NT * i < NP ? tid + NT * i : (tid & 63), off = (idx >> 5) * 1024 + (idx & 31) * 16;
      gr[i][0] = __builtin_amdgcn_raw_buffer_load_b128(rM, off, 0, 16); gr[i][1] = __builtin_amdgcn_raw_buffer_load_b128(rM, off + 512, 0, 16);
    }
  }
  DI void issue(char* ws, int tid) { rM = __builtin_amdgcn_make_buffer_rsrc(ws + WS_MODP, 0, 54 * 1024, 0x00020000); load(tid); }
  DI void finish(u64 tag, unsigned ldsb, unsigned* tmo, int tid) {
    const unsigned thi = (unsigned)(tag >> 32), tlo = (unsigned)tag;
    for (unsigned spins = 0;; ++spins) {
      bool ok = true;
#pragma unroll
      for (int i = 0; i < N; ++i) ok &= gr[i][0][2] == tlo && gr[i][0][3] == thi && gr[i][1][2] == tlo && gr[i][1][3] == thi;
      if (__all(ok)) break;
      if (spins > SPIN_CAP / 8) { if ((tid & 63) == 0) __hip_atomic_store((gu32*)tmo, 2u, __ATOMIC_RELAXED, __HIP_MEMORY_SCOPE_AGENT); break; }
      __builtin_amdgcn_s_sleep(4);
      load(tid);
    }
#pragma unroll
    for (int i = 0; i < N; ++i)
      if (tid + NT * i < NP) {
        const int idx = tid + NT * i;
        const f2 m = (f2){__uint_as_float(gr[i][0][0]), __uint_as_float(gr[i][0][1])} + (f2){__uint_as_float(gr[i][1][0]), __uint_as_float(gr[i][1][1])};
        if (idx < D) {
          f2 v = rb2(m);
          if (idx >= D / 2) v = rb2(v + 1.f);
          *(LAS f2*)(size_t)(ldsb + LDS_X1 + 1024 + idx * 8) = v;
        } else *(LAS unsigned*)(size_t)(ldsb + LDS_MOD + idx * 4) = pk(m);
      }
  }
};
DI u4 gated_residual(const u4 (&v)[8], const u4& res, const u4& g) {
  u4 o;
#pragma unroll
  for (int e = 0; e < 4; ++e) {
    f2 s = bf2(v[0][e]);
#pragma unroll
    for (int j = 1; j < 8; ++j) s += bf2(v[j][e]);
    const unsigned t = pk(bf2(pk(s)) * bf2(g[e]));
    o[e] = pk(bf2(res[e]) + bf2(t));
  }
  return o;
}

constexpr float QSCALE = 0.08838834764831845f * 1.4426950408889634f;
constexpr float THR = 40.0f;
struct QS {
  b4 wlo[2][8], whi[2][8];
  b8 hf[2][4];
  unsigned wb[4];
  unsigned hb[4];
  unsigned ring;
};
template <int PAR, bool ZERO, bool RD, bool DMA, int VM, typename PF>
DI void qstep(QS& s, f16v (&acc)[4][2], const unsigned (&voff)[8], const char* gp, unsigned hoff, PF&& pf) {
  const unsigned sl = s.ring + (PAR ^ 1) * QSLOT;
  auto rd = [&](auto ic) __attribute__((always_inline)) {
    constexpr int I = decltype(ic)::value;
    if constexpr (RD) {
      if constexpr (I < 4) {
        s.hf[PAR ^ 1][I] = ldA(s.hb[2 * (PAR ^ 1) + (I >> 1)] + hoff, (I & 1) * 32 * ROWB);
      } else {
        constexpr int J = I - 4, f = J >> 1, t = J & 1, ks = f >> 2, dt = f & 3;
        const b4 v = ldT(s.wb[dt], (PAR ^ 1) * QSLOT + 4096 * ks + 1024 * t);
        if constexpr (t == 0) s.wlo[PAR ^ 1][f] = v; else s.whi[PAR ^ 1][f] = v;
      }
    }
  };
  auto gap = [&](auto gc) __attribute__((always_inline)) {
    constexpr int G = decltype(gc)::value, ks = G >> 3, dt = (G >> 1) & 3, qb = G & 1, f = ks * 4 + dt;
    mfma32<false, ZERO && ks == 0>(acc[dt][qb], cat(s.wlo[PAR][f], s.whi[PAR][f]), s.hf[PAR][ks * 2 + qb]);
    if constexpr (G == 0 && RD) asm volatile("s_waitcnt vmcnt(%0)" :: "i"(VM) : "memory");
    if constexpr (G <= 6) { rd(IC<3 * G>{}); rd(IC<3 * G + 1>{}); if constexpr (3 * G + 2 < 20) rd(IC<3 * G + 2>{}); }
    if constexpr (DMA) {
      if constexpr (G == 9) { LGKM0(); setm0(sl + 4096); }
      if constexpr (G >= 10 && G <= 13) { dma<(2 * (G - 10) - 4) * 1024>(voff[2 * (G - 10)], gp); dma<(2 * (G - 10) + 1 - 4) * 1024>(voff[2 * (G - 10) + 1], gp); }
    }
    if constexpr (G == 15) pf();
    GAP();
  };
  UNROLL(16, gap);
}

struct AT {
  f16v S[2][2][2];
  f16v negm[2];
  f16v O[4][2];
  b8 q[2][8];
  b8 P[2][4];
  float m[2], l[2], rs0[2], rs1[2], fct[2], pm0[2], pm1[2];
  float one;
  unsigned kb_[2], vb_[2];
  unsigned voff[8];
  unsigned m0k, m0v;
  const char *gk, *gv;
  bool pend;
};
template <int MODE> DI void qk(f16v& s, const b8& k, const b8& q, const f16v& negm) {
  if constexpr (MODE == 0) asm volatile("v_mfma_f32_32x32x16_bf16 %0, %1, %2, 0" : "=&v"(s) : "v"(k), "a"(q));
  else if constexpr (MODE == 1) asm volatile("v_mfma_f32_32x32x16_bf16 %0, %1, %2, %3" : "=&v"(s) : "v"(k), "a"(q), "v"(negm));
  else asm volatile("v_mfma_f32_32x32x16_bf16 %0, %1, %2, %0" : "+v"(s) : "v"(k), "a"(q));
}
template <int XP, int KB, int E> DI void sm_exp(AT& a) { constexpr int qb = E >> 4, r = E & 15; a.S[XP][KB][qb][r] = __builtin_amdgcn_exp2f(a.S[XP][KB][qb][r]); }
template <int XP, int KB, int E> DI void sm_add(AT& a) {
  constexpr int qb = E >> 4, r = E & 15;
  if constexpr (r & 1) a.rs1[qb] = __builtin_fmaf(a.S[XP][KB][qb][r], a.one, a.rs1[qb]); else a.rs0[qb] += a.S[XP][KB][qb][r];
}
template <int XP, int C> DI void sm_pack(AT& a) {
  constexpr int kb = C >> 4, qb = (C >> 3) & 1, s = (C >> 2) & 1, p = C & 3;
  u4 w = __builtin_bit_cast(u4, a.P[qb][kb * 2 + s]);
  w[p] = pk(a.S[XP][kb][qb][8 * s + 2 * p], a.S[XP][kb][qb][8 * s + 2 * p + 1]);
  a.P[qb][kb * 2 + s] = __builtin_bit_cast(b8, w);
  if constexpr (p == 3) asm volatile("" : "+v"(a.P[qb][kb * 2 + s]));
}
template <int X, bool FIRST, bool DK, bool DV, typename PF> DI void phaseA(AT& a, int t, PF&& pf) {
  constexpr int XP = X ^ 1;
  const unsigned kslot = X * 16384;
  b8 kf[16];
  auto kread = [&](auto ic) __attribute__((always_inline)) { constexpr int I = decltype(ic)::value, ks = I >> 1, kb = I & 1; kf[I] = ldA(a.kb_[ks & 1], kslot + 8192 * kb + 512 * (ks >> 1)); };
  kread(IC<0>{}); kread(IC<1>{}); kread(IC<2>{}); kread(IC<3>{});
  const char* gk = a.gk + (size_t)(t + 1) * 16384; const char* gv = a.gv + (size_t)t * 16384;
  auto gap = [&](auto gc) __attribute__((always_inline)) {
    constexpr int G = decltype(gc)::value, ks = G >> 2, kb = (G >> 1) & 1, qb = G & 1, I = G >> 1;
    qk<(ks == 0) ? (FIRST ? 0 : 1) : 2>(a.S[X][kb][qb], kf[I], a.q[qb][ks], a.negm[qb]);
    if constexpr ((G & 1) == 0 && I + 4 < 16) kread(IC<I + 4>{});
    if constexpr (!FIRST && G == 8) asm volatile("" :: "v"(a.negm[0]), "v"(a.negm[1]));
    if constexpr (DK) { if constexpr (G == 0) setm0(a.m0k + (X ^ 1) * 16384); if constexpr ((G & 1) && G < 16) dma<((G >> 1) - 4) * 1024>(a.voff[G >> 1], gk); }
    if constexpr (DV) { if constexpr (G == 16) setm0(a.m0v + X * 16384); if constexpr ((G & 1) && G > 16) dma<(((G - 16) >> 1) - 4) * 1024>(a.voff[(G - 16) >> 1], gv); }
    if constexpr (!FIRST) {
      if constexpr (G < 16) { sm_exp<XP, 1, 2 * G>(a); sm_exp<XP, 1, 2 * G + 1>(a); sm_pack<XP, G>(a); }
      else { sm_pack<XP, G>(a); sm_add<XP, 1, 2 * (G - 16)>(a); sm_add<XP, 1, 2 * (G - 16) + 1>(a); }
    }
    GAP();
  };
  UNROLL(32, gap);
  pf();
  if constexpr (!FIRST) {
#pragma unroll
    for (int qb = 0; qb < 2; ++qb) { a.l[qb] += a.rs0[qb] + a.rs1[qb]; asm volatile("" : "+v"(a.l[qb])); }
  }
}
DI float swap32_max(float x) { auto r = __builtin_amdgcn_permlane32_swap(__float_as_uint(x), __float_as_uint(x), false, false); return fmaxx(__uint_as_float(r[0]), __uint_as_float(r[1])); }
template <int X, bool NOPV, bool ZERO> DI void phaseB(AT& a) {
  const unsigned vslot = (X ^ 1) * 16384;
  b4 vlo[16], vhi[16];
  auto vread = [&](auto ic) __attribute__((always_inline)) {
    constexpr int I = decltype(ic)::value, k4 = I >> 2, db = I & 3;
    vlo[I] = ldT(a.vb_[0], vslot + 2048 * (2 * k4) + 512 * db);
    vhi[I] = ldT(a.vb_[1], vslot + 2048 * (2 * k4 + 1) + 512 * db);
  };
  if constexpr (!NOPV) { vread(IC<0>{}); vread(IC<1>{}); vread(IC<2>{}); }
  float rmax[2];
  auto pv = [&](auto gc) __attribute__((always_inline)) {
    constexpr int G = decltype(gc)::value, I = G >> 1, k4 = I >> 2, db = I & 3, qb = G & 1;
    if constexpr (!NOPV) {
      mfma32<true, ZERO && k4 == 0>(a.O[db][qb], cat(vlo[I], vhi[I]), a.P[qb][k4]);
      if constexpr ((G & 1) == 0 && I + 3 < 16) vread(IC<I + 3>{});
    }
  };
  auto gap1 = [&](auto gc) __attribute__((always_inline)) {
    constexpr int G = decltype(gc)::value;
    pv(gc);
    if constexpr (G >= 4 && G < 12) {
      constexpr int i = G - 4, q = i >> 2, r = 4 * (i & 3);
      if constexpr ((i & 3) == 0) { a.pm0[q] = fmax3(a.S[X][0][q][0], a.S[X][0][q][1], a.S[X][1][q][0]); a.pm1[q] = fmax3(a.S[X][0][q][2], a.S[X][0][q][3], a.S[X][1][q][1]);
                                    a.pm0[q] = fmax3(a.pm0[q], a.S[X][1][q][2], a.S[X][1][q][3]); }
      else { a.pm0[q] = fmax3(a.pm0[q], a.S[X][0][q][r], a.S[X][0][q][r + 1]); a.pm1[q] = fmax3(a.pm1[q], a.S[X][0][q][r + 2], a.S[X][0][q][r + 3]);
             a.pm0[q] = fmax3(a.pm0[q], a.S[X][1][q][r], a.S[X][1][q][r + 1]); a.pm1[q] = fmax3(a.pm1[q], a.S[X][1][q][r + 2], a.S[X][1][q][r + 3]); }
    }
    if constexpr (G == 12 || G == 13) { constexpr int q = G - 12; rmax[q] = swap32_max(fmaxx(a.pm0[q], a.pm1[q])); }
    GAP();
  };
  UNROLL(14, gap1);
  if constexpr (NOPV) {
#pragma unroll
    for (int q = 0; q < 2; ++q) {
      a.m[q] = rmax[q]; a.l[q] = 0.f; a.fct[q] = 1.f;
#pragma unroll
      for (int kb = 0; kb < 2; ++kb)
#pragma unroll
        for (int e = 0; e < 16; ++e) a.S[X][kb][q][e] -= rmax[q];
#pragma unroll
      for (int e = 0; e < 16; ++e) a.negm[q][e] = -rmax[q];
      asm volatile("" : "+v"(a.negm[q]));
    }
  } else {
    const bool grew = (rmax[0] > THR) | (rmax[1] > THR);
    if (__builtin_expect(__any((int)grew), 0)) {
#pragma unroll
      for (int q = 0; q < 2; ++q) {
        const float dl = fmaxx(rmax[q], 0.f); a.m[q] += dl; a.fct[q] = __builtin_amdgcn_exp2f(-dl);
#pragma unroll
        for (int kb = 0; kb < 2; ++kb)
#pragma unroll
          for (int e = 0; e < 16; ++e) a.S[X][kb][q][e] -= dl;
#pragma unroll
        for (int e = 0; e < 16; ++e) a.negm[q][e] = -a.m[q];
        asm volatile("" : "+v"(a.negm[q]));
      }
      a.pend = true;
    }
  }
  a.rs0[0] = a.rs0[1] = a.rs1[0] = a.rs1[1] = 0.f;
  GAP();
  auto gap2 = [&](auto gc) __attribute__((always_inline)) {
    constexpr int G = decltype(gc)::value + 14;
    pv(IC<G>{});
    if constexpr (G >= 15) {
      constexpr int e0 = 2 * (G - 15);
      if constexpr (e0 < 32) { sm_exp<X, 0, e0>(a); sm_exp<X, 0, e0 + 1>(a); }
      if constexpr (G >= 16) { sm_add<X, 0, e0 - 2>(a); sm_add<X, 0, e0 - 1>(a); }
    }
    GAP();
  };
  UNROLL(18, gap2);
  asm volatile("" : "+v"(a.S[X][0][0]), "+v"(a.S[X][0][1]), "+v"(a.rs0[0]), "+v"(a.rs0[1]), "+v"(a.rs1[0]), "+v"(a.rs1[1]));
  if constexpr (!NOPV) {
    if (__builtin_expect(a.pend, 0)) {
      asm volatile("s_nop 7\n\ts_nop 7" ::: "memory");
#pragma unroll
      for (int db = 0; db < 4; ++db)
#pragma unroll
        for (int q = 0; q < 2; ++q) {
          asm volatile("" : "+a"(a.O[db][q]));
          f16v o = a.O[db][q];
#pragma unroll
          for (int e = 0; e < 16; ++e) o[e] *= a.fct[q];
          a.O[db][q] = o;
          asm volatile("" : "+a"(a.O[db][q]));
        }
      a.l[0] *= a.fct[0]; a.l[1] *= a.fct[1]; a.pend = false;
      asm volatile("s_nop 1" ::: "memory");
    }
  }
}

template <int X, bool FIRST, bool DK, bool DV, typename PF> DI void fastA(AT& a, int t, PF&& pf) {
  constexpr int XP = X ^ 1;
  const unsigned kslot = X * 16384;
  b8 kf[16];
  auto kread = [&](auto ic) __attribute__((always_inline)) { constexpr int I = decltype(ic)::value, ks = I >> 1, kb = I & 1; kf[I] = ldA(a.kb_[ks & 1], kslot + 8192 * kb + 512 * (ks >> 1)); };
  kread(IC<0>{}); kread(IC<1>{}); kread(IC<2>{}); kread(IC<3>{});
  const char* gk = a.gk + (size_t)(t + 1) * 16384; const char* gv = a.gv + (size_t)t * 16384;
  auto gap = [&](auto gc) __attribute__((always_inline)) {
    constexpr int G = decltype(gc)::value, ks = G >> 2, kb = (G >> 1) & 1, qb = G & 1, I = G >> 1;
    qk<(ks == 0) ? (FIRST ? 0 : 1) : 2>(a.S[X][kb][qb], kf[I], a.q[qb][ks], a.negm[qb]);
    if constexpr ((G & 1) == 0 && I + 4 < 16) kread(IC<I + 4>{});
    if constexpr (!FIRST && G == 8) asm volatile("" :: "v"(a.negm[0]), "v"(a.negm[1]));
    if constexpr (DK) { if constexpr (G == 0) setm0(a.m0k + (X ^ 1) * 16384); if constexpr ((G & 1) && G < 16) dma<((G >> 1) - 4) * 1024>(a.voff[G >> 1], gk); }
    if constexpr (DV) { if constexpr (G == 16) setm0(a.m0v + X * 16384); if constexpr ((G & 1) && G > 16) dma<(((G - 16) >> 1) - 4) * 1024>(a.voff[(G - 16) >> 1], gv); }
    if constexpr (!FIRST) {
      sm_exp<XP, 1, G>(a);
      if constexpr (G >= 1) sm_add<XP, 1, G - 1>(a);
      if constexpr (G >= 3 && (G & 1)) sm_pack<XP, 16 + (G - 3) / 2>(a);
    }
    GAP();
  };
  UNROLL(32, gap);
  pf();
  if constexpr (!FIRST) {
    sm_add<XP, 1, 31>(a); sm_pack<XP, 31>(a);
#pragma unroll
    for (int qb = 0; qb < 2; ++qb) { a.l[qb] += a.rs0[qb] + a.rs1[qb]; asm volatile("" : "+v"(a.l[qb])); }
  }
}
constexpr int fb_e(int G) { return G < 2 ? 0 : G - 2 + (G > 5) + (G > 9); }
constexpr int dr_e(int n) { return n < 8 ? n : n < 16 ? n + 8 : n < 24 ? n - 8 : n; }
template <int X, int MODE> DI void fastB(AT& a) {
  constexpr bool ZERO = MODE == 1, DRAIN = MODE == 3;
  if constexpr (MODE == 2) {
#pragma unroll
    for (int q = 0; q < 2; ++q) {
      float p0 = fmax3(a.S[X][0][q][0], a.S[X][1][q][0], a.S[X][0][q][2]), p1 = fmax3(a.S[X][0][q][1], a.S[X][1][q][1], a.S[X][0][q][3]);
      p0 = fmaxx(p0, a.S[X][1][q][2]); p1 = fmaxx(p1, a.S[X][1][q][3]);
#pragma unroll
      for (int r = 4; r < 16; r += 2) { p0 = fmax3(p0, a.S[X][0][q][r], a.S[X][1][q][r]); p1 = fmax3(p1, a.S[X][0][q][r + 1], a.S[X][1][q][r + 1]); }
      const float mx = swap32_max(fmaxx(p0, p1));
      f2 m2 = {mx, mx}; asm volatile("" : "+v"(m2));
      a.l[q] = 0.f;
#pragma unroll
      for (int kb = 0; kb < 2; ++kb)
#pragma unroll
        for (int e = 0; e < 16; e += 2) { const f2 v = (f2){a.S[X][kb][q][e], a.S[X][kb][q][e + 1]} - m2; a.S[X][kb][q][e] = v[0]; a.S[X][kb][q][e + 1] = v[1]; }
#pragma unroll
      for (int e = 0; e < 16; ++e) a.negm[q][e] = -mx;
      asm volatile("" : "+v"(a.negm[q]));
    }
    a.rs0[0] = a.rs0[1] = a.rs1[0] = a.rs1[1] = 0.f;
    { auto f = [&](auto ic) __attribute__((always_inline)) { sm_exp<X, 0, decltype(ic)::value>(a); }; UNROLL(32, f); }
    { auto f = [&](auto ic) __attribute__((always_inline)) { sm_add<X, 0, decltype(ic)::value>(a); }; UNROLL(32, f); }
    { auto f = [&](auto ic) __attribute__((always_inline)) { sm_pack<X, decltype(ic)::value>(a); }; UNROLL(16, f); }
  } else {
    const unsigned vslot = (DRAIN ? X : X ^ 1) * 16384;
    b4 vlo[16], vhi[16];
    auto vread = [&](auto ic) __attribute__((always_inline)) {
      constexpr int I = decltype(ic)::value, k4 = I >> 2, db = I & 3;
      vlo[I] = ldT(a.vb_[0], vslot + 2048 * (2 * k4) + 512 * db);
      vhi[I] = ldT(a.vb_[1], vslot + 2048 * (2 * k4 + 1) + 512 * db);
    };
    vread(IC<0>{}); vread(IC<1>{}); vread(IC<2>{});
    if constexpr (!DRAIN) a.rs0[0] = a.rs0[1] = a.rs1[0] = a.rs1[1] = 0.f;
    auto gap = [&](auto gc) __attribute__((always_inline)) {
      constexpr int G = decltype(gc)::value, I = G >> 1, k4 = I >> 2, db = I & 3, qb = G & 1;
      mfma32<true, ZERO && k4 == 0>(a.O[db][qb], cat(vlo[I], vhi[I]), a.P[qb][k4]);
      if constexpr ((G & 1) == 0 && I + 3 < 16) vread(IC<I + 3>{});
      if constexpr (DRAIN) {
        if constexpr (G < 16) { sm_exp<X, 1, dr_e(2 * G)>(a); sm_exp<X, 1, dr_e(2 * G) + 1>(a); }
        if constexpr (G >= 2 && G < 18) sm_pack<X, 16 + dr_e(2 * (G - 2)) / 2>(a);
        if constexpr (G >= 16) { sm_add<X, 1, dr_e(2 * (G - 16))>(a); sm_add<X, 1, dr_e(2 * (G - 16)) + 1>(a); }
      } else {
        if constexpr (fb_e(G) < fb_e(G + 1)) sm_exp<X, 0, fb_e(G)>(a);
        if constexpr (fb_e(G) + 1 < fb_e(G + 1)) sm_exp<X, 0, fb_e(G) + 1>(a);
        if constexpr (G >= 3 && fb_e(G - 1) < fb_e(G)) sm_add<X, 0, G >= 3 ? fb_e(G - 1) : 0>(a);
        if constexpr (G >= 3 && fb_e(G - 1) + 1 < fb_e(G)) sm_add<X, 0, G >= 3 ? fb_e(G - 1) + 1 : 0>(a);
        if constexpr (G >= 16 && G <= 29) sm_pack<X, G >= 16 ? G - 16 : 0>(a);
        if constexpr (G == 31) sm_pack<X, 14>(a);
      }
      GAP();
    };
    UNROLL(32, gap);
    if constexpr (!DRAIN) {
      sm_add<X, 0, 31>(a);
      static_assert(fb_e(32) == 32 && fb_e(28) >= 28 && fb_e(30) >= 30, "schedule");
      sm_pack<X, 15>(a);
      asm volatile("" : "+v"(a.S[X][0][0]), "+v"(a.S[X][0][1]), "+v"(a.rs0[0]), "+v"(a.rs0[1]), "+v"(a.rs1[0]), "+v"(a.rs1[1]));
    } else {
#pragma unroll
      for (int qb = 0; qb < 2; ++qb) a.l[qb] += a.rs0[qb] + a.rs1[qb];
    }
  }
}

struct Params { const u16 *x, *c, *wmod, *wq, *kc, *vc, *wo, *wg, *wu, *wd; u16* out; char* ws; float one; unsigned gap; };

__global__ __launch_bounds__(256, 1) void k_main(Params p) {
  extern __shared__ __attribute__((aligned(16))) char lds[];
  const int tid = threadIdx.x, w = __builtin_amdgcn_readfirstlane(tid >> 6), lane = tid & 63;
  const int j = blockIdx.x & 7, c = blockIdx.x >> 3;
  const unsigned ldsb = (unsigned)(size_t)lds;
  const int hh = lane >> 5, r32 = lane & 31;
  u64* const flags = (u64*)(p.ws + WS_FLAG);
  unsigned* const tmo = (unsigned*)(p.ws + WS_TMO);
  STAMP(0);
  PST(0);
  QS qs;
  unsigned voffQ[8];
#pragma unroll
  for (int i = 0; i < 8; ++i) voffQ[i] = (4 * i + (lane >> 4)) * 8192 + 16 * ((lane & 15) ^ ((lane >> 4) << 2)) - (i - 4) * 1024 + 4096;
  const char* gq = (const char*)p.wq + (size_t)(4 * j + w) * 256 - 4096;
  qs.ring = __builtin_amdgcn_readfirstlane(ldsb + ABYTES + w * 2 * QSLOT);
  constexpr size_t KS_Q = (size_t)32 * 8192;
  auto issueq = [&](int slot, const char* gp) __attribute__((always_inline)) {
    setm0(qs.ring + slot * QSLOT + 4096); asm volatile("s_nop 0");
    dma<-4096>(voffQ[0], gp); dma<-3072>(voffQ[1], gp); dma<-2048>(voffQ[2], gp); dma<-1024>(voffQ[3], gp);
    dma<0>(voffQ[4], gp); dma<1024>(voffQ[5], gp); dma<2048>(voffQ[6], gp); dma<3072>(voffQ[7], gp);
  };
  const int mg = blockIdx.x % 54, ms = (blockIdx.x / 54) & 1;
  u4 wm[9]; u16 cval[9];
#pragma unroll
  for (int i = 0; i < 9; ++i) {
    const int r = 288 * ms + (tid >> 3) + 32 * i;
    wm[i] = *(const u4*)(p.wmod + (size_t)r * (6 * D) + mg * 64 + (tid & 7) * 8); cval[i] = p.c[r];
  }
  const u64 e0 = __hip_atomic_load((gu64*)(p.ws + WS_EPOCH), __ATOMIC_RELAXED, __HIP_MEMORY_SCOPE_AGENT);
  AT a;
  u64 tag;
  {
    f16v Qt[4][2];
    QS& s = qs;
    {
      const int rr = lane >> 3, sub = lane & 7;
      tag = ((u64)(unsigned)__builtin_amdgcn_readfirstlane((unsigned)(e0 >> 32)) << 32 | (u64)(unsigned)__builtin_amdgcn_readfirstlane((unsigned)e0)) + 1;
      PST(9);
      if (blockIdx.x < 108) {
        float acc[8] = {};
#pragma unroll
        for (int i = 0; i < 9; ++i) {
          const float xc = bf2f(cval[i]);
          const float sc = rbf(xc * __builtin_amdgcn_rcpf(1.f + __expf(-xc)));
#pragma unroll
          for (int e = 0; e < 4; ++e) { acc[2 * e] += sc * bflo(wm[i][e]); acc[2 * e + 1] += sc * bfhi(wm[i][e]); }
        }
        LAS float* red = (LAS float*)(size_t)(ldsb + LDS_X1);
#pragma unroll
        for (int e = 0; e < 8; ++e) { float t = acc[e]; t += dpp<0x128>(t); t = swap32_add(swap16_add(t)); if (lane < 8) red[w * 64 + lane * 8 + e] = t; }
        asm volatile("s_waitcnt lgkmcnt(0)" ::: "memory");
        BAR();
        if (tid < 32) {
          f2 v = {0.f, 0.f};
#pragma unroll
          for (int k = 0; k < 4; ++k) v += *(LAS f2*)(red + k * 64 + 2 * tid);
          const __amdgpu_buffer_rsrc_t rP = __builtin_amdgcn_make_buffer_rsrc(p.ws + WS_MODP, 0, 54 * 1024, 0x00020000);
          __builtin_amdgcn_raw_buffer_store_b128((u4){__float_as_uint(v[0]), __float_as_uint(v[1]), (unsigned)tag, (unsigned)(tag >> 32)}, rP, mg * 1024 + ms * 512 + tid * 16, 0, 16);
        }
      }
      PST(10);
      PST(11);
      {
        const u64 t0_ = __builtin_amdgcn_s_memrealtime();
        if (c >= 16) {
          const unsigned dummy = __builtin_amdgcn_readfirstlane(ldsb + LDS_DUMMY);
          while (__builtin_amdgcn_s_memrealtime() - t0_ < PF_D) __builtin_amdgcn_s_sleep(2);
          const int li = tid < 144 ? tid : tid - 112;
          touch((const char*)p.wq + (size_t)512 * j * 2, (18 * (c - 16) + (li >> 3)) * 8192 + (li & 7) * 128, dummy);
          touch((const char*)p.wq + (size_t)512 * j * 2, (18 * c + (li >> 3)) * 8192 + (li & 7) * 128, dummy);
          touch((const char*)(w < 2 ? p.kc : p.vc) + ((size_t)(2 * j + (w & 1)) * SK + (SK - WIN)) * DH * 2, (32 * (c - 16 * (lane >> 5)) + (lane & 31)) * 128, dummy);
          while (__builtin_amdgcn_s_memrealtime() - t0_ < STAG_D) __builtin_amdgcn_s_sleep(2);
        }
      }
      u4 xv[2][9];
      GAP();
#pragma unroll
      for (int ps = 0; ps < 2; ++ps)
#pragma unroll
        for (int i = 0; i < 9; ++i) xv[ps][i] = *(const u4*)(p.x + (size_t)(c * 64 + 16 * w + 8 * ps + rr) * D + (sub + 8 * i) * 8);
      GAP();
      PST(1);
      STAMP(20);
      PST(2);
      f2 y[2][9][4];
      ModGather<1728, 256> mgat;
      {
#pragma unroll
        for (int ps = 0; ps < 2; ++ps) {
          f2 a1 = {0.f, 0.f}, a2 = {0.f, 0.f};
#pragma unroll
          for (int i = 0; i < 9; ++i)
#pragma unroll
            for (int e = 0; e < 4; ++e) { y[ps][i][e] = bf2(xv[ps][i][e]); a1 += y[ps][i][e]; a2 = __builtin_elementwise_fma(y[ps][i][e], y[ps][i][e], a2); }
          float s1 = a1[0] + a1[1], s2 = a2[0] + a2[1];
          s1 = sum8(s1); s2 = sum8(s2);
          const float mean = s1 * (1.f / D), var = __builtin_fmaxf(__builtin_fmaf(-mean, mean, s2 * (1.f / D)), 0.f);
          const float mr = rbf(mean), rs = __builtin_amdgcn_rcpf(rbf(__builtin_amdgcn_sqrtf(rbf(rbf(var) + 1e-5f))));
          f2 mr2 = {mr, mr}, rs2 = {rs, rs}; asm volatile("" : "+v"(mr2), "+v"(rs2));
          if (ps == 1) { GAP(); mgat.issue(p.ws, tid); GAP(); }
#pragma unroll
          for (int i = 0; i < 9; ++i) {
            f2 t[4];
#pragma unroll
            for (int e = 0; e < 4; ++e) t[e] = y[ps][i][e] - mr2;
#pragma unroll
            for (int e = 0; e < 4; ++e) t[e] = rb2(t[e]);
#pragma unroll
            for (int e = 0; e < 4; ++e) t[e] = t[e] * rs2;
#pragma unroll
            for (int e = 0; e < 4; ++e) y[ps][i][e] = rb2(t[e]);
            asm volatile("" : "+v"(y[ps][i][0]), "+v"(y[ps][i][1]), "+v"(y[ps][i][2]), "+v"(y[ps][i][3]));
          }
        }
      }
      STAMP(21);
      PST(4);
      mgat.finish(tag, ldsb, tmo, tid);
      STAMP(22);
      PST(5);
      issueq(0, gq); issueq(1, gq + KS_Q);
      asm volatile("s_waitcnt lgkmcnt(0)" ::: "memory");
      BAR();
      STAMP(23);
      PST(6);
      {
        const unsigned tb = ldsb + LDS_X1 + 1024 + sub * 32;
        f4 cst[2][4];
        auto ldc = [&](int i, f4 (&d)[4]) { d[0] = *(LAS f4*)(size_t)(tb + i * 256); d[1] = *(LAS f4*)(size_t)(tb + i * 256 + 16); d[2] = *(LAS f4*)(size_t)(tb + i * 256 + D * 4); d[3] = *(LAS f4*)(size_t)(tb + i * 256 + D * 4 + 16); };
        ldc(0, cst[0]);
        const unsigned ad0 = ldsb + (16 * w + rr) * ROWB + ((sub ^ ((rr >> 1) & 7)) * 16);
        const unsigned ad1 = ldsb + (16 * w + 8 + rr) * ROWB + ((sub ^ (((rr >> 1) + 4) & 7)) * 16);
#pragma unroll
        for (int i = 0; i < 9; ++i) {
          if (i + 1 < 9) ldc(i + 1, cst[(i + 1) & 1]);
          GAP();
          const f4 (&k)[4] = cst[i & 1];
          const f2 sh[4] = {{k[0][0], k[0][1]}, {k[0][2], k[0][3]}, {k[1][0], k[1][1]}, {k[1][2], k[1][3]}}, cc[4] = {{k[2][0], k[2][1]}, {k[2][2], k[2][3]}, {k[3][0], k[3][1]}, {k[3][2], k[3][3]}};
          f2 t[2][4]; u4 o[2];
#pragma unroll
          for (int ps = 0; ps < 2; ++ps)
#pragma unroll
            for (int e = 0; e < 4; ++e) t[ps][e] = y[ps][i][e] * cc[e];
#pragma unroll
          for (int ps = 0; ps < 2; ++ps)
#pragma unroll
            for (int e = 0; e < 4; ++e) t[ps][e] = rb2(t[ps][e]);
#pragma unroll
          for (int ps = 0; ps < 2; ++ps)
#pragma unroll
            for (int e = 0; e < 4; ++e) t[ps][e] = t[ps][e] + sh[e];
#pragma unroll
          for (int ps = 0; ps < 2; ++ps)
#pragma unroll
            for (int e = 0; e < 4; ++e) o[ps][e] = pk(t[ps][e]);
          *(LAS u4*)(size_t)(ad0 + i * 128) = o[0];
          *(LAS u4*)(size_t)(ad1 + i * 128) = o[1];
          GAP();
        }
      }
    }
    {
      const int blk = (lane >> 4) & 1, q = (lane & 15) >> 2, pp = lane & 3;
#pragma unroll
      for (int dt = 0; dt < 4; ++dt) s.wb[dt] = s.ring + (8 * hh + q) * 256 + 64 * (dt ^ q) + 32 * blk + 16 * (pp & 1) + 8 * (pp >> 1);
#pragma unroll
      for (int i = 0; i < 4; ++i) s.hb[i] = ldsb + r32 * ROWB + (((2 * i + hh) ^ ((r32 >> 1) & 7)) * 16);
    }
    asm volatile("s_waitcnt vmcnt(8) lgkmcnt(0)" ::: "memory");
    BAR();
    STAMP(1);
    PST(7);
#pragma unroll
    for (int f = 0; f < 8; ++f) { s.wlo[0][f] = ldT(s.wb[f & 3], 4096 * (f >> 2)); s.whi[0][f] = ldT(s.wb[f & 3], 4096 * (f >> 2) + 1024); }
#pragma unroll
    for (int i = 0; i < 4; ++i) s.hf[0][i] = ldA(s.hb[i >> 1], (i & 1) * 32 * ROWB);
    asm volatile("s_waitcnt lgkmcnt(0)" ::: "memory");
    issueq(0, gq + 2 * KS_Q);
    auto no = [&]() __attribute__((always_inline)) {};
    qstep<0, true, true, true, 8>(s, Qt, voffQ, gq + 3 * KS_Q, 0, no);
    qstep<1, false, true, true, 8>(s, Qt, voffQ, gq + 4 * KS_Q, 128, no);
    for (int n = 2; n < 14; n += 2) {
      qstep<0, false, true, true, 8>(s, Qt, voffQ, gq + (size_t)(n + 3) * KS_Q, 128 * ((n + 1) >> 1), no);
      qstep<1, false, true, true, 8>(s, Qt, voffQ, gq + (size_t)(n + 4) * KS_Q, 128 * ((n + 2) >> 1), no);
    }
    qstep<0, false, true, true, 8>(s, Qt, voffQ, gq + (size_t)17 * KS_Q, 128 * 7, no);
    qstep<1, false, true, false, 8>(s, Qt, voffQ, gq, 128 * 8, no);
    qstep<0, false, true, false, 0>(s, Qt, voffQ, gq, 128 * 8, no);
    qstep<1, false, false, false, 0>(s, Qt, voffQ, gq, 0, no);
    STAMP(2);
    PST(8);
    {
      const int kvl = w >> 1, half = w & 1;
      const unsigned kr = ldsb + kvl * 65536, vr = kr + 32768;
#pragma unroll
      for (int i = 0; i < 2; ++i) a.kb_[i] = kr + 2048 * (r32 >> 3) + 64 * (r32 & 7) + 16 * ((2 * i + hh) ^ ((r32 >> 2) & 3));
      const int blk = (lane >> 4) & 1, q = (lane & 15) >> 2, pp = lane & 3;
#pragma unroll
      for (int t = 0; t < 2; ++t) a.vb_[t] = vr + 64 * (4 * hh + q) + 16 * ((2 * blk + (pp >> 1)) ^ ((2 * t + hh) & 3)) + 8 * (pp & 1);
      const int st = lane >> 5, r7 = (lane >> 2) & 7, pos = lane & 3;
#pragma unroll
      for (int i = 0; i < 8; ++i) {
        const int rg = 4 * half + (i >> 1);
        a.voff[i] = rg * 2048 + r7 * 256 + (i & 1) * 128 + st * 64 + 16 * (pos ^ ((2 * (rg & 1) + (r7 >> 2)) & 3)) - (i - 4) * 1024 + 4096;
      }
      a.m0k = __builtin_amdgcn_readfirstlane(kr + 8192 * half + 4096); a.m0v = __builtin_amdgcn_readfirstlane(vr + 8192 * half + 4096);
      const size_t kvoff = ((size_t)(2 * j + kvl) * SK + (SK - WIN)) * DH * 2;
      a.gk = (const char*)p.kc + kvoff - 4096; a.gv = (const char*)p.vc + kvoff - 4096;
      a.pend = false; a.one = p.one;
    }
    asm volatile("s_waitcnt lgkmcnt(0)" ::: "memory");
    BAR();
    setm0(a.m0k); asm volatile("s_nop 0");
    dma<-4096>(a.voff[0], a.gk); dma<-3072>(a.voff[1], a.gk); dma<-2048>(a.voff[2], a.gk); dma<-1024>(a.voff[3], a.gk);
    dma<0>(a.voff[4], a.gk); dma<1024>(a.voff[5], a.gk); dma<2048>(a.voff[6], a.gk); dma<3072>(a.voff[7], a.gk);
    asm volatile("s_nop 7" ::: "memory");
#pragma unroll
    for (int dt = 0; dt < 4; ++dt)
#pragma unroll
      for (int qb = 0; qb < 2; ++qb) {
        asm volatile("" : "+v"(Qt[dt][qb]));
#pragma unroll
        for (int s2 = 0; s2 < 2; ++s2) {
          u4 w4;
#pragma unroll
          for (int e = 0; e < 4; ++e) w4[e] = pk((f2){Qt[dt][qb][8 * s2 + 2 * e], Qt[dt][qb][8 * s2 + 2 * e + 1]} * QSCALE);
          a.q[qb][2 * dt + s2] = __builtin_bit_cast(b8, w4);
          asm volatile("" : "+a"(a.q[qb][2 * dt + s2]));
        }
      }
  }
  STAMP(3);
  {
    auto tile_bar = [&]() __attribute__((always_inline)) { asm volatile("s_waitcnt vmcnt(0) lgkmcnt(0)" ::: "memory"); BAR(); };
    const char* pfp = lane >= 32 ? p.ws + WS_X + (size_t)c * 64 * SLICE + (size_t)j * SLICE + (18 * w + lane - 32) * 128
                    : w < 2 ? (const char*)(w == 0 ? p.wg : p.wu) + (size_t)576 * j * 2 + (size_t)(18 * c + lane / 9 - 3) * (DF * 2) + (lane % 9) * 128
                            : (const char*)p.wo + (size_t)512 * j * ROWB + (16 * c) * ROWB + (12 * (w - 2) + lane - 24) * 128;
    const int pfs = lane >= 32 ? 8 * SLICE : w < 2 ? 3 * DF * 2 : 24 * 128, pfn = w < 2 ? 27 : 12;
    auto pft = [&](int t) __attribute__((always_inline)) {
      const bool on = lane < pfn && t >= 1 && t <= 6;
      touchp(on ? pfp : (const char*)p.wo, __builtin_amdgcn_readfirstlane(ldsb + LDS_DUMMY));
      pfp += pfs;
    };
    auto tile_bar1 = [&]() __attribute__((always_inline)) { asm volatile("s_waitcnt vmcnt(1) lgkmcnt(0)" ::: "memory"); BAR(); };
    LAS unsigned* flag = (LAS unsigned*)(size_t)(ldsb + LDS_X1);
    if (tid == 0) *flag = 0u;
    auto k0 = [&]() __attribute__((always_inline)) {
      setm0(a.m0k); asm volatile("s_nop 0");
      dma<-4096>(a.voff[0], a.gk); dma<-3072>(a.voff[1], a.gk); dma<-2048>(a.voff[2], a.gk); dma<-1024>(a.voff[3], a.gk);
      dma<0>(a.voff[4], a.gk); dma<1024>(a.voff[5], a.gk); dma<2048>(a.voff[6], a.gk); dma<3072>(a.voff[7], a.gk);
    };
    tile_bar(); fastA<0, true, true, true>(a, 0, [&]() __attribute__((always_inline)) { pft(0); }); asm volatile("s_nop 7\n\ts_nop 3" ::: "memory"); GAP(); fastB<0, 2>(a);
    tile_bar1(); fastA<1, false, true, true>(a, 1, [&]() __attribute__((always_inline)) { pft(1); }); fastB<1, 1>(a);
    for (int t = 2; t < 6; t += 2) {
      tile_bar1(); fastA<0, false, true, true>(a, t, [&]() __attribute__((always_inline)) { pft(t); }); fastB<0, 0>(a);
      tile_bar1(); fastA<1, false, true, true>(a, t + 1, [&]() __attribute__((always_inline)) { pft(t + 1); }); fastB<1, 0>(a);
    }
    tile_bar1(); fastA<0, false, true, true>(a, 6, [&]() __attribute__((always_inline)) { pft(6); }); fastB<0, 0>(a);
    tile_bar1(); fastA<1, false, false, true>(a, 7, [&]() __attribute__((always_inline)) { pft(7); }); fastB<1, 0>(a);
    tile_bar1();
    fastB<1, 3>(a);
    {
      const bool bad = !(a.l[0] < 0x1p40f) || !(a.l[1] < 0x1p40f);
      if (__any((int)bad) && lane == 0) *flag = 1u;
      asm volatile("s_waitcnt lgkmcnt(0)" ::: "memory"); BAR();
      unsigned redo = __builtin_amdgcn_readfirstlane(*(volatile LAS unsigned*)flag); asm volatile("" : "+s"(redo));
      if (__builtin_expect(redo != 0u, 0)) {
        auto nopf = [&]() __attribute__((always_inline)) {};
        a.pend = false;
        auto upt = [&](const char* q) __attribute__((always_inline)) { const u64 v = (u64)q; return (const char*)((u64)(unsigned)__builtin_amdgcn_readfirstlane((unsigned)(v >> 32)) << 32 | (u64)(unsigned)__builtin_amdgcn_readfirstlane((unsigned)v)); };
        a.gk = upt(a.gk); a.gv = upt(a.gv); a.m0k = __builtin_amdgcn_readfirstlane(a.m0k); a.m0v = __builtin_amdgcn_readfirstlane(a.m0v);
        k0();
        tile_bar(); phaseA<0, true, true, true>(a, 0, nopf); asm volatile("s_nop 7\n\ts_nop 3" ::: "memory"); GAP(); phaseB<0, true, false>(a);
        tile_bar(); phaseA<1, false, true, true>(a, 1, nopf); phaseB<1, false, true>(a);
        for (int t = 2; t < 6; t += 2) {
          tile_bar(); phaseA<0, false, true, true>(a, t, nopf); phaseB<0, false, false>(a);
          tile_bar(); phaseA<1, false, true, true>(a, t + 1, nopf); phaseB<1, false, false>(a);
        }
        tile_bar(); phaseA<0, false, true, true>(a, 6, nopf); phaseB<0, false, false>(a);
        tile_bar(); phaseA<1, false, false, true>(a, 7, nopf); phaseB<1, false, false>(a);
        { auto f1 = [&](auto ic) __attribute__((always_inline)) { sm_exp<1, 1, decltype(ic)::value>(a); }; UNROLL(32, f1);
          auto f2_ = [&](auto ic) __attribute__((always_inline)) { sm_pack<1, decltype(ic)::value>(a); }; UNROLL(32, f2_);
          auto f3 = [&](auto ic) __attribute__((always_inline)) { sm_add<1, 1, decltype(ic)::value>(a); }; UNROLL(32, f3); }
#pragma unroll
        for (int qb = 0; qb < 2; ++qb) a.l[qb] += a.rs0[qb] + a.rs1[qb];
        tile_bar();
        const unsigned vslot = 16384;
        b4 vlo[16], vhi[16];
#pragma unroll
        for (int I = 0; I < 16; ++I) { vlo[I] = ldT(a.vb_[0], vslot + 2048 * (2 * (I >> 2)) + 512 * (I & 3)); vhi[I] = ldT(a.vb_[1], vslot + 2048 * (2 * (I >> 2) + 1) + 512 * (I & 3)); }
        asm volatile("s_nop 1" ::: "memory"); GAP();
#pragma unroll
        for (int G = 0; G < 32; ++G) { const int I = G >> 1; mfma32<true, false>(a.O[I & 3][G & 1], cat(vlo[I], vhi[I]), a.P[G & 1][I >> 2]); }
        asm volatile("s_waitcnt vmcnt(0) lgkmcnt(0)" ::: "memory"); BAR();
      }
    }
  }
  STAMP(4);
  int tid2 = threadIdx.x; asm volatile("" : "+v"(tid2));
  {
  const int tid = tid2, lane = tid2 & 63, hh = lane >> 5, r32 = lane & 31;
  GS s;
  unsigned voffB[9];
#pragma unroll
  for (int i = 0; i < 9; ++i) { const int idx = i * 64 + lane, imm = i < 8 ? (i - 4) * 1024 : 0; voffB[i] = (idx / 18) * ROWB + (idx % 18) * 16 - imm + 4096; }
  s.ring = __builtin_amdgcn_readfirstlane(ldsb + ABYTES + w * 2 * SLOT);
  const int g4 = lane >> 4, cl = lane & 15;
  s.wbase = s.ring + (4 * g4 + ((lane & 15) >> 2)) * 288 + 8 * (lane & 3);
  const unsigned sw = (cl >> 1) & 7;
  const unsigned ab_e = ldsb + cl * ROWB + ((g4 ^ sw) * 16), ab_o = ldsb + cl * ROWB + (((4 + g4) ^ sw) * 16);
  const __amdgpu_buffer_rsrc_t rX = __builtin_amdgcn_make_buffer_rsrc(p.ws + WS_X + (size_t)c * 64 * SLICE, 0, 64 * SLICE, 0x00020000);
  unsigned voffA[9];
#pragma unroll
  for (int i = 0; i < 9; ++i) { const int idx = i * 64 + lane, imm = i < 8 ? (i - 4) * 1024 : 0; voffA[i] = (idx / 18) * (DF * 2) + (idx % 18) * 16 - imm + 4096; }
  const char* gG = (const char*)p.wg + (size_t)(576 * j + 144 * w) * 2 - 4096;
  const char* gU = (const char*)p.wu + (size_t)(576 * j + 144 * w) * 2 - 4096;
  const char* gD = (const char*)p.wd + (size_t)(576 * j) * ROWB + 288 * w - 4096;
  f4 aG[9][4];
  {
    const char* gO = (const char*)p.wo + (size_t)(512 * j) * ROWB + 288 * w - 4096;
    issue_all(s, 0, voffB, gO); issue_all(s, 1, voffB, gO + (size_t)32 * ROWB);
    asm volatile("s_nop 7" ::: "memory");
#pragma unroll
    for (int qb = 0; qb < 2; ++qb) {
      const float il = __builtin_amdgcn_rcpf(swap32_add(a.l[qb]));
      const int row = 32 * qb + r32; const unsigned swr = (r32 >> 1) & 7;
#pragma unroll
      for (int db = 0; db < 4; ++db) {
        asm volatile("" : "+a"(a.O[db][qb]));
        const f16v o = a.O[db][qb];
#pragma unroll
        for (int u = 0; u < 2; ++u) {
          const u4 wv = {pk((f2){o[4 * u], o[4 * u + 1]} * il), pk((f2){o[4 * u + 2], o[4 * u + 3]} * il), pk((f2){o[4 * u + 8], o[4 * u + 9]} * il), pk((f2){o[4 * u + 10], o[4 * u + 11]} * il)};
          *(LAS u4*)(size_t)(ldsb + row * ROWB + (((4 * (4 * w + db) + 2 * u + hh) ^ swr) * 16)) = wv;
        }
      }
    }
    asm volatile("s_waitcnt vmcnt(9) lgkmcnt(0)" ::: "memory");
    BAR();
    STAMP(5);
    read_w(s, 0); read_a(s, 0, ab_e);
    asm volatile("s_waitcnt lgkmcnt(0)" ::: "memory");
    issue_all(s, 0, voffB, gO + (size_t)2 * 32 * ROWB);
    const Epi e1 = {rX, (unsigned)(j * SLICE), 8u * SLICE, ldsb + LDS_X1, j, w, lane};
    gemm_rows<16, 2, 0>(s, aG, voffB, gO, ab_e, ab_o, voffA, gG, gU, e1, [&]() __attribute__((always_inline)) {});
    STAMP(6);
  }
  {
    int zlr[3], zcc[3];
    const int t3 = tid < 192 ? tid : tid - 64;
    u4 xr[3], gv[3];
#pragma unroll
    for (int i = 0; i < 3; ++i) {
      zdecode(t3 + 192 * i, zlr[i], zcc[i]);
      xr[i] = *(const u4*)(p.x + (size_t)(c * 64 + j * 8 + zlr[i]) * D + zcc[i] * 8);
      gv[i] = *(LAS u4*)(size_t)(ldsb + LDS_MOD + (2 * D + zcc[i] * 8) * 2);
    }
    const int lrow = 2 * w + hh, grow = j * 8 + lrow;
    u2 cbv[3][2], sbv[3][2];
#pragma unroll
    for (int i = 0; i < 3; ++i) {
      const int pos = (i < 2 || r32 < 8) ? r32 + 32 * i : r32;
      const int q = pos ^ ((grow >> 1) & 7), k0 = 32 * (q >> 2) + 4 * (q & 3);
#pragma unroll
      for (int h = 0; h < 2; ++h) { cbv[i][h] = *(LAS u2*)(size_t)(ldsb + LDS_MOD + (4 * D + k0 + 16 * h) * 2); sbv[i][h] = *(LAS u2*)(size_t)(ldsb + LDS_MOD + (3 * D + k0 + 16 * h) * 2); }
    }
    asm volatile("s_waitcnt lgkmcnt(0)" ::: "memory");
    publish(flags + (0 * 256 + blockIdx.x) * FLAG_STRIDE, tag);
    STAMP(7);
    await8(flags + (0 * 256 + c * 8) * FLAG_STRIDE, tag, tmo, w, lane);
    STAMP(8);
    {
      u4 v[3][8];
#pragma unroll
      for (int i = 0; i < 3; ++i) {
#pragma unroll
        for (int jj = 0; jj < 7; ++jj) v[i][jj] = __builtin_amdgcn_raw_buffer_load_b128(rX, (j * 8 + jj + (jj >= j)) * SLICE + (t3 + 192 * i) * 16, 0, AUX_X);
      }
#pragma unroll
      for (int i = 0; i < 3; ++i) v[i][7] = *(LAS u4*)(size_t)(ldsb + LDS_X1 + (t3 + 192 * i) * 16);
#pragma unroll
      for (int i = 0; i < 3; ++i) {
        const u4 x1 = gated_residual(v[i], xr[i], gv[i]);
        if (tid < 192) {
          *(LAS u4*)(size_t)(ldsb + LDS_X1 + (tid + 192 * i) * 16) = x1;
          LAS f4* dst = (LAS f4*)(size_t)(ldsb + (zlr[i] * D + zcc[i] * 8) * 4);
          const f2 a0 = bf2(x1[0]), a1 = bf2(x1[1]), a2 = bf2(x1[2]), a3 = bf2(x1[3]);
          dst[0] = (f4){a0[0], a0[1], a1[0], a1[1]}; dst[1] = (f4){a2[0], a2[1], a3[0], a3[1]};
        }
      }
    }
    asm volatile("s_waitcnt lgkmcnt(0)" ::: "memory");
    BAR();
    STAMP(9);
    {
      f4 v[3][2];
      f2 sum = {0.f, 0.f};
#pragma unroll
      for (int i = 0; i < 3; ++i) {
        const bool on = (i < 2 || r32 < 8);
        const int pos = on ? r32 + 32 * i : r32;
        const int q = pos ^ ((grow >> 1) & 7), k0 = 32 * (q >> 2) + 4 * (q & 3);
#pragma unroll
        for (int h = 0; h < 2; ++h) {
          v[i][h] = *(LAS f4*)(size_t)(ldsb + (lrow * D + k0 + 16 * h) * 4);
          if (on) sum += (f2){v[i][h][0] + v[i][h][1], v[i][h][2] + v[i][h][3]};
        }
      }
      float sm = sum[0] + sum[1];
      sm = sum32(sm);
      const float mean = sm * (1.f / D);
      float q2 = 0.f;
#pragma unroll
      for (int i = 0; i < 3; ++i)
        if (i < 2 || r32 < 8) {
#pragma unroll
          for (int h = 0; h < 2; ++h)
#pragma unroll
            for (int e = 0; e < 4; ++e) { const float dl = v[i][h][e] - mean; q2 += dl * dl; }
        }
      q2 = sum32(q2);
      const float mr = rbf(mean), rs = __builtin_amdgcn_rcpf(rbf(__builtin_amdgcn_sqrtf(rbf(rbf(q2 * (1.f / D)) + 1e-5f))));
      const __amdgpu_buffer_rsrc_t rH = __builtin_amdgcn_make_buffer_rsrc(p.ws + WS_H2 + (size_t)c * ABYTES, 0, ABYTES, 0x00020000);
      u4 oh[3];
#pragma unroll
      for (int i = 0; i < 3; ++i) {
        u4& o = oh[i];
#pragma unroll
        for (int h = 0; h < 2; ++h)
#pragma unroll
          for (int e = 0; e < 2; ++e) {
            unsigned t = pk((f2){v[i][h][2 * e], v[i][h][2 * e + 1]} - mr);
            t = pk(bf2(t) * rs);
            t = pk(bf2(t) * bf2(pk(bf2(cbv[i][h][e]) + 1.f)));
            o[2 * h + e] = pk(bf2(t) + bf2(sbv[i][h][e]));
          }
        if (i < 2 || r32 < 8) __builtin_amdgcn_raw_buffer_store_b128(o, rH, grow * ROWB + (r32 + 32 * i) * 16, 0, 16);
      }
      publish(flags + (1 * 256 + blockIdx.x) * FLAG_STRIDE, tag);
      STAMP(10);
      if (w != 0) {
        const unsigned dummy = __builtin_amdgcn_readfirstlane(ldsb + LDS_DUMMY);
        int li = tid - 64; li = li < 162 ? li : li - 94;
        const int r = 18 * c + li / 9, l = li % 9;
        touch((const char*)p.wg + (size_t)576 * j * 2, r * (DF * 2) + l * 128, dummy);
        touch((const char*)p.wu + (size_t)576 * j * 2, r * (DF * 2) + l * 128, dummy);
      }
      await8(flags + (1 * 256 + c * 8) * FLAG_STRIDE, tag, tmo, w, lane);
      STAMP(11);
      u4 hv[16];
#pragma unroll
      for (int i = 0; i < 16; ++i) { const int y = i * 256 + tid < 4032 ? i * 256 + tid : tid; hv[i] = __builtin_amdgcn_raw_buffer_load_b128(rH, (y + (y >= j * 576 ? 576 : 0)) * 16, 0, AUX_H); }
#pragma unroll
      for (int i = 0; i < 3; ++i) if (i < 2 || r32 < 8) *(LAS u4*)(size_t)(ldsb + grow * ROWB + (r32 + 32 * i) * 16) = oh[i];
#pragma unroll
      for (int i = 0; i < 16; ++i) { const int y = i * 256 + tid; if (i < 15 || y < 4032) *(LAS u4*)(size_t)(ldsb + (y + (y >= j * 576 ? 576 : 0)) * 16) = hv[i]; }
    }
    asm volatile("s_waitcnt vmcnt(0) lgkmcnt(0)" ::: "memory");
    BAR();
  }
  STAMP(12);
  {
    constexpr size_t KS_GU = (size_t)32 * DF * 2, KS_D = (size_t)32 * ROWB;
    read_w(s, 0); read_a(s, 0, ab_e);
    asm volatile("s_waitcnt lgkmcnt(0)" ::: "memory");
    issue_all(s, 0, voffA, gG + KS_GU);
    f4 aU[9][4];
    auto no = [&]() __attribute__((always_inline)) {};
    auto pfd = [&](int h) __attribute__((always_inline)) {
      int li = 64 * h + 16 * w + lane; li = li < 162 ? li : 161;
      if (lane < 16) touch((const char*)p.wd + (size_t)576 * j * ROWB, (18 * c) * ROWB + li * 128, __builtin_amdgcn_readfirstlane(ldsb + LDS_DUMMY));
    };
    auto kpair = [&](auto zc, auto lastc, auto vmc, int k, auto&& pf) __attribute__((always_inline)) {
      constexpr bool Z = decltype(zc)::value, LAST = decltype(lastc)::value; constexpr int VM = decltype(vmc)::value;
      const char* u1 = gU + (size_t)(k + 1) * KS_GU; const char* g2 = gG + (size_t)(k + 2) * KS_GU;
      step<0, 0, Z, true, false, true, 9, 0>(s, aG, voffA, u1, 0, no);
      if constexpr (!LAST) {
        step<1, 0, Z, true, true, true, 9, NVU>(s, aU, voffA, g2, ab_o + 128 * (k >> 1), pf);
        step<0, 1, false, true, false, true, VM, 0>(s, aG, voffA, u1 + KS_GU, 0, no);
        step<1, 1, false, true, true, true, VM, NVU>(s, aU, voffA, g2 + KS_GU, ab_e + 128 * ((k + 2) >> 1), no);
      } else {
        step<1, 0, Z, true, true, true, 9, NVU>(s, aU, voffB, gD, ab_o + 128 * (k >> 1), no);
        step<0, 1, false, true, false, true, 9, 0>(s, aG, voffB, gD + KS_D, 0, no);
        step<1, 1, false, true, false, true, 9, NVU>(s, aU, voffB, gD + 2 * KS_D, 0, no);
      }
    };
    kpair(std::true_type{}, std::false_type{}, IC<9>{}, 0, no);
    for (int k = 2; k < 8; k += 2) kpair(std::false_type{}, std::false_type{}, IC<9>{}, k, no);
    for (int k = 8; k < 14; k += 2) kpair(std::false_type{}, std::false_type{}, IC<10>{}, k, [&]() __attribute__((always_inline)) { pfd((k - 8) >> 1); });
    kpair(std::false_type{}, std::false_type{}, IC<9>{}, 14, no);
    kpair(std::false_type{}, std::true_type{}, IC<9>{}, 16, no);
    STAMP(13);
    settle<0>(aG); settle<NVU>(aU);
    u2 act[9][4];
#pragma unroll
    for (int t = 0; t < 9; ++t)
#pragma unroll
      for (int m = 0; m < 4; ++m)
#pragma unroll
        for (int e = 0; e < 2; ++e) {
          const f2 x = {aG[t][m][2 * e], aG[t][m][2 * e + 1]}, u = {aU[t][m][2 * e], aU[t][m][2 * e + 1]};
          const f2 z = x * (x * x * -0.10294324f + -2.3022082f);
          const f2 dn = (f2){__builtin_amdgcn_exp2f(z[0]), __builtin_amdgcn_exp2f(z[1])} + 1.f;
          act[t][m][e] = pk((x * u) * (f2){__builtin_amdgcn_rcpf(dn[0]), __builtin_amdgcn_rcpf(dn[1])});
        }
    asm volatile("s_waitcnt lgkmcnt(0)" ::: "memory");
    BAR();
#pragma unroll
    for (int t = 0; t < 9; ++t) {
      const int tt = 9 * w + t, ks = tt >> 1, ah = tt & 1;
      const unsigned ad = ldsb + cl * ROWB + (((4 * ks + g4) ^ sw) * 16) + 8 * ah;
#pragma unroll
      for (int m = 0; m < 4; ++m) *(LAS u2*)(size_t)(ad + m * 16 * ROWB) = act[t][m];
    }
    asm volatile("s_waitcnt lgkmcnt(0)" ::: "memory");
    BAR();
    STAMP(14);
    read_a(s, 0, ab_e);
    const Epi e2 = {rX, (unsigned)(j * 8 * SLICE), (unsigned)SLICE, s.ring - w * 2304, j, w, lane};
    gemm_rows<18, 0, 0>(s, aG, voffB, gD, ab_e, ab_o, voffB, gD, gD, e2, [&]() __attribute__((always_inline)) {});
    STAMP(15);
  }
  {
    int tid3 = threadIdx.x; asm volatile("" : "+v"(tid3));
    const int tid = tid3, lane = tid3 & 63;
    asm volatile("s_waitcnt lgkmcnt(0)" ::: "memory");
    publish(flags + (2 * 256 + blockIdx.x) * FLAG_STRIDE, tag);
    STAMP(16);
    const int t3 = tid < 192 ? tid : tid - 64;
    int zlr[3], zcc[3];
    u4 gv[3], x1[3];
#pragma unroll
    for (int i = 0; i < 3; ++i) {
      zdecode(t3 + 192 * i, zlr[i], zcc[i]);
      gv[i] = *(LAS u4*)(size_t)(ldsb + LDS_MOD + (5 * D + zcc[i] * 8) * 2);
      x1[i] = *(LAS u4*)(size_t)(ldsb + LDS_X1 + (t3 + 192 * i) * 16);
    }
    if (blockIdx.x == 0 && w == 1) {
      for (unsigned spins = 0; spins <= SPIN_CAP; ++spins) {
        bool ok = true;
#pragma unroll
        for (int i = 0; i < 4; ++i) ok &= __hip_atomic_load((gu64*)(flags + (lane + 64 * i) * FLAG_STRIDE), __ATOMIC_RELAXED, __HIP_MEMORY_SCOPE_AGENT) == tag;
        if (__all(ok)) break;
        __builtin_amdgcn_s_sleep(4);
      }
      if (lane == 0) __hip_atomic_store((gu64*)(p.ws + WS_EPOCH), tag, __ATOMIC_RELAXED, __HIP_MEMORY_SCOPE_AGENT);
    }
    await8(flags + (2 * 256 + c * 8) * FLAG_STRIDE, tag, tmo, w, lane);
    STAMP(17);
    const __amdgpu_buffer_rsrc_t rO = __builtin_amdgcn_make_buffer_rsrc(p.out + (size_t)(c * 64 + j * 8) * D, 0, SLICE, 0x00020000); (void)rO;
    u4 v[3][8];
#pragma unroll
    for (int i = 0; i < 3; ++i)
#pragma unroll
      for (int jj = 0; jj < 7; ++jj) v[i][jj] = __builtin_amdgcn_raw_buffer_load_b128(rX, ((jj + (jj >= j)) * 8 + j) * SLICE + (t3 + 192 * i) * 16, 0, AUX_X);
#pragma unroll
    for (int i = 0; i < 3; ++i) { const int z = t3 + 192 * i, zw = z / 144; v[i][7] = *(LAS u4*)(size_t)(ldsb + ABYTES + zw * 2 * SLOT + (z - zw * 144) * 16); }
#pragma unroll
    for (int i = 0; i < 3; ++i) {
      const u4 o = gated_residual(v[i], x1[i], gv[i]);
      if (tid < 192) *(u4*)(p.out + (size_t)(c * 64 + j * 8 + zlr[i]) * D + zcc[i] * 8) = o;
    }
  }
  STAMP(18);
  asm volatile("s_waitcnt lgkmcnt(0)" ::: "memory");
  STAMP_FLUSH();
  }
}

extern "C" void kernel_launch(void* const* d_in, const int* in_sizes, int n_in, void* d_out, int out_size, void* d_ws, size_t ws_size, hipStream_t stream) {
  Params p;
  p.x = (const u16*)d_in[0]; p.kc = (const u16*)d_in[1]; p.vc = (const u16*)d_in[2]; p.wq = (const u16*)d_in[3]; p.wo = (const u16*)d_in[4];
  p.wg = (const u16*)d_in[6]; p.wu = (const u16*)d_in[7]; p.wd = (const u16*)d_in[8];
  p.c = (const u16*)d_in[9]; p.wmod = (const u16*)d_in[10];
  p.out = (u16*)d_out; p.ws = (char*)d_ws;
 p.one = 1.0f; p.gap = 0u;
  (void)hipFuncSetAttribute((const void*)k_main, hipFuncAttributeMaxDynamicSharedMemorySize, LDS_BYTES);
  k_main<<<256, 256, LDS_BYTES, stream>>>(p);
}
```

```cpp
#define PF_D2 150
#define STAG_D2 560
#include <hip/hip_runtime.h>
#include <stdint.h>
#include <utility>
#include <type_traits>

typedef unsigned short u16;
typedef unsigned long long u64;
typedef __bf16 b2 __attribute__((ext_vector_type(2)));
typedef __bf16 b4 __attribute__((ext_vector_type(4)));
typedef __bf16 b8 __attribute__((ext_vector_type(8)));
typedef float f2 __attribute__((ext_vector_type(2)));
typedef float f4 __attribute__((ext_vector_type(4)));
typedef float f16v __attribute__((ext_vector_type(16)));
typedef unsigned u2 __attribute__((ext_vector_type(2)));
typedef unsigned u4 __attribute__((ext_vector_type(4)));
typedef __attribute__((address_space(1))) unsigned gu32;
typedef __attribute__((address_space(1))) unsigned long long gu64;
#define LAS __attribute__((address_space(3)))
#define DI __device__ __forceinline__
#define GAP() __builtin_amdgcn_sched_barrier(0)
#define LGKM0() __builtin_amdgcn_s_waitcnt(0xC07F)
#define BAR() do { asm volatile("" ::: "memory"); __builtin_amdgcn_s_barrier(); asm volatile("" ::: "memory"); } while (0)
template <int V> using IC = std::integral_constant<int, V>;
#define AUX_X 16
#define AUX_H 16
#ifndef NVU
#define NVU 8
#endif
#define PFD_K 14
#define UNROLL(N, F) [&]<int... I_>(std::integer_sequence<int, I_...>) __attribute__((always_inline)) { (F(IC<I_>{}), ...); }(std::make_integer_sequence<int, N>{})

constexpr int S = 2048, D = 576, DH = 128, SK = 2048, WIN = 512, DF = 4608;
constexpr int ROWB = D * 2;
constexpr int ABYTES = 64 * ROWB;
constexpr int SLICE = 8 * ROWB;
constexpr int SLOT = 9216;
constexpr int QSLOT = 8192;
constexpr int LDS_DUMMY = ABYTES + 8 * SLOT;
constexpr int LDS_X1 = LDS_DUMMY + 256;
constexpr int LDS_MOD = LDS_X1 + SLICE;
constexpr int LDS_BYTES = LDS_MOD + 6 * D * 2;
static_assert(LDS_BYTES <= 163840, "LDS");
constexpr size_t WS_EPOCH = 0;
constexpr size_t WS_TMO = 256;
constexpr size_t WS_MODP = 131072;
constexpr size_t WS_FLAG = 32768;
constexpr int FLAG_STRIDE = 8;
constexpr size_t WS_X = 1048576;
constexpr size_t WS_H2 = WS_X + (size_t)32 * 64 * SLICE;
constexpr size_t WS_DBG = 200u << 20;
#define STAMP(i)
#define STAMP_FLUSH()
#define PST(i)

DI float bf2f(u16 v) { return __uint_as_float(((unsigned)v) << 16); }
DI float bflo(unsigned w) { return __uint_as_float(w << 16); }
DI float bfhi(unsigned w) { return __uint_as_float(w & 0xffff0000u); }
DI f2 bf2(unsigned w) { return (f2){bflo(w), bfhi(w)}; }
DI unsigned pk(float lo, float hi) { f2 v = {lo, hi}; b2 b = __builtin_convertvector(v, b2); return __builtin_bit_cast(unsigned, b); }
DI unsigned pk(f2 v) { b2 b = __builtin_convertvector(v, b2); return __builtin_bit_cast(unsigned, b); }
DI float rbf(float f) { return bflo(pk(f, f)); }
DI f2 rb2(f2 v) { return (f2){__uint_as_float(pk(0.f, v[0])), __uint_as_float(pk(0.f, v[1]))}; }
template <int CTRL> DI float dpp(float v) { return __uint_as_float(__builtin_amdgcn_update_dpp(0u, __float_as_uint(v), CTRL, 0xf, 0xf, false)); }
DI float sum8(float v) { v += dpp<0xB1>(v); v += dpp<0x4E>(v); v += dpp<0x141>(v); return v; }
DI float swap16_add(float x) { auto r = __builtin_amdgcn_permlane16_swap(__float_as_uint(x), __float_as_uint(x), false, false); return __uint_as_float(r[0]) + __uint_as_float(r[1]); }
DI float swap32_add(float x) { auto r = __builtin_amdgcn_permlane32_swap(__float_as_uint(x), __float_as_uint(x), false, false); return __uint_as_float(r[0]) + __uint_as_float(r[1]); }
DI float sum32(float v) { v = sum8(v); v += dpp<0x140>(v); return swap16_add(v); }
DI u16 f2bf(float f) { return (u16)(pk(f, f) & 0xffffu); }
DI float fmaxx(float a, float b) { return __builtin_elementwise_maximum(a, b); }
DI float fmax3(float a, float b, float c) { return __builtin_elementwise_maximum(__builtin_elementwise_maximum(a, b), c); }

DI void zdecode(int z, int& lr, int& cc) {
  const int w = z / 144, y = z - w * 144;
  if (y < 128) { const int tp = y >> 5; lr = (y >> 2) & 7; cc = 18 * w + 4 * tp + (y & 3); }
  else { const int y2 = y - 128; lr = y2 >> 1; cc = 18 * w + 16 + (y2 & 1); }
}

template <int IMM> DI void dma(unsigned voff, const char* g) { asm volatile("global_load_lds_dwordx4 %0, %1 offset:%2" :: "v"(voff), "s"(g), "i"(IMM) : "memory"); }
DI void setm0(unsigned a) { asm volatile("s_mov_b32 m0, %0" :: "s"(a) : "memory"); }
DI void touch(const char* p, unsigned voff, unsigned ldsdummy) {
  asm volatile("s_mov_b32 m0, %0\n\ts_nop 0\n\tglobal_load_lds_dword %1, %2" :: "s"(ldsdummy), "v"(voff), "s"(p) : "memory");
}
DI void touchp(const char* a, unsigned ldsdummy) {
  asm volatile("s_mov_b32 m0, %0\n\ts_nop 0\n\tglobal_load_lds_dword %1, off" :: "s"(ldsdummy), "v"(a) : "memory");
}
DI b4 ldT(unsigned base, int off) { return __builtin_amdgcn_ds_read_tr16_b64_v4bf16((LAS b4*)(size_t)(base + off)); }
DI b8 ldA(unsigned base, int off) { return *(LAS b8*)(size_t)(base + off); }
DI b8 cat(const b4& lo, const b4& hi) { return __builtin_shufflevector(lo, hi, 0, 1, 2, 3, 4, 5, 6, 7); }
template <bool AG, bool ZERO> DI void mfma16(f4& c, const b8& a, const b8& b) {
  if constexpr (AG) {
    if constexpr (ZERO) asm volatile("v_mfma_f32_16x16x32_bf16 %0, %1, %2, 0" : "=a"(c) : "v"(a), "v"(b));
    else asm volatile("v_mfma_f32_16x16x32_bf16 %0, %1, %2, %0" : "+a"(c) : "v"(a), "v"(b));
  } else {
    if constexpr (ZERO) asm volatile("v_mfma_f32_16x16x32_bf16 %0, %1, %2, 0" : "=&v"(c) : "v"(a), "v"(b));
    else asm volatile("v_mfma_f32_16x16x32_bf16 %0, %1, %2, %0" : "+v"(c) : "v"(a), "v"(b));
  }
}
template <bool AG, bool ZERO> DI void mfma32(f16v& c, const b8& a, const b8& b) {
  if constexpr (AG) {
    if constexpr (ZERO) asm volatile("v_mfma_f32_32x32x16_bf16 %0, %1, %2, 0" : "=a"(c) : "v"(a), "v"(b));
    else asm volatile("v_mfma_f32_32x32x16_bf16 %0, %1, %2, %0" : "+a"(c) : "v"(a), "v"(b));
  } else {
    if constexpr (ZERO) asm volatile("v_mfma_f32_32x32x16_bf16 %0, %1, %2, 0" : "=&v"(c) : "v"(a), "v"(b));
    else asm volatile("v_mfma_f32_32x32x16_bf16 %0, %1, %2, %0" : "+v"(c) : "v"(a), "v"(b));
  }
}
template <int NVG> DI void settle(f4 (&acc)[9][4]) {
  asm volatile("s_nop 7\n\ts_nop 7" ::: "memory");
#pragma unroll
  for (int t = 0; t < 9; ++t)
#pragma unroll
    for (int m = 0; m < 4; ++m) { if (t * 4 + m < 36 - NVG) asm volatile("" : "+a"(acc[t][m])); else asm volatile("" : "+v"(acc[t][m])); }
}

struct GS {
  b4 wlo[2][9], whi[2][9];
  b8 af[2][4];
  unsigned wbase, ring;
};
DI void issue_all(const GS& s, int slot, const unsigned (&voff)[9], const char* gp) {
  const unsigned sl = s.ring + slot * SLOT;
  setm0(sl + 4096); asm volatile("s_nop 0");
  dma<-4096>(voff[0], gp); dma<-3072>(voff[1], gp); dma<-2048>(voff[2], gp); dma<-1024>(voff[3], gp);
  dma<0>(voff[4], gp); dma<1024>(voff[5], gp); dma<2048>(voff[6], gp); dma<3072>(voff[7], gp);
  setm0(sl + 8192); asm volatile("s_nop 0"); dma<0>(voff[8], gp);
}
template <int PAR, int AP, bool ZERO, bool RDW, bool RDA, bool DMA, int VM, int NVG, typename PF>
DI void step(GS& s, f4 (&acc)[9][4], const unsigned (&voff)[9], const char* gp, unsigned ab, PF&& pf) {
  const unsigned sl = s.ring + (PAR ^ 1) * SLOT;
  const unsigned wb = s.wbase + (PAR ^ 1) * SLOT;
  auto mm = [&](auto ic) __attribute__((always_inline)) {
    constexpr int I = decltype(ic)::value, t = I >> 2, m = I & 3;
    mfma16<(I < 36 - NVG), ZERO>(acc[t][m], cat(s.wlo[PAR][t], s.whi[PAR][t]), s.af[AP][m]);
  };
  auto rd = [&](auto ic) __attribute__((always_inline)) {
    constexpr int I = decltype(ic)::value;
    if constexpr (I < 4) { if constexpr (RDA) s.af[AP ^ 1][I] = ldA(ab, I * 16 * ROWB); }
    else if constexpr (RDW) {
      if constexpr (((I - 4) & 1) == 0) s.wlo[PAR ^ 1][(I - 4) >> 1] = ldT(wb, 32 * ((I - 4) >> 1));
      else s.whi[PAR ^ 1][(I - 4) >> 1] = ldT(wb, 32 * ((I - 4) >> 1) + 4608);
    }
  };
  auto gap = [&](auto gc) __attribute__((always_inline)) {
    constexpr int G = decltype(gc)::value;
    mm(IC<2 * G>{});
    if constexpr (G == 0 && RDW) asm volatile("s_waitcnt vmcnt(%0)" :: "i"(VM) : "memory");
    if constexpr (G <= 10) rd(IC<2 * G>{});
    if constexpr (DMA) {
      if constexpr (G == 12) { LGKM0(); setm0(sl + 4096); }
      if constexpr (G >= 13 && G <= 16) dma<(2 * (G - 13) - 4) * 1024>(voff[2 * (G - 13)], gp);
      if constexpr (G == 17) dma<0>(voff[8], gp);
    }
    GAP();
    mm(IC<2 * G + 1>{});
    if constexpr (G <= 10) rd(IC<2 * G + 1>{});
    if constexpr (DMA) {
      if constexpr (G >= 13 && G <= 16) dma<(2 * (G - 13) + 1 - 4) * 1024>(voff[2 * (G - 13) + 1], gp);
      if constexpr (G == 16) setm0(sl + 8192);
    }
    if constexpr (G == 17) pf();
    GAP();
  };
  UNROLL(18, gap);
}
DI void read_w(GS& s, int par) {
#pragma unroll
  for (int t = 0; t < 9; ++t) { s.wlo[par][t] = ldT(s.wbase + par * SLOT, 32 * t); s.whi[par][t] = ldT(s.wbase + par * SLOT, 32 * t + 4608); }
}
DI void read_a(GS& s, int set, unsigned ab) {
#pragma unroll
  for (int m = 0; m < 4; ++m) s.af[set][m] = ldA(ab, m * 16 * ROWB);
}
struct Epi { __amdgpu_buffer_rsrc_t rs; unsigned base, dstride, ownlds; int own, w, lane; };
template <int U> DI void epi_unit(f4 (&acc)[9][4], const Epi& e) {
  const int G = e.lane >> 4, c = e.lane & 15, lr = c & 7, dh = c >> 3;
  constexpr int ta = U < 16 ? 2 * (U >> 2) : 8, tb = U < 16 ? ta + 1 : 8, ma = U < 16 ? (U & 3) : 2 * (U - 16), mb = U < 16 ? ma : ma + 1;
  asm volatile("" : "+a"(acc[ta][ma]), "+a"(acc[tb][mb]));
  const f4 a = acc[ta][ma], b = acc[tb][mb];
  auto r0 = __builtin_amdgcn_permlane16_swap(pk(a[0], a[1]), pk(b[0], b[1]), false, false);
  auto r1 = __builtin_amdgcn_permlane16_swap(pk(a[2], a[3]), pk(b[2], b[3]), false, false);
  const u4 v = {r0[0], r1[0], r0[1], r1[1]};
  const int d = U < 16 ? 2 * ma + dh : 2 * (ma + (G & 1)) + dh;
  const unsigned off = U < 16 ? e.w * 2304 + (U >> 2) * 512 + lr * 64 + (2 * (G & 1) + (G >> 1)) * 16 : e.w * 2304 + 2048 + lr * 32 + (G >> 1) * 16;
  __builtin_amdgcn_raw_buffer_store_b128(v, e.rs, d == e.own ? 0x7ffffff0u : e.base + d * e.dstride + off, 0, 16);
  if ((U < 16 ? ma : ma >> 1) == (U < 16 ? e.own >> 1 : e.own >> 2)) { if (d == e.own) *(LAS u4*)(size_t)(e.ownlds + off) = v; }
}
template <bool NEXT, int VM> DI void final2(GS& s, f4 (&acc)[9][4], const unsigned (&nvoff)[9], const char* nx1, unsigned ab, const Epi& e) {
  const unsigned sl = s.ring + SLOT, wb = s.wbase + SLOT;
  asm volatile("s_waitcnt vmcnt(%0)" :: "i"(VM) : "memory");
#pragma unroll
  for (int m = 0; m < 4; ++m) s.af[1][m] = ldA(ab, m * 16 * ROWB);
  auto rdw = [&](auto ic) __attribute__((always_inline)) { constexpr int t = decltype(ic)::value; s.wlo[1][t] = ldT(wb, 32 * t); s.whi[1][t] = ldT(wb, 32 * t + 4608); };
  rdw(IC<0>{}); rdw(IC<1>{});
  GAP();
  auto grp = [&](auto gc) __attribute__((always_inline)) {
    constexpr int G = decltype(gc)::value, t = G >> 1, st = G & 1;
    auto mm = [&](auto mc) __attribute__((always_inline)) { constexpr int m = decltype(mc)::value; mfma16<true, false>(acc[t][m], cat(s.wlo[st][t], s.whi[st][t]), s.af[st][m]); };
    mm(IC<0>{});
    if constexpr (st == 0 && t + 2 < 9) rdw(IC<t + 2>{});
    if constexpr (NEXT && G == 15) { LGKM0(); setm0(sl + 4096); }
    GAP();
    constexpr int U = G >= 5 ? G - 5 : -1;
    mm(IC<1>{});
    if constexpr (U >= 0 && U < 16 && (U >> 2) * 4 + 5 <= G) epi_unit<(U >= 0 && U < 16) ? U : 0>(acc, e);
    GAP();
    mm(IC<2>{});
    if constexpr (NEXT && G == 16) { dma<-4096>(nvoff[0], nx1); dma<-3072>(nvoff[1], nx1); dma<-2048>(nvoff[2], nx1); dma<-1024>(nvoff[3], nx1); }
    if constexpr (NEXT && G == 17) { dma<0>(nvoff[4], nx1); dma<1024>(nvoff[5], nx1); dma<2048>(nvoff[6], nx1); dma<3072>(nvoff[7], nx1); setm0(sl + 8192); }
    GAP();
    mm(IC<3>{});
    GAP();
  };
  UNROLL(18, grp);
  if constexpr (NEXT) dma<0>(nvoff[8], nx1);
  asm volatile("s_nop 7" ::: "memory");
  epi_unit<13>(acc, e); epi_unit<14>(acc, e); epi_unit<15>(acc, e); epi_unit<16>(acc, e); epi_unit<17>(acc, e);
}
template <int NS, int NEXT, int NPF, typename PF> DI void gemm_rows(GS& s, f4 (&acc)[9][4], const unsigned (&voff)[9], const char* gW, unsigned ab_e, unsigned ab_o,
                                              const unsigned (&nvoff)[9], const char* nx0, const char* nx1, const Epi& e, PF&& pf) {
  constexpr size_t KS = (size_t)32 * ROWB;
  auto no = [&]() __attribute__((always_inline)) {};
  step<0, 0, true, true, true, true, 9, 0>(s, acc, voff, gW + 3 * KS, ab_o, no);
  step<1, 1, false, true, true, true, 9, 0>(s, acc, voff, gW + 4 * KS, ab_e + 128, pf);
  step<0, 0, false, true, true, true, 9 + NPF, 0>(s, acc, voff, gW + 5 * KS, ab_o + 128, no);
  step<1, 1, false, true, true, true, 9 + NPF, 0>(s, acc, voff, gW + 6 * KS, ab_e + 256, no);
  for (int k = 4; k < NS - 4; k += 2) {
    step<0, 0, false, true, true, true, 9, 0>(s, acc, voff, gW + (size_t)(k + 3) * KS, ab_o + 128 * (k >> 1), no);
    step<1, 1, false, true, true, true, 9, 0>(s, acc, voff, gW + (size_t)(k + 4) * KS, ab_e + 128 * ((k + 2) >> 1), no);
  }
  step<0, 0, false, true, true, true, 9, 0>(s, acc, voff, gW + (size_t)(NS - 1) * KS, ab_o + 128 * ((NS - 4) >> 1), no);
  step<1, 1, false, true, true, NEXT != 0, 9, 0>(s, acc, nvoff, nx0, ab_e + 128 * ((NS - 2) >> 1), no);
  final2<NEXT != 0, NEXT != 0 ? 9 : 0>(s, acc, nvoff, nx1, ab_o + 128 * ((NS - 2) >> 1), e);
}

constexpr unsigned SPIN_CAP = 1u << 17;
DI void publish(u64* flag, u64 tag) {
  asm volatile("s_waitcnt vmcnt(0)" ::: "memory");
  BAR();
  if (threadIdx.x == 0) __hip_atomic_store((gu64*)flag, tag, __ATOMIC_RELAXED, __HIP_MEMORY_SCOPE_AGENT);
}
DI void await8(u64* flags8, u64 tag, unsigned* tmo, int w, int lane) {
  if (w == 0) {
    for (unsigned spins = 0;; ++spins) {
      const u64 v = __hip_atomic_load((gu64*)(flags8 + (lane & 7) * FLAG_STRIDE), __ATOMIC_RELAXED, __HIP_MEMORY_SCOPE_AGENT);
      if (__all(v == tag)) break;
      if (spins > SPIN_CAP) { if (lane == 0) __hip_atomic_store((gu32*)tmo, 1u, __ATOMIC_RELAXED, __HIP_MEMORY_SCOPE_AGENT); break; }
      __builtin_amdgcn_s_sleep(1);
    }
  }
  BAR();
}
template <int NP, int NT> struct ModGather {
  static constexpr int N = (NP + NT - 1) / NT;
  u4 gr[N][2];
  __amdgpu_buffer_rsrc_t rM;
  DI void load(int tid) {
#pragma unroll
    for (int i = 0; i < N; ++i) {
      const int idx = tid + NT * i < NP ? tid + NT * i : (tid & 63), off = (idx >> 5) * 1024 + (idx & 31) * 16;
      gr[i][0] = __builtin_amdgcn_raw_buffer_load_b128(rM, off, 0, 16); gr[i][1] = __builtin_amdgcn_raw_buffer_load_b128(rM, off + 512, 0, 16);
    }
  }
  DI void issue(char* ws, int tid) { rM = __builtin_amdgcn_make_buffer_rsrc(ws + WS_MODP, 0, 54 * 1024, 0x00020000); load(tid); }
  DI void finish(u64 tag, unsigned ldsb, unsigned* tmo, int tid) {
    const unsigned thi = (unsigned)(tag >> 32), tlo = (unsigned)tag;
    for (unsigned spins = 0;; ++spins) {
      bool ok = true;
#pragma unroll
      for (int i = 0; i < N; ++i) ok &= gr[i][0][2] == tlo && gr[i][0][3] == thi && gr[i][1][2] == tlo && gr[i][1][3] == thi;
      if (__all(ok)) break;
      if (spins > SPIN_CAP / 8) { if ((tid & 63) == 0) __hip_atomic_store((gu32*)tmo, 2u, __ATOMIC_RELAXED, __HIP_MEMORY_SCOPE_AGENT); break; }
      __builtin_amdgcn_s_sleep(4);
      load(tid);
    }
#pragma unroll
    for (int i = 0; i < N; ++i)
      if (tid + NT * i < NP) {
        const int idx = tid + NT * i;
        const f2 m = (f2){__uint_as_float(gr[i][0][0]), __uint_as_float(gr[i][0][1])} + (f2){__uint_as_float(gr[i][1][0]), __uint_as_float(gr[i][1][1])};
        if (idx < D) {
          f2 v = rb2(m);
          if (idx >= D / 2) v = rb2(v + 1.f);
          *(LAS f2*)(size_t)(ldsb + LDS_X1 + 1024 + idx * 8) = v;
        } else *(LAS unsigned*)(size_t)(ldsb + LDS_MOD + idx * 4) = pk(m);
      }
  }
};
DI u4 gated_residual(const u4 (&v)[8], const u4& res, const u4& g) {
  u4 o;
#pragma unroll
  for (int e = 0; e < 4; ++e) {
    f2 s = bf2(v[0][e]);
#pragma unroll
    for (int j = 1; j < 8; ++j) s += bf2(v[j][e]);
    const unsigned t = pk(bf2(pk(s)) * bf2(g[e]));
    o[e] = pk(bf2(res[e]) + bf2(t));
  }
  return o;
}

constexpr float QSCALE = 0.08838834764831845f * 1.4426950408889634f;
constexpr float THR = 40.0f;
struct QS {
  b4 wlo[2][8], whi[2][8];
  b8 hf[2][4];
  unsigned wb[4];
  unsigned hb[4];
  unsigned ring;
};
template <int PAR, bool ZERO, bool RD, bool DMA, int VM, typename PF>
DI void qstep(QS& s, f16v (&acc)[4][2], const unsigned (&voff)[8], const char* gp, unsigned hoff, PF&& pf) {
  const unsigned sl = s.ring + (PAR ^ 1) * QSLOT;
  auto rd = [&](auto ic) __attribute__((always_inline)) {
    constexpr int I = decltype(ic)::value;
    if constexpr (RD) {
      if constexpr (I < 4) {
        s.hf[PAR ^ 1][I] = ldA(s.hb[2 * (PAR ^ 1) + (I >> 1)] + hoff, (I & 1) * 32 * ROWB);
      } else {
        constexpr int J = I - 4, f = J >> 1, t = J & 1, ks = f >> 2, dt = f & 3;
        const b4 v = ldT(s.wb[dt], (PAR ^ 1) * QSLOT + 4096 * ks + 1024 * t);
        if constexpr (t == 0) s.wlo[PAR ^ 1][f] = v; else s.whi[PAR ^ 1][f] = v;
      }
    }
  };
  auto gap = [&](auto gc) __attribute__((always_inline)) {
    constexpr int G = decltype(gc)::value, ks = G >> 3, dt = (G >> 1) & 3, qb = G & 1, f = ks * 4 + dt;
    mfma32<false, ZERO && ks == 0>(acc[dt][qb], cat(s.wlo[PAR][f], s.whi[PAR][f]), s.hf[PAR][ks * 2 + qb]);
    if constexpr (G == 0 && RD) asm volatile("s_waitcnt vmcnt(%0)" :: "i"(VM) : "memory");
    if constexpr (G <= 6) { rd(IC<3 * G>{}); rd(IC<3 * G + 1>{}); if constexpr (3 * G + 2 < 20) rd(IC<3 * G + 2>{}); }
    if constexpr (DMA) {
      if constexpr (G == 9) { LGKM0(); setm0(sl + 4096); }
      if constexpr (G >= 10 && G <= 13) { dma<(2 * (G - 10) - 4) * 1024>(voff[2 * (G - 10)], gp); dma<(2 * (G - 10) + 1 - 4) * 1024>(voff[2 * (G - 10) + 1], gp); }
    }
    if constexpr (G == 15) pf();
    GAP();
  };
  UNROLL(16, gap);
}

struct AT {
  f16v S[2][2][2];
  f16v negm[2];
  f16v O[4][2];
  b8 q[2][8];
  b8 P[2][4];
  float m[2], l[2], rs0[2], rs1[2], fct[2], pm0[2], pm1[2];
  float one;
  unsigned kb_[2], vb_[2];
  unsigned voff[8];
  unsigned m0k, m0v;
  const char *gk, *gv;
  bool pend;
};
template <int MODE> DI void qk(f16v& s, const b8& k, const b8& q, const f16v& negm) {
  if constexpr (MODE == 0) asm volatile("v_mfma_f32_32x32x16_bf16 %0, %1, %2, 0" : "=&v"(s) : "v"(k), "a"(q));
  else if constexpr (MODE == 1) asm volatile("v_mfma_f32_32x32x16_bf16 %0, %1, %2, %3" : "=&v"(s) : "v"(k), "a"(q), "v"(negm));
  else asm volatile("v_mfma_f32_32x32x16_bf16 %0, %1, %2, %0" : "+v"(s) : "v"(k), "a"(q));
}
template <int XP, int KB, int E> DI void sm_exp(AT& a) { constexpr int qb = E >> 4, r = E & 15; a.S[XP][KB][qb][r] = __builtin_amdgcn_exp2f(a.S[XP][KB][qb][r]); }
template <int XP, int KB, int E> DI void sm_add(AT& a) {
  constexpr int qb = E >> 4, r = E & 15;
  if constexpr (r & 1) a.rs1[qb] = __builtin_fmaf(a.S[XP][KB][qb][r], a.one, a.rs1[qb]); else a.rs0[qb] += a.S[XP][KB][qb][r];
}
template <int XP, int C> DI void sm_pack(AT& a) {
  constexpr int kb = C >> 4, qb = (C >> 3) & 1, s = (C >> 2) & 1, p = C & 3;
  u4 w = __builtin_bit_cast(u4, a.P[qb][kb * 2 + s]);
  w[p] = pk(a.S[XP][kb][qb][8 * s + 2 * p], a.S[XP][kb][qb][8 * s + 2 * p + 1]);
  a.P[qb][kb * 2 + s] = __builtin_bit_cast(b8, w);
  if constexpr (p == 3) asm volatile("" : "+v"(a.P[qb][kb * 2 + s]));
}
template <int X, bool FIRST, bool DK, bool DV, typename PF> DI void phaseA(AT& a, int t, PF&& pf) {
  constexpr int XP = X ^ 1;
  const unsigned kslot = X * 16384;
  b8 kf[16];
  auto kread = [&](auto ic) __attribute__((always_inline)) { constexpr int I = decltype(ic)::value, ks = I >> 1, kb = I & 1; kf[I] = ldA(a.kb_[ks & 1], kslot + 8192 * kb + 512 * (ks >> 1)); };
  kread(IC<0>{}); kread(IC<1>{}); kread(IC<2>{}); kread(IC<3>{});
  const char* gk = a.gk + (size_t)(t + 1) * 16384; const char* gv = a.gv + (size_t)t * 16384;
  auto gap = [&](auto gc) __attribute__((always_inline)) {
    constexpr int G = decltype(gc)::value, ks = G >> 2, kb = (G >> 1) & 1, qb = G & 1, I = G >> 1;
    qk<(ks == 0) ? (FIRST ? 0 : 1) : 2>(a.S[X][kb][qb], kf[I], a.q[qb][ks], a.negm[qb]);
    if constexpr ((G & 1) == 0 && I + 4 < 16) kread(IC<I + 4>{});
    if constexpr (!FIRST && G == 8) asm volatile("" :: "v"(a.negm[0]), "v"(a.negm[1]));
    if constexpr (DK) { if constexpr (G == 0) setm0(a.m0k + (X ^ 1) * 16384); if constexpr ((G & 1) && G < 16) dma<((G >> 1) - 4) * 1024>(a.voff[G >> 1], gk); }
    if constexpr (DV) { if constexpr (G == 16) setm0(a.m0v + X * 16384); if constexpr ((G & 1) && G > 16) dma<(((G - 16) >> 1) - 4) * 1024>(a.voff[(G - 16) >> 1], gv); }
    if constexpr (!FIRST) {
      if constexpr (G < 16) { sm_exp<XP, 1, 2 * G>(a); sm_exp<XP, 1, 2 * G + 1>(a); sm_pack<XP, G>(a); }
      else { sm_pack<XP, G>(a); sm_add<XP, 1, 2 * (G - 16)>(a); sm_add<XP, 1, 2 * (G - 16) + 1>(a); }
    }
    GAP();
  };
  UNROLL(32, gap);
  pf();
  if constexpr (!FIRST) {
#pragma unroll
    for (int qb = 0; qb < 2; ++qb) { a.l[qb] += a.rs0[qb] + a.rs1[qb]; asm volatile("" : "+v"(a.l[qb])); }
  }
}
DI float swap32_max(float x) { auto r = __builtin_amdgcn_permlane32_swap(__float_as_uint(x), __float_as_uint(x), false, false); return fmaxx(__uint_as_float(r[0]), __uint_as_float(r[1])); }
template <int X, bool NOPV, bool ZERO> DI void phaseB(AT& a) {
  const unsigned vslot = (X ^ 1) * 16384;
  b4 vlo[16], vhi[16];
  auto vread = [&](auto ic) __attribute__((always_inline)) {
    constexpr int I = decltype(ic)::value, k4 = I >> 2, db = I & 3;
    vlo[I] = ldT(a.vb_[0], vslot + 2048 * (2 * k4) + 512 * db);
    vhi[I] = ldT(a.vb_[1], vslot + 2048 * (2 * k4 + 1) + 512 * db);
  };
  if constexpr (!NOPV) { vread(IC<0>{}); vread(IC<1>{}); vread(IC<2>{}); }
  float rmax[2];
  auto pv = [&](auto gc) __attribute__((always_inline)) {
    constexpr int G = decltype(gc)::value, I = G >> 1, k4 = I >> 2, db = I & 3, qb = G & 1;
    if constexpr (!NOPV) {
      mfma32<true, ZERO && k4 == 0>(a.O[db][qb], cat(vlo[I], vhi[I]), a.P[qb][k4]);
      if constexpr ((G & 1) == 0 && I + 3 < 16) vread(IC<I + 3>{});
    }
  };
  auto gap1 = [&](auto gc) __attribute__((always_inline)) {
    constexpr int G = decltype(gc)::value;
    pv(gc);
    if constexpr (G >= 4 && G < 12) {
      constexpr int i = G - 4, q = i >> 2, r = 4 * (i & 3);
      if constexpr ((i & 3) == 0) { a.pm0[q] = fmax3(a.S[X][0][q][0], a.S[X][0][q][1], a.S[X][1][q][0]); a.pm1[q] = fmax3(a.S[X][0][q][2], a.S[X][0][q][3], a.S[X][1][q][1]);
                                    a.pm0[q] = fmax3(a.pm0[q], a.S[X][1][q][2], a.S[X][1][q][3]); }
      else { a.pm0[q] = fmax3(a.pm0[q], a.S[X][0][q][r], a.S[X][0][q][r + 1]); a.pm1[q] = fmax3(a.pm1[q], a.S[X][0][q][r + 2], a.S[X][0][q][r + 3]);
             a.pm0[q] = fmax3(a.pm0[q], a.S[X][1][q][r], a.S[X][1][q][r + 1]); a.pm1[q] = fmax3(a.pm1[q], a.S[X][1][q][r + 2], a.S[X][1][q][r + 3]); }
    }
    if constexpr (G == 12 || G == 13) { constexpr int q = G - 12; rmax[q] = swap32_max(fmaxx(a.pm0[q], a.pm1[q])); }
    GAP();
  };
  UNROLL(14, gap1);
  if constexpr (NOPV) {
#pragma unroll
    for (int q = 0; q < 2; ++q) {
      a.m[q] = rmax[q]; a.l[q] = 0.f; a.fct[q] = 1.f;
#pragma unroll
      for (int kb = 0; kb < 2; ++kb)
#pragma unroll
        for (int e = 0; e < 16; ++e) a.S[X][kb][q][e] -= rmax[q];
#pragma unroll
      for (int e = 0; e < 16; ++e) a.negm[q][e] = -rmax[q];
      asm volatile("" : "+v"(a.negm[q]));
    }
  } else {
    const bool grew = (rmax[0] > THR) | (rmax[1] > THR);
    if (__builtin_expect(__any((int)grew), 0)) {
#pragma unroll
      for (int q = 0; q < 2; ++q) {
        const float dl = fmaxx(rmax[q], 0.f); a.m[q] += dl; a.fct[q] = __builtin_amdgcn_exp2f(-dl);
#pragma unroll
        for (int kb = 0; kb < 2; ++kb)
#pragma unroll
          for (int e = 0; e < 16; ++e) a.S[X][kb][q][e] -= dl;
#pragma unroll
        for (int e = 0; e < 16; ++e) a.negm[q][e] = -a.m[q];
        asm volatile("" : "+v"(a.negm[q]));
      }
      a.pend = true;
    }
  }
  a.rs0[0] = a.rs0[1] = a.rs1[0] = a.rs1[1] = 0.f;
  GAP();
  auto gap2 = [&](auto gc) __attribute__((always_inline)) {
    constexpr int G = decltype(gc)::value + 14;
    pv(IC<G>{});
    if constexpr (G >= 15) {
      constexpr int e0 = 2 * (G - 15);
      if constexpr (e0 < 32) { sm_exp<X, 0, e0>(a); sm_exp<X, 0, e0 + 1>(a); }
      if constexpr (G >= 16) { sm_add<X, 0, e0 - 2>(a); sm_add<X, 0, e0 - 1>(a); }
    }
    GAP();
  };
  UNROLL(18, gap2);
  asm volatile("" : "+v"(a.S[X][0][0]), "+v"(a.S[X][0][1]), "+v"(a.rs0[0]), "+v"(a.rs0[1]), "+v"(a.rs1[0]), "+v"(a.rs1[1]));
  if constexpr (!NOPV) {
    if (__builtin_expect(a.pend, 0)) {
      asm volatile("s_nop 7\n\ts_nop 7" ::: "memory");
#pragma unroll
      for (int db = 0; db < 4; ++db)
#pragma unroll
        for (int q = 0; q < 2; ++q) {
          asm volatile("" : "+a"(a.O[db][q]));
          f16v o = a.O[db][q];
#pragma unroll
          for (int e = 0; e < 16; ++e) o[e] *= a.fct[q];
          a.O[db][q] = o;
          asm volatile("" : "+a"(a.O[db][q]));
        }
      a.l[0] *= a.fct[0]; a.l[1] *= a.fct[1]; a.pend = false;
      asm volatile("s_nop 1" ::: "memory");
    }
  }
}

template <int X, bool FIRST, bool DK, bool DV, typename PF> DI void fastA(AT& a, int t, PF&& pf) {
  constexpr int XP = X ^ 1;
  const unsigned kslot = X * 16384;
  b8 kf[16];
  auto kread = [&](auto ic) __attribute__((always_inline)) { constexpr int I = decltype(ic)::value, ks = I >> 1, kb = I & 1; kf[I] = ldA(a.kb_[ks & 1], kslot + 8192 * kb + 512 * (ks >> 1)); };
  kread(IC<0>{}); kread(IC<1>{}); kread(IC<2>{}); kread(IC<3>{});
  const char* gk = a.gk + (size_t)(t + 1) * 16384; const char* gv = a.gv + (size_t)t * 16384;
  auto gap = [&](auto gc) __attribute__((always_inline)) {
    constexpr int G = decltype(gc)::value, ks = G >> 2, kb = (G >> 1) & 1, qb = G & 1, I = G >> 1;
    qk<(ks == 0) ? (FIRST ? 0 : 1) : 2>(a.S[X][kb][qb], kf[I], a.q[qb][ks], a.negm[qb]);
    if constexpr ((G & 1) == 0 && I + 4 < 16) kread(IC<I + 4>{});
    if constexpr (!FIRST && G == 8) asm volatile("" :: "v"(a.negm[0]), "v"(a.negm[1]));
    if constexpr (DK) { if constexpr (G == 0) setm0(a.m0k + (X ^ 1) * 16384); if constexpr ((G & 1) && G < 16) dma<((G >> 1) - 4) * 1024>(a.voff[G >> 1], gk); }
    if constexpr (DV) { if constexpr (G == 16) setm0(a.m0v + X * 16384); if constexpr ((G & 1) && G > 16) dma<(((G - 16) >> 1) - 4) * 1024>(a.voff[(G - 16) >> 1], gv); }
    if constexpr (!FIRST) {
      sm_exp<XP, 1, G>(a);
      if constexpr (G >= 1) sm_add<XP, 1, G - 1>(a);
      if constexpr (G >= 3 && (G & 1)) sm_pack<XP, 16 + (G - 3) / 2>(a);
    }
    GAP();
  };
  UNROLL(32, gap);
  pf();
  if constexpr (!FIRST) {
    sm_add<XP, 1, 31>(a); sm_pack<XP, 31>(a);
#pragma unroll
    for (int qb = 0; qb < 2; ++qb) { a.l[qb] += a.rs0[qb] + a.rs1[qb]; asm volatile("" : "+v"(a.l[qb])); }
  }
}
constexpr int fb_e(int G) { return G < 2 ? 0 : G - 2 + (G > 5) + (G > 9); }
constexpr int dr_e(int n) { return n < 8 ? n : n < 16 ? n + 8 : n < 24 ? n - 8 : n; }
template <int X, int MODE> DI void fastB(AT& a) {
  constexpr bool ZERO = MODE == 1, DRAIN = MODE == 3;
  if constexpr (MODE == 2) {
#pragma unroll
    for (int q = 0; q < 2; ++q) {
      float p0 = fmax3(a.S[X][0][q][0], a.S[X][1][q][0], a.S[X][0][q][2]), p1 = fmax3(a.S[X][0][q][1], a.S[X][1][q][1], a.S[X][0][q][3]);
      p0 = fmaxx(p0, a.S[X][1][q][2]); p1 = fmaxx(p1, a.S[X][1][q][3]);
#pragma unroll
      for (int r = 4; r < 16; r += 2) { p0 = fmax3(p0, a.S[X][0][q][r], a.S[X][1][q][r]); p1 = fmax3(p1, a.S[X][0][q][r + 1], a.S[X][1][q][r + 1]); }
      const float mx = swap32_max(fmaxx(p0, p1));
      f2 m2 = {mx, mx}; asm volatile("" : "+v"(m2));
      a.l[q] = 0.f;
#pragma unroll
      for (int kb = 0; kb < 2; ++kb)
#pragma unroll
        for (int e = 0; e < 16; e += 2) { const f2 v = (f2){a.S[X][kb][q][e], a.S[X][kb][q][e + 1]} - m2; a.S[X][kb][q][e] = v[0]; a.S[X][kb][q][e + 1] = v[1]; }
#pragma unroll
      for (int e = 0; e < 16; ++e) a.negm[q][e] = -mx;
      asm volatile("" : "+v"(a.negm[q]));
    }
    a.rs0[0] = a.rs0[1] = a.rs1[0] = a.rs1[1] = 0.f;
    { auto f = [&](auto ic) __attribute__((always_inline)) { sm_exp<X, 0, decltype(ic)::value>(a); }; UNROLL(32, f); }
    { auto f = [&](auto ic) __attribute__((always_inline)) { sm_add<X, 0, decltype(ic)::value>(a); }; UNROLL(32, f); }
    { auto f = [&](auto ic) __attribute__((always_inline)) { sm_pack<X, decltype(ic)::value>(a); }; UNROLL(16, f); }
  } else {
    const unsigned vslot = (DRAIN ? X : X ^ 1) * 16384;
    b4 vlo[16], vhi[16];
    auto vread = [&](auto ic) __attribute__((always_inline)) {
      constexpr int I = decltype(ic)::value, k4 = I >> 2, db = I & 3;
      vlo[I] = ldT(a.vb_[0], vslot + 2048 * (2 * k4) + 512 * db);
      vhi[I] = ldT(a.vb_[1], vslot + 2048 * (2 * k4 + 1) + 512 * db);
    };
    vread(IC<0>{}); vread(IC<1>{}); vread(IC<2>{});
    if constexpr (!DRAIN) a.rs0[0] = a.rs0[1] = a.rs1[0] = a.rs1[1] = 0.f;
    auto gap = [&](auto gc) __attribute__((always_inline)) {
      constexpr int G = decltype(gc)::value, I = G >> 1, k4 = I >> 2, db = I & 3, qb = G & 1;
      mfma32<true, ZERO && k4 == 0>(a.O[db][qb], cat(vlo[I], vhi[I]), a.P[qb][k4]);
      if constexpr ((G & 1) == 0 && I + 3 < 16) vread(IC<I + 3>{});
      if constexpr (DRAIN) {
        if constexpr (G < 16) { sm_exp<X, 1, dr_e(2 * G)>(a); sm_exp<X, 1, dr_e(2 * G) + 1>(a); }
        if constexpr (G >= 2 && G < 18) sm_pack<X, 16 + dr_e(2 * (G - 2)) / 2>(a);
        if constexpr (G >= 16) { sm_add<X, 1, dr_e(2 * (G - 16))>(a); sm_add<X, 1, dr_e(2 * (G - 16)) + 1>(a); }
      } else {
        if constexpr (fb_e(G) < fb_e(G + 1)) sm_exp<X, 0, fb_e(G)>(a);
        if constexpr (fb_e(G) + 1 < fb_e(G + 1)) sm_exp<X, 0, fb_e(G) + 1>(a);
        if constexpr (G >= 3 && fb_e(G - 1) < fb_e(G)) sm_add<X, 0, G >= 3 ? fb_e(G - 1) : 0>(a);
        if constexpr (G >= 3 && fb_e(G - 1) + 1 < fb_e(G)) sm_add<X, 0, G >= 3 ? fb_e(G - 1) + 1 : 0>(a);
        if constexpr (G >= 16 && G <= 29) sm_pack<X, G >= 16 ? G - 16 : 0>(a);
        if constexpr (G == 31) sm_pack<X, 14>(a);
      }
      GAP();
    };
    UNROLL(32, gap);
    if constexpr (!DRAIN) {
      sm_add<X, 0, 31>(a);
      static_assert(fb_e(32) == 32 && fb_e(28) >= 28 && fb_e(30) >= 30, "schedule");
      sm_pack<X, 15>(a);
      asm volatile("" : "+v"(a.S[X][0][0]), "+v"(a.S[X][0][1]), "+v"(a.rs0[0]), "+v"(a.rs0[1]), "+v"(a.rs1[0]), "+v"(a.rs1[1]));
    } else {
#pragma unroll
      for (int qb = 0; qb < 2; ++qb) a.l[qb] += a.rs0[qb] + a.rs1[qb];
    }
  }
}

struct Params { const u16 *x, *c, *wmod, *wq, *kc, *vc, *wo, *wg, *wu, *wd; u16* out; char* ws; float one; unsigned gap; };

__global__ __launch_bounds__(256, 1) void k_main(Params p) {
  extern __shared__ __attribute__((aligned(16))) char lds[];
  const int tid = threadIdx.x, w = __builtin_amdgcn_readfirstlane(tid >> 6), lane = tid & 63;
  const int j = blockIdx.x & 7, c = blockIdx.x >> 3;
  const unsigned ldsb = (unsigned)(size_t)lds;
  const int hh = lane >> 5, r32 = lane & 31;
  u64* const flags = (u64*)(p.ws + WS_FLAG);
  unsigned* const tmo = (unsigned*)(p.ws + WS_TMO);
  STAMP(0);
  PST(0);
  QS qs;
  unsigned voffQ[8];
#pragma unroll
  for (int i = 0; i < 8; ++i) voffQ[i] = (4 * i + (lane >> 4)) * 8192 + 16 * ((lane & 15) ^ ((lane >> 4) << 2)) - (i - 4) * 1024 + 4096;
  const char* gq = (const char*)p.wq + (size_t)(4 * j + w) * 256 - 4096;
  qs.ring = __builtin_amdgcn_readfirstlane(ldsb + ABYTES + w * 2 * QSLOT);
  constexpr size_t KS_Q = (size_t)32 * 8192;
  auto issueq = [&](int slot, const char* gp) __attribute__((always_inline)) {
    setm0(qs.ring + slot * QSLOT + 4096); asm volatile("s_nop 0");
    dma<-4096>(voffQ[0], gp); dma<-3072>(voffQ[1], gp); dma<-2048>(voffQ[2], gp); dma<-1024>(voffQ[3], gp);
    dma<0>(voffQ[4], gp); dma<1024>(voffQ[5], gp); dma<2048>(voffQ[6], gp); dma<3072>(voffQ[7], gp);
  };
  const u64 e0 = __hip_atomic_load((gu64*)(p.ws + WS_EPOCH), __ATOMIC_RELAXED, __HIP_MEMORY_SCOPE_AGENT);
  AT a;
  u64 tag;
  {
    f16v Qt[4][2];
    QS& s = qs;
    {
      const int rr = lane >> 3, sub = lane & 7;
      if (c >= 16) {
        const u64 t0_ = __builtin_amdgcn_s_memrealtime();
        const int mb = blockIdx.x - 128, mg = mb % 54, ms = (mb / 54) & 1;
        u4 wm[9]; u16 cval[9];
#pragma unroll
        for (int i = 0; i < 9; ++i) {
          const int r = 288 * ms + (tid >> 3) + 32 * i;
          wm[i] = *(const u4*)(p.wmod + (size_t)r * (6 * D) + mg * 64 + (tid & 7) * 8); cval[i] = p.c[r];
        }
        GAP();
        const u64 tag = ((u64)(unsigned)__builtin_amdgcn_readfirstlane((unsigned)(e0 >> 32)) << 32 | (u64)(unsigned)__builtin_amdgcn_readfirstlane((unsigned)e0)) + 1;
        if (mb < 108) {
          float acc[8] = {};
#pragma unroll
          for (int i = 0; i < 9; ++i) {
            const float xc = bf2f(cval[i]);
            const float sc = rbf(xc * __builtin_amdgcn_rcpf(1.f + __expf(-xc)));
#pragma unroll
            for (int e = 0; e < 4; ++e) { acc[2 * e] += sc * bflo(wm[i][e]); acc[2 * e + 1] += sc * bfhi(wm[i][e]); }
          }
          LAS float* red = (LAS float*)(size_t)(ldsb + LDS_X1);
#pragma unroll
          for (int e = 0; e < 8; ++e) { float t = acc[e]; t += dpp<0x128>(t); t = swap32_add(swap16_add(t)); if (lane < 8) red[w * 64 + lane * 8 + e] = t; }
          asm volatile("s_waitcnt lgkmcnt(0)" ::: "memory");
          BAR();
          if (tid < 32) {
            f2 v = {0.f, 0.f};
#pragma unroll
            for (int k = 0; k < 4; ++k) v += *(LAS f2*)(red + k * 64 + 2 * tid);
            const __amdgpu_buffer_rsrc_t rP = __builtin_amdgcn_make_buffer_rsrc(p.ws + WS_MODP, 0, 54 * 1024, 0x00020000);
            __builtin_amdgcn_raw_buffer_store_b128((u4){__float_as_uint(v[0]), __float_as_uint(v[1]), (unsigned)tag, (unsigned)(tag >> 32)}, rP, mg * 1024 + ms * 512 + tid * 16, 0, 16);
          }
        }
        const unsigned dummy = __builtin_amdgcn_readfirstlane(ldsb + LDS_DUMMY);
        while (__builtin_amdgcn_s_memrealtime() - t0_ < PF_D2) __builtin_amdgcn_s_sleep(2);
        const int li = tid < 144 ? tid : tid - 112;
        touch((const char*)p.wq + (size_t)512 * j * 2, (18 * (c - 16) + (li >> 3)) * 8192 + (li & 7) * 128, dummy);
        touch((const char*)p.wq + (size_t)512 * j * 2, (18 * c + (li >> 3)) * 8192 + (li & 7) * 128, dummy);
        touch((const char*)(w < 2 ? p.kc : p.vc) + ((size_t)(2 * j + (w & 1)) * SK + (SK - WIN)) * DH * 2, (32 * (c - 16 * (lane >> 5)) + (lane & 31)) * 128, dummy);
        while (__builtin_amdgcn_s_memrealtime() - t0_ < STAG_D2) __builtin_amdgcn_s_sleep(2);
      }
      PST(10);
      PST(11);
      u4 xv[2][9];
      GAP();
#pragma unroll
      for (int ps = 0; ps < 2; ++ps)
#pragma unroll
        for (int i = 0; i < 9; ++i) xv[ps][i] = *(const u4*)(p.x + (size_t)(c * 64 + 16 * w + 8 * ps + rr) * D + (sub + 8 * i) * 8);
      GAP();
      tag = ((u64)(unsigned)__builtin_amdgcn_readfirstlane((unsigned)(e0 >> 32)) << 32 | (u64)(unsigned)__builtin_amdgcn_readfirstlane((unsigned)e0)) + 1;
      PST(1);
      STAMP(20);
      PST(2);
      f2 y[2][9][4];
      ModGather<1728, 256> mgat;
      {
#pragma unroll
        for (int ps = 0; ps < 2; ++ps) {
          f2 a1 = {0.f, 0.f}, a2 = {0.f, 0.f};
#pragma unroll
          for (int i = 0; i < 9; ++i)
#pragma unroll
            for (int e = 0; e < 4; ++e) { y[ps][i][e] = bf2(xv[ps][i][e]); a1 += y[ps][i][e]; a2 = __builtin_elementwise_fma(y[ps][i][e], y[ps][i][e], a2); }
          float s1 = a1[0] + a1[1], s2 = a2[0] + a2[1];
          s1 = sum8(s1); s2 = sum8(s2);
          const float mean = s1 * (1.f / D), var = __builtin_fmaxf(__builtin_fmaf(-mean, mean, s2 * (1.f / D)), 0.f);
          const float mr = rbf(mean), rs = __builtin_amdgcn_rcpf(rbf(__builtin_amdgcn_sqrtf(rbf(rbf(var) + 1e-5f))));
          f2 mr2 = {mr, mr}, rs2 = {rs, rs}; asm volatile("" : "+v"(mr2), "+v"(rs2));
          if (ps == 1) { GAP(); mgat.issue(p.ws, tid); GAP(); }
#pragma unroll
          for (int i = 0; i < 9; ++i) {
            f2 t[4];
#pragma unroll
            for (int e = 0; e < 4; ++e) t[e] = y[ps][i][e] - mr2;
#pragma unroll
            for (int e = 0; e < 4; ++e) t[e] = rb2(t[e]);
#pragma unroll
            for (int e = 0; e < 4; ++e) t[e] = t[e] * rs2;
#pragma unroll
            for (int e = 0; e < 4; ++e) y[ps][i][e] = rb2(t[e]);
            asm volatile("" : "+v"(y[ps][i][0]), "+v"(y[ps][i][1]), "+v"(y[ps][i][2]), "+v"(y[ps][i][3]));
          }
        }
      }
      STAMP(21);
      PST(4);
      mgat.finish(tag, ldsb, tmo, tid);
      STAMP(22);
      PST(5);
      issueq(0, gq); issueq(1, gq + KS_Q);
      asm volatile("s_waitcnt lgkmcnt(0)" ::: "memory");
      BAR();
      STAMP(23);
      PST(6);
      {
        const unsigned tb = ldsb + LDS_X1 + 1024 + sub * 32;
        f4 cst[2][4];
        auto ldc = [&](int i, f4 (&d)[4]) { d[0] = *(LAS f4*)(size_t)(tb + i * 256); d[1] = *(LAS f4*)(size_t)(tb + i * 256 + 16); d[2] = *(LAS f4*)(size_t)(tb + i * 256 + D * 4); d[3] = *(LAS f4*)(size_t)(tb + i * 256 + D * 4 + 16); };
        ldc(0, cst[0]);
        const unsigned ad0 = ldsb + (16 * w + rr) * ROWB + ((sub ^ ((rr >> 1) & 7)) * 16);
        const unsigned ad1 = ldsb + (16 * w + 8 + rr) * ROWB + ((sub ^ (((rr >> 1) + 4) & 7)) * 16);
#pragma unroll
        for (int i = 0; i < 9; ++i) {
          if (i + 1 < 9) ldc(i + 1, cst[(i + 1) & 1]);
          GAP();
          const f4 (&k)[4] = cst[i & 1];
          const f2 sh[4] = {{k[0][0], k[0][1]}, {k[0][2], k[0][3]}, {k[1][0], k[1][1]}, {k[1][2], k[1][3]}}, cc[4] = {{k[2][0], k[2][1]}, {k[2][2], k[2][3]}, {k[3][0], k[3][1]}, {k[3][2], k[3][3]}};
          f2 t[2][4]; u4 o[2];
#pragma unroll
          for (int ps = 0; ps < 2; ++ps)
#pragma unroll
            for (int e = 0; e < 4; ++e) t[ps][e] = y[ps][i][e] * cc[e];
#pragma unroll
          for (int ps = 0; ps < 2; ++ps)
#pragma unroll
            for (int e = 0; e < 4; ++e) t[ps][e] = rb2(t[ps][e]);
#pragma unroll
          for (int ps = 0; ps < 2; ++ps)
#pragma unroll
            for (int e = 0; e < 4; ++e) t[ps][e] = t[ps][e] + sh[e];
#pragma unroll
          for (int ps = 0; ps < 2; ++ps)
#pragma unroll
            for (int e = 0; e < 4; ++e) o[ps][e] = pk(t[ps][e]);
          *(LAS u4*)(size_t)(ad0 + i * 128) = o[0];
          *(LAS u4*)(size_t)(ad1 + i * 128) = o[1];
          GAP();
        }
      }
    }
    {
      const int blk = (lane >> 4) & 1, q = (lane & 15) >> 2, pp = lane & 3;
#pragma unroll
      for (int dt = 0; dt < 4; ++dt) s.wb[dt] = s.ring + (8 * hh + q) * 256 + 64 * (dt ^ q) + 32 * blk + 16 * (pp & 1) + 8 * (pp >> 1);
#pragma unroll
      for (int i = 0; i < 4; ++i) s.hb[i] = ldsb + r32 * ROWB + (((2 * i + hh) ^ ((r32 >> 1) & 7)) * 16);
    }
    asm volatile("s_waitcnt vmcnt(8) lgkmcnt(0)" ::: "memory");
    BAR();
    STAMP(1);
    PST(7);
#pragma unroll
    for (int f = 0; f < 8; ++f) { s.wlo[0][f] = ldT(s.wb[f & 3], 4096 * (f >> 2)); s.whi[0][f] = ldT(s.wb[f & 3], 4096 * (f >> 2) + 1024); }
#pragma unroll
    for (int i = 0; i < 4; ++i) s.hf[0][i] = ldA(s.hb[i >> 1], (i & 1) * 32 * ROWB);
    asm volatile("s_waitcnt lgkmcnt(0)" ::: "memory");
    issueq(0, gq + 2 * KS_Q);
    auto no = [&]() __attribute__((always_inline)) {};
    qstep<0, true, true, true, 8>(s, Qt, voffQ, gq + 3 * KS_Q, 0, no);
    qstep<1, false, true, true, 8>(s, Qt, voffQ, gq + 4 * KS_Q, 128, no);
    for (int n = 2; n < 14; n += 2) {
      qstep<0, false, true, true, 8>(s, Qt, voffQ, gq + (size_t)(n + 3) * KS_Q, 128 * ((n + 1) >> 1), no);
      qstep<1, false, true, true, 8>(s, Qt, voffQ, gq + (size_t)(n + 4) * KS_Q, 128 * ((n + 2) >> 1), no);
    }
    qstep<0, false, true, true, 8>(s, Qt, voffQ, gq + (size_t)17 * KS_Q, 128 * 7, no);
    qstep<1, false, true, false, 8>(s, Qt, voffQ, gq, 128 * 8, no);
    qstep<0, false, true, false, 0>(s, Qt, voffQ, gq, 128 * 8, no);
    qstep<1, false, false, false, 0>(s, Qt, voffQ, gq, 0, no);
    STAMP(2);
    PST(8);
    {
      const int kvl = w >> 1, half = w & 1;
      const unsigned kr = ldsb + kvl * 65536, vr = kr + 32768;
#pragma unroll
      for (int i = 0; i < 2; ++i) a.kb_[i] = kr + 2048 * (r32 >> 3) + 64 * (r32 & 7) + 16 * ((2 * i + hh) ^ ((r32 >> 2) & 3));
      const int blk = (lane >> 4) & 1, q = (lane & 15) >> 2, pp = lane & 3;
#pragma unroll
      for (int t = 0; t < 2; ++t) a.vb_[t] = vr + 64 * (4 * hh + q) + 16 * ((2 * blk + (pp >> 1)) ^ ((2 * t + hh) & 3)) + 8 * (pp & 1);
      const int st = lane >> 5, r7 = (lane >> 2) & 7, pos = lane & 3;
#pragma unroll
      for (int i = 0; i < 8; ++i) {
        const int rg = 4 * half + (i >> 1);
        a.voff[i] = rg * 2048 + r7 * 256 + (i & 1) * 128 + st * 64 + 16 * (pos ^ ((2 * (rg & 1) + (r7 >> 2)) & 3)) - (i - 4) * 1024 + 4096;
      }
      a.m0k = __builtin_amdgcn_readfirstlane(kr + 8192 * half + 4096); a.m0v = __builtin_amdgcn_readfirstlane(vr + 8192 * half + 4096);
      const size_t kvoff = ((size_t)(2 * j + kvl) * SK + (SK - WIN)) * DH * 2;
      a.gk = (const char*)p.kc + kvoff - 4096; a.gv = (const char*)p.vc + kvoff - 4096;
      a.pend = false; a.one = p.one;
    }
    asm volatile("s_waitcnt lgkmcnt(0)" ::: "memory");
    BAR();
    setm0(a.m0k); asm volatile("s_nop 0");
    dma<-4096>(a.voff[0], a.gk); dma<-3072>(a.voff[1], a.gk); dma<-2048>(a.voff[2], a.gk); dma<-1024>(a.voff[3], a.gk);
    dma<0>(a.voff[4], a.gk); dma<1024>(a.voff[5], a.gk); dma<2048>(a.voff[6], a.gk); dma<3072>(a.voff[7], a.gk);
    asm volatile("s_nop 7" ::: "memory");
#pragma unroll
    for (int dt = 0; dt < 4; ++dt)
#pragma unroll
      for (int qb = 0; qb < 2; ++qb) {
        asm volatile("" : "+v"(Qt[dt][qb]));
#pragma unroll
        for (int s2 = 0; s2 < 2; ++s2) {
          u4 w4;
#pragma unroll
          for (int e = 0; e < 4; ++e) w4[e] = pk((f2){Qt[dt][qb][8 * s2 + 2 * e], Qt[dt][qb][8 * s2 + 2 * e + 1]} * QSCALE);
          a.q[qb][2 * dt + s2] = __builtin_bit_cast(b8, w4);
          asm volatile("" : "+a"(a.q[qb][2 * dt + s2]));
        }
      }
  }
  STAMP(3);
  {
    auto tile_bar = [&]() __attribute__((always_inline)) { asm volatile("s_waitcnt vmcnt(0) lgkmcnt(0)" ::: "memory"); BAR(); };
    const char* pfp = lane >= 32 ? p.ws + WS_X + (size_t)c * 64 * SLICE + (size_t)j * SLICE + (18 * w + lane - 32) * 128
                    : w < 2 ? (const char*)(w == 0 ? p.wg : p.wu) + (size_t)576 * j * 2 + (size_t)(18 * c + lane / 9 - 3) * (DF * 2) + (lane % 9) * 128
                            : (const char*)p.wo + (size_t)512 * j * ROWB + (16 * c) * ROWB + (12 * (w - 2) + lane - 24) * 128;
    const int pfs = lane >= 32 ? 8 * SLICE : w < 2 ? 3 * DF * 2 : 24 * 128, pfn = w < 2 ? 27 : 12;
    auto pft = [&](int t) __attribute__((always_inline)) {
      const bool on = lane < pfn && t >= 1 && t <= 6;
      touchp(on ? pfp : (const char*)p.wo, __builtin_amdgcn_readfirstlane(ldsb + LDS_DUMMY));
      pfp += pfs;
    };
    auto tile_bar1 = [&]() __attribute__((always_inline)) { asm volatile("s_waitcnt vmcnt(1) lgkmcnt(0)" ::: "memory"); BAR(); };
    LAS unsigned* flag = (LAS unsigned*)(size_t)(ldsb + LDS_X1);
    if (tid == 0) *flag = 0u;
    auto k0 = [&]() __attribute__((always_inline)) {
      setm0(a.m0k); asm volatile("s_nop 0");
      dma<-4096>(a.voff[0], a.gk); dma<-3072>(a.voff[1], a.gk); dma<-2048>(a.voff[2], a.gk); dma<-1024>(a.voff[3], a.gk);
      dma<0>(a.voff[4], a.gk); dma<1024>(a.voff[5], a.gk); dma<2048>(a.voff[6], a.gk); dma<3072>(a.voff[7], a.gk);
    };
    tile_bar(); fastA<0, true, true, true>(a, 0, [&]() __attribute__((always_inline)) { pft(0); }); asm volatile("s_nop 7\n\ts_nop 3" ::: "memory"); GAP(); fastB<0, 2>(a);
    tile_bar1(); fastA<1, false, true, true>(a, 1, [&]() __attribute__((always_inline)) { pft(1); }); fastB<1, 1>(a);
    for (int t = 2; t < 6; t += 2) {
      tile_bar1(); fastA<0, false, true, true>(a, t, [&]() __attribute__((always_inline)) { pft(t); }); fastB<0, 0>(a);
      tile_bar1(); fastA<1, false, true, true>(a, t + 1, [&]() __attribute__((always_inline)) { pft(t + 1); }); fastB<1, 0>(a);
    }
    tile_bar1(); fastA<0, false, true, true>(a, 6, [&]() __attribute__((always_inline)) { pft(6); }); fastB<0, 0>(a);
    tile_bar1(); fastA<1, false, false, true>(a, 7, [&]() __attribute__((always_inline)) { pft(7); }); fastB<1, 0>(a);
    tile_bar1();
    fastB<1, 3>(a);
    {
      const bool bad = !(a.l[0] < 0x1p40f) || !(a.l[1] < 0x1p40f);
      if (__any((int)bad) && lane == 0) *flag = 1u;
      asm volatile("s_waitcnt lgkmcnt(0)" ::: "memory"); BAR();
      unsigned redo = __builtin_amdgcn_readfirstlane(*(volatile LAS unsigned*)flag); asm volatile("" : "+s"(redo));
      if (__builtin_expect(redo != 0u, 0)) {
        auto nopf = [&]() __attribute__((always_inline)) {};
        a.pend = false;
        auto upt = [&](const char* q) __attribute__((always_inline)) { const u64 v = (u64)q; return (const char*)((u64)(unsigned)__builtin_amdgcn_readfirstlane((unsigned)(v >> 32)) << 32 | (u64)(unsigned)__builtin_amdgcn_readfirstlane((unsigned)v)); };
        a.gk = upt(a.gk); a.gv = upt(a.gv); a.m0k = __builtin_amdgcn_readfirstlane(a.m0k); a.m0v = __builtin_amdgcn_readfirstlane(a.m0v);
        k0();
        tile_bar(); phaseA<0, true, true, true>(a, 0, nopf); asm volatile("s_nop 7\n\ts_nop 3" ::: "memory"); GAP(); phaseB<0, true, false>(a);
        tile_bar(); phaseA<1, false, true, true>(a, 1, nopf); phaseB<1, false, true>(a);
        for (int t = 2; t < 6; t += 2) {
          tile_bar(); phaseA<0, false, true, true>(a, t, nopf); phaseB<0, false, false>(a);
          tile_bar(); phaseA<1, false, true, true>(a, t + 1, nopf); phaseB<1, false, false>(a);
        }
        tile_bar(); phaseA<0, false, true, true>(a, 6, nopf); phaseB<0, false, false>(a);
        tile_bar(); phaseA<1, false, false, true>(a, 7, nopf); phaseB<1, false, false>(a);
        { auto f1 = [&](auto ic) __attribute__((always_inline)) { sm_exp<1, 1, decltype(ic)::value>(a); }; UNROLL(32, f1);
          auto f2_ = [&](auto ic) __attribute__((always_inline)) { sm_pack<1, decltype(ic)::value>(a); }; UNROLL(32, f2_);
          auto f3 = [&](auto ic) __attribute__((always_inline)) { sm_add<1, 1, decltype(ic)::value>(a); }; UNROLL(32, f3); }
#pragma unroll
        for (int qb = 0; qb < 2; ++qb) a.l[qb] += a.rs0[qb] + a.rs1[qb];
        tile_bar();
        const unsigned vslot = 16384;
        b4 vlo[16], vhi[16];
#pragma unroll
        for (int I = 0; I < 16; ++I) { vlo[I] = ldT(a.vb_[0], vslot + 2048 * (2 * (I >> 2)) + 512 * (I & 3)); vhi[I] = ldT(a.vb_[1], vslot + 2048 * (2 * (I >> 2) + 1) + 512 * (I & 3)); }
        asm volatile("s_nop 1" ::: "memory"); GAP();
#pragma unroll
        for (int G = 0; G < 32; ++G) { const int I = G >> 1; mfma32<true, false>(a.O[I & 3][G & 1], cat(vlo[I], vhi[I]), a.P[G & 1][I >> 2]); }
        asm volatile("s_waitcnt vmcnt(0) lgkmcnt(0)" ::: "memory"); BAR();
      }
    }
  }
  STAMP(4);
  int tid2 = threadIdx.x; asm volatile("" : "+v"(tid2));
  {
  const int tid = tid2, lane = tid2 & 63, hh = lane >> 5, r32 = lane & 31;
  GS s;
  unsigned voffB[9];
#pragma unroll
  for (int i = 0; i < 9; ++i) { const int idx = i * 64 + lane, imm = i < 8 ? (i - 4) * 1024 : 0; voffB[i] = (idx / 18) * ROWB + (idx % 18) * 16 - imm + 4096; }
  s.ring = __builtin_amdgcn_readfirstlane(ldsb + ABYTES + w * 2 * SLOT);
  const int g4 = lane >> 4, cl = lane & 15;
  s.wbase = s.ring + (4 * g4 + ((lane & 15) >> 2)) * 288 + 8 * (lane & 3);
  const unsigned sw = (cl >> 1) & 7;
  const unsigned ab_e = ldsb + cl * ROWB + ((g4 ^ sw) * 16), ab_o = ldsb + cl * ROWB + (((4 + g4) ^ sw) * 16);
  const __amdgpu_buffer_rsrc_t rX = __builtin_amdgcn_make_buffer_rsrc(p.ws + WS_X + (size_t)c * 64 * SLICE, 0, 64 * SLICE, 0x00020000);
  unsigned voffA[9];
#pragma unroll
  for (int i = 0; i < 9; ++i) { const int idx = i * 64 + lane, imm = i < 8 ? (i - 4) * 1024 : 0; voffA[i] = (idx / 18) * (DF * 2) + (idx % 18) * 16 - imm + 4096; }
  const char* gG = (const char*)p.wg + (size_t)(576 * j + 144 * w) * 2 - 4096;
  const char* gU = (const char*)p.wu + (size_t)(576 * j + 144 * w) * 2 - 4096;
  const char* gD = (const char*)p.wd + (size_t)(576 * j) * ROWB + 288 * w - 4096;
  f4 aG[9][4];
  {
    const char* gO = (const char*)p.wo + (size_t)(512 * j) * ROWB + 288 * w - 4096;
    issue_all(s, 0, voffB, gO); issue_all(s, 1, voffB, gO + (size_t)32 * ROWB);
    asm volatile("s_nop 7" ::: "memory");
#pragma unroll
    for (int qb = 0; qb < 2; ++qb) {
      const float il = __builtin_amdgcn_rcpf(swap32_add(a.l[qb]));
      const int row = 32 * qb + r32; const unsigned swr = (r32 >> 1) & 7;
#pragma unroll
      for (int db = 0; db < 4; ++db) {
        asm volatile("" : "+a"(a.O[db][qb]));
        const f16v o = a.O[db][qb];
#pragma unroll
        for (int u = 0; u < 2; ++u) {
          const u4 wv = {pk((f2){o[4 * u], o[4 * u + 1]} * il), pk((f2){o[4 * u + 2], o[4 * u + 3]} * il), pk((f2){o[4 * u + 8], o[4 * u + 9]} * il), pk((f2){o[4 * u + 10], o[4 * u + 11]} * il)};
          *(LAS u4*)(size_t)(ldsb + row * ROWB + (((4 * (4 * w + db) + 2 * u + hh) ^ swr) * 16)) = wv;
        }
      }
    }
    asm volatile("s_waitcnt vmcnt(9) lgkmcnt(0)" ::: "memory");
    BAR();
    STAMP(5);
    read_w(s, 0); read_a(s, 0, ab_e);
    asm volatile("s_waitcnt lgkmcnt(0)" ::: "memory");
    issue_all(s, 0, voffB, gO + (size_t)2 * 32 * ROWB);
    const Epi e1 = {rX, (unsigned)(j * SLICE), 8u * SLICE, ldsb + LDS_X1, j, w, lane};
    gemm_rows<16, 2, 0>(s, aG, voffB, gO, ab_e, ab_o, voffA, gG, gU, e1, [&]() __attribute__((always_inline)) {});
    STAMP(6);
  }
  {
    int zlr[3], zcc[3];
    const int t3 = tid < 192 ? tid : tid - 64;
    u4 xr[3], gv[3];
#pragma unroll
    for (int i = 0; i < 3; ++i) {
      zdecode(t3 + 192 * i, zlr[i], zcc[i]);
      xr[i] = *(const u4*)(p.x + (size_t)(c * 64 + j * 8 + zlr[i]) * D + zcc[i] * 8);
      gv[i] = *(LAS u4*)(size_t)(ldsb + LDS_MOD + (2 * D + zcc[i] * 8) * 2);
    }
    const int lrow = 2 * w + hh, grow = j * 8 + lrow;
    u2 cbv[3][2], sbv[3][2];
#pragma unroll
    for (int i = 0; i < 3; ++i) {
      const int pos = (i < 2 || r32 < 8) ? r32 + 32 * i : r32;
      const int q = pos ^ ((grow >> 1) & 7), k0 = 32 * (q >> 2) + 4 * (q & 3);
#pragma unroll
      for (int h = 0; h < 2; ++h) { cbv[i][h] = *(LAS u2*)(size_t)(ldsb + LDS_MOD + (4 * D + k0 + 16 * h) * 2); sbv[i][h] = *(LAS u2*)(size_t)(ldsb + LDS_MOD + (3 * D + k0 + 16 * h) * 2); }
    }
    asm volatile("s_waitcnt lgkmcnt(0)" ::: "memory");
    publish(flags + (0 * 256 + blockIdx.x) * FLAG_STRIDE, tag);
    STAMP(7);
    await8(flags + (0 * 256 + c * 8) * FLAG_STRIDE, tag, tmo, w, lane);
    STAMP(8);
    {
      u4 v[3][8];
#pragma unroll
      for (int i = 0; i < 3; ++i) {
#pragma unroll
        for (int jj = 0; jj < 7; ++jj) v[i][jj] = __builtin_amdgcn_raw_buffer_load_b128(rX, (j * 8 + jj + (jj >= j)) * SLICE + (t3 + 192 * i) * 16, 0, AUX_X);
      }
#pragma unroll
      for (int i = 0; i < 3; ++i) v[i][7] = *(LAS u4*)(size_t)(ldsb + LDS_X1 + (t3 + 192 * i) * 16);
#pragma unroll
      for (int i = 0; i < 3; ++i) {
        const u4 x1 = gated_residual(v[i], xr[i], gv[i]);
        if (tid < 192) {
          *(LAS u4*)(size_t)(ldsb + LDS_X1 + (tid + 192 * i) * 16) = x1;
          LAS f4* dst = (LAS f4*)(size_t)(ldsb + (zlr[i] * D + zcc[i] * 8) * 4);
          const f2 a0 = bf2(x1[0]), a1 = bf2(x1[1]), a2 = bf2(x1[2]), a3 = bf2(x1[3]);
          dst[0] = (f4){a0[0], a0[1], a1[0], a1[1]}; dst[1] = (f4){a2[0], a2[1], a3[0], a3[1]};
        }
      }
    }
    asm volatile("s_waitcnt lgkmcnt(0)" ::: "memory");
    BAR();
    STAMP(9);
    {
      f4 v[3][2];
      f2 sum = {0.f, 0.f};
#pragma unroll
      for (int i = 0; i < 3; ++i) {
        const bool on = (i < 2 || r32 < 8);
        const int pos = on ? r32 + 32 * i : r32;
        const int q = pos ^ ((grow >> 1) & 7), k0 = 32 * (q >> 2) + 4 * (q & 3);
#pragma unroll
        for (int h = 0; h < 2; ++h) {
          v[i][h] = *(LAS f4*)(size_t)(ldsb + (lrow * D + k0 + 16 * h) * 4);
          if (on) sum += (f2){v[i][h][0] + v[i][h][1], v[i][h][2] + v[i][h][3]};
        }
      }
      float sm = sum[0] + sum[1];
      sm = sum32(sm);
      const float mean = sm * (1.f / D);
      float q2 = 0.f;
#pragma unroll
      for (int i = 0; i < 3; ++i)
        if (i < 2 || r32 < 8) {
#pragma unroll
          for (int h = 0; h < 2; ++h)
#pragma unroll
            for (int e = 0; e < 4; ++e) { const float dl = v[i][h][e] - mean; q2 += dl * dl; }
        }
      q2 = sum32(q2);
      const float mr = rbf(mean), rs = __builtin_amdgcn_rcpf(rbf(__builtin_amdgcn_sqrtf(rbf(rbf(q2 * (1.f / D)) + 1e-5f))));
      const __amdgpu_buffer_rsrc_t rH = __builtin_amdgcn_make_buffer_rsrc(p.ws + WS_H2 + (size_t)c * ABYTES, 0, ABYTES, 0x00020000);
      u4 oh[3];
#pragma unroll
      for (int i = 0; i < 3; ++i) {
        u4& o = oh[i];
#pragma unroll
        for (int h = 0; h < 2; ++h)
#pragma unroll
          for (int e = 0; e < 2; ++e) {
            unsigned t = pk((f2){v[i][h][2 * e], v[i][h][2 * e + 1]} - mr);
            t = pk(bf2(t) * rs);
            t = pk(bf2(t) * bf2(pk(bf2(cbv[i][h][e]) + 1.f)));
            o[2 * h + e] = pk(bf2(t) + bf2(sbv[i][h][e]));
          }
        if (i < 2 || r32 < 8) __builtin_amdgcn_raw_buffer_store_b128(o, rH, grow * ROWB + (r32 + 32 * i) * 16, 0, 16);
      }
      publish(flags + (1 * 256 + blockIdx.x) * FLAG_STRIDE, tag);
      STAMP(10);
      if (w != 0) {
        const unsigned dummy = __builtin_amdgcn_readfirstlane(ldsb + LDS_DUMMY);
        int li = tid - 64; li = li < 162 ? li : li - 94;
        const int r = 18 * c + li / 9, l = li % 9;
        touch((const char*)p.wg + (size_t)576 * j * 2, r * (DF * 2) + l * 128, dummy);
        touch((const char*)p.wu + (size_t)576 * j * 2, r * (DF * 2) + l * 128, dummy);
      }
      await8(flags + (1 * 256 + c * 8) * FLAG_STRIDE, tag, tmo, w, lane);
      STAMP(11);
      u4 hv[16];
#pragma unroll
      for (int i = 0; i < 16; ++i) { const int y = i * 256 + tid < 4032 ? i * 256 + tid : tid; hv[i] = __builtin_amdgcn_raw_buffer_load_b128(rH, (y + (y >= j * 576 ? 576 : 0)) * 16, 0, AUX_H); }
#pragma unroll
      for (int i = 0; i < 3; ++i) if (i < 2 || r32 < 8) *(LAS u4*)(size_t)(ldsb + grow * ROWB + (r32 + 32 * i) * 16) = oh[i];
#pragma unroll
      for (int i = 0; i < 16; ++i) { const int y = i * 256 + tid; if (i < 15 || y < 4032) *(LAS u4*)(size_t)(ldsb + (y + (y >= j * 576 ? 576 : 0)) * 16) = hv[i]; }
    }
    asm volatile("s_waitcnt vmcnt(0) lgkmcnt(0)" ::: "memory");
    BAR();
  }
  STAMP(12);
  {
    constexpr size_t KS_GU = (size_t)32 * DF * 2, KS_D = (size_t)32 * ROWB;
    read_w(s, 0); read_a(s, 0, ab_e);
    asm volatile("s_waitcnt lgkmcnt(0)" ::: "memory");
    issue_all(s, 0, voffA, gG + KS_GU);
    f4 aU[9][4];
    auto no = [&]() __attribute__((always_inline)) {};
    auto pfd = [&](int h) __attribute__((always_inline)) {
      int li = 64 * h + 16 * w + lane; li = li < 162 ? li : 161;
      if (lane < 16) touch((const char*)p.wd + (size_t)576 * j * ROWB, (18 * c) * ROWB + li * 128, __builtin_amdgcn_readfirstlane(ldsb + LDS_DUMMY));
    };
    auto kpair = [&](auto zc, auto lastc, auto vmc, int k, auto&& pf) __attribute__((always_inline)) {
      constexpr bool Z = decltype(zc)::value, LAST = decltype(lastc)::value; constexpr int VM = decltype(vmc)::value;
      const char* u1 = gU + (size_t)(k + 1) * KS_GU; const char* g2 = gG + (size_t)(k + 2) * KS_GU;
      step<0, 0, Z, true, false, true, 9, 0>(s, aG, voffA, u1, 0, no);
      if constexpr (!LAST) {
        step<1, 0, Z, true, true, true, 9, NVU>(s, aU, voffA, g2, ab_o + 128 * (k >> 1), pf);
        step<0, 1, false, true, false, true, VM, 0>(s, aG, voffA, u1 + KS_GU, 0, no);
        step<1, 1, false, true, true, true, VM, NVU>(s, aU, voffA, g2 + KS_GU, ab_e + 128 * ((k + 2) >> 1), no);
      } else {
        step<1, 0, Z, true, true, true, 9, NVU>(s, aU, voffB, gD, ab_o + 128 * (k >> 1), no);
        step<0, 1, false, true, false, true, 9, 0>(s, aG, voffB, gD + KS_D, 0, no);
        step<1, 1, false, true, false, true, 9, NVU>(s, aU, voffB, gD + 2 * KS_D, 0, no);
      }
    };
    kpair(std::true_type{}, std::false_type{}, IC<9>{}, 0, no);
    for (int k = 2; k < 8; k += 2) kpair(std::false_type{}, std::false_type{}, IC<9>{}, k, no);
    for (int k = 8; k < 14; k += 2) kpair(std::false_type{}, std::false_type{}, IC<10>{}, k, [&]() __attribute__((always_inline)) { pfd((k - 8) >> 1); });
    kpair(std::false_type{}, std::false_type{}, IC<9>{}, 14, no);
    kpair(std::false_type{}, std::true_type{}, IC<9>{}, 16, no);
    STAMP(13);
    settle<0>(aG); settle<NVU>(aU);
    u2 act[9][4];
#pragma unroll
    for (int t = 0; t < 9; ++t)
#pragma unroll
      for (int m = 0; m < 4; ++m)
#pragma unroll
        for (int e = 0; e < 2; ++e) {
          const f2 x = {aG[t][m][2 * e], aG[t][m][2 * e + 1]}, u = {aU[t][m][2 * e], aU[t][m][2 * e + 1]};
          const f2 z = x * (x * x * -0.10294324f + -2.3022082f);
          const f2 dn = (f2){__builtin_amdgcn_exp2f(z[0]), __builtin_amdgcn_exp2f(z[1])} + 1.f;
          act[t][m][e] = pk((x * u) * (f2){__builtin_amdgcn_rcpf(dn[0]), __builtin_amdgcn_rcpf(dn[1])});
        }
    asm volatile("s_waitcnt lgkmcnt(0)" ::: "memory");
    BAR();
#pragma unroll
    for (int t = 0; t < 9; ++t) {
      const int tt = 9 * w + t, ks = tt >> 1, ah = tt & 1;
      const unsigned ad = ldsb + cl * ROWB + (((4 * ks + g4) ^ sw) * 16) + 8 * ah;
#pragma unroll
      for (int m = 0; m < 4; ++m) *(LAS u2*)(size_t)(ad + m * 16 * ROWB) = act[t][m];
    }
    asm volatile("s_waitcnt lgkmcnt(0)" ::: "memory");
    BAR();
    STAMP(14);
    read_a(s, 0, ab_e);
    const Epi e2 = {rX, (unsigned)(j * 8 * SLICE), (unsigned)SLICE, s.ring - w * 2304, j, w, lane};
    gemm_rows<18, 0, 0>(s, aG, voffB, gD, ab_e, ab_o, voffB, gD, gD, e2, [&]() __attribute__((always_inline)) {});
    STAMP(15);
  }
  {
    int tid3 = threadIdx.x; asm volatile("" : "+v"(tid3));
    const int tid = tid3, lane = tid3 & 63;
    asm volatile("s_waitcnt lgkmcnt(0)" ::: "memory");
    publish(flags + (2 * 256 + blockIdx.x) * FLAG_STRIDE, tag);
    STAMP(16);
    const int t3 = tid < 192 ? tid : tid - 64;
    int zlr[3], zcc[3];
    u4 gv[3], x1[3];
#pragma unroll
    for (int i = 0; i < 3; ++i) {
      zdecode(t3 + 192 * i, zlr[i], zcc[i]);
      gv[i] = *(LAS u4*)(size_t)(ldsb + LDS_MOD + (5 * D + zcc[i] * 8) * 2);
      x1[i] = *(LAS u4*)(size_t)(ldsb + LDS_X1 + (t3 + 192 * i) * 16);
    }
    if (blockIdx.x == 0 && w == 1) {
      for (unsigned spins = 0; spins <= SPIN_CAP; ++spins) {
        bool ok = true;
#pragma unroll
        for (int i = 0; i < 4; ++i) ok &= __hip_atomic_load((gu64*)(flags + (lane + 64 * i) * FLAG_STRIDE), __ATOMIC_RELAXED, __HIP_MEMORY_SCOPE_AGENT) == tag;
        if (__all(ok)) break;
        __builtin_amdgcn_s_sleep(4);
      }
      if (lane == 0) __hip_atomic_store((gu64*)(p.ws + WS_EPOCH), tag, __ATOMIC_RELAXED, __HIP_MEMORY_SCOPE_AGENT);
    }
    await8(flags + (2 * 256 + c * 8) * FLAG_STRIDE, tag, tmo, w, lane);
    STAMP(17);
    const __amdgpu_buffer_rsrc_t rO = __builtin_amdgcn_make_buffer_rsrc(p.out + (size_t)(c * 64 + j * 8) * D, 0, SLICE, 0x00020000); (void)rO;
    u4 v[3][8];
#pragma unroll
    for (int i = 0; i < 3; ++i)
#pragma unroll
      for (int jj = 0; jj < 7; ++jj) v[i][jj] = __builtin_amdgcn_raw_buffer_load_b128(rX, ((jj + (jj >= j)) * 8 + j) * SLICE + (t3 + 192 * i) * 16, 0, AUX_X);
#pragma unroll
    for (int i = 0; i < 3; ++i) { const int z = t3 + 192 * i, zw = z / 144; v[i][7] = *(LAS u4*)(size_t)(ldsb + ABYTES + zw * 2 * SLOT + (z - zw * 144) * 16); }
#pragma unroll
    for (int i = 0; i < 3; ++i) {
      const u4 o = gated_residual(v[i], x1[i], gv[i]);
      if (tid < 192) *(u4*)(p.out + (size_t)(c * 64 + j * 8 + zlr[i]) * D + zcc[i] * 8) = o;
    }
  }
  STAMP(18);
  asm volatile("s_waitcnt lgkmcnt(0)" ::: "memory");
  STAMP_FLUSH();
  }
}

extern "C" void kernel_launch(void* const* d_in, const int* in_sizes, int n_in, void* d_out, int out_size, void* d_ws, size_t ws_size, hipStream_t stream) {
  Params p;
  p.x = (const u16*)d_in[0]; p.kc = (const u16*)d_in[1]; p.vc = (const u16*)d_in[2]; p.wq = (const u16*)d_in[3]; p.wo = (const u16*)d_in[4];
  p.wg = (const u16*)d_in[6]; p.wu = (const u16*)d_in[7]; p.wd = (const u16*)d_in[8];
  p.c = (const u16*)d_in[9]; p.wmod = (const u16*)d_in[10];
  p.out = (u16*)d_out; p.ws = (char*)d_ws;
 p.one = 1.0f; p.gap = 0u;
  (void)hipFuncSetAttribute((const void*)k_main, hipFuncAttributeMaxDynamicSharedMemorySize, LDS_BYTES);
  k_main<<<256, 256, LDS_BYTES, stream>>>(p);
}
```

```cpp
#define XPRE 250
#define STAG_D2 640
#include <hip/hip_runtime.h>
#include <stdint.h>
#include <utility>
#include <type_traits>

typedef unsigned short u16;
typedef unsigned long long u64;
typedef __bf16 b2 __attribute__((ext_vector_type(2)));
typedef __bf16 b4 __attribute__((ext_vector_type(4)));
typedef __bf16 b8 __attribute__((ext_vector_type(8)));
typedef float f2 __attribute__((ext_vector_type(2)));
typedef float f4 __attribute__((ext_vector_type(4)));
typedef float f16v __attribute__((ext_vector_type(16)));
typedef unsigned u2 __attribute__((ext_vector_type(2)));
typedef unsigned u4 __attribute__((ext_vector_type(4)));
typedef __attribute__((address_space(1))) unsigned gu32;
typedef __attribute__((address_space(1))) unsigned long long gu64;
#define LAS __attribute__((address_space(3)))
#define DI __device__ __forceinline__
#define GAP() __builtin_amdgcn_sched_barrier(0)
#define LGKM0() __builtin_amdgcn_s_waitcnt(0xC07F)
#define BAR() do { asm volatile("" ::: "memory"); __builtin_amdgcn_s_barrier(); asm volatile("" ::: "memory"); } while (0)
template <int V> using IC = std::integral_constant<int, V>;
#define AUX_X 16
#define AUX_H 16
#define PF_D2 150
#ifndef NVU
#define NVU 8
#endif
#define PFD_K 14
#define UNROLL(N, F) [&]<int... I_>(std::integer_sequence<int, I_...>) __attribute__((always_inline)) { (F(IC<I_>{}), ...); }(std::make_integer_sequence<int, N>{})

constexpr int S = 2048, D = 576, DH = 128, SK = 2048, WIN = 512, DF = 4608;
constexpr int ROWB = D * 2;
constexpr int ABYTES = 64 * ROWB;
constexpr int SLICE = 8 * ROWB;
constexpr int SLOT = 9216;
constexpr int QSLOT = 8192;
constexpr int LDS_DUMMY = ABYTES + 8 * SLOT;
constexpr int LDS_X1 = LDS_DUMMY + 256;
constexpr int LDS_MOD = LDS_X1 + SLICE;
constexpr int LDS_BYTES = LDS_MOD + 6 * D * 2;
static_assert(LDS_BYTES <= 163840, "LDS");
constexpr size_t WS_EPOCH = 0;
constexpr size_t WS_TMO = 256;
constexpr size_t WS_MODP = 131072;
constexpr size_t WS_FLAG = 32768;
constexpr int FLAG_STRIDE = 8;
constexpr size_t WS_X = 1048576;
constexpr size_t WS_H2 = WS_X + (size_t)32 * 64 * SLICE;
constexpr size_t WS_DBG = 200u << 20;
#define STAMP(i)
#define STAMP_FLUSH()
#define PST(i)

DI float bf2f(u16 v) { return __uint_as_float(((unsigned)v) << 16); }
DI float bflo(unsigned w) { return __uint_as_float(w << 16); }
DI float bfhi(unsigned w) { return __uint_as_float(w & 0xffff0000u); }
DI f2 bf2(unsigned w) { return (f2){bflo(w), bfhi(w)}; }
DI unsigned pk(float lo, float hi) { f2 v = {lo, hi}; b2 b = __builtin_convertvector(v, b2); return __builtin_bit_cast(unsigned, b); }
DI unsigned pk(f2 v) { b2 b = __builtin_convertvector(v, b2); return __builtin_bit_cast(unsigned, b); }
DI float rbf(float f) { return bflo(pk(f, f)); }
DI f2 rb2(f2 v) { return (f2){__uint_as_float(pk(0.f, v[0])), __uint_as_float(pk(0.f, v[1]))}; }
template <int CTRL> DI float dpp(float v) { return __uint_as_float(__builtin_amdgcn_update_dpp(0u, __float_as_uint(v), CTRL, 0xf, 0xf, false)); }
DI float sum8(float v) { v += dpp<0xB1>(v); v += dpp<0x4E>(v); v += dpp<0x141>(v); return v; }
DI float swap16_add(float x) { auto r = __builtin_amdgcn_permlane16_swap(__float_as_uint(x), __float_as_uint(x), false, false); return __uint_as_float(r[0]) + __uint_as_float(r[1]); }
DI float swap32_add(float x) { auto r = __builtin_amdgcn_permlane32_swap(__float_as_uint(x), __float_as_uint(x), false, false); return __uint_as_float(r[0]) + __uint_as_float(r[1]); }
DI float sum32(float v) { v = sum8(v); v += dpp<0x140>(v); return swap16_add(v); }
DI u16 f2bf(float f) { return (u16)(pk(f, f) & 0xffffu); }
DI float fmaxx(float a, float b) { return __builtin_elementwise_maximum(a, b); }
DI float fmax3(float a, float b, float c) { return __builtin_elementwise_maximum(__builtin_elementwise_maximum(a, b), c); }

DI void zdecode(int z, int& lr, int& cc) {
  const int w = z / 144, y = z - w * 144;
  if (y < 128) { const int tp = y >> 5; lr = (y >> 2) & 7; cc = 18 * w + 4 * tp + (y & 3); }
  else { const int y2 = y - 128; lr = y2 >> 1; cc = 18 * w + 16 + (y2 & 1); }
}

template <int IMM> DI void dma(unsigned voff, const char* g) { asm volatile("global_load_lds_dwordx4 %0, %1 offset:%2" :: "v"(voff), "s"(g), "i"(IMM) : "memory"); }
DI void setm0(unsigned a) { asm volatile("s_mov_b32 m0, %0" :: "s"(a) : "memory"); }
DI void touch(const char* p, unsigned voff, unsigned ldsdummy) {
  asm volatile("s_mov_b32 m0, %0\n\ts_nop 0\n\tglobal_load_lds_dword %1, %2" :: "s"(ldsdummy), "v"(voff), "s"(p) : "memory");
}
DI void touchp(const char* a, unsigned ldsdummy) {
  asm volatile("s_mov_b32 m0, %0\n\ts_nop 0\n\tglobal_load_lds_dword %1, off" :: "s"(ldsdummy), "v"(a) : "memory");
}
DI b4 ldT(unsigned base, int off) { return __builtin_amdgcn_ds_read_tr16_b64_v4bf16((LAS b4*)(size_t)(base + off)); }
DI b8 ldA(unsigned base, int off) { return *(LAS b8*)(size_t)(base + off); }
DI b8 cat(const b4& lo, const b4& hi) { return __builtin_shufflevector(lo, hi, 0, 1, 2, 3, 4, 5, 6, 7); }
template <bool AG, bool ZERO> DI void mfma16(f4& c, const b8& a, const b8& b) {
  if constexpr (AG) {
    if constexpr (ZERO) asm volatile("v_mfma_f32_16x16x32_bf16 %0, %1, %2, 0" : "=a"(c) : "v"(a), "v"(b));
    else asm volatile("v_mfma_f32_16x16x32_bf16 %0, %1, %2, %0" : "+a"(c) : "v"(a), "v"(b));
  } else {
    if constexpr (ZERO) asm volatile("v_mfma_f32_16x16x32_bf16 %0, %1, %2, 0" : "=&v"(c) : "v"(a), "v"(b));
    else asm volatile("v_mfma_f32_16x16x32_bf16 %0, %1, %2, %0" : "+v"(c) : "v"(a), "v"(b));
  }
}
template <bool AG, bool ZERO> DI void mfma32(f16v& c, const b8& a, const b8& b) {
  if constexpr (AG) {
    if constexpr (ZERO) asm volatile("v_mfma_f32_32x32x16_bf16 %0, %1, %2, 0" : "=a"(c) : "v"(a), "v"(b));
    else asm volatile("v_mfma_f32_32x32x16_bf16 %0, %1, %2, %0" : "+a"(c) : "v"(a), "v"(b));
  } else {
    if constexpr (ZERO) asm volatile("v_mfma_f32_32x32x16_bf16 %0, %1, %2, 0" : "=&v"(c) : "v"(a), "v"(b));
    else asm volatile("v_mfma_f32_32x32x16_bf16 %0, %1, %2, %0" : "+v"(c) : "v"(a), "v"(b));
  }
}
template <int NVG> DI void settle(f4 (&acc)[9][4]) {
  asm volatile("s_nop 7\n\ts_nop 7" ::: "memory");
#pragma unroll
  for (int t = 0; t < 9; ++t)
#pragma unroll
    for (int m = 0; m < 4; ++m) { if (t * 4 + m < 36 - NVG) asm volatile("" : "+a"(acc[t][m])); else asm volatile("" : "+v"(acc[t][m])); }
}

struct GS {
  b4 wlo[2][9], whi[2][9];
  b8 af[2][4];
  unsigned wbase, ring;
};
DI void issue_all(const GS& s, int slot, const unsigned (&voff)[9], const char* gp) {
  const unsigned sl = s.ring + slot * SLOT;
  setm0(sl + 4096); asm volatile("s_nop 0");
  dma<-4096>(voff[0], gp); dma<-3072>(voff[1], gp); dma<-2048>(voff[2], gp); dma<-1024>(voff[3], gp);
  dma<0>(voff[4], gp); dma<1024>(voff[5], gp); dma<2048>(voff[6], gp); dma<3072>(voff[7], gp);
  setm0(sl + 8192); asm volatile("s_nop 0"); dma<0>(voff[8], gp);
}
template <int PAR, int AP, bool ZERO, bool RDW, bool RDA, bool DMA, int VM, int NVG, typename PF>
DI void step(GS& s, f4 (&acc)[9][4], const unsigned (&voff)[9], const char* gp, unsigned ab, PF&& pf) {
  const unsigned sl = s.ring + (PAR ^ 1) * SLOT;
  const unsigned wb = s.wbase + (PAR ^ 1) * SLOT;
  auto mm = [&](auto ic) __attribute__((always_inline)) {
    constexpr int I = decltype(ic)::value, t = I >> 2, m = I & 3;
    mfma16<(I < 36 - NVG), ZERO>(acc[t][m], cat(s.wlo[PAR][t], s.whi[PAR][t]), s.af[AP][m]);
  };
  auto rd = [&](auto ic) __attribute__((always_inline)) {
    constexpr int I = decltype(ic)::value;
    if constexpr (I < 4) { if constexpr (RDA) s.af[AP ^ 1][I] = ldA(ab, I * 16 * ROWB); }
    else if constexpr (RDW) {
      if constexpr (((I - 4) & 1) == 0) s.wlo[PAR ^ 1][(I - 4) >> 1] = ldT(wb, 32 * ((I - 4) >> 1));
      else s.whi[PAR ^ 1][(I - 4) >> 1] = ldT(wb, 32 * ((I - 4) >> 1) + 4608);
    }
  };
  auto gap = [&](auto gc) __attribute__((always_inline)) {
    constexpr int G = decltype(gc)::value;
    mm(IC<2 * G>{});
    if constexpr (G == 0 && RDW) asm volatile("s_waitcnt vmcnt(%0)" :: "i"(VM) : "memory");
    if constexpr (G <= 10) rd(IC<2 * G>{});
    if constexpr (DMA) {
      if constexpr (G == 12) { LGKM0(); setm0(sl + 4096); }
      if constexpr (G >= 13 && G <= 16) dma<(2 * (G - 13) - 4) * 1024>(voff[2 * (G - 13)], gp);
      if constexpr (G == 17) dma<0>(voff[8], gp);
    }
    GAP();
    mm(IC<2 * G + 1>{});
    if constexpr (G <= 10) rd(IC<2 * G + 1>{});
    if constexpr (DMA) {
      if constexpr (G >= 13 && G <= 16) dma<(2 * (G - 13) + 1 - 4) * 1024>(voff[2 * (G - 13) + 1], gp);
      if constexpr (G == 16) setm0(sl + 8192);
    }
    if constexpr (G == 17) pf();
    GAP();
  };
  UNROLL(18, gap);
}
DI void read_w(GS& s, int par) {
#pragma unroll
  for (int t = 0; t < 9; ++t) { s.wlo[par][t] = ldT(s.wbase + par * SLOT, 32 * t); s.whi[par][t] = ldT(s.wbase + par * SLOT, 32 * t + 4608); }
}
DI void read_a(GS& s, int set, unsigned ab) {
#pragma unroll
  for (int m = 0; m < 4; ++m) s.af[set][m] = ldA(ab, m * 16 * ROWB);
}
struct Epi { __amdgpu_buffer_rsrc_t rs; unsigned base, dstride, ownlds; int own, w, lane; };
template <int U> DI void epi_unit(f4 (&acc)[9][4], const Epi& e) {
  const int G = e.lane >> 4, c = e.lane & 15, lr = c & 7, dh = c >> 3;
  constexpr int ta = U < 16 ? 2 * (U >> 2) : 8, tb = U < 16 ? ta + 1 : 8, ma = U < 16 ? (U & 3) : 2 * (U - 16), mb = U < 16 ? ma : ma + 1;
  asm volatile("" : "+a"(acc[ta][ma]), "+a"(acc[tb][mb]));
  const f4 a = acc[ta][ma], b = acc[tb][mb];
  auto r0 = __builtin_amdgcn_permlane16_swap(pk(a[0], a[1]), pk(b[0], b[1]), false, false);
  auto r1 = __builtin_amdgcn_permlane16_swap(pk(a[2], a[3]), pk(b[2], b[3]), false, false);
  const u4 v = {r0[0], r1[0], r0[1], r1[1]};
  const int d = U < 16 ? 2 * ma + dh : 2 * (ma + (G & 1)) + dh;
  const unsigned off = U < 16 ? e.w * 2304 + (U >> 2) * 512 + lr * 64 + (2 * (G & 1) + (G >> 1)) * 16 : e.w * 2304 + 2048 + lr * 32 + (G >> 1) * 16;
  __builtin_amdgcn_raw_buffer_store_b128(v, e.rs, d == e.own ? 0x7ffffff0u : e.base + d * e.dstride + off, 0, 16);
  if ((U < 16 ? ma : ma >> 1) == (U < 16 ? e.own >> 1 : e.own >> 2)) { if (d == e.own) *(LAS u4*)(size_t)(e.ownlds + off) = v; }
}
template <bool NEXT, int VM> DI void final2(GS& s, f4 (&acc)[9][4], const unsigned (&nvoff)[9], const char* nx1, unsigned ab, const Epi& e) {
  const unsigned sl = s.ring + SLOT, wb = s.wbase + SLOT;
  asm volatile("s_waitcnt vmcnt(%0)" :: "i"(VM) : "memory");
#pragma unroll
  for (int m = 0; m < 4; ++m) s.af[1][m] = ldA(ab, m * 16 * ROWB);
  auto rdw = [&](auto ic) __attribute__((always_inline)) { constexpr int t = decltype(ic)::value; s.wlo[1][t] = ldT(wb, 32 * t); s.whi[1][t] = ldT(wb, 32 * t + 4608); };
  rdw(IC<0>{}); rdw(IC<1>{});
  GAP();
  auto grp = [&](auto gc) __attribute__((always_inline)) {
    constexpr int G = decltype(gc)::value, t = G >> 1, st = G & 1;
    auto mm = [&](auto mc) __attribute__((always_inline)) { constexpr int m = decltype(mc)::value; mfma16<true, false>(acc[t][m], cat(s.wlo[st][t], s.whi[st][t]), s.af[st][m]); };
    mm(IC<0>{});
    if constexpr (st == 0 && t + 2 < 9) rdw(IC<t + 2>{});
    if constexpr (NEXT && G == 15) { LGKM0(); setm0(sl + 4096); }
    GAP();
    constexpr int U = G >= 5 ? G - 5 : -1;
    mm(IC<1>{});
    if constexpr (U >= 0 && U < 16 && (U >> 2) * 4 + 5 <= G) epi_unit<(U >= 0 && U < 16) ? U : 0>(acc, e);
    GAP();
    mm(IC<2>{});
    if constexpr (NEXT && G == 16) { dma<-4096>(nvoff[0], nx1); dma<-3072>(nvoff[1], nx1); dma<-2048>(nvoff[2], nx1); dma<-1024>(nvoff[3], nx1); }
    if constexpr (NEXT && G == 17) { dma<0>(nvoff[4], nx1); dma<1024>(nvoff[5], nx1); dma<2048>(nvoff[6], nx1); dma<3072>(nvoff[7], nx1); setm0(sl + 8192); }
    GAP();
    mm(IC<3>{});
    GAP();
  };
  UNROLL(18, grp);
  if constexpr (NEXT) dma<0>(nvoff[8], nx1);
  asm volatile("s_nop 7" ::: "memory");
  epi_unit<13>(acc, e); epi_unit<14>(acc, e); epi_unit<15>(acc, e); epi_unit<16>(acc, e); epi_unit<17>(acc, e);
}
template <int NS, int NEXT, int NPF, typename PF> DI void gemm_rows(GS& s, f4 (&acc)[9][4], const unsigned (&voff)[9], const char* gW, unsigned ab_e, unsigned ab_o,
                                              const unsigned (&nvoff)[9], const char* nx0, const char* nx1, const Epi& e, PF&& pf) {
  constexpr size_t KS = (size_t)32 * ROWB;
  auto no = [&]() __attribute__((always_inline)) {};
  step<0, 0, true, true, true, true, 9, 0>(s, acc, voff, gW + 3 * KS, ab_o, no);
  step<1, 1, false, true, true, true, 9, 0>(s, acc, voff, gW + 4 * KS, ab_e + 128, pf);
  step<0, 0, false, true, true, true, 9 + NPF, 0>(s, acc, voff, gW + 5 * KS, ab_o + 128, no);
  step<1, 1, false, true, true, true, 9 + NPF, 0>(s, acc, voff, gW + 6 * KS, ab_e + 256, no);
  for (int k = 4; k < NS - 4; k += 2) {
    step<0, 0, false, true, true, true, 9, 0>(s, acc, voff, gW + (size_t)(k + 3) * KS, ab_o + 128 * (k >> 1), no);
    step<1, 1, false, true, true, true, 9, 0>(s, acc, voff, gW + (size_t)(k + 4) * KS, ab_e + 128 * ((k + 2) >> 1), no);
  }
  step<0, 0, false, true, true, true, 9, 0>(s, acc, voff, gW + (size_t)(NS - 1) * KS, ab_o + 128 * ((NS - 4) >> 1), no);
  step<1, 1, false, true, true, NEXT != 0, 9, 0>(s, acc, nvoff, nx0, ab_e + 128 * ((NS - 2) >> 1), no);
  final2<NEXT != 0, NEXT != 0 ? 9 : 0>(s, acc, nvoff, nx1, ab_o + 128 * ((NS - 2) >> 1), e);
}

constexpr unsigned SPIN_CAP = 1u << 17;
DI void publish(u64* flag, u64 tag) {
  asm volatile("s_waitcnt vmcnt(0)" ::: "memory");
  BAR();
  if (threadIdx.x == 0) __hip_atomic_store((gu64*)flag, tag, __ATOMIC_RELAXED, __HIP_MEMORY_SCOPE_AGENT);
}
DI void await8(u64* flags8, u64 tag, unsigned* tmo, int w, int lane) {
  if (w == 0) {
    for (unsigned spins = 0;; ++spins) {
      const u64 v = __hip_atomic_load((gu64*)(flags8 + (lane & 7) * FLAG_STRIDE), __ATOMIC_RELAXED, __HIP_MEMORY_SCOPE_AGENT);
      if (__all(v == tag)) break;
      if (spins > SPIN_CAP) { if (lane == 0) __hip_atomic_store((gu32*)tmo, 1u, __ATOMIC_RELAXED, __HIP_MEMORY_SCOPE_AGENT); break; }
      __builtin_amdgcn_s_sleep(1);
    }
  }
  BAR();
}
template <int NP, int NT> struct ModGather {
  static constexpr int N = (NP + NT - 1) / NT;
  u4 gr[N][2];
  __amdgpu_buffer_rsrc_t rM;
  DI void load(int tid) {
#pragma unroll
    for (int i = 0; i < N; ++i) {
      const int idx = tid + NT * i < NP ? tid + NT * i : (tid & 63), off = (idx >> 5) * 1024 + (idx & 31) * 16;
      gr[i][0] = __builtin_amdgcn_raw_buffer_load_b128(rM, off, 0, 16); gr[i][1] = __builtin_amdgcn_raw_buffer_load_b128(rM, off + 512, 0, 16);
    }
  }
  DI void issue(char* ws, int tid) { rM = __builtin_amdgcn_make_buffer_rsrc(ws + WS_MODP, 0, 54 * 1024, 0x00020000); load(tid); }
  DI void finish(u64 tag, unsigned ldsb, unsigned* tmo, int tid) {
    const unsigned thi = (unsigned)(tag >> 32), tlo = (unsigned)tag;
    for (unsigned spins = 0;; ++spins) {
      bool ok = true;
#pragma unroll
      for (int i = 0; i < N; ++i) ok &= gr[i][0][2] == tlo && gr[i][0][3] == thi && gr[i][1][2] == tlo && gr[i][1][3] == thi;
      if (__all(ok)) break;
      if (spins > SPIN_CAP / 8) { if ((tid & 63) == 0) __hip_atomic_store((gu32*)tmo, 2u, __ATOMIC_RELAXED, __HIP_MEMORY_SCOPE_AGENT); break; }
      __builtin_amdgcn_s_sleep(4);
      asm volatile("" ::: "memory");
      load(tid);
    }
#pragma unroll
    for (int i = 0; i < N; ++i)
      if (tid + NT * i < NP) {
        const int idx = tid + NT * i;
        const f2 m = (f2){__uint_as_float(gr[i][0][0]), __uint_as_float(gr[i][0][1])} + (f2){__uint_as_float(gr[i][1][0]), __uint_as_float(gr[i][1][1])};
        if (idx < D) {
          f2 v = rb2(m);
          if (idx >= D / 2) v = rb2(v + 1.f);
          *(LAS f2*)(size_t)(ldsb + LDS_X1 + 1024 + idx * 8) = v;
        } else *(LAS unsigned*)(size_t)(ldsb + LDS_MOD + idx * 4) = pk(m);
      }
  }
};
DI u4 gated_residual(const u4 (&v)[8], const u4& res, const u4& g) {
  u4 o;
#pragma unroll
  for (int e = 0; e < 4; ++e) {
    f2 s = bf2(v[0][e]);
#pragma unroll
    for (int j = 1; j < 8; ++j) s += bf2(v[j][e]);
    const unsigned t = pk(bf2(pk(s)) * bf2(g[e]));
    o[e] = pk(bf2(res[e]) + bf2(t));
  }
  return o;
}

constexpr float QSCALE = 0.08838834764831845f * 1.4426950408889634f;
constexpr float THR = 40.0f;
struct QS {
  b4 wlo[2][8], whi[2][8];
  b8 hf[2][4];
  unsigned wb[4];
  unsigned hb[4];
  unsigned ring;
};
template <int PAR, bool ZERO, bool RD, bool DMA, int VM, typename PF>
DI void qstep(QS& s, f16v (&acc)[4][2], const unsigned (&voff)[8], const char* gp, unsigned hoff, PF&& pf) {
  const unsigned sl = s.ring + (PAR ^ 1) * QSLOT;
  auto rd = [&](auto ic) __attribute__((always_inline)) {
    constexpr int I = decltype(ic)::value;
    if constexpr (RD) {
      if constexpr (I < 4) {
        s.hf[PAR ^ 1][I] = ldA(s.hb[2 * (PAR ^ 1) + (I >> 1)] + hoff, (I & 1) * 32 * ROWB);
      } else {
        constexpr int J = I - 4, f = J >> 1, t = J & 1, ks = f >> 2, dt = f & 3;
        const b4 v = ldT(s.wb[dt], (PAR ^ 1) * QSLOT + 4096 * ks + 1024 * t);
        if constexpr (t == 0) s.wlo[PAR ^ 1][f] = v; else s.whi[PAR ^ 1][f] = v;
      }
    }
  };
  auto gap = [&](auto gc) __attribute__((always_inline)) {
    constexpr int G = decltype(gc)::value, ks = G >> 3, dt = (G >> 1) & 3, qb = G & 1, f = ks * 4 + dt;
    mfma32<false, ZERO && ks == 0>(acc[dt][qb], cat(s.wlo[PAR][f], s.whi[PAR][f]), s.hf[PAR][ks * 2 + qb]);
    if constexpr (G == 0 && RD) asm volatile("s_waitcnt vmcnt(%0)" :: "i"(VM) : "memory");
    if constexpr (G <= 6) { rd(IC<3 * G>{}); rd(IC<3 * G + 1>{}); if constexpr (3 * G + 2 < 20) rd(IC<3 * G + 2>{}); }
    if constexpr (DMA) {
      if constexpr (G == 9) { LGKM0(); setm0(sl + 4096); }
      if constexpr (G >= 10 && G <= 13) { dma<(2 * (G - 10) - 4) * 1024>(voff[2 * (G - 10)], gp); dma<(2 * (G - 10) + 1 - 4) * 1024>(voff[2 * (G - 10) + 1], gp); }
    }
    if constexpr (G == 15) pf();
    GAP();
  };
  UNROLL(16, gap);
}

struct AT {
  f16v S[2][2][2];
  f16v negm[2];
  f16v O[4][2];
  b8 q[2][8];
  b8 P[2][4];
  float m[2], l[2], rs0[2], rs1[2], fct[2], pm0[2], pm1[2];
  float one;
  unsigned kb_[2], vb_[2];
  unsigned voff[8];
  unsigned m0k, m0v;
  const char *gk, *gv;
  bool pend;
};
template <int MODE> DI void qk(f16v& s, const b8& k, const b8& q, const f16v& negm) {
  if constexpr (MODE == 0) asm volatile("v_mfma_f32_32x32x16_bf16 %0, %1, %2, 0" : "=&v"(s) : "v"(k), "a"(q));
  else if constexpr (MODE == 1) asm volatile("v_mfma_f32_32x32x16_bf16 %0, %1, %2, %3" : "=&v"(s) : "v"(k), "a"(q), "v"(negm));
  else asm volatile("v_mfma_f32_32x32x16_bf16 %0, %1, %2, %0" : "+v"(s) : "v"(k), "a"(q));
}
template <int XP, int KB, int E> DI void sm_exp(AT& a) { constexpr int qb = E >> 4, r = E & 15; a.S[XP][KB][qb][r] = __builtin_amdgcn_exp2f(a.S[XP][KB][qb][r]); }
template <int XP, int KB, int E> DI void sm_add(AT& a) {
  constexpr int qb = E >> 4, r = E & 15;
  if constexpr (r & 1) a.rs1[qb] = __builtin_fmaf(a.S[XP][KB][qb][r], a.one, a.rs1[qb]); else a.rs0[qb] += a.S[XP][KB][qb][r];
}
template <int XP, int C> DI void sm_pack(AT& a) {
  constexpr int kb = C >> 4, qb = (C >> 3) & 1, s = (C >> 2) & 1, p = C & 3;
  u4 w = __builtin_bit_cast(u4, a.P[qb][kb * 2 + s]);
  w[p] = pk(a.S[XP][kb][qb][8 * s + 2 * p], a.S[XP][kb][qb][8 * s + 2 * p + 1]);
  a.P[qb][kb * 2 + s] = __builtin_bit_cast(b8, w);
  if constexpr (p == 3) asm volatile("" : "+v"(a.P[qb][kb * 2 + s]));
}
template <int X, bool FIRST, bool DK, bool DV, typename PF> DI void phaseA(AT& a, int t, PF&& pf) {
  constexpr int XP = X ^ 1;
  const unsigned kslot = X * 16384;
  b8 kf[16];
  auto kread = [&](auto ic) __attribute__((always_inline)) { constexpr int I = decltype(ic)::value, ks = I >> 1, kb = I & 1; kf[I] = ldA(a.kb_[ks & 1], kslot + 8192 * kb + 512 * (ks >> 1)); };
  kread(IC<0>{}); kread(IC<1>{}); kread(IC<2>{}); kread(IC<3>{});
  const char* gk = a.gk + (size_t)(t + 1) * 16384; const char* gv = a.gv + (size_t)t * 16384;
  auto gap = [&](auto gc) __attribute__((always_inline)) {
    constexpr int G = decltype(gc)::value, ks = G >> 2, kb = (G >> 1) & 1, qb = G & 1, I = G >> 1;
    qk<(ks == 0) ? (FIRST ? 0 : 1) : 2>(a.S[X][kb][qb], kf[I], a.q[qb][ks], a.negm[qb]);
    if constexpr ((G & 1) == 0 && I + 4 < 16) kread(IC<I + 4>{});
    if constexpr (!FIRST && G == 8) asm volatile("" :: "v"(a.negm[0]), "v"(a.negm[1]));
    if constexpr (DK) { if constexpr (G == 0) setm0(a.m0k + (X ^ 1) * 16384); if constexpr ((G & 1) && G < 16) dma<((G >> 1) - 4) * 1024>(a.voff[G >> 1], gk); }
    if constexpr (DV) { if constexpr (G == 16) setm0(a.m0v + X * 16384); if constexpr ((G & 1) && G > 16) dma<(((G - 16) >> 1) - 4) * 1024>(a.voff[(G - 16) >> 1], gv); }
    if constexpr (!FIRST) {
      if constexpr (G < 16) { sm_exp<XP, 1, 2 * G>(a); sm_exp<XP, 1, 2 * G + 1>(a); sm_pack<XP, G>(a); }
      else { sm_pack<XP, G>(a); sm_add<XP, 1, 2 * (G - 16)>(a); sm_add<XP, 1, 2 * (G - 16) + 1>(a); }
    }
    GAP();
  };
  UNROLL(32, gap);
  pf();
  if constexpr (!FIRST) {
#pragma unroll
    for (int qb = 0; qb < 2; ++qb) { a.l[qb] += a.rs0[qb] + a.rs1[qb]; asm volatile("" : "+v"(a.l[qb])); }
  }
}
DI float swap32_max(float x) { auto r = __builtin_amdgcn_permlane32_swap(__float_as_uint(x), __float_as_uint(x), false, false); return fmaxx(__uint_as_float(r[0]), __uint_as_float(r[1])); }
template <int X, bool NOPV, bool ZERO> DI void phaseB(AT& a) {
  const unsigned vslot = (X ^ 1) * 16384;
  b4 vlo[16], vhi[16];
  auto vread = [&](auto ic) __attribute__((always_inline)) {
    constexpr int I = decltype(ic)::value, k4 = I >> 2, db = I & 3;
    vlo[I] = ldT(a.vb_[0], vslot + 2048 * (2 * k4) + 512 * db);
    vhi[I] = ldT(a.vb_[1], vslot + 2048 * (2 * k4 + 1) + 512 * db);
  };
  if constexpr (!NOPV) { vread(IC<0>{}); vread(IC<1>{}); vread(IC<2>{}); }
  float rmax[2];
  auto pv = [&](auto gc) __attribute__((always_inline)) {
    constexpr int G = decltype(gc)::value, I = G >> 1, k4 = I >> 2, db = I & 3, qb = G & 1;
    if constexpr (!NOPV) {
      mfma32<true, ZERO && k4 == 0>(a.O[db][qb], cat(vlo[I], vhi[I]), a.P[qb][k4]);
      if constexpr ((G & 1) == 0 && I + 3 < 16) vread(IC<I + 3>{});
    }
  };
  auto gap1 = [&](auto gc) __attribute__((always_inline)) {
    constexpr int G = decltype(gc)::value;
    pv(gc);
    if constexpr (G >= 4 && G < 12) {
      constexpr int i = G - 4, q = i >> 2, r = 4 * (i & 3);
      if constexpr ((i & 3) == 0) { a.pm0[q] = fmax3(a.S[X][0][q][0], a.S[X][0][q][1], a.S[X][1][q][0]); a.pm1[q] = fmax3(a.S[X][0][q][2], a.S[X][0][q][3], a.S[X][1][q][1]);
                                    a.pm0[q] = fmax3(a.pm0[q], a.S[X][1][q][2], a.S[X][1][q][3]); }
      else { a.pm0[q] = fmax3(a.pm0[q], a.S[X][0][q][r], a.S[X][0][q][r + 1]); a.pm1[q] = fmax3(a.pm1[q], a.S[X][0][q][r + 2], a.S[X][0][q][r + 3]);
             a.pm0[q] = fmax3(a.pm0[q], a.S[X][1][q][r], a.S[X][1][q][r + 1]); a.pm1[q] = fmax3(a.pm1[q], a.S[X][1][q][r + 2], a.S[X][1][q][r + 3]); }
    }
    if constexpr (G == 12 || G == 13) { constexpr int q = G - 12; rmax[q] = swap32_max(fmaxx(a.pm0[q], a.pm1[q])); }
    GAP();
  };
  UNROLL(14, gap1);
  if constexpr (NOPV) {
#pragma unroll
    for (int q = 0; q < 2; ++q) {
      a.m[q] = rmax[q]; a.l[q] = 0.f; a.fct[q] = 1.f;
#pragma unroll
      for (int kb = 0; kb < 2; ++kb)
#pragma unroll
        for (int e = 0; e < 16; ++e) a.S[X][kb][q][e] -= rmax[q];
#pragma unroll
      for (int e = 0; e < 16; ++e) a.negm[q][e] = -rmax[q];
      asm volatile("" : "+v"(a.negm[q]));
    }
  } else {
    const bool grew = (rmax[0] > THR) | (rmax[1] > THR);
    if (__builtin_expect(__any((int)grew), 0)) {
#pragma unroll
      for (int q = 0; q < 2; ++q) {
        const float dl = fmaxx(rmax[q], 0.f); a.m[q] += dl; a.fct[q] = __builtin_amdgcn_exp2f(-dl);
#pragma unroll
        for (int kb = 0; kb < 2; ++kb)
#pragma unroll
          for (int e = 0; e < 16; ++e) a.S[X][kb][q][e] -= dl;
#pragma unroll
        for (int e = 0; e < 16; ++e) a.negm[q][e] = -a.m[q];
        asm volatile("" : "+v"(a.negm[q]));
      }
      a.pend = true;
    }
  }
  a.rs0[0] = a.rs0[1] = a.rs1[0] = a.rs1[1] = 0.f;
  GAP();
  auto gap2 = [&](auto gc) __attribute__((always_inline)) {
    constexpr int G = decltype(gc)::value + 14;
    pv(IC<G>{});
    if constexpr (G >= 15) {
      constexpr int e0 = 2 * (G - 15);
      if constexpr (e0 < 32) { sm_exp<X, 0, e0>(a); sm_exp<X, 0, e0 + 1>(a); }
      if constexpr (G >= 16) { sm_add<X, 0, e0 - 2>(a); sm_add<X, 0, e0 - 1>(a); }
    }
    GAP();
  };
  UNROLL(18, gap2);
  asm volatile("" : "+v"(a.S[X][0][0]), "+v"(a.S[X][0][1]), "+v"(a.rs0[0]), "+v"(a.rs0[1]), "+v"(a.rs1[0]), "+v"(a.rs1[1]));
  if constexpr (!NOPV) {
    if (__builtin_expect(a.pend, 0)) {
      asm volatile("s_nop 7\n\ts_nop 7" ::: "memory");
#pragma unroll
      for (int db = 0; db < 4; ++db)
#pragma unroll
        for (int q = 0; q < 2; ++q) {
          asm volatile("" : "+a"(a.O[db][q]));
          f16v o = a.O[db][q];
#pragma unroll
          for (int e = 0; e < 16; ++e) o[e] *= a.fct[q];
          a.O[db][q] = o;
          asm volatile("" : "+a"(a.O[db][q]));
        }
      a.l[0] *= a.fct[0]; a.l[1] *= a.fct[1]; a.pend = false;
      asm volatile("s_nop 1" ::: "memory");
    }
  }
}

template <int X, bool FIRST, bool DK, bool DV, typename PF> DI void fastA(AT& a, int t, PF&& pf) {
  constexpr int XP = X ^ 1;
  const unsigned kslot = X * 16384;
  b8 kf[16];
  auto kread = [&](auto ic) __attribute__((always_inline)) { constexpr int I = decltype(ic)::value, ks = I >> 1, kb = I & 1; kf[I] = ldA(a.kb_[ks & 1], kslot + 8192 * kb + 512 * (ks >> 1)); };
  kread(IC<0>{}); kread(IC<1>{}); kread(IC<2>{}); kread(IC<3>{});
  const char* gk = a.gk + (size_t)(t + 1) * 16384; const char* gv = a.gv + (size_t)t * 16384;
  auto gap = [&](auto gc) __attribute__((always_inline)) {
    constexpr int G = decltype(gc)::value, ks = G >> 2, kb = (G >> 1) & 1, qb = G & 1, I = G >> 1;
    qk<(ks == 0) ? (FIRST ? 0 : 1) : 2>(a.S[X][kb][qb], kf[I], a.q[qb][ks], a.negm[qb]);
    if constexpr ((G & 1) == 0 && I + 4 < 16) kread(IC<I + 4>{});
    if constexpr (!FIRST && G == 8) asm volatile("" :: "v"(a.negm[0]), "v"(a.negm[1]));
    if constexpr (DK) { if constexpr (G == 0) setm0(a.m0k + (X ^ 1) * 16384); if constexpr ((G & 1) && G < 16) dma<((G >> 1) - 4) * 1024>(a.voff[G >> 1], gk); }
    if constexpr (DV) { if constexpr (G == 16) setm0(a.m0v + X * 16384); if constexpr ((G & 1) && G > 16) dma<(((G - 16) >> 1) - 4) * 1024>(a.voff[(G - 16) >> 1], gv); }
    if constexpr (!FIRST) {
      sm_exp<XP, 1, G>(a);
      if constexpr (G >= 1) sm_add<XP, 1, G - 1>(a);
      if constexpr (G >= 3 && (G & 1)) sm_pack<XP, 16 + (G - 3) / 2>(a);
    }
    GAP();
  };
  UNROLL(32, gap);
  pf();
  if constexpr (!FIRST) {
    sm_add<XP, 1, 31>(a); sm_pack<XP, 31>(a);
#pragma unroll
    for (int qb = 0; qb < 2; ++qb) { a.l[qb] += a.rs0[qb] + a.rs1[qb]; asm volatile("" : "+v"(a.l[qb])); }
  }
}
constexpr int fb_e(int G) { return G < 2 ? 0 : G - 2 + (G > 5) + (G > 9); }
constexpr int dr_e(int n) { return n < 8 ? n : n < 16 ? n + 8 : n < 24 ? n - 8 : n; }
template <int X, int MODE> DI void fastB(AT& a) {
  constexpr bool ZERO = MODE == 1, DRAIN = MODE == 3;
  if constexpr (MODE == 2) {
#pragma unroll
    for (int q = 0; q < 2; ++q) {
      float p0 = fmax3(a.S[X][0][q][0], a.S[X][1][q][0], a.S[X][0][q][2]), p1 = fmax3(a.S[X][0][q][1], a.S[X][1][q][1], a.S[X][0][q][3]);
      p0 = fmaxx(p0, a.S[X][1][q][2]); p1 = fmaxx(p1, a.S[X][1][q][3]);
#pragma unroll
      for (int r = 4; r < 16; r += 2) { p0 = fmax3(p0, a.S[X][0][q][r], a.S[X][1][q][r]); p1 = fmax3(p1, a.S[X][0][q][r + 1], a.S[X][1][q][r + 1]); }
      const float mx = swap32_max(fmaxx(p0, p1));
      f2 m2 = {mx, mx}; asm volatile("" : "+v"(m2));
      a.l[q] = 0.f;
#pragma unroll
      for (int kb = 0; kb < 2; ++kb)
#pragma unroll
        for (int e = 0; e < 16; e += 2) { const f2 v = (f2){a.S[X][kb][q][e], a.S[X][kb][q][e + 1]} - m2; a.S[X][kb][q][e] = v[0]; a.S[X][kb][q][e + 1] = v[1]; }
#pragma unroll
      for (int e = 0; e < 16; ++e) a.negm[q][e] = -mx;
      asm volatile("" : "+v"(a.negm[q]));
    }
    a.rs0[0] = a.rs0[1] = a.rs1[0] = a.rs1[1] = 0.f;
    { auto f = [&](auto ic) __attribute__((always_inline)) { sm_exp<X, 0, decltype(ic)::value>(a); }; UNROLL(32, f); }
    { auto f = [&](auto ic) __attribute__((always_inline)) { sm_add<X, 0, decltype(ic)::value>(a); }; UNROLL(32, f); }
    { auto f = [&](auto ic) __attribute__((always_inline)) { sm_pack<X, decltype(ic)::value>(a); }; UNROLL(16, f); }
  } else {
    const unsigned vslot = (DRAIN ? X : X ^ 1) * 16384;
    b4 vlo[16], vhi[16];
    auto vread = [&](auto ic) __attribute__((always_inline)) {
      constexpr int I = decltype(ic)::value, k4 = I >> 2, db = I & 3;
      vlo[I] = ldT(a.vb_[0], vslot + 2048 * (2 * k4) + 512 * db);
      vhi[I] = ldT(a.vb_[1], vslot + 2048 * (2 * k4 + 1) + 512 * db);
    };
    vread(IC<0>{}); vread(IC<1>{}); vread(IC<2>{});
    if constexpr (!DRAIN) a.rs0[0] = a.rs0[1] = a.rs1[0] = a.rs1[1] = 0.f;
    auto gap = [&](auto gc) __attribute__((always_inline)) {
      constexpr int G = decltype(gc)::value, I = G >> 1, k4 = I >> 2, db = I & 3, qb = G & 1;
      mfma32<true, ZERO && k4 == 0>(a.O[db][qb], cat(vlo[I], vhi[I]), a.P[qb][k4]);
      if constexpr ((G & 1) == 0 && I + 3 < 16) vread(IC<I + 3>{});
      if constexpr (DRAIN) {
        if constexpr (G < 16) { sm_exp<X, 1, dr_e(2 * G)>(a); sm_exp<X, 1, dr_e(2 * G) + 1>(a); }
        if constexpr (G >= 2 && G < 18) sm_pack<X, 16 + dr_e(2 * (G - 2)) / 2>(a);
        if constexpr (G >= 16) { sm_add<X, 1, dr_e(2 * (G - 16))>(a); sm_add<X, 1, dr_e(2 * (G - 16)) + 1>(a); }
      } else {
        if constexpr (fb_e(G) < fb_e(G + 1)) sm_exp<X, 0, fb_e(G)>(a);
        if constexpr (fb_e(G) + 1 < fb_e(G + 1)) sm_exp<X, 0, fb_e(G) + 1>(a);
        if constexpr (G >= 3 && fb_e(G - 1) < fb_e(G)) sm_add<X, 0, G >= 3 ? fb_e(G - 1) : 0>(a);
        if constexpr (G >= 3 && fb_e(G - 1) + 1 < fb_e(G)) sm_add<X, 0, G >= 3 ? fb_e(G - 1) + 1 : 0>(a);
        if constexpr (G >= 16 && G <= 29) sm_pack<X, G >= 16 ? G - 16 : 0>(a);
        if constexpr (G == 31) sm_pack<X, 14>(a);
      }
      GAP();
    };
    UNROLL(32, gap);
    if constexpr (!DRAIN) {
      sm_add<X, 0, 31>(a);
      static_assert(fb_e(32) == 32 && fb_e(28) >= 28 && fb_e(30) >= 30, "schedule");
      sm_pack<X, 15>(a);
      asm volatile("" : "+v"(a.S[X][0][0]), "+v"(a.S[X][0][1]), "+v"(a.rs0[0]), "+v"(a.rs0[1]), "+v"(a.rs1[0]), "+v"(a.rs1[1]));
    } else {
#pragma unroll
      for (int qb = 0; qb < 2; ++qb) a.l[qb] += a.rs0[qb] + a.rs1[qb];
    }
  }
}

struct Params { const u16 *x, *c, *wmod, *wq, *kc, *vc, *wo, *wg, *wu, *wd; u16* out; char* ws; float one; unsigned gap; };

__global__ __launch_bounds__(256, 1) void k_main(Params p) {
  extern __shared__ __attribute__((aligned(16))) char lds[];
  const int tid = threadIdx.x, w = __builtin_amdgcn_readfirstlane(tid >> 6), lane = tid & 63;
  const int j = blockIdx.x & 7, c = blockIdx.x >> 3;
  const unsigned ldsb = (unsigned)(size_t)lds;
  const int hh = lane >> 5, r32 = lane & 31;
  u64* const flags = (u64*)(p.ws + WS_FLAG);
  unsigned* const tmo = (unsigned*)(p.ws + WS_TMO);
  STAMP(0);
  PST(0);
  QS qs;
  unsigned voffQ[8];
#pragma unroll
  for (int i = 0; i < 8; ++i) voffQ[i] = (4 * i + (lane >> 4)) * 8192 + 16 * ((lane & 15) ^ ((lane >> 4) << 2)) - (i - 4) * 1024 + 4096;
  const char* gq = (const char*)p.wq + (size_t)(4 * j + w) * 256 - 4096;
  qs.ring = __builtin_amdgcn_readfirstlane(ldsb + ABYTES + w * 2 * QSLOT);
  constexpr size_t KS_Q = (size_t)32 * 8192;
  auto issueq = [&](int slot, const char* gp) __attribute__((always_inline)) {
    setm0(qs.ring + slot * QSLOT + 4096); asm volatile("s_nop 0");
    dma<-4096>(voffQ[0], gp); dma<-3072>(voffQ[1], gp); dma<-2048>(voffQ[2], gp); dma<-1024>(voffQ[3], gp);
    dma<0>(voffQ[4], gp); dma<1024>(voffQ[5], gp); dma<2048>(voffQ[6], gp); dma<3072>(voffQ[7], gp);
  };
  const u64 e0 = __hip_atomic_load((gu64*)(p.ws + WS_EPOCH), __ATOMIC_RELAXED, __HIP_MEMORY_SCOPE_AGENT);
  AT a;
  u64 tag;
  {
    f16v Qt[4][2];
    QS& s = qs;
    {
      const int rr = lane >> 3, sub = lane & 7;
      if (c >= 16) {
        const u64 t0_ = __builtin_amdgcn_s_memrealtime();
        const int mb = blockIdx.x - 128, mg = mb % 54, ms = (mb / 54) & 1;
        u4 wm[9]; u16 cval[9];
#pragma unroll
        for (int i = 0; i < 9; ++i) {
          const int r = 288 * ms + (tid >> 3) + 32 * i;
          wm[i] = *(const u4*)(p.wmod + (size_t)r * (6 * D) + mg * 64 + (tid & 7) * 8); cval[i] = p.c[r];
        }
        GAP();
        const u64 tag = ((u64)(unsigned)__builtin_amdgcn_readfirstlane((unsigned)(e0 >> 32)) << 32 | (u64)(unsigned)__builtin_amdgcn_readfirstlane((unsigned)e0)) + 1;
        if (mb < 108) {
          float acc[8] = {};
#pragma unroll
          for (int i = 0; i < 9; ++i) {
            const float xc = bf2f(cval[i]);
            const float sc = rbf(xc * __builtin_amdgcn_rcpf(1.f + __expf(-xc)));
#pragma unroll
            for (int e = 0; e < 4; ++e) { acc[2 * e] += sc * bflo(wm[i][e]); acc[2 * e + 1] += sc * bfhi(wm[i][e]); }
          }
          LAS float* red = (LAS float*)(size_t)(ldsb + LDS_X1);
#pragma unroll
          for (int e = 0; e < 8; ++e) { float t = acc[e]; t += dpp<0x128>(t); t = swap32_add(swap16_add(t)); if (lane < 8) red[w * 64 + lane * 8 + e] = t; }
          asm volatile("s_waitcnt lgkmcnt(0)" ::: "memory");
          BAR();
          if (tid < 32) {
            f2 v = {0.f, 0.f};
#pragma unroll
            for (int k = 0; k < 4; ++k) v += *(LAS f2*)(red + k * 64 + 2 * tid);
            const __amdgpu_buffer_rsrc_t rP = __builtin_amdgcn_make_buffer_rsrc(p.ws + WS_MODP, 0, 54 * 1024, 0x00020000);
            __builtin_amdgcn_raw_buffer_store_b128((u4){__float_as_uint(v[0]), __float_as_uint(v[1]), (unsigned)tag, (unsigned)(tag >> 32)}, rP, mg * 1024 + ms * 512 + tid * 16, 0, 16);
          }
        }
        const unsigned dummy = __builtin_amdgcn_readfirstlane(ldsb + LDS_DUMMY);
        while (__builtin_amdgcn_s_memrealtime() - t0_ < PF_D2) __builtin_amdgcn_s_sleep(2);
        const int li = tid < 144 ? tid : tid - 112;
        touch((const char*)p.wq + (size_t)512 * j * 2, (18 * (c - 16) + (li >> 3)) * 8192 + (li & 7) * 128, dummy);
        touch((const char*)p.wq + (size_t)512 * j * 2, (18 * c + (li >> 3)) * 8192 + (li & 7) * 128, dummy);
        touch((const char*)(w < 2 ? p.kc : p.vc) + ((size_t)(2 * j + (w & 1)) * SK + (SK - WIN)) * DH * 2, (32 * (c - 16 * (lane >> 5)) + (lane & 31)) * 128, dummy);
        {
          while (__builtin_amdgcn_s_memrealtime() - t0_ < XPRE) __builtin_amdgcn_s_sleep(2);
#pragma unroll
          for (int h = 0; h < 2; ++h)
#pragma unroll
            for (int i = 0; i < 3; ++i) {
              const int L = tid + 256 * i < 576 ? tid + 256 * i : tid, d_ = L / 72, l_ = L - d_ * 72;
              touch(p.ws + WS_X + (size_t)(c - 16 * h) * 64 * SLICE, (d_ * 8 + j) * SLICE + l_ * 128, dummy);
            }
        }
        while (__builtin_amdgcn_s_memrealtime() - t0_ < STAG_D2) __builtin_amdgcn_s_sleep(2);
      }
      PST(10);
      PST(11);
      u4 xv[2][9];
      GAP();
#pragma unroll
      for (int ps = 0; ps < 2; ++ps)
#pragma unroll
        for (int i = 0; i < 9; ++i) xv[ps][i] = *(const u4*)(p.x + (size_t)(c * 64 + 16 * w + 8 * ps + rr) * D + (sub + 8 * i) * 8);
      GAP();
      tag = ((u64)(unsigned)__builtin_amdgcn_readfirstlane((unsigned)(e0 >> 32)) << 32 | (u64)(unsigned)__builtin_amdgcn_readfirstlane((unsigned)e0)) + 1;
      PST(1);
      STAMP(20);
      PST(2);
      f2 y[2][9][4];
      ModGather<1728, 256> mgat;
      {
#pragma unroll
        for (int ps = 0; ps < 2; ++ps) {
          f2 a1 = {0.f, 0.f}, a2 = {0.f, 0.f};
#pragma unroll
          for (int i = 0; i < 9; ++i)
#pragma unroll
            for (int e = 0; e < 4; ++e) { y[ps][i][e] = bf2(xv[ps][i][e]); a1 += y[ps][i][e]; a2 = __builtin_elementwise_fma(y[ps][i][e], y[ps][i][e], a2); }
          float s1 = a1[0] + a1[1], s2 = a2[0] + a2[1];
          s1 = sum8(s1); s2 = sum8(s2);
          const float mean = s1 * (1.f / D), var = __builtin_fmaxf(__builtin_fmaf(-mean, mean, s2 * (1.f / D)), 0.f);
          const float mr = rbf(mean), rs = __builtin_amdgcn_rcpf(rbf(__builtin_amdgcn_sqrtf(rbf(rbf(var) + 1e-5f))));
          f2 mr2 = {mr, mr}, rs2 = {rs, rs}; asm volatile("" : "+v"(mr2), "+v"(rs2));
          if (ps == 1) { GAP(); mgat.issue(p.ws, tid); GAP(); }
#pragma unroll
          for (int i = 0; i < 9; ++i) {
            f2 t[4];
#pragma unroll
            for (int e = 0; e < 4; ++e) t[e] = y[ps][i][e] - mr2;
#pragma unroll
            for (int e = 0; e < 4; ++e) t[e] = rb2(t[e]);
#pragma unroll
            for (int e = 0; e < 4; ++e) t[e] = t[e] * rs2;
#pragma unroll
            for (int e = 0; e < 4; ++e) y[ps][i][e] = rb2(t[e]);
            asm volatile("" : "+v"(y[ps][i][0]), "+v"(y[ps][i][1]), "+v"(y[ps][i][2]), "+v"(y[ps][i][3]));
          }
        }
      }
      STAMP(21);
      PST(4);
      mgat.finish(tag, ldsb, tmo, tid);
      STAMP(22);
      PST(5);
      issueq(0, gq); issueq(1, gq + KS_Q);
      asm volatile("s_waitcnt lgkmcnt(0)" ::: "memory");
      BAR();
      STAMP(23);
      PST(6);
      {
        const unsigned tb = ldsb + LDS_X1 + 1024 + sub * 32;
        f4 cst[2][4];
        auto ldc = [&](int i, f4 (&d)[4]) { d[0] = *(LAS f4*)(size_t)(tb + i * 256); d[1] = *(LAS f4*)(size_t)(tb + i * 256 + 16); d[2] = *(LAS f4*)(size_t)(tb + i * 256 + D * 4); d[3] = *(LAS f4*)(size_t)(tb + i * 256 + D * 4 + 16); };
        ldc(0, cst[0]);
        const unsigned ad0 = ldsb + (16 * w + rr) * ROWB + ((sub ^ ((rr >> 1) & 7)) * 16);
        const unsigned ad1 = ldsb + (16 * w + 8 + rr) * ROWB + ((sub ^ (((rr >> 1) + 4) & 7)) * 16);
#pragma unroll
        for (int i = 0; i < 9; ++i) {
          if (i + 1 < 9) ldc(i + 1, cst[(i + 1) & 1]);
          GAP();
          const f4 (&k)[4] = cst[i & 1];
          const f2 sh[4] = {{k[0][0], k[0][1]}, {k[0][2], k[0][3]}, {k[1][0], k[1][1]}, {k[1][2], k[1][3]}}, cc[4] = {{k[2][0], k[2][1]}, {k[2][2], k[2][3]}, {k[3][0], k[3][1]}, {k[3][2], k[3][3]}};
          f2 t[2][4]; u4 o[2];
#pragma unroll
          for (int ps = 0; ps < 2; ++ps)
#pragma unroll
            for (int e = 0; e < 4; ++e) t[ps][e] = y[ps][i][e] * cc[e];
#pragma unroll
          for (int ps = 0; ps < 2; ++ps)
#pragma unroll
            for (int e = 0; e < 4; ++e) t[ps][e] = rb2(t[ps][e]);
#pragma unroll
          for (int ps = 0; ps < 2; ++ps)
#pragma unroll
            for (int e = 0; e < 4; ++e) t[ps][e] = t[ps][e] + sh[e];
#pragma unroll
          for (int ps = 0; ps < 2; ++ps)
#pragma unroll
            for (int e = 0; e < 4; ++e) o[ps][e] = pk(t[ps][e]);
          *(LAS u4*)(size_t)(ad0 + i * 128) = o[0];
          *(LAS u4*)(size_t)(ad1 + i * 128) = o[1];
          GAP();
        }
      }
    }
    {
      const int blk = (lane >> 4) & 1, q = (lane & 15) >> 2, pp = lane & 3;
#pragma unroll
      for (int dt = 0; dt < 4; ++dt) s.wb[dt] = s.ring + (8 * hh + q) * 256 + 64 * (dt ^ q) + 32 * blk + 16 * (pp & 1) + 8 * (pp >> 1);
#pragma unroll
      for (int i = 0; i < 4; ++i) s.hb[i] = ldsb + r32 * ROWB + (((2 * i + hh) ^ ((r32 >> 1) & 7)) * 16);
    }
    asm volatile("s_waitcnt vmcnt(8) lgkmcnt(0)" ::: "memory");
    BAR();
    STAMP(1);
    PST(7);
#pragma unroll
    for (int f = 0; f < 8; ++f) { s.wlo[0][f] = ldT(s.wb[f & 3], 4096 * (f >> 2)); s.whi[0][f] = ldT(s.wb[f & 3], 4096 * (f >> 2) + 1024); }
#pragma unroll
    for (int i = 0; i < 4; ++i) s.hf[0][i] = ldA(s.hb[i >> 1], (i & 1) * 32 * ROWB);
    asm volatile("s_waitcnt lgkmcnt(0)" ::: "memory");
    issueq(0, gq + 2 * KS_Q);
    auto no = [&]() __attribute__((always_inline)) {};
    qstep<0, true, true, true, 8>(s, Qt, voffQ, gq + 3 * KS_Q, 0, no);
    qstep<1, false, true, true, 8>(s, Qt, voffQ, gq + 4 * KS_Q, 128, no);
    for (int n = 2; n < 14; n += 2) {
      qstep<0, false, true, true, 8>(s, Qt, voffQ, gq + (size_t)(n + 3) * KS_Q, 128 * ((n + 1) >> 1), no);
      qstep<1, false, true, true, 8>(s, Qt, voffQ, gq + (size_t)(n + 4) * KS_Q, 128 * ((n + 2) >> 1), no);
    }
    qstep<0, false, true, true, 8>(s, Qt, voffQ, gq + (size_t)17 * KS_Q, 128 * 7, no);
    qstep<1, false, true, false, 8>(s, Qt, voffQ, gq, 128 * 8, no);
    qstep<0, false, true, false, 0>(s, Qt, voffQ, gq, 128 * 8, no);
    qstep<1, false, false, false, 0>(s, Qt, voffQ, gq, 0, no);
    STAMP(2);
    PST(8);
    {
      const int kvl = w >> 1, half = w & 1;
      const unsigned kr = ldsb + kvl * 65536, vr = kr + 32768;
#pragma unroll
      for (int i = 0; i < 2; ++i) a.kb_[i] = kr + 2048 * (r32 >> 3) + 64 * (r32 & 7) + 16 * ((2 * i + hh) ^ ((r32 >> 2) & 3));
      const int blk = (lane >> 4) & 1, q = (lane & 15) >> 2, pp = lane & 3;
#pragma unroll
      for (int t = 0; t < 2; ++t) a.vb_[t] = vr + 64 * (4 * hh + q) + 16 * ((2 * blk + (pp >> 1)) ^ ((2 * t + hh) & 3)) + 8 * (pp & 1);
      const int st = lane >> 5, r7 = (lane >> 2) & 7, pos = lane & 3;
#pragma unroll
      for (int i = 0; i < 8; ++i) {
        const int rg = 4 * half + (i >> 1);
        a.voff[i] = rg * 2048 + r7 * 256 + (i & 1) * 128 + st * 64 + 16 * (pos ^ ((2 * (rg & 1) + (r7 >> 2)) & 3)) - (i - 4) * 1024 + 4096;
      }
      a.m0k = __builtin_amdgcn_readfirstlane(kr + 8192 * half + 4096); a.m0v = __builtin_amdgcn_readfirstlane(vr + 8192 * half + 4096);
      const size_t kvoff = ((size_t)(2 * j + kvl) * SK + (SK - WIN)) * DH * 2;
      a.gk = (const char*)p.kc + kvoff - 4096; a.gv = (const char*)p.vc + kvoff - 4096;
      a.pend = false; a.one = p.one;
    }
    asm volatile("s_waitcnt lgkmcnt(0)" ::: "memory");
    BAR();
    setm0(a.m0k); asm volatile("s_nop 0");
    dma<-4096>(a.voff[0], a.gk); dma<-3072>(a.voff[1], a.gk); dma<-2048>(a.voff[2], a.gk); dma<-1024>(a.voff[3], a.gk);
    dma<0>(a.voff[4], a.gk); dma<1024>(a.voff[5], a.gk); dma<2048>(a.voff[6], a.gk); dma<3072>(a.voff[7], a.gk);
    asm volatile("s_nop 7" ::: "memory");
#pragma unroll
    for (int dt = 0; dt < 4; ++dt)
#pragma unroll
      for (int qb = 0; qb < 2; ++qb) {
        asm volatile("" : "+v"(Qt[dt][qb]));
#pragma unroll
        for (int s2 = 0; s2 < 2; ++s2) {
          u4 w4;
#pragma unroll
          for (int e = 0; e < 4; ++e) w4[e] = pk((f2){Qt[dt][qb][8 * s2 + 2 * e], Qt[dt][qb][8 * s2 + 2 * e + 1]} * QSCALE);
          a.q[qb][2 * dt + s2] = __builtin_bit_cast(b8, w4);
          asm volatile("" : "+a"(a.q[qb][2 * dt + s2]));
        }
      }
  }
  STAMP(3);
  {
    auto tile_bar = [&]() __attribute__((always_inline)) { asm volatile("s_waitcnt vmcnt(0) lgkmcnt(0)" ::: "memory"); BAR(); };
    const char* pfp = lane >= 32 ? p.ws + WS_X + (size_t)c * 64 * SLICE + (size_t)j * SLICE + (18 * w + lane - 32) * 128
                    : w < 2 ? (const char*)(w == 0 ? p.wg : p.wu) + (size_t)576 * j * 2 + (size_t)(18 * c + lane / 9 - 3) * (DF * 2) + (lane % 9) * 128
                            : (const char*)p.wo + (size_t)512 * j * ROWB + (16 * c) * ROWB + (12 * (w - 2) + lane - 24) * 128;
    const int pfs = lane >= 32 ? 8 * SLICE : w < 2 ? 3 * DF * 2 : 24 * 128, pfn = w < 2 ? 27 : 12;
    auto pft = [&](int t) __attribute__((always_inline)) {
      const bool on = lane < pfn && t >= 1 && t <= 6;
      touchp(on ? pfp : (const char*)p.wo, __builtin_amdgcn_readfirstlane(ldsb + LDS_DUMMY));
      pfp += pfs;
    };
    auto tile_bar1 = [&]() __attribute__((always_inline)) { asm volatile("s_waitcnt vmcnt(1) lgkmcnt(0)" ::: "memory"); BAR(); };
    LAS unsigned* flag = (LAS unsigned*)(size_t)(ldsb + LDS_X1);
    if (tid == 0) *flag = 0u;
    auto k0 = [&]() __attribute__((always_inline)) {
      setm0(a.m0k); asm volatile("s_nop 0");
      dma<-4096>(a.voff[0], a.gk); dma<-3072>(a.voff[1], a.gk); dma<-2048>(a.voff[2], a.gk); dma<-1024>(a.voff[3], a.gk);
      dma<0>(a.voff[4], a.gk); dma<1024>(a.voff[5], a.gk); dma<2048>(a.voff[6], a.gk); dma<3072>(a.voff[7], a.gk);
    };
    tile_bar(); fastA<0, true, true, true>(a, 0, [&]() __attribute__((always_inline)) { pft(0); }); asm volatile("s_nop 7\n\ts_nop 3" ::: "memory"); GAP(); fastB<0, 2>(a);
    tile_bar1(); fastA<1, false, true, true>(a, 1, [&]() __attribute__((always_inline)) { pft(1); }); fastB<1, 1>(a);
    for (int t = 2; t < 6; t += 2) {
      tile_bar1(); fastA<0, false, true, true>(a, t, [&]() __attribute__((always_inline)) { pft(t); }); fastB<0, 0>(a);
      tile_bar1(); fastA<1, false, true, true>(a, t + 1, [&]() __attribute__((always_inline)) { pft(t + 1); }); fastB<1, 0>(a);
    }
    tile_bar1(); fastA<0, false, true, true>(a, 6, [&]() __attribute__((always_inline)) { pft(6); }); fastB<0, 0>(a);
    tile_bar1(); fastA<1, false, false, true>(a, 7, [&]() __attribute__((always_inline)) { pft(7); }); fastB<1, 0>(a);
    tile_bar1();
    fastB<1, 3>(a);
    {
      const bool bad = !(a.l[0] < 0x1p40f) || !(a.l[1] < 0x1p40f);
      if (__any((int)bad) && lane == 0) *flag = 1u;
      asm volatile("s_waitcnt lgkmcnt(0)" ::: "memory"); BAR();
      unsigned redo = __builtin_amdgcn_readfirstlane(*(volatile LAS unsigned*)flag); asm volatile("" : "+s"(redo));
      if (__builtin_expect(redo != 0u, 0)) {
        auto nopf = [&]() __attribute__((always_inline)) {};
        a.pend = false;
        auto upt = [&](const char* q) __attribute__((always_inline)) { const u64 v = (u64)q; return (const char*)((u64)(unsigned)__builtin_amdgcn_readfirstlane((unsigned)(v >> 32)) << 32 | (u64)(unsigned)__builtin_amdgcn_readfirstlane((unsigned)v)); };
        a.gk = upt(a.gk); a.gv = upt(a.gv); a.m0k = __builtin_amdgcn_readfirstlane(a.m0k); a.m0v = __builtin_amdgcn_readfirstlane(a.m0v);
        k0();
        tile_bar(); phaseA<0, true, true, true>(a, 0, nopf); asm volatile("s_nop 7\n\ts_nop 3" ::: "memory"); GAP(); phaseB<0, true, false>(a);
        tile_bar(); phaseA<1, false, true, true>(a, 1, nopf); phaseB<1, false, true>(a);
        for (int t = 2; t < 6; t += 2) {
          tile_bar(); phaseA<0, false, true, true>(a, t, nopf); phaseB<0, false, false>(a);
          tile_bar(); phaseA<1, false, true, true>(a, t + 1, nopf); phaseB<1, false, false>(a);
        }
        tile_bar(); phaseA<0, false, true, true>(a, 6, nopf); phaseB<0, false, false>(a);
        tile_bar(); phaseA<1, false, false, true>(a, 7, nopf); phaseB<1, false, false>(a);
        { auto f1 = [&](auto ic) __attribute__((always_inline)) { sm_exp<1, 1, decltype(ic)::value>(a); }; UNROLL(32, f1);
          auto f2_ = [&](auto ic) __attribute__((always_inline)) { sm_pack<1, decltype(ic)::value>(a); }; UNROLL(32, f2_);
          auto f3 = [&](auto ic) __attribute__((always_inline)) { sm_add<1, 1, decltype(ic)::value>(a); }; UNROLL(32, f3); }
#pragma unroll
        for (int qb = 0; qb < 2; ++qb) a.l[qb] += a.rs0[qb] + a.rs1[qb];
        tile_bar();
        const unsigned vslot = 16384;
        b4 vlo[16], vhi[16];
#pragma unroll
        for (int I = 0; I < 16; ++I) { vlo[I] = ldT(a.vb_[0], vslot + 2048 * (2 * (I >> 2)) + 512 * (I & 3)); vhi[I] = ldT(a.vb_[1], vslot + 2048 * (2 * (I >> 2) + 1) + 512 * (I & 3)); }
        asm volatile("s_nop 1" ::: "memory"); GAP();
#pragma unroll
        for (int G = 0; G < 32; ++G) { const int I = G >> 1; mfma32<true, false>(a.O[I & 3][G & 1], cat(vlo[I], vhi[I]), a.P[G & 1][I >> 2]); }
        asm volatile("s_waitcnt vmcnt(0) lgkmcnt(0)" ::: "memory"); BAR();
      }
    }
  }
  STAMP(4);
  int tid2 = threadIdx.x; asm volatile("" : "+v"(tid2));
  {
  const int tid = tid2, lane = tid2 & 63, hh = lane >> 5, r32 = lane & 31;
  GS s;
  unsigned voffB[9];
#pragma unroll
  for (int i = 0; i < 9; ++i) { const int idx = i * 64 + lane, imm = i < 8 ? (i - 4) * 1024 : 0; voffB[i] = (idx / 18) * ROWB + (idx % 18) * 16 - imm + 4096; }
  s.ring = __builtin_amdgcn_readfirstlane(ldsb + ABYTES + w * 2 * SLOT);
  const int g4 = lane >> 4, cl = lane & 15;
  s.wbase = s.ring + (4 * g4 + ((lane & 15) >> 2)) * 288 + 8 * (lane & 3);
  const unsigned sw = (cl >> 1) & 7;
  const unsigned ab_e = ldsb + cl * ROWB + ((g4 ^ sw) * 16), ab_o = ldsb + cl * ROWB + (((4 + g4) ^ sw) * 16);
  const __amdgpu_buffer_rsrc_t rX = __builtin_amdgcn_make_buffer_rsrc(p.ws + WS_X + (size_t)c * 64 * SLICE, 0, 64 * SLICE, 0x00020000);
  unsigned voffA[9];
#pragma unroll
  for (int i = 0; i < 9; ++i) { const int idx = i * 64 + lane, imm = i < 8 ? (i - 4) * 1024 : 0; voffA[i] = (idx / 18) * (DF * 2) + (idx % 18) * 16 - imm + 4096; }
  const char* gG = (const char*)p.wg + (size_t)(576 * j + 144 * w) * 2 - 4096;
  const char* gU = (const char*)p.wu + (size_t)(576 * j + 144 * w) * 2 - 4096;
  const char* gD = (const char*)p.wd + (size_t)(576 * j) * ROWB + 288 * w - 4096;
  f4 aG[9][4];
  {
    const char* gO = (const char*)p.wo + (size_t)(512 * j) * ROWB + 288 * w - 4096;
    issue_all(s, 0, voffB, gO); issue_all(s, 1, voffB, gO + (size_t)32 * ROWB);
    asm volatile("s_nop 7" ::: "memory");
#pragma unroll
    for (int qb = 0; qb < 2; ++qb) {
      const float il = __builtin_amdgcn_rcpf(swap32_add(a.l[qb]));
      const int row = 32 * qb + r32; const unsigned swr = (r32 >> 1) & 7;
#pragma unroll
      for (int db = 0; db < 4; ++db) {
        asm volatile("" : "+a"(a.O[db][qb]));
        const f16v o = a.O[db][qb];
#pragma unroll
        for (int u = 0; u < 2; ++u) {
          const u4 wv = {pk((f2){o[4 * u], o[4 * u + 1]} * il), pk((f2){o[4 * u + 2], o[4 * u + 3]} * il), pk((f2){o[4 * u + 8], o[4 * u + 9]} * il), pk((f2){o[4 * u + 10], o[4 * u + 11]} * il)};
          *(LAS u4*)(size_t)(ldsb + row * ROWB + (((4 * (4 * w + db) + 2 * u + hh) ^ swr) * 16)) = wv;
        }
      }
    }
    asm volatile("s_waitcnt vmcnt(9) lgkmcnt(0)" ::: "memory");
    BAR();
    STAMP(5);
    read_w(s, 0); read_a(s, 0, ab_e);
    asm volatile("s_waitcnt lgkmcnt(0)" ::: "memory");
    issue_all(s, 0, voffB, gO + (size_t)2 * 32 * ROWB);
    const Epi e1 = {rX, (unsigned)(j * SLICE), 8u * SLICE, ldsb + LDS_X1, j, w, lane};
    gemm_rows<16, 2, 0>(s, aG, voffB, gO, ab_e, ab_o, voffA, gG, gU, e1, [&]() __attribute__((always_inline)) {});
    STAMP(6);
  }
  {
    int zlr[3], zcc[3];
    const int t3 = tid < 192 ? tid : tid - 64;
    u4 xr[3], gv[3];
#pragma unroll
    for (int i = 0; i < 3; ++i) {
      zdecode(t3 + 192 * i, zlr[i], zcc[i]);
      xr[i] = *(const u4*)(p.x + (size_t)(c * 64 + j * 8 + zlr[i]) * D + zcc[i] * 8);
      gv[i] = *(LAS u4*)(size_t)(ldsb + LDS_MOD + (2 * D + zcc[i] * 8) * 2);
    }
    const int lrow = 2 * w + hh, grow = j * 8 + lrow;
    u2 cbv[3][2], sbv[3][2];
#pragma unroll
    for (int i = 0; i < 3; ++i) {
      const int pos = (i < 2 || r32 < 8) ? r32 + 32 * i : r32;
      const int q = pos ^ ((grow >> 1) & 7), k0 = 32 * (q >> 2) + 4 * (q & 3);
#pragma unroll
      for (int h = 0; h < 2; ++h) { cbv[i][h] = *(LAS u2*)(size_t)(ldsb + LDS_MOD + (4 * D + k0 + 16 * h) * 2); sbv[i][h] = *(LAS u2*)(size_t)(ldsb + LDS_MOD + (3 * D + k0 + 16 * h) * 2); }
    }
    asm volatile("s_waitcnt lgkmcnt(0)" ::: "memory");
    publish(flags + (0 * 256 + blockIdx.x) * FLAG_STRIDE, tag);
    STAMP(7);
    await8(flags + (0 * 256 + c * 8) * FLAG_STRIDE, tag, tmo, w, lane);
    STAMP(8);
    {
      u4 v[3][8];
#pragma unroll
      for (int i = 0; i < 3; ++i) {
#pragma unroll
        for (int jj = 0; jj < 7; ++jj) v[i][jj] = __builtin_amdgcn_raw_buffer_load_b128(rX, (j * 8 + jj + (jj >= j)) * SLICE + (t3 + 192 * i) * 16, 0, AUX_X);
      }
#pragma unroll
      for (int i = 0; i < 3; ++i) v[i][7] = *(LAS u4*)(size_t)(ldsb + LDS_X1 + (t3 + 192 * i) * 16);
#pragma unroll
      for (int i = 0; i < 3; ++i) {
        const u4 x1 = gated_residual(v[i], xr[i], gv[i]);
        if (tid < 192) {
          *(LAS u4*)(size_t)(ldsb + LDS_X1 + (tid + 192 * i) * 16) = x1;
          LAS f4* dst = (LAS f4*)(size_t)(ldsb + (zlr[i] * D + zcc[i] * 8) * 4);
          const f2 a0 = bf2(x1[0]), a1 = bf2(x1[1]), a2 = bf2(x1[2]), a3 = bf2(x1[3]);
          dst[0] = (f4){a0[0], a0[1], a1[0], a1[1]}; dst[1] = (f4){a2[0], a2[1], a3[0], a3[1]};
        }
      }
    }
    asm volatile("s_waitcnt lgkmcnt(0)" ::: "memory");
    BAR();
    STAMP(9);
    {
      f4 v[3][2];
      f2 sum = {0.f, 0.f};
#pragma unroll
      for (int i = 0; i < 3; ++i) {
        const bool on = (i < 2 || r32 < 8);
        const int pos = on ? r32 + 32 * i : r32;
        const int q = pos ^ ((grow >> 1) & 7), k0 = 32 * (q >> 2) + 4 * (q & 3);
#pragma unroll
        for (int h = 0; h < 2; ++h) {
          v[i][h] = *(LAS f4*)(size_t)(ldsb + (lrow * D + k0 + 16 * h) * 4);
          if (on) sum += (f2){v[i][h][0] + v[i][h][1], v[i][h][2] + v[i][h][3]};
        }
      }
      float sm = sum[0] + sum[1];
      sm = sum32(sm);
      const float mean = sm * (1.f / D);
      float q2 = 0.f;
#pragma unroll
      for (int i = 0; i < 3; ++i)
        if (i < 2 || r32 < 8) {
#pragma unroll
          for (int h = 0; h < 2; ++h)
#pragma unroll
            for (int e = 0; e < 4; ++e) { const float dl = v[i][h][e] - mean; q2 += dl * dl; }
        }
      q2 = sum32(q2);
      const float mr = rbf(mean), rs = __builtin_amdgcn_rcpf(rbf(__builtin_amdgcn_sqrtf(rbf(rbf(q2 * (1.f / D)) + 1e-5f))));
      const __amdgpu_buffer_rsrc_t rH = __builtin_amdgcn_make_buffer_rsrc(p.ws + WS_H2 + (size_t)c * ABYTES, 0, ABYTES, 0x00020000);
      u4 oh[3];
#pragma unroll
      for (int i = 0; i < 3; ++i) {
        u4& o = oh[i];
#pragma unroll
        for (int h = 0; h < 2; ++h)
#pragma unroll
          for (int e = 0; e < 2; ++e) {
            unsigned t = pk((f2){v[i][h][2 * e], v[i][h][2 * e + 1]} - mr);
            t = pk(bf2(t) * rs);
            t = pk(bf2(t) * bf2(pk(bf2(cbv[i][h][e]) + 1.f)));
            o[2 * h + e] = pk(bf2(t) + bf2(sbv[i][h][e]));
          }
        if (i < 2 || r32 < 8) __builtin_amdgcn_raw_buffer_store_b128(o, rH, grow * ROWB + (r32 + 32 * i) * 16, 0, 16);
      }
      publish(flags + (1 * 256 + blockIdx.x) * FLAG_STRIDE, tag);
      STAMP(10);
      if (w != 0) {
        const unsigned dummy = __builtin_amdgcn_readfirstlane(ldsb + LDS_DUMMY);
        int li = tid - 64; li = li < 162 ? li : li - 94;
        const int r = 18 * c + li / 9, l = li % 9;
        touch((const char*)p.wg + (size_t)576 * j * 2, r * (DF * 2) + l * 128, dummy);
        touch((const char*)p.wu + (size_t)576 * j * 2, r * (DF * 2) + l * 128, dummy);
      }
      await8(flags + (1 * 256 + c * 8) * FLAG_STRIDE, tag, tmo, w, lane);
      STAMP(11);
      u4 hv[16];
#pragma unroll
      for (int i = 0; i < 16; ++i) { const int y = i * 256 + tid < 4032 ? i * 256 + tid : tid; hv[i] = __builtin_amdgcn_raw_buffer_load_b128(rH, (y + (y >= j * 576 ? 576 : 0)) * 16, 0, AUX_H); }
#pragma unroll
      for (int i = 0; i < 3; ++i) if (i < 2 || r32 < 8) *(LAS u4*)(size_t)(ldsb + grow * ROWB + (r32 + 32 * i) * 16) = oh[i];
#pragma unroll
      for (int i = 0; i < 16; ++i) { const int y = i * 256 + tid; if (i < 15 || y < 4032) *(LAS u4*)(size_t)(ldsb + (y + (y >= j * 576 ? 576 : 0)) * 16) = hv[i]; }
    }
    asm volatile("s_waitcnt vmcnt(0) lgkmcnt(0)" ::: "memory");
    BAR();
  }
  STAMP(12);
  {
    constexpr size_t KS_GU = (size_t)32 * DF * 2, KS_D = (size_t)32 * ROWB;
    read_w(s, 0); read_a(s, 0, ab_e);
    asm volatile("s_waitcnt lgkmcnt(0)" ::: "memory");
    issue_all(s, 0, voffA, gG + KS_GU);
    f4 aU[9][4];
    auto no = [&]() __attribute__((always_inline)) {};
    auto pfd = [&](int h) __attribute__((always_inline)) {
      int li = 64 * h + 16 * w + lane; li = li < 162 ? li : 161;
      if (lane < 16) touch((const char*)p.wd + (size_t)576 * j * ROWB, (18 * c) * ROWB + li * 128, __builtin_amdgcn_readfirstlane(ldsb + LDS_DUMMY));
    };
    auto kpair = [&](auto zc, auto lastc, auto vmc, int k, auto&& pf) __attribute__((always_inline)) {
      constexpr bool Z = decltype(zc)::value, LAST = decltype(lastc)::value; constexpr int VM = decltype(vmc)::value;
      const char* u1 = gU + (size_t)(k + 1) * KS_GU; const char* g2 = gG + (size_t)(k + 2) * KS_GU;
      step<0, 0, Z, true, false, true, 9, 0>(s, aG, voffA, u1, 0, no);
      if constexpr (!LAST) {
        step<1, 0, Z, true, true, true, 9, NVU>(s, aU, voffA, g2, ab_o + 128 * (k >> 1), pf);
        step<0, 1, false, true, false, true, VM, 0>(s, aG, voffA, u1 + KS_GU, 0, no);
        step<1, 1, false, true, true, true, VM, NVU>(s, aU, voffA, g2 + KS_GU, ab_e + 128 * ((k + 2) >> 1), no);
      } else {
        step<1, 0, Z, true, true, true, 9, NVU>(s, aU, voffB, gD, ab_o + 128 * (k >> 1), no);
        step<0, 1, false, true, false, true, 9, 0>(s, aG, voffB, gD + KS_D, 0, no);
        step<1, 1, false, true, false, true, 9, NVU>(s, aU, voffB, gD + 2 * KS_D, 0, no);
      }
    };
    kpair(std::true_type{}, std::false_type{}, IC<9>{}, 0, no);
    for (int k = 2; k < 8; k += 2) kpair(std::false_type{}, std::false_type{}, IC<9>{}, k, no);
    for (int k = 8; k < 14; k += 2) kpair(std::false_type{}, std::false_type{}, IC<10>{}, k, [&]() __attribute__((always_inline)) { pfd((k - 8) >> 1); });
    kpair(std::false_type{}, std::false_type{}, IC<9>{}, 14, no);
    kpair(std::false_type{}, std::true_type{}, IC<9>{}, 16, no);
    STAMP(13);
    settle<0>(aG); settle<NVU>(aU);
    u2 act[9][4];
#pragma unroll
    for (int t = 0; t < 9; ++t)
#pragma unroll
      for (int m = 0; m < 4; ++m)
#pragma unroll
        for (int e = 0; e < 2; ++e) {
          const f2 x = {aG[t][m][2 * e], aG[t][m][2 * e + 1]}, u = {aU[t][m][2 * e], aU[t][m][2 * e + 1]};
          const f2 z = x * (x * x * -0.10294324f + -2.3022082f);
          const f2 dn = (f2){__builtin_amdgcn_exp2f(z[0]), __builtin_amdgcn_exp2f(z[1])} + 1.f;
          act[t][m][e] = pk((x * u) * (f2){__builtin_amdgcn_rcpf(dn[0]), __builtin_amdgcn_rcpf(dn[1])});
        }
    asm volatile("s_waitcnt lgkmcnt(0)" ::: "memory");
    BAR();
#pragma unroll
    for (int t = 0; t < 9; ++t) {
      const int tt = 9 * w + t, ks = tt >> 1, ah = tt & 1;
      const unsigned ad = ldsb + cl * ROWB + (((4 * ks + g4) ^ sw) * 16) + 8 * ah;
#pragma unroll
      for (int m = 0; m < 4; ++m) *(LAS u2*)(size_t)(ad + m * 16 * ROWB) = act[t][m];
    }
    asm volatile("s_waitcnt lgkmcnt(0)" ::: "memory");
    BAR();
    STAMP(14);
    read_a(s, 0, ab_e);
    const Epi e2 = {rX, (unsigned)(j * 8 * SLICE), (unsigned)SLICE, s.ring - w * 2304, j, w, lane};
    gemm_rows<18, 0, 0>(s, aG, voffB, gD, ab_e, ab_o, voffB, gD, gD, e2, [&]() __attribute__((always_inline)) {});
    STAMP(15);
  }
  {
    int tid3 = threadIdx.x; asm volatile("" : "+v"(tid3));
    const int tid = tid3, lane = tid3 & 63;
    asm volatile("s_waitcnt lgkmcnt(0)" ::: "memory");
    publish(flags + (2 * 256 + blockIdx.x) * FLAG_STRIDE, tag);
    STAMP(16);
    const int t3 = tid < 192 ? tid : tid - 64;
    int zlr[3], zcc[3];
    u4 gv[3], x1[3];
#pragma unroll
    for (int i = 0; i < 3; ++i) {
      zdecode(t3 + 192 * i, zlr[i], zcc[i]);
      gv[i] = *(LAS u4*)(size_t)(ldsb + LDS_MOD + (5 * D + zcc[i] * 8) * 2);
      x1[i] = *(LAS u4*)(size_t)(ldsb + LDS_X1 + (t3 + 192 * i) * 16);
    }
    if (blockIdx.x == 0 && w == 1) {
      for (unsigned spins = 0; spins <= SPIN_CAP; ++spins) {
        bool ok = true;
#pragma unroll
        for (int i = 0; i < 4; ++i) ok &= __hip_atomic_load((gu64*)(flags + (lane + 64 * i) * FLAG_STRIDE), __ATOMIC_RELAXED, __HIP_MEMORY_SCOPE_AGENT) == tag;
        if (__all(ok)) break;
        __builtin_amdgcn_s_sleep(4);
      }
      if (lane == 0) __hip_atomic_store((gu64*)(p.ws + WS_EPOCH), tag, __ATOMIC_RELAXED, __HIP_MEMORY_SCOPE_AGENT);
    }
    await8(flags + (2 * 256 + c * 8) * FLAG_STRIDE, tag, tmo, w, lane);
    STAMP(17);
    const __amdgpu_buffer_rsrc_t rO = __builtin_amdgcn_make_buffer_rsrc(p.out + (size_t)(c * 64 + j * 8) * D, 0, SLICE, 0x00020000); (void)rO;
    u4 v[3][8];
#pragma unroll
    for (int i = 0; i < 3; ++i)
#pragma unroll
      for (int jj = 0; jj < 7; ++jj) v[i][jj] = __builtin_amdgcn_raw_buffer_load_b128(rX, ((jj + (jj >= j)) * 8 + j) * SLICE + (t3 + 192 * i) * 16, 0, AUX_X);
#pragma unroll
    for (int i = 0; i < 3; ++i) { const int z = t3 + 192 * i, zw = z / 144; v[i][7] = *(LAS u4*)(size_t)(ldsb + ABYTES + zw * 2 * SLOT + (z - zw * 144) * 16); }
#pragma unroll
    for (int i = 0; i < 3; ++i) {
      const u4 o = gated_residual(v[i], x1[i], gv[i]);
      if (tid < 192) *(u4*)(p.out + (size_t)(c * 64 + j * 8 + zlr[i]) * D + zcc[i] * 8) = o;
    }
  }
  STAMP(18);
  asm volatile("s_waitcnt lgkmcnt(0)" ::: "memory");
  STAMP_FLUSH();
  }
}

extern "C" void kernel_launch(void* const* d_in, const int* in_sizes, int n_in, void* d_out, int out_size, void* d_ws, size_t ws_size, hipStream_t stream) {
  Params p;
  p.x = (const u16*)d_in[0]; p.kc = (const u16*)d_in[1]; p.vc = (const u16*)d_in[2]; p.wq = (const u16*)d_in[3]; p.wo = (const u16*)d_in[4];
  p.wg = (const u16*)d_in[6]; p.wu = (const u16*)d_in[7]; p.wd = (const u16*)d_in[8];
  p.c = (const u16*)d_in[9]; p.wmod = (const u16*)d_in[10];
  p.out = (u16*)d_out; p.ws = (char*)d_ws;
 p.one = 1.0f; p.gap = 0u;
  (void)hipFuncSetAttribute((const void*)k_main, hipFuncAttributeMaxDynamicSharedMemorySize, LDS_BYTES);
  k_main<<<256, 256, LDS_BYTES, stream>>>(p);
}
```

```cpp
#define PRA 7
#define PRB 12
#include <hip/hip_runtime.h>
#include <stdint.h>
#include <utility>
#include <type_traits>

typedef unsigned short u16;
typedef unsigned long long u64;
typedef __bf16 b2 __attribute__((ext_vector_type(2)));
typedef __bf16 b4 __attribute__((ext_vector_type(4)));
typedef __bf16 b8 __attribute__((ext_vector_type(8)));
typedef float f2 __attribute__((ext_vector_type(2)));
typedef float f4 __attribute__((ext_vector_type(4)));
typedef float f16v __attribute__((ext_vector_type(16)));
typedef unsigned u2 __attribute__((ext_vector_type(2)));
typedef unsigned u4 __attribute__((ext_vector_type(4)));
typedef __attribute__((address_space(1))) unsigned gu32;
typedef __attribute__((address_space(1))) unsigned long long gu64;
#define LAS __attribute__((address_space(3)))
#define DI __device__ __forceinline__
#define GAP() __builtin_amdgcn_sched_barrier(0)
#define LGKM0() __builtin_amdgcn_s_waitcnt(0xC07F)
#define BAR() do { asm volatile("" ::: "memory"); __builtin_amdgcn_s_barrier(); asm volatile("" ::: "memory"); } while (0)
template <int V> using IC = std::integral_constant<int, V>;
#define AUX_X 16
#define AUX_H 16
#define PF_D2 150
#define STAG_D2 560
#ifndef NVU
#define NVU 8
#endif
#define PFD_K 14
#define UNROLL(N, F) [&]<int... I_>(std::integer_sequence<int, I_...>) __attribute__((always_inline)) { (F(IC<I_>{}), ...); }(std::make_integer_sequence<int, N>{})

constexpr int S = 2048, D = 576, DH = 128, SK = 2048, WIN = 512, DF = 4608;
constexpr int ROWB = D * 2;
constexpr int ABYTES = 64 * ROWB;
constexpr int SLICE = 8 * ROWB;
constexpr int SLOT = 9216;
constexpr int QSLOT = 8192;
constexpr int LDS_DUMMY = ABYTES + 8 * SLOT;
constexpr int LDS_X1 = LDS_DUMMY + 256;
constexpr int LDS_MOD = LDS_X1 + SLICE;
constexpr int LDS_BYTES = LDS_MOD + 6 * D * 2;
static_assert(LDS_BYTES <= 163840, "LDS");
constexpr size_t WS_EPOCH = 0;
constexpr size_t WS_TMO = 256;
constexpr size_t WS_MODP = 131072;
constexpr size_t WS_FLAG = 32768;
constexpr int FLAG_STRIDE = 8;
constexpr size_t WS_X = 1048576;
constexpr size_t WS_H2 = WS_X + (size_t)32 * 64 * SLICE;
constexpr size_t WS_DBG = 200u << 20;
#define STAMP(i)
#define STAMP_FLUSH()
#define PST(i)

DI float bf2f(u16 v) { return __uint_as_float(((unsigned)v) << 16); }
DI float bflo(unsigned w) { return __uint_as_float(w << 16); }
DI float bfhi(unsigned w) { return __uint_as_float(w & 0xffff0000u); }
DI f2 bf2(unsigned w) { return (f2){bflo(w), bfhi(w)}; }
DI unsigned pk(float lo, float hi) { f2 v = {lo, hi}; b2 b = __builtin_convertvector(v, b2); return __builtin_bit_cast(unsigned, b); }
DI unsigned pk(f2 v) { b2 b = __builtin_convertvector(v, b2); return __builtin_bit_cast(unsigned, b); }
DI float rbf(float f) { return bflo(pk(f, f)); }
DI f2 rb2(f2 v) { return (f2){__uint_as_float(pk(0.f, v[0])), __uint_as_float(pk(0.f, v[1]))}; }
template <int CTRL> DI float dpp(float v) { return __uint_as_float(__builtin_amdgcn_update_dpp(0u, __float_as_uint(v), CTRL, 0xf, 0xf, false)); }
DI float sum8(float v) { v += dpp<0xB1>(v); v += dpp<0x4E>(v); v += dpp<0x141>(v); return v; }
DI float swap16_add(float x) { auto r = __builtin_amdgcn_permlane16_swap(__float_as_uint(x), __float_as_uint(x), false, false); return __uint_as_float(r[0]) + __uint_as_float(r[1]); }
DI float swap32_add(float x) { auto r = __builtin_amdgcn_permlane32_swap(__float_as_uint(x), __float_as_uint(x), false, false); return __uint_as_float(r[0]) + __uint_as_float(r[1]); }
DI float sum32(float v) { v = sum8(v); v += dpp<0x140>(v); return swap16_add(v); }
DI u16 f2bf(float f) { return (u16)(pk(f, f) & 0xffffu); }
DI float fmaxx(float a, float b) { return __builtin_elementwise_maximum(a, b); }
DI float fmax3(float a, float b, float c) { return __builtin_elementwise_maximum(__builtin_elementwise_maximum(a, b), c); }

DI void zdecode(int z, int& lr, int& cc) {
  const int w = z / 144, y = z - w * 144;
  if (y < 128) { const int tp = y >> 5; lr = (y >> 2) & 7; cc = 18 * w + 4 * tp + (y & 3); }
  else { const int y2 = y - 128; lr = y2 >> 1; cc = 18 * w + 16 + (y2 & 1); }
}

template <int IMM> DI void dma(unsigned voff, const char* g) { asm volatile("global_load_lds_dwordx4 %0, %1 offset:%2" :: "v"(voff), "s"(g), "i"(IMM) : "memory"); }
DI void setm0(unsigned a) { asm volatile("s_mov_b32 m0, %0" :: "s"(a) : "memory"); }
DI void touch(const char* p, unsigned voff, unsigned ldsdummy) {
  asm volatile("s_mov_b32 m0, %0\n\ts_nop 0\n\tglobal_load_lds_dword %1, %2" :: "s"(ldsdummy), "v"(voff), "s"(p) : "memory");
}
DI void touchp(const char* a, unsigned ldsdummy) {
  asm volatile("s_mov_b32 m0, %0\n\ts_nop 0\n\tglobal_load_lds_dword %1, off" :: "s"(ldsdummy), "v"(a) : "memory");
}
DI b4 ldT(unsigned base, int off) { return __builtin_amdgcn_ds_read_tr16_b64_v4bf16((LAS b4*)(size_t)(base + off)); }
DI b8 ldA(unsigned base, int off) { return *(LAS b8*)(size_t)(base + off); }
DI b8 cat(const b4& lo, const b4& hi) { return __builtin_shufflevector(lo, hi, 0, 1, 2, 3, 4, 5, 6, 7); }
template <bool AG, bool ZERO> DI void mfma16(f4& c, const b8& a, const b8& b) {
  if constexpr (AG) {
    if constexpr (ZERO) asm volatile("v_mfma_f32_16x16x32_bf16 %0, %1, %2, 0" : "=a"(c) : "v"(a), "v"(b));
    else asm volatile("v_mfma_f32_16x16x32_bf16 %0, %1, %2, %0" : "+a"(c) : "v"(a), "v"(b));
  } else {
    if constexpr (ZERO) asm volatile("v_mfma_f32_16x16x32_bf16 %0, %1, %2, 0" : "=&v"(c) : "v"(a), "v"(b));
    else asm volatile("v_mfma_f32_16x16x32_bf16 %0, %1, %2, %0" : "+v"(c) : "v"(a), "v"(b));
  }
}
template <bool AG, bool ZERO> DI void mfma32(f16v& c, const b8& a, const b8& b) {
  if constexpr (AG) {
    if constexpr (ZERO) asm volatile("v_mfma_f32_32x32x16_bf16 %0, %1, %2, 0" : "=a"(c) : "v"(a), "v"(b));
    else asm volatile("v_mfma_f32_32x32x16_bf16 %0, %1, %2, %0" : "+a"(c) : "v"(a), "v"(b));
  } else {
    if constexpr (ZERO) asm volatile("v_mfma_f32_32x32x16_bf16 %0, %1, %2, 0" : "=&v"(c) : "v"(a), "v"(b));
    else asm volatile("v_mfma_f32_32x32x16_bf16 %0, %1, %2, %0" : "+v"(c) : "v"(a), "v"(b));
  }
}
template <int NVG> DI void settle(f4 (&acc)[9][4]) {
  asm volatile("s_nop 7\n\ts_nop 7" ::: "memory");
#pragma unroll
  for (int t = 0; t < 9; ++t)
#pragma unroll
    for (int m = 0; m < 4; ++m) { if (t * 4 + m < 36 - NVG) asm volatile("" : "+a"(acc[t][m])); else asm volatile("" : "+v"(acc[t][m])); }
}

struct GS {
  b4 wlo[2][9], whi[2][9];
  b8 af[2][4];
  unsigned wbase, ring;
};
DI void issue_all(const GS& s, int slot, const unsigned (&voff)[9], const char* gp) {
  const unsigned sl = s.ring + slot * SLOT;
  setm0(sl + 4096); asm volatile("s_nop 0");
  dma<-4096>(voff[0], gp); dma<-3072>(voff[1], gp); dma<-2048>(voff[2], gp); dma<-1024>(voff[3], gp);
  dma<0>(voff[4], gp); dma<1024>(voff[5], gp); dma<2048>(voff[6], gp); dma<3072>(voff[7], gp);
  setm0(sl + 8192); asm volatile("s_nop 0"); dma<0>(voff[8], gp);
}
template <int PAR, int AP, bool ZERO, bool RDW, bool RDA, bool DMA, int VM, int NVG, typename PF>
DI void step(GS& s, f4 (&acc)[9][4], const unsigned (&voff)[9], const char* gp, unsigned ab, PF&& pf) {
  const unsigned sl = s.ring + (PAR ^ 1) * SLOT;
  const unsigned wb = s.wbase + (PAR ^ 1) * SLOT;
  auto mm = [&](auto ic) __attribute__((always_inline)) {
    constexpr int I = decltype(ic)::value, t = I >> 2, m = I & 3;
    mfma16<(I < 36 - NVG), ZERO>(acc[t][m], cat(s.wlo[PAR][t], s.whi[PAR][t]), s.af[AP][m]);
  };
  auto rd = [&](auto ic) __attribute__((always_inline)) {
    constexpr int I = decltype(ic)::value;
    if constexpr (I < 4) { if constexpr (RDA) s.af[AP ^ 1][I] = ldA(ab, I * 16 * ROWB); }
    else if constexpr (RDW) {
      if constexpr (((I - 4) & 1) == 0) s.wlo[PAR ^ 1][(I - 4) >> 1] = ldT(wb, 32 * ((I - 4) >> 1));
      else s.whi[PAR ^ 1][(I - 4) >> 1] = ldT(wb, 32 * ((I - 4) >> 1) + 4608);
    }
  };
  auto gap = [&](auto gc) __attribute__((always_inline)) {
    constexpr int G = decltype(gc)::value;
    mm(IC<2 * G>{});
    if constexpr (G == 0 && RDW) asm volatile("s_waitcnt vmcnt(%0)" :: "i"(VM) : "memory");
    if constexpr (G <= 10) rd(IC<2 * G>{});
    if constexpr (DMA) {
      if constexpr (G == 12) { LGKM0(); setm0(sl + 4096); }
      if constexpr (G >= 13 && G <= 16) dma<(2 * (G - 13) - 4) * 1024>(voff[2 * (G - 13)], gp);
      if constexpr (G == 17) dma<0>(voff[8], gp);
    }
    GAP();
    mm(IC<2 * G + 1>{});
    if constexpr (G <= 10) rd(IC<2 * G + 1>{});
    if constexpr (DMA) {
      if constexpr (G >= 13 && G <= 16) dma<(2 * (G - 13) + 1 - 4) * 1024>(voff[2 * (G - 13) + 1], gp);
      if constexpr (G == 16) setm0(sl + 8192);
    }
    if constexpr (G == 17) pf();
    GAP();
  };
  UNROLL(18, gap);
}
DI void read_w(GS& s, int par) {
#pragma unroll
  for (int t = 0; t < 9; ++t) { s.wlo[par][t] = ldT(s.wbase + par * SLOT, 32 * t); s.whi[par][t] = ldT(s.wbase + par * SLOT, 32 * t + 4608); }
}
DI void read_a(GS& s, int set, unsigned ab) {
#pragma unroll
  for (int m = 0; m < 4; ++m) s.af[set][m] = ldA(ab, m * 16 * ROWB);
}
struct Epi { __amdgpu_buffer_rsrc_t rs; unsigned base, dstride, ownlds; int own, w, lane; };
template <int U> DI void epi_unit(f4 (&acc)[9][4], const Epi& e) {
  const int G = e.lane >> 4, c = e.lane & 15, lr = c & 7, dh = c >> 3;
  constexpr int ta = U < 16 ? 2 * (U >> 2) : 8, tb = U < 16 ? ta + 1 : 8, ma = U < 16 ? (U & 3) : 2 * (U - 16), mb = U < 16 ? ma : ma + 1;
  asm volatile("" : "+a"(acc[ta][ma]), "+a"(acc[tb][mb]));
  const f4 a = acc[ta][ma], b = acc[tb][mb];
  auto r0 = __builtin_amdgcn_permlane16_swap(pk(a[0], a[1]), pk(b[0], b[1]), false, false);
  auto r1 = __builtin_amdgcn_permlane16_swap(pk(a[2], a[3]), pk(b[2], b[3]), false, false);
  const u4 v = {r0[0], r1[0], r0[1], r1[1]};
  const int d = U < 16 ? 2 * ma + dh : 2 * (ma + (G & 1)) + dh;
  const unsigned off = U < 16 ? e.w * 2304 + (U >> 2) * 512 + lr * 64 + (2 * (G & 1) + (G >> 1)) * 16 : e.w * 2304 + 2048 + lr * 32 + (G >> 1) * 16;
  __builtin_amdgcn_raw_buffer_store_b128(v, e.rs, d == e.own ? 0x7ffffff0u : e.base + d * e.dstride + off, 0, 16);
  if ((U < 16 ? ma : ma >> 1) == (U < 16 ? e.own >> 1 : e.own >> 2)) { if (d == e.own) *(LAS u4*)(size_t)(e.ownlds + off) = v; }
}
template <bool NEXT, int VM> DI void final2(GS& s, f4 (&acc)[9][4], const unsigned (&nvoff)[9], const char* nx1, unsigned ab, const Epi& e) {
  const unsigned sl = s.ring + SLOT, wb = s.wbase + SLOT;
  asm volatile("s_waitcnt vmcnt(%0)" :: "i"(VM) : "memory");
#pragma unroll
  for (int m = 0; m < 4; ++m) s.af[1][m] = ldA(ab, m * 16 * ROWB);
  auto rdw = [&](auto ic) __attribute__((always_inline)) { constexpr int t = decltype(ic)::value; s.wlo[1][t] = ldT(wb, 32 * t); s.whi[1][t] = ldT(wb, 32 * t + 4608); };
  rdw(IC<0>{}); rdw(IC<1>{});
  GAP();
  auto grp = [&](auto gc) __attribute__((always_inline)) {
    constexpr int G = decltype(gc)::value, t = G >> 1, st = G & 1;
    auto mm = [&](auto mc) __attribute__((always_inline)) { constexpr int m = decltype(mc)::value; mfma16<true, false>(acc[t][m], cat(s.wlo[st][t], s.whi[st][t]), s.af[st][m]); };
    mm(IC<0>{});
    if constexpr (st == 0 && t + 2 < 9) rdw(IC<t + 2>{});
    if constexpr (NEXT && G == 15) { LGKM0(); setm0(sl + 4096); }
    GAP();
    constexpr int U = G >= 5 ? G - 5 : -1;
    mm(IC<1>{});
    if constexpr (U >= 0 && U < 16 && (U >> 2) * 4 + 5 <= G) epi_unit<(U >= 0 && U < 16) ? U : 0>(acc, e);
    GAP();
    mm(IC<2>{});
    if constexpr (NEXT && G == 16) { dma<-4096>(nvoff[0], nx1); dma<-3072>(nvoff[1], nx1); dma<-2048>(nvoff[2], nx1); dma<-1024>(nvoff[3], nx1); }
    if constexpr (NEXT && G == 17) { dma<0>(nvoff[4], nx1); dma<1024>(nvoff[5], nx1); dma<2048>(nvoff[6], nx1); dma<3072>(nvoff[7], nx1); setm0(sl + 8192); }
    GAP();
    mm(IC<3>{});
    GAP();
  };
  UNROLL(18, grp);
  if constexpr (NEXT) dma<0>(nvoff[8], nx1);
  asm volatile("s_nop 7" ::: "memory");
  epi_unit<13>(acc, e); epi_unit<14>(acc, e); epi_unit<15>(acc, e); epi_unit<16>(acc, e); epi_unit<17>(acc, e);
}
template <int NS, int NEXT, int NPF, typename PF> DI void gemm_rows(GS& s, f4 (&acc)[9][4], const unsigned (&voff)[9], const char* gW, unsigned ab_e, unsigned ab_o,
                                              const unsigned (&nvoff)[9], const char* nx0, const char* nx1, const Epi& e, PF&& pf) {
  constexpr size_t KS = (size_t)32 * ROWB;
  auto no = [&]() __attribute__((always_inline)) {};
  step<0, 0, true, true, true, true, 9, 0>(s, acc, voff, gW + 3 * KS, ab_o, no);
  step<1, 1, false, true, true, true, 9, 0>(s, acc, voff, gW + 4 * KS, ab_e + 128, pf);
  step<0, 0, false, true, true, true, 9 + NPF, 0>(s, acc, voff, gW + 5 * KS, ab_o + 128, no);
  step<1, 1, false, true, true, true, 9 + NPF, 0>(s, acc, voff, gW + 6 * KS, ab_e + 256, no);
  for (int k = 4; k < NS - 4; k += 2) {
    step<0, 0, false, true, true, true, 9, 0>(s, acc, voff, gW + (size_t)(k + 3) * KS, ab_o + 128 * (k >> 1), no);
    step<1, 1, false, true, true, true, 9, 0>(s, acc, voff, gW + (size_t)(k + 4) * KS, ab_e + 128 * ((k + 2) >> 1), no);
  }
  step<0, 0, false, true, true, true, 9, 0>(s, acc, voff, gW + (size_t)(NS - 1) * KS, ab_o + 128 * ((NS - 4) >> 1), no);
  step<1, 1, false, true, true, NEXT != 0, 9, 0>(s, acc, nvoff, nx0, ab_e + 128 * ((NS - 2) >> 1), no);
  final2<NEXT != 0, NEXT != 0 ? 9 : 0>(s, acc, nvoff, nx1, ab_o + 128 * ((NS - 2) >> 1), e);
}

constexpr unsigned SPIN_CAP = 1u << 17;
DI void publish(u64* flag, u64 tag) {
  asm volatile("s_waitcnt vmcnt(0)" ::: "memory");
  BAR();
  if (threadIdx.x == 0) __hip_atomic_store((gu64*)flag, tag, __ATOMIC_RELAXED, __HIP_MEMORY_SCOPE_AGENT);
}
DI void await8(u64* flags8, u64 tag, unsigned* tmo, int w, int lane) {
  if (w == 0) {
    for (unsigned spins = 0;; ++spins) {
      const u64 v = __hip_atomic_load((gu64*)(flags8 + (lane & 7) * FLAG_STRIDE), __ATOMIC_RELAXED, __HIP_MEMORY_SCOPE_AGENT);
      if (__all(v == tag)) break;
      if (spins > SPIN_CAP) { if (lane == 0) __hip_atomic_store((gu32*)tmo, 1u, __ATOMIC_RELAXED, __HIP_MEMORY_SCOPE_AGENT); break; }
      __builtin_amdgcn_s_sleep(1);
    }
  }
  BAR();
}
template <int NP, int NT> struct ModGather {
  static constexpr int N = (NP + NT - 1) / NT;
  u4 gr[N][2];
  __amdgpu_buffer_rsrc_t rM;
  DI void load(int tid) {
#pragma unroll
    for (int i = 0; i < N; ++i) {
      const int idx = tid + NT * i < NP ? tid + NT * i : (tid & 63), off = (idx >> 5) * 1024 + (idx & 31) * 16;
      gr[i][0] = __builtin_amdgcn_raw_buffer_load_b128(rM, off, 0, 16); gr[i][1] = __builtin_amdgcn_raw_buffer_load_b128(rM, off + 512, 0, 16);
    }
  }
  DI void issue(char* ws, int tid) { rM = __builtin_amdgcn_make_buffer_rsrc(ws + WS_MODP, 0, 54 * 1024, 0x00020000); load(tid); }
  DI void finish(u64 tag, unsigned ldsb, unsigned* tmo, int tid) {
    const unsigned thi = (unsigned)(tag >> 32), tlo = (unsigned)tag;
    for (unsigned spins = 0;; ++spins) {
      bool ok = true;
#pragma unroll
      for (int i = 0; i < N; ++i) ok &= gr[i][0][2] == tlo && gr[i][0][3] == thi && gr[i][1][2] == tlo && gr[i][1][3] == thi;
      if (__all(ok)) break;
      if (spins > SPIN_CAP / 8) { if ((tid & 63) == 0) __hip_atomic_store((gu32*)tmo, 2u, __ATOMIC_RELAXED, __HIP_MEMORY_SCOPE_AGENT); break; }
      __builtin_amdgcn_s_sleep(4);
      asm volatile("" ::: "memory");
      load(tid);
    }
#pragma unroll
    for (int i = 0; i < N; ++i)
      if (tid + NT * i < NP) {
        const int idx = tid + NT * i;
        const f2 m = (f2){__uint_as_float(gr[i][0][0]), __uint_as_float(gr[i][0][1])} + (f2){__uint_as_float(gr[i][1][0]), __uint_as_float(gr[i][1][1])};
        if (idx < D) {
          f2 v = rb2(m);
          if (idx >= D / 2) v = rb2(v + 1.f);
          *(LAS f2*)(size_t)(ldsb + LDS_X1 + 1024 + idx * 8) = v;
        } else *(LAS unsigned*)(size_t)(ldsb + LDS_MOD + idx * 4) = pk(m);
      }
  }
};
DI u4 gated_residual(const u4 (&v)[8], const u4& res, const u4& g) {
  u4 o;
#pragma unroll
  for (int e = 0; e < 4; ++e) {
    f2 s = bf2(v[0][e]);
#pragma unroll
    for (int j = 1; j < 8; ++j) s += bf2(v[j][e]);
    const unsigned t = pk(bf2(pk(s)) * bf2(g[e]));
    o[e] = pk(bf2(res[e]) + bf2(t));
  }
  return o;
}

constexpr float QSCALE = 0.08838834764831845f * 1.4426950408889634f;
constexpr float THR = 40.0f;
struct QS {
  b4 wlo[2][8], whi[2][8];
  b8 hf[2][4];
  unsigned wb[4];
  unsigned hb[4];
  unsigned ring;
};
template <int PAR, bool ZERO, bool RD, bool DMA, int VM, typename PF>
DI void qstep(QS& s, f16v (&acc)[4][2], const unsigned (&voff)[8], const char* gp, unsigned hoff, PF&& pf) {
  const unsigned sl = s.ring + (PAR ^ 1) * QSLOT;
  auto rd = [&](auto ic) __attribute__((always_inline)) {
    constexpr int I = decltype(ic)::value;
    if constexpr (RD) {
      if constexpr (I < 4) {
        s.hf[PAR ^ 1][I] = ldA(s.hb[2 * (PAR ^ 1) + (I >> 1)] + hoff, (I & 1) * 32 * ROWB);
      } else {
        constexpr int J = I - 4, f = J >> 1, t = J & 1, ks = f >> 2, dt = f & 3;
        const b4 v = ldT(s.wb[dt], (PAR ^ 1) * QSLOT + 4096 * ks + 1024 * t);
        if constexpr (t == 0) s.wlo[PAR ^ 1][f] = v; else s.whi[PAR ^ 1][f] = v;
      }
    }
  };
  auto gap = [&](auto gc) __attribute__((always_inline)) {
    constexpr int G = decltype(gc)::value, ks = G >> 3, dt = (G >> 1) & 3, qb = G & 1, f = ks * 4 + dt;
    mfma32<false, ZERO && ks == 0>(acc[dt][qb], cat(s.wlo[PAR][f], s.whi[PAR][f]), s.hf[PAR][ks * 2 + qb]);
    if constexpr (G == 0 && RD) asm volatile("s_waitcnt vmcnt(%0)" :: "i"(VM) : "memory");
    if constexpr (G <= 6) { rd(IC<3 * G>{}); rd(IC<3 * G + 1>{}); if constexpr (3 * G + 2 < 20) rd(IC<3 * G + 2>{}); }
    if constexpr (DMA) {
      if constexpr (G == 9) { LGKM0(); setm0(sl + 4096); }
      if constexpr (G >= 10 && G <= 13) { dma<(2 * (G - 10) - 4) * 1024>(voff[2 * (G - 10)], gp); dma<(2 * (G - 10) + 1 - 4) * 1024>(voff[2 * (G - 10) + 1], gp); }
    }
    if constexpr (G == 15) pf();
    GAP();
  };
  UNROLL(16, gap);
}

struct AT {
  f16v S[2][2][2];
  f16v negm[2];
  f16v O[4][2];
  b8 q[2][8];
  b8 P[2][4];
  float m[2], l[2], rs0[2], rs1[2], fct[2], pm0[2], pm1[2];
  float one;
  unsigned kb_[2], vb_[2];
  unsigned voff[8];
  unsigned m0k, m0v;
  const char *gk, *gv;
  bool pend;
};
template <int MODE> DI void qk(f16v& s, const b8& k, const b8& q, const f16v& negm) {
  if constexpr (MODE == 0) asm volatile("v_mfma_f32_32x32x16_bf16 %0, %1, %2, 0" : "=&v"(s) : "v"(k), "a"(q));
  else if constexpr (MODE == 1) asm volatile("v_mfma_f32_32x32x16_bf16 %0, %1, %2, %3" : "=&v"(s) : "v"(k), "a"(q), "v"(negm));
  else asm volatile("v_mfma_f32_32x32x16_bf16 %0, %1, %2, %0" : "+v"(s) : "v"(k), "a"(q));
}
template <int XP, int KB, int E> DI void sm_exp(AT& a) { constexpr int qb = E >> 4, r = E & 15; a.S[XP][KB][qb][r] = __builtin_amdgcn_exp2f(a.S[XP][KB][qb][r]); }
template <int XP, int KB, int E> DI void sm_add(AT& a) {
  constexpr int qb = E >> 4, r = E & 15;
  if constexpr (r & 1) a.rs1[qb] = __builtin_fmaf(a.S[XP][KB][qb][r], a.one, a.rs1[qb]); else a.rs0[qb] += a.S[XP][KB][qb][r];
}
template <int XP, int C> DI void sm_pack(AT& a) {
  constexpr int kb = C >> 4, qb = (C >> 3) & 1, s = (C >> 2) & 1, p = C & 3;
  u4 w = __builtin_bit_cast(u4, a.P[qb][kb * 2 + s]);
  w[p] = pk(a.S[XP][kb][qb][8 * s + 2 * p], a.S[XP][kb][qb][8 * s + 2 * p + 1]);
  a.P[qb][kb * 2 + s] = __builtin_bit_cast(b8, w);
  if constexpr (p == 3) asm volatile("" : "+v"(a.P[qb][kb * 2 + s]));
}
template <int X, bool FIRST, bool DK, bool DV, typename PF> DI void phaseA(AT& a, int t, PF&& pf) {
  constexpr int XP = X ^ 1;
  const unsigned kslot = X * 16384;
  b8 kf[16];
  auto kread = [&](auto ic) __attribute__((always_inline)) { constexpr int I = decltype(ic)::value, ks = I >> 1, kb = I & 1; kf[I] = ldA(a.kb_[ks & 1], kslot + 8192 * kb + 512 * (ks >> 1)); };
  kread(IC<0>{}); kread(IC<1>{}); kread(IC<2>{}); kread(IC<3>{});
  const char* gk = a.gk + (size_t)(t + 1) * 16384; const char* gv = a.gv + (size_t)t * 16384;
  auto gap = [&](auto gc) __attribute__((always_inline)) {
    constexpr int G = decltype(gc)::value, ks = G >> 2, kb = (G >> 1) & 1, qb = G & 1, I = G >> 1;
    qk<(ks == 0) ? (FIRST ? 0 : 1) : 2>(a.S[X][kb][qb], kf[I], a.q[qb][ks], a.negm[qb]);
    if constexpr ((G & 1) == 0 && I + 4 < 16) kread(IC<I + 4>{});
    if constexpr (!FIRST && G == 8) asm volatile("" :: "v"(a.negm[0]), "v"(a.negm[1]));
    if constexpr (DK) { if constexpr (G == 0) setm0(a.m0k + (X ^ 1) * 16384); if constexpr ((G & 1) && G < 16) dma<((G >> 1) - 4) * 1024>(a.voff[G >> 1], gk); }
    if constexpr (DV) { if constexpr (G == 16) setm0(a.m0v + X * 16384); if constexpr ((G & 1) && G > 16) dma<(((G - 16) >> 1) - 4) * 1024>(a.voff[(G - 16) >> 1], gv); }
    if constexpr (!FIRST) {
      if constexpr (G < 16) { sm_exp<XP, 1, 2 * G>(a); sm_exp<XP, 1, 2 * G + 1>(a); sm_pack<XP, G>(a); }
      else { sm_pack<XP, G>(a); sm_add<XP, 1, 2 * (G - 16)>(a); sm_add<XP, 1, 2 * (G - 16) + 1>(a); }
    }
    GAP();
  };
  UNROLL(32, gap);
  pf();
  if constexpr (!FIRST) {
#pragma unroll
    for (int qb = 0; qb < 2; ++qb) { a.l[qb] += a.rs0[qb] + a.rs1[qb]; asm volatile("" : "+v"(a.l[qb])); }
  }
}
DI float swap32_max(float x) { auto r = __builtin_amdgcn_permlane32_swap(__float_as_uint(x), __float_as_uint(x), false, false); return fmaxx(__uint_as_float(r[0]), __uint_as_float(r[1])); }
template <int X, bool NOPV, bool ZERO> DI void phaseB(AT& a) {
  const unsigned vslot = (X ^ 1) * 16384;
  b4 vlo[16], vhi[16];
  auto vread = [&](auto ic) __attribute__((always_inline)) {
    constexpr int I = decltype(ic)::value, k4 = I >> 2, db = I & 3;
    vlo[I] = ldT(a.vb_[0], vslot + 2048 * (2 * k4) + 512 * db);
    vhi[I] = ldT(a.vb_[1], vslot + 2048 * (2 * k4 + 1) + 512 * db);
  };
  if constexpr (!NOPV) { vread(IC<0>{}); vread(IC<1>{}); vread(IC<2>{}); }
  float rmax[2];
  auto pv = [&](auto gc) __attribute__((always_inline)) {
    constexpr int G = decltype(gc)::value, I = G >> 1, k4 = I >> 2, db = I & 3, qb = G & 1;
    if constexpr (!NOPV) {
      mfma32<true, ZERO && k4 == 0>(a.O[db][qb], cat(vlo[I], vhi[I]), a.P[qb][k4]);
      if constexpr ((G & 1) == 0 && I + 3 < 16) vread(IC<I + 3>{});
    }
  };
  auto gap1 = [&](auto gc) __attribute__((always_inline)) {
    constexpr int G = decltype(gc)::value;
    pv(gc);
    if constexpr (G >= 4 && G < 12) {
      constexpr int i = G - 4, q = i >> 2, r = 4 * (i & 3);
      if constexpr ((i & 3) == 0) { a.pm0[q] = fmax3(a.S[X][0][q][0], a.S[X][0][q][1], a.S[X][1][q][0]); a.pm1[q] = fmax3(a.S[X][0][q][2], a.S[X][0][q][3], a.S[X][1][q][1]);
                                    a.pm0[q] = fmax3(a.pm0[q], a.S[X][1][q][2], a.S[X][1][q][3]); }
      else { a.pm0[q] = fmax3(a.pm0[q], a.S[X][0][q][r], a.S[X][0][q][r + 1]); a.pm1[q] = fmax3(a.pm1[q], a.S[X][0][q][r + 2], a.S[X][0][q][r + 3]);
             a.pm0[q] = fmax3(a.pm0[q], a.S[X][1][q][r], a.S[X][1][q][r + 1]); a.pm1[q] = fmax3(a.pm1[q], a.S[X][1][q][r + 2], a.S[X][1][q][r + 3]); }
    }
    if constexpr (G == 12 || G == 13) { constexpr int q = G - 12; rmax[q] = swap32_max(fmaxx(a.pm0[q], a.pm1[q])); }
    GAP();
  };
  UNROLL(14, gap1);
  if constexpr (NOPV) {
#pragma unroll
    for (int q = 0; q < 2; ++q) {
      a.m[q] = rmax[q]; a.l[q] = 0.f; a.fct[q] = 1.f;
#pragma unroll
      for (int kb = 0; kb < 2; ++kb)
#pragma unroll
        for (int e = 0; e < 16; ++e) a.S[X][kb][q][e] -= rmax[q];
#pragma unroll
      for (int e = 0; e < 16; ++e) a.negm[q][e] = -rmax[q];
      asm volatile("" : "+v"(a.negm[q]));
    }
  } else {
    const bool grew = (rmax[0] > THR) | (rmax[1] > THR);
    if (__builtin_expect(__any((int)grew), 0)) {
#pragma unroll
      for (int q = 0; q < 2; ++q) {
        const float dl = fmaxx(rmax[q], 0.f); a.m[q] += dl; a.fct[q] = __builtin_amdgcn_exp2f(-dl);
#pragma unroll
        for (int kb = 0; kb < 2; ++kb)
#pragma unroll
          for (int e = 0; e < 16; ++e) a.S[X][kb][q][e] -= dl;
#pragma unroll
        for (int e = 0; e < 16; ++e) a.negm[q][e] = -a.m[q];
        asm volatile("" : "+v"(a.negm[q]));
      }
      a.pend = true;
    }
  }
  a.rs0[0] = a.rs0[1] = a.rs1[0] = a.rs1[1] = 0.f;
  GAP();
  auto gap2 = [&](auto gc) __attribute__((always_inline)) {
    constexpr int G = decltype(gc)::value + 14;
    pv(IC<G>{});
    if constexpr (G >= 15) {
      constexpr int e0 = 2 * (G - 15);
      if constexpr (e0 < 32) { sm_exp<X, 0, e0>(a); sm_exp<X, 0, e0 + 1>(a); }
      if constexpr (G >= 16) { sm_add<X, 0, e0 - 2>(a); sm_add<X, 0, e0 - 1>(a); }
    }
    GAP();
  };
  UNROLL(18, gap2);
  asm volatile("" : "+v"(a.S[X][0][0]), "+v"(a.S[X][0][1]), "+v"(a.rs0[0]), "+v"(a.rs0[1]), "+v"(a.rs1[0]), "+v"(a.rs1[1]));
  if constexpr (!NOPV) {
    if (__builtin_expect(a.pend, 0)) {
      asm volatile("s_nop 7\n\ts_nop 7" ::: "memory");
#pragma unroll
      for (int db = 0; db < 4; ++db)
#pragma unroll
        for (int q = 0; q < 2; ++q) {
          asm volatile("" : "+a"(a.O[db][q]));
          f16v o = a.O[db][q];
#pragma unroll
          for (int e = 0; e < 16; ++e) o[e] *= a.fct[q];
          a.O[db][q] = o;
          asm volatile("" : "+a"(a.O[db][q]));
        }
      a.l[0] *= a.fct[0]; a.l[1] *= a.fct[1]; a.pend = false;
      asm volatile("s_nop 1" ::: "memory");
    }
  }
}

template <int X, bool FIRST, bool DK, bool DV, typename PF> DI void fastA(AT& a, int t, PF&& pf) {
  constexpr int XP = X ^ 1;
  const unsigned kslot = X * 16384;
  b8 kf[16];
  auto kread = [&](auto ic) __attribute__((always_inline)) { constexpr int I = decltype(ic)::value, ks = I >> 1, kb = I & 1; kf[I] = ldA(a.kb_[ks & 1], kslot + 8192 * kb + 512 * (ks >> 1)); };
  kread(IC<0>{}); kread(IC<1>{}); kread(IC<2>{}); kread(IC<3>{});
  const char* gk = a.gk + (size_t)(t + 1) * 16384; const char* gv = a.gv + (size_t)t * 16384;
  auto gap = [&](auto gc) __attribute__((always_inline)) {
    constexpr int G = decltype(gc)::value, ks = G >> 2, kb = (G >> 1) & 1, qb = G & 1, I = G >> 1;
    qk<(ks == 0) ? (FIRST ? 0 : 1) : 2>(a.S[X][kb][qb], kf[I], a.q[qb][ks], a.negm[qb]);
    if constexpr ((G & 1) == 0 && I + 4 < 16) kread(IC<I + 4>{});
    if constexpr (!FIRST && G == 8) asm volatile("" :: "v"(a.negm[0]), "v"(a.negm[1]));
    if constexpr (DK) { if constexpr (G == 0) setm0(a.m0k + (X ^ 1) * 16384); if constexpr ((G & 1) && G < 16) dma<((G >> 1) - 4) * 1024>(a.voff[G >> 1], gk); }
    if constexpr (DV) { if constexpr (G == 16) setm0(a.m0v + X * 16384); if constexpr ((G & 1) && G > 16) dma<(((G - 16) >> 1) - 4) * 1024>(a.voff[(G - 16) >> 1], gv); }
    if constexpr (!FIRST) {
      sm_exp<XP, 1, G>(a);
      if constexpr (G >= 1) sm_add<XP, 1, G - 1>(a);
      if constexpr (G >= 3 && (G & 1)) sm_pack<XP, 16 + (G - 3) / 2>(a);
    }
    GAP();
  };
  UNROLL(32, gap);
  pf();
  if constexpr (!FIRST) {
    sm_add<XP, 1, 31>(a); sm_pack<XP, 31>(a);
#pragma unroll
    for (int qb = 0; qb < 2; ++qb) { a.l[qb] += a.rs0[qb] + a.rs1[qb]; asm volatile("" : "+v"(a.l[qb])); }
  }
}
constexpr int fb_e(int G) { return G < 2 ? 0 : G - 2 + (G > 5) + (G > 9); }
constexpr int dr_e(int n) { return n < 8 ? n : n < 16 ? n + 8 : n < 24 ? n - 8 : n; }
template <int X, int MODE> DI void fastB(AT& a) {
  constexpr bool ZERO = MODE == 1, DRAIN = MODE == 3;
  if constexpr (MODE == 2) {
#pragma unroll
    for (int q = 0; q < 2; ++q) {
      float p0 = fmax3(a.S[X][0][q][0], a.S[X][1][q][0], a.S[X][0][q][2]), p1 = fmax3(a.S[X][0][q][1], a.S[X][1][q][1], a.S[X][0][q][3]);
      p0 = fmaxx(p0, a.S[X][1][q][2]); p1 = fmaxx(p1, a.S[X][1][q][3]);
#pragma unroll
      for (int r = 4; r < 16; r += 2) { p0 = fmax3(p0, a.S[X][0][q][r], a.S[X][1][q][r]); p1 = fmax3(p1, a.S[X][0][q][r + 1], a.S[X][1][q][r + 1]); }
      const float mx = swap32_max(fmaxx(p0, p1));
      f2 m2 = {mx, mx}; asm volatile("" : "+v"(m2));
      a.l[q] = 0.f;
#pragma unroll
      for (int kb = 0; kb < 2; ++kb)
#pragma unroll
        for (int e = 0; e < 16; e += 2) { const f2 v = (f2){a.S[X][kb][q][e], a.S[X][kb][q][e + 1]} - m2; a.S[X][kb][q][e] = v[0]; a.S[X][kb][q][e + 1] = v[1]; }
#pragma unroll
      for (int e = 0; e < 16; ++e) a.negm[q][e] = -mx;
      asm volatile("" : "+v"(a.negm[q]));
    }
    a.rs0[0] = a.rs0[1] = a.rs1[0] = a.rs1[1] = 0.f;
    { auto f = [&](auto ic) __attribute__((always_inline)) { sm_exp<X, 0, decltype(ic)::value>(a); }; UNROLL(32, f); }
    { auto f = [&](auto ic) __attribute__((always_inline)) { sm_add<X, 0, decltype(ic)::value>(a); }; UNROLL(32, f); }
    { auto f = [&](auto ic) __attribute__((always_inline)) { sm_pack<X, decltype(ic)::value>(a); }; UNROLL(16, f); }
  } else {
    const unsigned vslot = (DRAIN ? X : X ^ 1) * 16384;
    b4 vlo[16], vhi[16];
    auto vread = [&](auto ic) __attribute__((always_inline)) {
      constexpr int I = decltype(ic)::value, k4 = I >> 2, db = I & 3;
      vlo[I] = ldT(a.vb_[0], vslot + 2048 * (2 * k4) + 512 * db);
      vhi[I] = ldT(a.vb_[1], vslot + 2048 * (2 * k4 + 1) + 512 * db);
    };
    vread(IC<0>{}); vread(IC<1>{}); vread(IC<2>{});
    if constexpr (!DRAIN) a.rs0[0] = a.rs0[1] = a.rs1[0] = a.rs1[1] = 0.f;
    auto gap = [&](auto gc) __attribute__((always_inline)) {
      constexpr int G = decltype(gc)::value, I = G >> 1, k4 = I >> 2, db = I & 3, qb = G & 1;
      mfma32<true, ZERO && k4 == 0>(a.O[db][qb], cat(vlo[I], vhi[I]), a.P[qb][k4]);
      if constexpr ((G & 1) == 0 && I + 3 < 16) vread(IC<I + 3>{});
      if constexpr (DRAIN) {
        if constexpr (G < 16) { sm_exp<X, 1, dr_e(2 * G)>(a); sm_exp<X, 1, dr_e(2 * G) + 1>(a); }
        if constexpr (G >= 2 && G < 18) sm_pack<X, 16 + dr_e(2 * (G - 2)) / 2>(a);
        if constexpr (G >= 16) { sm_add<X, 1, dr_e(2 * (G - 16))>(a); sm_add<X, 1, dr_e(2 * (G - 16)) + 1>(a); }
      } else {
        if constexpr (fb_e(G) < fb_e(G + 1)) sm_exp<X, 0, fb_e(G)>(a);
        if constexpr (fb_e(G) + 1 < fb_e(G + 1)) sm_exp<X, 0, fb_e(G) + 1>(a);
        if constexpr (G >= 3 && fb_e(G - 1) < fb_e(G)) sm_add<X, 0, G >= 3 ? fb_e(G - 1) : 0>(a);
        if constexpr (G >= 3 && fb_e(G - 1) + 1 < fb_e(G)) sm_add<X, 0, G >= 3 ? fb_e(G - 1) + 1 : 0>(a);
        if constexpr (G >= 16 && G <= 29) sm_pack<X, G >= 16 ? G - 16 : 0>(a);
        if constexpr (G == 31) sm_pack<X, 14>(a);
      }
      GAP();
    };
    UNROLL(32, gap);
    if constexpr (!DRAIN) {
      sm_add<X, 0, 31>(a);
      static_assert(fb_e(32) == 32 && fb_e(28) >= 28 && fb_e(30) >= 30, "schedule");
      sm_pack<X, 15>(a);
      asm volatile("" : "+v"(a.S[X][0][0]), "+v"(a.S[X][0][1]), "+v"(a.rs0[0]), "+v"(a.rs0[1]), "+v"(a.rs1[0]), "+v"(a.rs1[1]));
    } else {
#pragma unroll
      for (int qb = 0; qb < 2; ++qb) a.l[qb] += a.rs0[qb] + a.rs1[qb];
    }
  }
}

struct Params { const u16 *x, *c, *wmod, *wq, *kc, *vc, *wo, *wg, *wu, *wd; u16* out; char* ws; float one; unsigned gap; };

__global__ __launch_bounds__(256, 1) void k_main(Params p) {
  extern __shared__ __attribute__((aligned(16))) char lds[];
  const int tid = threadIdx.x, w = __builtin_amdgcn_readfirstlane(tid >> 6), lane = tid & 63;
  const int j = blockIdx.x & 7, c = blockIdx.x >> 3;
  const unsigned ldsb = (unsigned)(size_t)lds;
  const int hh = lane >> 5, r32 = lane & 31;
  u64* const flags = (u64*)(p.ws + WS_FLAG);
  unsigned* const tmo = (unsigned*)(p.ws + WS_TMO);
  STAMP(0);
  PST(0);
  QS qs;
  unsigned voffQ[8];
#pragma unroll
  for (int i = 0; i < 8; ++i) voffQ[i] = (4 * i + (lane >> 4)) * 8192 + 16 * ((lane & 15) ^ ((lane >> 4) << 2)) - (i - 4) * 1024 + 4096;
  const char* gq = (const char*)p.wq + (size_t)(4 * j + w) * 256 - 4096;
  qs.ring = __builtin_amdgcn_readfirstlane(ldsb + ABYTES + w * 2 * QSLOT);
  constexpr size_t KS_Q = (size_t)32 * 8192;
  auto issueq = [&](int slot, const char* gp) __attribute__((always_inline)) {
    setm0(qs.ring + slot * QSLOT + 4096); asm volatile("s_nop 0");
    dma<-4096>(voffQ[0], gp); dma<-3072>(voffQ[1], gp); dma<-2048>(voffQ[2], gp); dma<-1024>(voffQ[3], gp);
    dma<0>(voffQ[4], gp); dma<1024>(voffQ[5], gp); dma<2048>(voffQ[6], gp); dma<3072>(voffQ[7], gp);
  };
  const u64 e0 = __hip_atomic_load((gu64*)(p.ws + WS_EPOCH), __ATOMIC_RELAXED, __HIP_MEMORY_SCOPE_AGENT);
  AT a;
  u64 tag;
  {
    f16v Qt[4][2];
    QS& s = qs;
    {
      const int rr = lane >> 3, sub = lane & 7;
      if (c >= 16) {
        const u64 t0_ = __builtin_amdgcn_s_memrealtime();
        const int mb = blockIdx.x - 128, mg = mb % 54, ms = (mb / 54) & 1;
        u4 wm[9]; u16 cval[9];
#pragma unroll
        for (int i = 0; i < 9; ++i) {
          const int r = 288 * ms + (tid >> 3) + 32 * i;
          wm[i] = *(const u4*)(p.wmod + (size_t)r * (6 * D) + mg * 64 + (tid & 7) * 8); cval[i] = p.c[r];
        }
        GAP();
        const u64 tag = ((u64)(unsigned)__builtin_amdgcn_readfirstlane((unsigned)(e0 >> 32)) << 32 | (u64)(unsigned)__builtin_amdgcn_readfirstlane((unsigned)e0)) + 1;
        if (mb < 108) {
          float acc[8] = {};
#pragma unroll
          for (int i = 0; i < 9; ++i) {
            const float xc = bf2f(cval[i]);
            const float sc = rbf(xc * __builtin_amdgcn_rcpf(1.f + __expf(-xc)));
#pragma unroll
            for (int e = 0; e < 4; ++e) { acc[2 * e] += sc * bflo(wm[i][e]); acc[2 * e + 1] += sc * bfhi(wm[i][e]); }
          }
          LAS float* red = (LAS float*)(size_t)(ldsb + LDS_X1);
#pragma unroll
          for (int e = 0; e < 8; ++e) { float t = acc[e]; t += dpp<0x128>(t); t = swap32_add(swap16_add(t)); if (lane < 8) red[w * 64 + lane * 8 + e] = t; }
          asm volatile("s_waitcnt lgkmcnt(0)" ::: "memory");
          BAR();
          if (tid < 32) {
            f2 v = {0.f, 0.f};
#pragma unroll
            for (int k = 0; k < 4; ++k) v += *(LAS f2*)(red + k * 64 + 2 * tid);
            const __amdgpu_buffer_rsrc_t rP = __builtin_amdgcn_make_buffer_rsrc(p.ws + WS_MODP, 0, 54 * 1024, 0x00020000);
            __builtin_amdgcn_raw_buffer_store_b128((u4){__float_as_uint(v[0]), __float_as_uint(v[1]), (unsigned)tag, (unsigned)(tag >> 32)}, rP, mg * 1024 + ms * 512 + tid * 16, 0, 16);
          }
        }
        const unsigned dummy = __builtin_amdgcn_readfirstlane(ldsb + LDS_DUMMY);
        while (__builtin_amdgcn_s_memrealtime() - t0_ < PF_D2) __builtin_amdgcn_s_sleep(2);
        const int li = tid < 144 ? tid : tid - 112;
        touch((const char*)p.wq + (size_t)512 * j * 2, (18 * (c - 16) + (li >> 3)) * 8192 + (li & 7) * 128, dummy);
        touch((const char*)p.wq + (size_t)512 * j * 2, (18 * c + (li >> 3)) * 8192 + (li & 7) * 128, dummy);
        touch((const char*)(w < 2 ? p.kc : p.vc) + ((size_t)(2 * j + (w & 1)) * SK + (SK - WIN)) * DH * 2, (32 * (c - 16 * (lane >> 5)) + (lane & 31)) * 128, dummy);
        while (__builtin_amdgcn_s_memrealtime() - t0_ < STAG_D2) __builtin_amdgcn_s_sleep(2);
      }
      PST(10);
      PST(11);
      u4 xv[2][9];
      GAP();
#pragma unroll
      for (int ps = 0; ps < 2; ++ps)
#pragma unroll
        for (int i = 0; i < 9; ++i) xv[ps][i] = *(const u4*)(p.x + (size_t)(c * 64 + 16 * w + 8 * ps + rr) * D + (sub + 8 * i) * 8);
      GAP();
      tag = ((u64)(unsigned)__builtin_amdgcn_readfirstlane((unsigned)(e0 >> 32)) << 32 | (u64)(unsigned)__builtin_amdgcn_readfirstlane((unsigned)e0)) + 1;
      PST(1);
      STAMP(20);
      PST(2);
      f2 y[2][9][4];
      ModGather<1728, 256> mgat;
      {
#pragma unroll
        for (int ps = 0; ps < 2; ++ps) {
          f2 a1 = {0.f, 0.f}, a2 = {0.f, 0.f};
#pragma unroll
          for (int i = 0; i < 9; ++i)
#pragma unroll
            for (int e = 0; e < 4; ++e) { y[ps][i][e] = bf2(xv[ps][i][e]); a1 += y[ps][i][e]; a2 = __builtin_elementwise_fma(y[ps][i][e], y[ps][i][e], a2); }
          float s1 = a1[0] + a1[1], s2 = a2[0] + a2[1];
          s1 = sum8(s1); s2 = sum8(s2);
          const float mean = s1 * (1.f / D), var = __builtin_fmaxf(__builtin_fmaf(-mean, mean, s2 * (1.f / D)), 0.f);
          const float mr = rbf(mean), rs = __builtin_amdgcn_rcpf(rbf(__builtin_amdgcn_sqrtf(rbf(rbf(var) + 1e-5f))));
          f2 mr2 = {mr, mr}, rs2 = {rs, rs}; asm volatile("" : "+v"(mr2), "+v"(rs2));
          if (ps == 1) { GAP(); mgat.issue(p.ws, tid); GAP(); }
#pragma unroll
          for (int i = 0; i < 9; ++i) {
            f2 t[4];
#pragma unroll
            for (int e = 0; e < 4; ++e) t[e] = y[ps][i][e] - mr2;
#pragma unroll
            for (int e = 0; e < 4; ++e) t[e] = rb2(t[e]);
#pragma unroll
            for (int e = 0; e < 4; ++e) t[e] = t[e] * rs2;
#pragma unroll
            for (int e = 0; e < 4; ++e) y[ps][i][e] = rb2(t[e]);
            asm volatile("" : "+v"(y[ps][i][0]), "+v"(y[ps][i][1]), "+v"(y[ps][i][2]), "+v"(y[ps][i][3]));
          }
        }
      }
      STAMP(21);
      PST(4);
      mgat.finish(tag, ldsb, tmo, tid);
      STAMP(22);
      PST(5);
      issueq(0, gq); issueq(1, gq + KS_Q);
      asm volatile("s_waitcnt lgkmcnt(0)" ::: "memory");
      BAR();
      STAMP(23);
      PST(6);
      {
        const unsigned tb = ldsb + LDS_X1 + 1024 + sub * 32;
        f4 cst[2][4];
        auto ldc = [&](int i, f4 (&d)[4]) { d[0] = *(LAS f4*)(size_t)(tb + i * 256); d[1] = *(LAS f4*)(size_t)(tb + i * 256 + 16); d[2] = *(LAS f4*)(size_t)(tb + i * 256 + D * 4); d[3] = *(LAS f4*)(size_t)(tb + i * 256 + D * 4 + 16); };
        ldc(0, cst[0]);
        const unsigned ad0 = ldsb + (16 * w + rr) * ROWB + ((sub ^ ((rr >> 1) & 7)) * 16);
        const unsigned ad1 = ldsb + (16 * w + 8 + rr) * ROWB + ((sub ^ (((rr >> 1) + 4) & 7)) * 16);
#pragma unroll
        for (int i = 0; i < 9; ++i) {
          if (i + 1 < 9) ldc(i + 1, cst[(i + 1) & 1]);
          GAP();
          const f4 (&k)[4] = cst[i & 1];
          const f2 sh[4] = {{k[0][0], k[0][1]}, {k[0][2], k[0][3]}, {k[1][0], k[1][1]}, {k[1][2], k[1][3]}}, cc[4] = {{k[2][0], k[2][1]}, {k[2][2], k[2][3]}, {k[3][0], k[3][1]}, {k[3][2], k[3][3]}};
          f2 t[2][4]; u4 o[2];
#pragma unroll
          for (int ps = 0; ps < 2; ++ps)
#pragma unroll
            for (int e = 0; e < 4; ++e) t[ps][e] = y[ps][i][e] * cc[e];
#pragma unroll
          for (int ps = 0; ps < 2; ++ps)
#pragma unroll
            for (int e = 0; e < 4; ++e) t[ps][e] = rb2(t[ps][e]);
#pragma unroll
          for (int ps = 0; ps < 2; ++ps)
#pragma unroll
            for (int e = 0; e < 4; ++e) t[ps][e] = t[ps][e] + sh[e];
#pragma unroll
          for (int ps = 0; ps < 2; ++ps)
#pragma unroll
            for (int e = 0; e < 4; ++e) o[ps][e] = pk(t[ps][e]);
          *(LAS u4*)(size_t)(ad0 + i * 128) = o[0];
          *(LAS u4*)(size_t)(ad1 + i * 128) = o[1];
          GAP();
        }
      }
    }
    {
      const int blk = (lane >> 4) & 1, q = (lane & 15) >> 2, pp = lane & 3;
#pragma unroll
      for (int dt = 0; dt < 4; ++dt) s.wb[dt] = s.ring + (8 * hh + q) * 256 + 64 * (dt ^ q) + 32 * blk + 16 * (pp & 1) + 8 * (pp >> 1);
#pragma unroll
      for (int i = 0; i < 4; ++i) s.hb[i] = ldsb + r32 * ROWB + (((2 * i + hh) ^ ((r32 >> 1) & 7)) * 16);
    }
    asm volatile("s_waitcnt vmcnt(8) lgkmcnt(0)" ::: "memory");
    BAR();
    STAMP(1);
    PST(7);
#pragma unroll
    for (int f = 0; f < 8; ++f) { s.wlo[0][f] = ldT(s.wb[f & 3], 4096 * (f >> 2)); s.whi[0][f] = ldT(s.wb[f & 3], 4096 * (f >> 2) + 1024); }
#pragma unroll
    for (int i = 0; i < 4; ++i) s.hf[0][i] = ldA(s.hb[i >> 1], (i & 1) * 32 * ROWB);
    asm volatile("s_waitcnt lgkmcnt(0)" ::: "memory");
    issueq(0, gq + 2 * KS_Q);
    auto no = [&]() __attribute__((always_inline)) {};
    qstep<0, true, true, true, 8>(s, Qt, voffQ, gq + 3 * KS_Q, 0, no);
    qstep<1, false, true, true, 8>(s, Qt, voffQ, gq + 4 * KS_Q, 128, no);
    for (int n = 2; n < 14; n += 2) {
      qstep<0, false, true, true, 8>(s, Qt, voffQ, gq + (size_t)(n + 3) * KS_Q, 128 * ((n + 1) >> 1), no);
      qstep<1, false, true, true, 8>(s, Qt, voffQ, gq + (size_t)(n + 4) * KS_Q, 128 * ((n + 2) >> 1), no);
    }
    qstep<0, false, true, true, 8>(s, Qt, voffQ, gq + (size_t)17 * KS_Q, 128 * 7, no);
    qstep<1, false, true, false, 8>(s, Qt, voffQ, gq, 128 * 8, no);
    qstep<0, false, true, false, 0>(s, Qt, voffQ, gq, 128 * 8, no);
    qstep<1, false, false, false, 0>(s, Qt, voffQ, gq, 0, no);
    STAMP(2);
    PST(8);
    {
      const int kvl = w >> 1, half = w & 1;
      const unsigned kr = ldsb + kvl * 65536, vr = kr + 32768;
#pragma unroll
      for (int i = 0; i < 2; ++i) a.kb_[i] = kr + 2048 * (r32 >> 3) + 64 * (r32 & 7) + 16 * ((2 * i + hh) ^ ((r32 >> 2) & 3));
      const int blk = (lane >> 4) & 1, q = (lane & 15) >> 2, pp = lane & 3;
#pragma unroll
      for (int t = 0; t < 2; ++t) a.vb_[t] = vr + 64 * (4 * hh + q) + 16 * ((2 * blk + (pp >> 1)) ^ ((2 * t + hh) & 3)) + 8 * (pp & 1);
      const int st = lane >> 5, r7 = (lane >> 2) & 7, pos = lane & 3;
#pragma unroll
      for (int i = 0; i < 8; ++i) {
        const int rg = 4 * half + (i >> 1);
        a.voff[i] = rg * 2048 + r7 * 256 + (i & 1) * 128 + st * 64 + 16 * (pos ^ ((2 * (rg & 1) + (r7 >> 2)) & 3)) - (i - 4) * 1024 + 4096;
      }
      a.m0k = __builtin_amdgcn_readfirstlane(kr + 8192 * half + 4096); a.m0v = __builtin_amdgcn_readfirstlane(vr + 8192 * half + 4096);
      const size_t kvoff = ((size_t)(2 * j + kvl) * SK + (SK - WIN)) * DH * 2;
      a.gk = (const char*)p.kc + kvoff - 4096; a.gv = (const char*)p.vc + kvoff - 4096;
      a.pend = false; a.one = p.one;
    }
    asm volatile("s_waitcnt lgkmcnt(0)" ::: "memory");
    BAR();
    setm0(a.m0k); asm volatile("s_nop 0");
    dma<-4096>(a.voff[0], a.gk); dma<-3072>(a.voff[1], a.gk); dma<-2048>(a.voff[2], a.gk); dma<-1024>(a.voff[3], a.gk);
    dma<0>(a.voff[4], a.gk); dma<1024>(a.voff[5], a.gk); dma<2048>(a.voff[6], a.gk); dma<3072>(a.voff[7], a.gk);
    asm volatile("s_nop 7" ::: "memory");
#pragma unroll
    for (int dt = 0; dt < 4; ++dt)
#pragma unroll
      for (int qb = 0; qb < 2; ++qb) {
        asm volatile("" : "+v"(Qt[dt][qb]));
#pragma unroll
        for (int s2 = 0; s2 < 2; ++s2) {
          u4 w4;
#pragma unroll
          for (int e = 0; e < 4; ++e) w4[e] = pk((f2){Qt[dt][qb][8 * s2 + 2 * e], Qt[dt][qb][8 * s2 + 2 * e + 1]} * QSCALE);
          a.q[qb][2 * dt + s2] = __builtin_bit_cast(b8, w4);
          asm volatile("" : "+a"(a.q[qb][2 * dt + s2]));
        }
      }
  }
  STAMP(3);
  {
    auto tile_bar = [&]() __attribute__((always_inline)) { asm volatile("s_waitcnt vmcnt(0) lgkmcnt(0)" ::: "memory"); BAR(); };
    const char* pfp = lane >= 32 ? p.ws + WS_X + (size_t)c * 64 * SLICE + (size_t)j * SLICE + (18 * w + lane - 32) * 128
                    : w < 2 ? (const char*)(w == 0 ? p.wg : p.wu) + (size_t)576 * j * 2 + (size_t)(18 * c + lane / 9 - 3) * (DF * 2) + (lane % 9) * 128
                            : (const char*)p.wo + (size_t)512 * j * ROWB + (16 * c) * ROWB + (12 * (w - 2) + lane - 24) * 128;
    const int pfs = lane >= 32 ? 8 * SLICE : w < 2 ? 3 * DF * 2 : 24 * 128, pfn = w < 2 ? 27 : 12;
    auto pft = [&](int t) __attribute__((always_inline)) {
      const bool on = lane < pfn && t >= 1 && t <= 6;
      touchp(on ? pfp : (const char*)p.wo, __builtin_amdgcn_readfirstlane(ldsb + LDS_DUMMY));
      pfp += pfs;
    };
    auto tile_bar1 = [&]() __attribute__((always_inline)) { asm volatile("s_waitcnt vmcnt(1) lgkmcnt(0)" ::: "memory"); BAR(); };
    LAS unsigned* flag = (LAS unsigned*)(size_t)(ldsb + LDS_X1);
    if (tid == 0) *flag = 0u;
    auto k0 = [&]() __attribute__((always_inline)) {
      setm0(a.m0k); asm volatile("s_nop 0");
      dma<-4096>(a.voff[0], a.gk); dma<-3072>(a.voff[1], a.gk); dma<-2048>(a.voff[2], a.gk); dma<-1024>(a.voff[3], a.gk);
      dma<0>(a.voff[4], a.gk); dma<1024>(a.voff[5], a.gk); dma<2048>(a.voff[6], a.gk); dma<3072>(a.voff[7], a.gk);
    };
    tile_bar(); fastA<0, true, true, true>(a, 0, [&]() __attribute__((always_inline)) { pft(0); }); asm volatile("s_nop 7\n\ts_nop 3" ::: "memory"); GAP(); fastB<0, 2>(a);
    tile_bar1(); fastA<1, false, true, true>(a, 1, [&]() __attribute__((always_inline)) { pft(1); }); fastB<1, 1>(a);
    for (int t = 2; t < 6; t += 2) {
      tile_bar1(); fastA<0, false, true, true>(a, t, [&]() __attribute__((always_inline)) { pft(t); }); fastB<0, 0>(a);
      tile_bar1(); fastA<1, false, true, true>(a, t + 1, [&]() __attribute__((always_inline)) { pft(t + 1); }); fastB<1, 0>(a);
    }
    tile_bar1(); fastA<0, false, true, true>(a, 6, [&]() __attribute__((always_inline)) { pft(6); }); fastB<0, 0>(a);
    tile_bar1(); fastA<1, false, false, true>(a, 7, [&]() __attribute__((always_inline)) { pft(7); }); fastB<1, 0>(a);
    tile_bar1();
    fastB<1, 3>(a);
    {
      const bool bad = !(a.l[0] < 0x1p40f) || !(a.l[1] < 0x1p40f);
      if (__any((int)bad) && lane == 0) *flag = 1u;
      asm volatile("s_waitcnt lgkmcnt(0)" ::: "memory"); BAR();
      unsigned redo = __builtin_amdgcn_readfirstlane(*(volatile LAS unsigned*)flag); asm volatile("" : "+s"(redo));
      if (__builtin_expect(redo != 0u, 0)) {
        auto nopf = [&]() __attribute__((always_inline)) {};
        a.pend = false;
        auto upt = [&](const char* q) __attribute__((always_inline)) { const u64 v = (u64)q; return (const char*)((u64)(unsigned)__builtin_amdgcn_readfirstlane((unsigned)(v >> 32)) << 32 | (u64)(unsigned)__builtin_amdgcn_readfirstlane((unsigned)v)); };
        a.gk = upt(a.gk); a.gv = upt(a.gv); a.m0k = __builtin_amdgcn_readfirstlane(a.m0k); a.m0v = __builtin_amdgcn_readfirstlane(a.m0v);
        k0();
        tile_bar(); phaseA<0, true, true, true>(a, 0, nopf); asm volatile("s_nop 7\n\ts_nop 3" ::: "memory"); GAP(); phaseB<0, true, false>(a);
        tile_bar(); phaseA<1, false, true, true>(a, 1, nopf); phaseB<1, false, true>(a);
        for (int t = 2; t < 6; t += 2) {
          tile_bar(); phaseA<0, false, true, true>(a, t, nopf); phaseB<0, false, false>(a);
          tile_bar(); phaseA<1, false, true, true>(a, t + 1, nopf); phaseB<1, false, false>(a);
        }
        tile_bar(); phaseA<0, false, true, true>(a, 6, nopf); phaseB<0, false, false>(a);
        tile_bar(); phaseA<1, false, false, true>(a, 7, nopf); phaseB<1, false, false>(a);
        { auto f1 = [&](auto ic) __attribute__((always_inline)) { sm_exp<1, 1, decltype(ic)::value>(a); }; UNROLL(32, f1);
          auto f2_ = [&](auto ic) __attribute__((always_inline)) { sm_pack<1, decltype(ic)::value>(a); }; UNROLL(32, f2_);
          auto f3 = [&](auto ic) __attribute__((always_inline)) { sm_add<1, 1, decltype(ic)::value>(a); }; UNROLL(32, f3); }
#pragma unroll
        for (int qb = 0; qb < 2; ++qb) a.l[qb] += a.rs0[qb] + a.rs1[qb];
        tile_bar();
        const unsigned vslot = 16384;
        b4 vlo[16], vhi[16];
#pragma unroll
        for (int I = 0; I < 16; ++I) { vlo[I] = ldT(a.vb_[0], vslot + 2048 * (2 * (I >> 2)) + 512 * (I & 3)); vhi[I] = ldT(a.vb_[1], vslot + 2048 * (2 * (I >> 2) + 1) + 512 * (I & 3)); }
        asm volatile("s_nop 1" ::: "memory"); GAP();
#pragma unroll
        for (int G = 0; G < 32; ++G) { const int I = G >> 1; mfma32<true, false>(a.O[I & 3][G & 1], cat(vlo[I], vhi[I]), a.P[G & 1][I >> 2]); }
        asm volatile("s_waitcnt vmcnt(0) lgkmcnt(0)" ::: "memory"); BAR();
      }
    }
  }
  STAMP(4);
  int tid2 = threadIdx.x; asm volatile("" : "+v"(tid2));
  {
  const int tid = tid2, lane = tid2 & 63, hh = lane >> 5, r32 = lane & 31;
  GS s;
  unsigned voffB[9];
#pragma unroll
  for (int i = 0; i < 9; ++i) { const int idx = i * 64 + lane, imm = i < 8 ? (i - 4) * 1024 : 0; voffB[i] = (idx / 18) * ROWB + (idx % 18) * 16 - imm + 4096; }
  s.ring = __builtin_amdgcn_readfirstlane(ldsb + ABYTES + w * 2 * SLOT);
  const int g4 = lane >> 4, cl = lane & 15;
  s.wbase = s.ring + (4 * g4 + ((lane & 15) >> 2)) * 288 + 8 * (lane & 3);
  const unsigned sw = (cl >> 1) & 7;
  const unsigned ab_e = ldsb + cl * ROWB + ((g4 ^ sw) * 16), ab_o = ldsb + cl * ROWB + (((4 + g4) ^ sw) * 16);
  const __amdgpu_buffer_rsrc_t rX = __builtin_amdgcn_make_buffer_rsrc(p.ws + WS_X + (size_t)c * 64 * SLICE, 0, 64 * SLICE, 0x00020000);
  unsigned voffA[9];
#pragma unroll
  for (int i = 0; i < 9; ++i) { const int idx = i * 64 + lane, imm = i < 8 ? (i - 4) * 1024 : 0; voffA[i] = (idx / 18) * (DF * 2) + (idx % 18) * 16 - imm + 4096; }
  const char* gG = (const char*)p.wg + (size_t)(576 * j + 144 * w) * 2 - 4096;
  const char* gU = (const char*)p.wu + (size_t)(576 * j + 144 * w) * 2 - 4096;
  const char* gD = (const char*)p.wd + (size_t)(576 * j) * ROWB + 288 * w - 4096;
  f4 aG[9][4];
  {
    const char* gO = (const char*)p.wo + (size_t)(512 * j) * ROWB + 288 * w - 4096;
    issue_all(s, 0, voffB, gO); issue_all(s, 1, voffB, gO + (size_t)32 * ROWB);
    asm volatile("s_nop 7" ::: "memory");
#pragma unroll
    for (int qb = 0; qb < 2; ++qb) {
      const float il = __builtin_amdgcn_rcpf(swap32_add(a.l[qb]));
      const int row = 32 * qb + r32; const unsigned swr = (r32 >> 1) & 7;
#pragma unroll
      for (int db = 0; db < 4; ++db) {
        asm volatile("" : "+a"(a.O[db][qb]));
        const f16v o = a.O[db][qb];
#pragma unroll
        for (int u = 0; u < 2; ++u) {
          const u4 wv = {pk((f2){o[4 * u], o[4 * u + 1]} * il), pk((f2){o[4 * u + 2], o[4 * u + 3]} * il), pk((f2){o[4 * u + 8], o[4 * u + 9]} * il), pk((f2){o[4 * u + 10], o[4 * u + 11]} * il)};
          *(LAS u4*)(size_t)(ldsb + row * ROWB + (((4 * (4 * w + db) + 2 * u + hh) ^ swr) * 16)) = wv;
        }
      }
    }
    asm volatile("s_waitcnt vmcnt(9) lgkmcnt(0)" ::: "memory");
    BAR();
    STAMP(5);
    read_w(s, 0); read_a(s, 0, ab_e);
    asm volatile("s_waitcnt lgkmcnt(0)" ::: "memory");
    issue_all(s, 0, voffB, gO + (size_t)2 * 32 * ROWB);
    const Epi e1 = {rX, (unsigned)(j * SLICE), 8u * SLICE, ldsb + LDS_X1, j, w, lane};
    gemm_rows<16, 2, 0>(s, aG, voffB, gO, ab_e, ab_o, voffA, gG, gU, e1, [&]() __attribute__((always_inline)) {});
    STAMP(6);
  }
  {
    int zlr[3], zcc[3];
    const int t3 = tid < 192 ? tid : tid - 64;
    u4 xr[3], gv[3];
#pragma unroll
    for (int i = 0; i < 3; ++i) {
      zdecode(t3 + 192 * i, zlr[i], zcc[i]);
      xr[i] = *(const u4*)(p.x + (size_t)(c * 64 + j * 8 + zlr[i]) * D + zcc[i] * 8);
      gv[i] = *(LAS u4*)(size_t)(ldsb + LDS_MOD + (2 * D + zcc[i] * 8) * 2);
    }
    const int lrow = 2 * w + hh, grow = j * 8 + lrow;
    u2 cbv[3][2], sbv[3][2];
#pragma unroll
    for (int i = 0; i < 3; ++i) {
      const int pos = (i < 2 || r32 < 8) ? r32 + 32 * i : r32;
      const int q = pos ^ ((grow >> 1) & 7), k0 = 32 * (q >> 2) + 4 * (q & 3);
#pragma unroll
      for (int h = 0; h < 2; ++h) { cbv[i][h] = *(LAS u2*)(size_t)(ldsb + LDS_MOD + (4 * D + k0 + 16 * h) * 2); sbv[i][h] = *(LAS u2*)(size_t)(ldsb + LDS_MOD + (3 * D + k0 + 16 * h) * 2); }
    }
    asm volatile("s_waitcnt lgkmcnt(0)" ::: "memory");
    publish(flags + (0 * 256 + blockIdx.x) * FLAG_STRIDE, tag);
    STAMP(7);
    await8(flags + (0 * 256 + c * 8) * FLAG_STRIDE, tag, tmo, w, lane);
    STAMP(8);
    {
      u4 v[3][8];
#pragma unroll
      for (int i = 0; i < 3; ++i) {
#pragma unroll
        for (int jj = 0; jj < 7; ++jj) v[i][jj] = __builtin_amdgcn_raw_buffer_load_b128(rX, (j * 8 + jj + (jj >= j)) * SLICE + (t3 + 192 * i) * 16, 0, AUX_X);
      }
#pragma unroll
      for (int i = 0; i < 3; ++i) v[i][7] = *(LAS u4*)(size_t)(ldsb + LDS_X1 + (t3 + 192 * i) * 16);
#pragma unroll
      for (int i = 0; i < 3; ++i) {
        const u4 x1 = gated_residual(v[i], xr[i], gv[i]);
        if (tid < 192) {
          *(LAS u4*)(size_t)(ldsb + LDS_X1 + (tid + 192 * i) * 16) = x1;
          LAS f4* dst = (LAS f4*)(size_t)(ldsb + (zlr[i] * D + zcc[i] * 8) * 4);
          const f2 a0 = bf2(x1[0]), a1 = bf2(x1[1]), a2 = bf2(x1[2]), a3 = bf2(x1[3]);
          dst[0] = (f4){a0[0], a0[1], a1[0], a1[1]}; dst[1] = (f4){a2[0], a2[1], a3[0], a3[1]};
        }
      }
    }
    asm volatile("s_waitcnt lgkmcnt(0)" ::: "memory");
    BAR();
    STAMP(9);
    {
      f4 v[3][2];
      f2 sum = {0.f, 0.f};
#pragma unroll
      for (int i = 0; i < 3; ++i) {
        const bool on = (i < 2 || r32 < 8);
        const int pos = on ? r32 + 32 * i : r32;
        const int q = pos ^ ((grow >> 1) & 7), k0 = 32 * (q >> 2) + 4 * (q & 3);
#pragma unroll
        for (int h = 0; h < 2; ++h) {
          v[i][h] = *(LAS f4*)(size_t)(ldsb + (lrow * D + k0 + 16 * h) * 4);
          if (on) sum += (f2){v[i][h][0] + v[i][h][1], v[i][h][2] + v[i][h][3]};
        }
      }
      float sm = sum[0] + sum[1];
      sm = sum32(sm);
      const float mean = sm * (1.f / D);
      float q2 = 0.f;
#pragma unroll
      for (int i = 0; i < 3; ++i)
        if (i < 2 || r32 < 8) {
#pragma unroll
          for (int h = 0; h < 2; ++h)
#pragma unroll
            for (int e = 0; e < 4; ++e) { const float dl = v[i][h][e] - mean; q2 += dl * dl; }
        }
      q2 = sum32(q2);
      const float mr = rbf(mean), rs = __builtin_amdgcn_rcpf(rbf(__builtin_amdgcn_sqrtf(rbf(rbf(q2 * (1.f / D)) + 1e-5f))));
      const __amdgpu_buffer_rsrc_t rH = __builtin_amdgcn_make_buffer_rsrc(p.ws + WS_H2 + (size_t)c * ABYTES, 0, ABYTES, 0x00020000);
      u4 oh[3];
#pragma unroll
      for (int i = 0; i < 3; ++i) {
        u4& o = oh[i];
#pragma unroll
        for (int h = 0; h < 2; ++h)
#pragma unroll
          for (int e = 0; e < 2; ++e) {
            unsigned t = pk((f2){v[i][h][2 * e], v[i][h][2 * e + 1]} - mr);
            t = pk(bf2(t) * rs);
            t = pk(bf2(t) * bf2(pk(bf2(cbv[i][h][e]) + 1.f)));
            o[2 * h + e] = pk(bf2(t) + bf2(sbv[i][h][e]));
          }
        if (i < 2 || r32 < 8) __builtin_amdgcn_raw_buffer_store_b128(o, rH, grow * ROWB + (r32 + 32 * i) * 16, 0, 16);
      }
      publish(flags + (1 * 256 + blockIdx.x) * FLAG_STRIDE, tag);
      STAMP(10);
      if (w != 0) {
        const unsigned dummy = __builtin_amdgcn_readfirstlane(ldsb + LDS_DUMMY);
        int li = tid - 64; li = li < 162 ? li : li - 94;
        const int r = 18 * c + li / 9, l = li % 9;
        touch((const char*)p.wg + (size_t)576 * j * 2, r * (DF * 2) + l * 128, dummy);
        touch((const char*)p.wu + (size_t)576 * j * 2, r * (DF * 2) + l * 128, dummy);
      }
      await8(flags + (1 * 256 + c * 8) * FLAG_STRIDE, tag, tmo, w, lane);
      STAMP(11);
      u4 hv[16];
#pragma unroll
      for (int i = 0; i < 16; ++i) { const int y = i * 256 + tid < 4032 ? i * 256 + tid : tid; hv[i] = __builtin_amdgcn_raw_buffer_load_b128(rH, (y + (y >= j * 576 ? 576 : 0)) * 16, 0, AUX_H); }
#pragma unroll
      for (int i = 0; i < 3; ++i) if (i < 2 || r32 < 8) *(LAS u4*)(size_t)(ldsb + grow * ROWB + (r32 + 32 * i) * 16) = oh[i];
#pragma unroll
      for (int i = 0; i < 16; ++i) { const int y = i * 256 + tid; if (i < 15 || y < 4032) *(LAS u4*)(size_t)(ldsb + (y + (y >= j * 576 ? 576 : 0)) * 16) = hv[i]; }
    }
    asm volatile("s_waitcnt vmcnt(0) lgkmcnt(0)" ::: "memory");
    BAR();
  }
  STAMP(12);
  {
    constexpr size_t KS_GU = (size_t)32 * DF * 2, KS_D = (size_t)32 * ROWB;
    read_w(s, 0); read_a(s, 0, ab_e);
    asm volatile("s_waitcnt lgkmcnt(0)" ::: "memory");
    issue_all(s, 0, voffA, gG + KS_GU);
    f4 aU[9][4];
    auto no = [&]() __attribute__((always_inline)) {};
    auto pfd = [&](int h) __attribute__((always_inline)) {
      int li = 64 * h + 16 * w + lane; li = li < 162 ? li : 161;
      if (lane < 16) touch((const char*)p.wd + (size_t)576 * j * ROWB, (18 * c) * ROWB + li * 128, __builtin_amdgcn_readfirstlane(ldsb + LDS_DUMMY));
    };
    auto kpair = [&](auto zc, auto lastc, auto vmc, int k, auto&& pf) __attribute__((always_inline)) {
      constexpr bool Z = decltype(zc)::value, LAST = decltype(lastc)::value; constexpr int VM = decltype(vmc)::value;
      const char* u1 = gU + (size_t)(k + 1) * KS_GU; const char* g2 = gG + (size_t)(k + 2) * KS_GU;
      step<0, 0, Z, true, false, true, 9, 0>(s, aG, voffA, u1, 0, no);
      if constexpr (!LAST) {
        step<1, 0, Z, true, true, true, 9, NVU>(s, aU, voffA, g2, ab_o + 128 * (k >> 1), pf);
        step<0, 1, false, true, false, true, VM, 0>(s, aG, voffA, u1 + KS_GU, 0, no);
        step<1, 1, false, true, true, true, VM, NVU>(s, aU, voffA, g2 + KS_GU, ab_e + 128 * ((k + 2) >> 1), no);
      } else {
        step<1, 0, Z, true, true, true, 9, NVU>(s, aU, voffB, gD, ab_o + 128 * (k >> 1), no);
        step<0, 1, false, true, false, true, 9, 0>(s, aG, voffB, gD + KS_D, 0, no);
        step<1, 1, false, true, false, true, 9, NVU>(s, aU, voffB, gD + 2 * KS_D, 0, no);
      }
    };
    kpair(std::true_type{}, std::false_type{}, IC<9>{}, 0, no);
    for (int k = 2; k < 8; k += 2) kpair(std::false_type{}, std::false_type{}, IC<9>{}, k, no);
    for (int k = 8; k < 14; k += 2) kpair(std::false_type{}, std::false_type{}, IC<10>{}, k, [&]() __attribute__((always_inline)) { pfd((k - 8) >> 1); });
    kpair(std::false_type{}, std::false_type{}, IC<9>{}, 14, no);
    kpair(std::false_type{}, std::true_type{}, IC<9>{}, 16, no);
    STAMP(13);
    settle<0>(aG); settle<NVU>(aU);
    u2 act[9][4];
#pragma unroll
    for (int t = 0; t < 9; ++t)
#pragma unroll
      for (int m = 0; m < 4; ++m)
#pragma unroll
        for (int e = 0; e < 2; ++e) {
          const f2 x = {aG[t][m][2 * e], aG[t][m][2 * e + 1]}, u = {aU[t][m][2 * e], aU[t][m][2 * e + 1]};
          const f2 z = x * (x * x * -0.10294324f + -2.3022082f);
          const f2 dn = (f2){__builtin_amdgcn_exp2f(z[0]), __builtin_amdgcn_exp2f(z[1])} + 1.f;
          act[t][m][e] = pk((x * u) * (f2){__builtin_amdgcn_rcpf(dn[0]), __builtin_amdgcn_rcpf(dn[1])});
        }
    asm volatile("s_waitcnt lgkmcnt(0)" ::: "memory");
    BAR();
#pragma unroll
    for (int t = 0; t < 9; ++t) {
      const int tt = 9 * w + t, ks = tt >> 1, ah = tt & 1;
      const unsigned ad = ldsb + cl * ROWB + (((4 * ks + g4) ^ sw) * 16) + 8 * ah;
#pragma unroll
      for (int m = 0; m < 4; ++m) *(LAS u2*)(size_t)(ad + m * 16 * ROWB) = act[t][m];
    }
    asm volatile("s_waitcnt lgkmcnt(0)" ::: "memory");
    BAR();
    STAMP(14);
    read_a(s, 0, ab_e);
    const Epi e2 = {rX, (unsigned)(j * 8 * SLICE), (unsigned)SLICE, s.ring - w * 2304, j, w, lane};
    gemm_rows<18, 0, 0>(s, aG, voffB, gD, ab_e, ab_o, voffB, gD, gD, e2, [&]() __attribute__((always_inline)) {});
    STAMP(15);
  }
  {
    int tid3 = threadIdx.x; asm volatile("" : "+v"(tid3));
    const int tid = tid3, lane = tid3 & 63;
    asm volatile("s_waitcnt lgkmcnt(0)" ::: "memory");
    publish(flags + (2 * 256 + blockIdx.x) * FLAG_STRIDE, tag);
    STAMP(16);
    const int t3 = tid < 192 ? tid : tid - 64;
    int zlr[3], zcc[3];
    u4 gv[3], x1[3];
#pragma unroll
    for (int i = 0; i < 3; ++i) {
      zdecode(t3 + 192 * i, zlr[i], zcc[i]);
      gv[i] = *(LAS u4*)(size_t)(ldsb + LDS_MOD + (5 * D + zcc[i] * 8) * 2);
      x1[i] = *(LAS u4*)(size_t)(ldsb + LDS_X1 + (t3 + 192 * i) * 16);
    }
    if (blockIdx.x == 0 && w == 1) {
      for (unsigned spins = 0; spins <= SPIN_CAP; ++spins) {
        bool ok = true;
#pragma unroll
        for (int i = 0; i < 4; ++i) ok &= __hip_atomic_load((gu64*)(flags + (lane + 64 * i) * FLAG_STRIDE), __ATOMIC_RELAXED, __HIP_MEMORY_SCOPE_AGENT) == tag;
        if (__all(ok)) break;
        __builtin_amdgcn_s_sleep(4);
      }
      if (lane == 0) __hip_atomic_store((gu64*)(p.ws + WS_EPOCH), tag, __ATOMIC_RELAXED, __HIP_MEMORY_SCOPE_AGENT);
    }
    await8(flags + (2 * 256 + c * 8) * FLAG_STRIDE, tag, tmo, w, lane);
    STAMP(17);
    const __amdgpu_buffer_rsrc_t rO = __builtin_amdgcn_make_buffer_rsrc(p.out + (size_t)(c * 64 + j * 8) * D, 0, SLICE, 0x00020000); (void)rO;
    u4 v[3][8];
#pragma unroll
    for (int i = 0; i < 3; ++i)
#pragma unroll
      for (int jj = 0; jj < 7; ++jj) v[i][jj] = __builtin_amdgcn_raw_buffer_load_b128(rX, ((jj + (jj >= j)) * 8 + j) * SLICE + (t3 + 192 * i) * 16, 0, AUX_X);
#pragma unroll
    for (int i = 0; i < 3; ++i) { const int z = t3 + 192 * i, zw = z / 144; v[i][7] = *(LAS u4*)(size_t)(ldsb + ABYTES + zw * 2 * SLOT + (z - zw * 144) * 16); }
#pragma unroll
    for (int i = 0; i < 3; ++i) {
      const u4 o = gated_residual(v[i], x1[i], gv[i]);
      if (tid < 192) *(u4*)(p.out + (size_t)(c * 64 + j * 8 + zlr[i]) * D + zcc[i] * 8) = o;
    }
  }
  STAMP(18);
  asm volatile("s_waitcnt lgkmcnt(0)" ::: "memory");
  STAMP_FLUSH();
  }
}

extern "C" void kernel_launch(void* const* d_in, const int* in_sizes, int n_in, void* d_out, int out_size, void* d_ws, size_t ws_size, hipStream_t stream) {
  Params p;
  p.x = (const u16*)d_in[0]; p.kc = (const u16*)d_in[1]; p.vc = (const u16*)d_in[2]; p.wq = (const u16*)d_in[3]; p.wo = (const u16*)d_in[4];
  p.wg = (const u16*)d_in[6]; p.wu = (const u16*)d_in[7]; p.wd = (const u16*)d_in[8];
  p.c = (const u16*)d_in[9]; p.wmod = (const u16*)d_in[10];
  p.out = (u16*)d_out; p.ws = (char*)d_ws;
 p.one = 1.0f; p.gap = 0u;
  (void)hipFuncSetAttribute((const void*)k_main, hipFuncAttributeMaxDynamicSharedMemorySize, LDS_BYTES);
  k_main<<<256, 256, LDS_BYTES, stream>>>(p);
}
```

```cpp
#include <hip/hip_runtime.h>
#include <stdint.h>
#include <utility>
#include <type_traits>

typedef unsigned short u16;
typedef unsigned long long u64;
typedef __bf16 b2 __attribute__((ext_vector_type(2)));
typedef __bf16 b4 __attribute__((ext_vector_type(4)));
typedef __bf16 b8 __attribute__((ext_vector_type(8)));
typedef float f2 __attribute__((ext_vector_type(2)));
typedef float f4 __attribute__((ext_vector_type(4)));
typedef float f16v __attribute__((ext_vector_type(16)));
typedef unsigned u2 __attribute__((ext_vector_type(2)));
typedef unsigned u4 __attribute__((ext_vector_type(4)));
typedef __attribute__((address_space(1))) unsigned gu32;
typedef __attribute__((address_space(1))) unsigned long long gu64;
#define LAS __attribute__((address_space(3)))
#define DI __device__ __forceinline__
#define GAP() __builtin_amdgcn_sched_barrier(0)
#define LGKM0() __builtin_amdgcn_s_waitcnt(0xC07F)
#define BAR() do { asm volatile("" ::: "memory"); __builtin_amdgcn_s_barrier(); asm volatile("" ::: "memory"); } while (0)
template <int V> using IC = std::integral_constant<int, V>;
#define UNROLL(N, F) [&]<int... I_>(std::integer_sequence<int, I_...>) __attribute__((always_inline)) { (F(IC<I_>{}), ...); }(std::make_integer_sequence<int, N>{})

constexpr int S = 2048, D = 576, DH = 128, SK = 2048, WIN = 512, DF = 4608;
constexpr int ROWB = D * 2;
constexpr int ABYTES = 64 * ROWB;
constexpr int SLICE = 8 * ROWB;
constexpr int SLOT = 9216;
constexpr int QSLOT = 8192;
constexpr int LDS_DUMMY = ABYTES + 8 * SLOT;
constexpr int LDS_X1 = LDS_DUMMY + 256;
constexpr int LDS_MOD = LDS_X1 + SLICE;
constexpr int LDS_BYTES = LDS_MOD + 6 * D * 2;
static_assert(LDS_BYTES <= 163840, "LDS");
constexpr size_t WS_EPOCH = 0;
constexpr size_t WS_TMO = 256;
constexpr size_t WS_MODP = 131072;
constexpr size_t WS_FLAG = 32768;
constexpr int FLAG_STRIDE = 8;
constexpr size_t WS_X = 1048576;
constexpr size_t WS_H2 = WS_X + (size_t)32 * 64 * SLICE;

DI float bf2f(u16 v) { return __uint_as_float(((unsigned)v) << 16); }
DI float bflo(unsigned w) { return __uint_as_float(w << 16); }
DI float bfhi(unsigned w) { return __uint_as_float(w & 0xffff0000u); }
DI f2 bf2(unsigned w) { return (f2){bflo(w), bfhi(w)}; }
DI unsigned pk(float lo, float hi) { f2 v = {lo, hi}; b2 b = __builtin_convertvector(v, b2); return __builtin_bit_cast(unsigned, b); }
DI unsigned pk(f2 v) { b2 b = __builtin_convertvector(v, b2); return __builtin_bit_cast(unsigned, b); }
DI float rbf(float f) { return bflo(pk(f, f)); }
DI f2 rb2(f2 v) { return (f2){__uint_as_float(pk(0.f, v[0])), __uint_as_float(pk(0.f, v[1]))}; }
template <int CTRL> DI float dpp(float v) { return __uint_as_float(__builtin_amdgcn_update_dpp(0u, __float_as_uint(v), CTRL, 0xf, 0xf, false)); }
DI float sum8(float v) { v += dpp<0xB1>(v); v += dpp<0x4E>(v); v += dpp<0x141>(v); return v; }
DI float swap16_add(float x) { auto r = __builtin_amdgcn_permlane16_swap(__float_as_uint(x), __float_as_uint(x), false, false); return __uint_as_float(r[0]) + __uint_as_float(r[1]); }
DI float swap32_add(float x) { auto r = __builtin_amdgcn_permlane32_swap(__float_as_uint(x), __float_as_uint(x), false, false); return __uint_as_float(r[0]) + __uint_as_float(r[1]); }
DI float sum32(float v) { v = sum8(v); v += dpp<0x140>(v); return swap16_add(v); }
DI u16 f2bf(float f) { return (u16)(pk(f, f) & 0xffffu); }
DI float fmaxx(float a, float b) { return __builtin_elementwise_maximum(a, b); }
DI float fmax3(float a, float b, float c) { return __builtin_elementwise_maximum(__builtin_elementwise_maximum(a, b), c); }

DI void zdecode(int z, int& lr, int& cc) {
  const int w = z / 144, y = z - w * 144;
  if (y < 128) { const int tp = y >> 5; lr = (y >> 2) & 7; cc = 18 * w + 4 * tp + (y & 3); }
  else { const int y2 = y - 128; lr = y2 >> 1; cc = 18 * w + 16 + (y2 & 1); }
}

template <int IMM> DI void dma(unsigned voff, const char* g) { asm volatile("global_load_lds_dwordx4 %0, %1 offset:%2" :: "v"(voff), "s"(g), "i"(IMM) : "memory"); }
DI void setm0(unsigned a) { asm volatile("s_mov_b32 m0, %0" :: "s"(a) : "memory"); }
DI void touch(const char* p, unsigned voff, unsigned ldsdummy) {
  asm volatile("s_mov_b32 m0, %0\n\ts_nop 0\n\tglobal_load_lds_dword %1, %2" :: "s"(ldsdummy), "v"(voff), "s"(p) : "memory");
}
DI void touchp(const char* a, unsigned ldsdummy) {
  asm volatile("s_mov_b32 m0, %0\n\ts_nop 0\n\tglobal_load_lds_dword %1, off" :: "s"(ldsdummy), "v"(a) : "memory");
}
DI b4 ldT(unsigned base, int off) { return __builtin_amdgcn_ds_read_tr16_b64_v4bf16((LAS b4*)(size_t)(base + off)); }
DI b8 ldA(unsigned base, int off) { return *(LAS b8*)(size_t)(base + off); }
DI b8 cat(const b4& lo, const b4& hi) { return __builtin_shufflevector(lo, hi, 0, 1, 2, 3, 4, 5, 6, 7); }
template <bool AG, bool ZERO> DI void mfma16(f4& c, const b8& a, const b8& b) {
  if constexpr (AG) {
    if constexpr (ZERO) asm volatile("v_mfma_f32_16x16x32_bf16 %0, %1, %2, 0" : "=a"(c) : "v"(a), "v"(b));
    else asm volatile("v_mfma_f32_16x16x32_bf16 %0, %1, %2, %0" : "+a"(c) : "v"(a), "v"(b));
  } else {
    if constexpr (ZERO) asm volatile("v_mfma_f32_16x16x32_bf16 %0, %1, %2, 0" : "=&v"(c) : "v"(a), "v"(b));
    else asm volatile("v_mfma_f32_16x16x32_bf16 %0, %1, %2, %0" : "+v"(c) : "v"(a), "v"(b));
  }
}
template <bool AG, bool ZERO> DI void mfma32(f16v& c, const b8& a, const b8& b) {
  if constexpr (AG) {
    if constexpr (ZERO) asm volatile("v_mfma_f32_32x32x16_bf16 %0, %1, %2, 0" : "=a"(c) : "v"(a), "v"(b));
    else asm volatile("v_mfma_f32_32x32x16_bf16 %0, %1, %2, %0" : "+a"(c) : "v"(a), "v"(b));
  } else {
    if constexpr (ZERO) asm volatile("v_mfma_f32_32x32x16_bf16 %0, %1, %2, 0" : "=&v"(c) : "v"(a), "v"(b));
    else asm volatile("v_mfma_f32_32x32x16_bf16 %0, %1, %2, %0" : "+v"(c) : "v"(a), "v"(b));
  }
}
template <int NVG> DI void settle(f4 (&acc)[9][4]) {
  asm volatile("s_nop 7\n\ts_nop 7" ::: "memory");
#pragma unroll
  for (int t = 0; t < 9; ++t)
#pragma unroll
    for (int m = 0; m < 4; ++m) { if (t * 4 + m < 36 - NVG) asm volatile("" : "+a"(acc[t][m])); else asm volatile("" : "+v"(acc[t][m])); }
}

struct GS {
  b4 wlo[2][9], whi[2][9];
  b8 af[2][4];
  unsigned wbase, ring;
};
DI void issue_all(const GS& s, int slot, const unsigned (&voff)[9], const char* gp) {
  const unsigned sl = s.ring + slot * SLOT;
  setm0(sl + 4096); asm volatile("s_nop 0");
  dma<-4096>(voff[0], gp); dma<-3072>(voff[1], gp); dma<-2048>(voff[2], gp); dma<-1024>(voff[3], gp);
  dma<0>(voff[4], gp); dma<1024>(voff[5], gp); dma<2048>(voff[6], gp); dma<3072>(voff[7], gp);
  setm0(sl + 8192); asm volatile("s_nop 0"); dma<0>(voff[8], gp);
}
template <int PAR, int AP, bool ZERO, bool RDW, bool RDA, bool DMA, int VM, int NVG, typename PF>
DI void step(GS& s, f4 (&acc)[9][4], const unsigned (&voff)[9], const char* gp, unsigned ab, PF&& pf) {
  const unsigned sl = s.ring + (PAR ^ 1) * SLOT;
  const unsigned wb = s.wbase + (PAR ^ 1) * SLOT;
  auto mm = [&](auto ic) __attribute__((always_inline)) {
    constexpr int I = decltype(ic)::value, t = I >> 2, m = I & 3;
    mfma16<(I < 36 - NVG), ZERO>(acc[t][m], cat(s.wlo[PAR][t], s.whi[PAR][t]), s.af[AP][m]);
  };
  auto rd = [&](auto ic) __attribute__((always_inline)) {
    constexpr int I = decltype(ic)::value;
    if constexpr (I < 4) { if constexpr (RDA) s.af[AP ^ 1][I] = ldA(ab, I * 16 * ROWB); }
    else if constexpr (RDW) {
      if constexpr (((I - 4) & 1) == 0) s.wlo[PAR ^ 1][(I - 4) >> 1] = ldT(wb, 32 * ((I - 4) >> 1));
      else s.whi[PAR ^ 1][(I - 4) >> 1] = ldT(wb, 32 * ((I - 4) >> 1) + 4608);
    }
  };
  auto gap = [&](auto gc) __attribute__((always_inline)) {
    constexpr int G = decltype(gc)::value;
    mm(IC<2 * G>{});
    if constexpr (G == 0 && RDW) asm volatile("s_waitcnt vmcnt(%0)" :: "i"(VM) : "memory");
    if constexpr (G <= 10) rd(IC<2 * G>{});
    if constexpr (DMA) {
      if constexpr (G == 12) { LGKM0(); setm0(sl + 4096); }
      if constexpr (G >= 13 && G <= 16) dma<(2 * (G - 13) - 4) * 1024>(voff[2 * (G - 13)], gp);
      if constexpr (G == 17) dma<0>(voff[8], gp);
    }
    GAP();
    mm(IC<2 * G + 1>{});
    if constexpr (G <= 10) rd(IC<2 * G + 1>{});
    if constexpr (DMA) {
      if constexpr (G >= 13 && G <= 16) dma<(2 * (G - 13) + 1 - 4) * 1024>(voff[2 * (G - 13) + 1], gp);
      if constexpr (G == 16) setm0(sl + 8192);
    }
    if constexpr (G == 17) pf();
    GAP();
  };
  UNROLL(18, gap);
}
DI void read_w(GS& s, int par) {
#pragma unroll
  for (int t = 0; t < 9; ++t) { s.wlo[par][t] = ldT(s.wbase + par * SLOT, 32 * t); s.whi[par][t] = ldT(s.wbase + par * SLOT, 32 * t + 4608); }
}
DI void read_a(GS& s, int set, unsigned ab) {
#pragma unroll
  for (int m = 0; m < 4; ++m) s.af[set][m] = ldA(ab, m * 16 * ROWB);
}
struct Epi { __amdgpu_buffer_rsrc_t rs; unsigned base, dstride, ownlds; int own, w, lane; };
template <int U> DI void epi_unit(f4 (&acc)[9][4], const Epi& e) {
  const int G = e.lane >> 4, c = e.lane & 15, lr = c & 7, dh = c >> 3;
  constexpr int ta = U < 16 ? 2 * (U >> 2) : 8, tb = U < 16 ? ta + 1 : 8, ma = U < 16 ? (U & 3) : 2 * (U - 16), mb = U < 16 ? ma : ma + 1;
  asm volatile("" : "+a"(acc[ta][ma]), "+a"(acc[tb][mb]));
  const f4 a = acc[ta][ma], b = acc[tb][mb];
  auto r0 = __builtin_amdgcn_permlane16_swap(pk(a[0], a[1]), pk(b[0], b[1]), false, false);
  auto r1 = __builtin_amdgcn_permlane16_swap(pk(a[2], a[3]), pk(b[2], b[3]), false, false);
  const u4 v = {r0[0], r1[0], r0[1], r1[1]};
  const int d = U < 16 ? 2 * ma + dh : 2 * (ma + (G & 1)) + dh;
  const unsigned off = U < 16 ? e.w * 2304 + (U >> 2) * 512 + lr * 64 + (2 * (G & 1) + (G >> 1)) * 16 : e.w * 2304 + 2048 + lr * 32 + (G >> 1) * 16;
  __builtin_amdgcn_raw_buffer_store_b128(v, e.rs, d == e.own ? 0x7ffffff0u : e.base + d * e.dstride + off, 0, 16);
  if ((U < 16 ? ma : ma >> 1) == (U < 16 ? e.own >> 1 : e.own >> 2)) { if (d == e.own) *(LAS u4*)(size_t)(e.ownlds + off) = v; }
}
template <bool NEXT, int VM> DI void final2(GS& s, f4 (&acc)[9][4], const unsigned (&nvoff)[9], const char* nx1, unsigned ab, const Epi& e) {
  const unsigned sl = s.ring + SLOT, wb = s.wbase + SLOT;
  asm volatile("s_waitcnt vmcnt(%0)" :: "i"(VM) : "memory");
#pragma unroll
  for (int m = 0; m < 4; ++m) s.af[1][m] = ldA(ab, m * 16 * ROWB);
  auto rdw = [&](auto ic) __attribute__((always_inline)) { constexpr int t = decltype(ic)::value; s.wlo[1][t] = ldT(wb, 32 * t); s.whi[1][t] = ldT(wb, 32 * t + 4608); };
  rdw(IC<0>{}); rdw(IC<1>{});
  GAP();
  auto grp = [&](auto gc) __attribute__((always_inline)) {
    constexpr int G = decltype(gc)::value, t = G >> 1, st = G & 1;
    auto mm = [&](auto mc) __attribute__((always_inline)) { constexpr int m = decltype(mc)::value; mfma16<true, false>(acc[t][m], cat(s.wlo[st][t], s.whi[st][t]), s.af[st][m]); };
    mm(IC<0>{});
    if constexpr (st == 0 && t + 2 < 9) rdw(IC<t + 2>{});
    if constexpr (NEXT && G == 15) { LGKM0(); setm0(sl + 4096); }
    GAP();
    constexpr int U = G >= 5 ? G - 5 : -1;
    mm(IC<1>{});
    if constexpr (U >= 0 && U < 16 && (U >> 2) * 4 + 5 <= G) epi_unit<(U >= 0 && U < 16) ? U : 0>(acc, e);
    GAP();
    mm(IC<2>{});
    if constexpr (NEXT && G == 16) { dma<-4096>(nvoff[0], nx1); dma<-3072>(nvoff[1], nx1); dma<-2048>(nvoff[2], nx1); dma<-1024>(nvoff[3], nx1); }
    if constexpr (NEXT && G == 17) { dma<0>(nvoff[4], nx1); dma<1024>(nvoff[5], nx1); dma<2048>(nvoff[6], nx1); dma<3072>(nvoff[7], nx1); setm0(sl + 8192); }
    GAP();
    mm(IC<3>{});
    GAP();
  };
  UNROLL(18, grp);
  if constexpr (NEXT) dma<0>(nvoff[8], nx1);
  asm volatile("s_nop 7" ::: "memory");
  epi_unit<13>(acc, e); epi_unit<14>(acc, e); epi_unit<15>(acc, e); epi_unit<16>(acc, e); epi_unit<17>(acc, e);
}
template <int NS, int NEXT, int NPF, typename PF> DI void gemm_rows(GS& s, f4 (&acc)[9][4], const unsigned (&voff)[9], const char* gW, unsigned ab_e, unsigned ab_o,
                                              const unsigned (&nvoff)[9], const char* nx0, const char* nx1, const Epi& e, PF&& pf) {
  constexpr size_t KS = (size_t)32 * ROWB;
  auto no = [&]() __attribute__((always_inline)) {};
  step<0, 0, true, true, true, true, 9, 0>(s, acc, voff, gW + 3 * KS, ab_o, no);
  step<1, 1, false, true, true, true, 9, 0>(s, acc, voff, gW + 4 * KS, ab_e + 128, pf);
  step<0, 0, false, true, true, true, 9 + NPF, 0>(s, acc, voff, gW + 5 * KS, ab_o + 128, no);
  step<1, 1, false, true, true, true, 9 + NPF, 0>(s, acc, voff, gW + 6 * KS, ab_e + 256, no);
  for (int k = 4; k < NS - 4; k += 2) {
    step<0, 0, false, true, true, true, 9, 0>(s, acc, voff, gW + (size_t)(k + 3) * KS, ab_o + 128 * (k >> 1), no);
    step<1, 1, false, true, true, true, 9, 0>(s, acc, voff, gW + (size_t)(k + 4) * KS, ab_e + 128 * ((k + 2) >> 1), no);
  }
  step<0, 0, false, true, true, true, 9, 0>(s, acc, voff, gW + (size_t)(NS - 1) * KS, ab_o + 128 * ((NS - 4) >> 1), no);
  step<1, 1, false, true, true, NEXT != 0, 9, 0>(s, acc, nvoff, nx0, ab_e + 128 * ((NS - 2) >> 1), no);
  final2<NEXT != 0, NEXT != 0 ? 9 : 0>(s, acc, nvoff, nx1, ab_o + 128 * ((NS - 2) >> 1), e);
}

constexpr unsigned SPIN_CAP = 1u << 17;
DI void publish(u64* flag, u64 tag) {
  asm volatile("s_waitcnt vmcnt(0)" ::: "memory");
  BAR();
  if (threadIdx.x == 0) __hip_atomic_store((gu64*)flag, tag, __ATOMIC_RELAXED, __HIP_MEMORY_SCOPE_AGENT);
}
DI void await8(u64* flags8, u64 tag, unsigned* tmo, int w, int lane) {
  if (w == 0) {
    for (unsigned spins = 0;; ++spins) {
      const u64 v = __hip_atomic_load((gu64*)(flags8 + (lane & 7) * FLAG_STRIDE), __ATOMIC_RELAXED, __HIP_MEMORY_SCOPE_AGENT);
      if (__all(v == tag)) break;
      if (spins > SPIN_CAP) { if (lane == 0) __hip_atomic_store((gu32*)tmo, 1u, __ATOMIC_RELAXED, __HIP_MEMORY_SCOPE_AGENT); break; }
      __builtin_amdgcn_s_sleep(1);
    }
  }
  BAR();
}
template <int NP, int NT> struct ModGather {
  static constexpr int N = (NP + NT - 1) / NT;
  u4 gr[N][2];
  __amdgpu_buffer_rsrc_t rM;
  DI void load(int tid) {
#pragma unroll
    for (int i = 0; i < N; ++i) {
      const int idx = tid + NT * i < NP ? tid + NT * i : (tid & 63), off = (idx >> 5) * 1024 + (idx & 31) * 16;
      gr[i][0] = __builtin_amdgcn_raw_buffer_load_b128(rM, off, 0, 16); gr[i][1] = __builtin_amdgcn_raw_buffer_load_b128(rM, off + 512, 0, 16);
    }
  }
  DI void issue(char* ws, int tid) { rM = __builtin_amdgcn_make_buffer_rsrc(ws + WS_MODP, 0, 54 * 1024, 0x00020000); load(tid); }
  DI void finish(u64 tag, unsigned ldsb, unsigned* tmo, int tid) {
    const unsigned thi = (unsigned)(tag >> 32), tlo = (unsigned)tag;
    for (unsigned spins = 0;; ++spins) {
      bool ok = true;
#pragma unroll
      for (int i = 0; i < N; ++i) ok &= gr[i][0][2] == tlo && gr[i][0][3] == thi && gr[i][1][2] == tlo && gr[i][1][3] == thi;
      if (__all(ok)) break;
      if (spins > SPIN_CAP / 8) { if ((tid & 63) == 0) __hip_atomic_store((gu32*)tmo, 2u, __ATOMIC_RELAXED, __HIP_MEMORY_SCOPE_AGENT); break; }
      __builtin_amdgcn_s_sleep(4);
      asm volatile("" ::: "memory");
      load(tid);
    }
#pragma unroll
    for (int i = 0; i < N; ++i)
      if (tid + NT * i < NP) {
        const int idx = tid + NT * i;
        const f2 m = (f2){__uint_as_float(gr[i][0][0]), __uint_as_float(gr[i][0][1])} + (f2){__uint_as_float(gr[i][1][0]), __uint_as_float(gr[i][1][1])};
        if (idx < D) {
          f2 v = rb2(m);
          if (idx >= D / 2) v = rb2(v + 1.f);
          *(LAS f2*)(size_t)(ldsb + LDS_X1 + 1024 + idx * 8) = v;
        } else *(LAS unsigned*)(size_t)(ldsb + LDS_MOD + idx * 4) = pk(m);
      }
  }
};
DI u4 gated_residual(const u4 (&v)[8], const u4& res, const u4& g) {
  u4 o;
#pragma unroll
  for (int e = 0; e < 4; ++e) {
    f2 s = bf2(v[0][e]);
#pragma unroll
    for (int j = 1; j < 8; ++j) s += bf2(v[j][e]);
    const unsigned t = pk(bf2(pk(s)) * bf2(g[e]));
    o[e] = pk(bf2(res[e]) + bf2(t));
  }
  return o;
}

constexpr float QSCALE = 0.08838834764831845f * 1.4426950408889634f;
constexpr float THR = 40.0f;
struct QS {
  b4 wlo[2][8], whi[2][8];
  b8 hf[2][4];
  unsigned wb[4];
  unsigned hb[4];
  unsigned ring;
};
template <int PAR, bool ZERO, bool RD, bool DMA, int VM, typename PF>
DI void qstep(QS& s, f16v (&acc)[4][2], const unsigned (&voff)[8], const char* gp, unsigned hoff, PF&& pf) {
  const unsigned sl = s.ring + (PAR ^ 1) * QSLOT;
  auto rd = [&](auto ic) __attribute__((always_inline)) {
    constexpr int I = decltype(ic)::value;
    if constexpr (RD) {
      if constexpr (I < 4) {
        s.hf[PAR ^ 1][I] = ldA(s.hb[2 * (PAR ^ 1) + (I >> 1)] + hoff, (I & 1) * 32 * ROWB);
      } else {
        constexpr int J = I - 4, f = J >> 1, t = J & 1, ks = f >> 2, dt = f & 3;
        const b4 v = ldT(s.wb[dt], (PAR ^ 1) * QSLOT + 4096 * ks + 1024 * t);
        if constexpr (t == 0) s.wlo[PAR ^ 1][f] = v; else s.whi[PAR ^ 1][f] = v;
      }
    }
  };
  auto gap = [&](auto gc) __attribute__((always_inline)) {
    constexpr int G = decltype(gc)::value, ks = G >> 3, dt = (G >> 1) & 3, qb = G & 1, f = ks * 4 + dt;
    mfma32<false, ZERO && ks == 0>(acc[dt][qb], cat(s.wlo[PAR][f], s.whi[PAR][f]), s.hf[PAR][ks * 2 + qb]);
    if constexpr (G == 0 && RD) asm volatile("s_waitcnt vmcnt(%0)" :: "i"(VM) : "memory");
    if constexpr (G <= 6) { rd(IC<3 * G>{}); rd(IC<3 * G + 1>{}); if constexpr (3 * G + 2 < 20) rd(IC<3 * G + 2>{}); }
    if constexpr (DMA) {
      if constexpr (G == 9) { LGKM0(); setm0(sl + 4096); }
      if constexpr (G >= 10 && G <= 13) { dma<(2 * (G - 10) - 4) * 1024>(voff[2 * (G - 10)], gp); dma<(2 * (G - 10) + 1 - 4) * 1024>(voff[2 * (G - 10) + 1], gp); }
    }
    if constexpr (G == 15) pf();
    GAP();
  };
  UNROLL(16, gap);
}

struct AT {
  f16v S[2][2][2];
  f16v negm[2];
  f16v O[4][2];
  b8 q[2][8];
  b8 P[2][4];
  float m[2], l[2], rs0[2], rs1[2], fct[2], pm0[2], pm1[2];
  float one;
  unsigned kb_[2], vb_[2];
  unsigned voff[8];
  unsigned m0k, m0v;
  const char *gk, *gv;
  bool pend;
};
template <int MODE> DI void qk(f16v& s, const b8& k, const b8& q, const f16v& negm) {
  if constexpr (MODE == 0) asm volatile("v_mfma_f32_32x32x16_bf16 %0, %1, %2, 0" : "=&v"(s) : "v"(k), "a"(q));
  else if constexpr (MODE == 1) asm volatile("v_mfma_f32_32x32x16_bf16 %0, %1, %2, %3" : "=&v"(s) : "v"(k), "a"(q), "v"(negm));
  else asm volatile("v_mfma_f32_32x32x16_bf16 %0, %1, %2, %0" : "+v"(s) : "v"(k), "a"(q));
}
template <int XP, int KB, int E> DI void sm_exp(AT& a) { constexpr int qb = E >> 4, r = E & 15; a.S[XP][KB][qb][r] = __builtin_amdgcn_exp2f(a.S[XP][KB][qb][r]); }
template <int XP, int KB, int E> DI void sm_add(AT& a) {
  constexpr int qb = E >> 4, r = E & 15;
  if constexpr (r & 1) a.rs1[qb] = __builtin_fmaf(a.S[XP][KB][qb][r], a.one, a.rs1[qb]); else a.rs0[qb] += a.S[XP][KB][qb][r];
}
template <int XP, int C> DI void sm_pack(AT& a) {
  constexpr int kb = C >> 4, qb = (C >> 3) & 1, s = (C >> 2) & 1, p = C & 3;
  u4 w = __builtin_bit_cast(u4, a.P[qb][kb * 2 + s]);
  w[p] = pk(a.S[XP][kb][qb][8 * s + 2 * p], a.S[XP][kb][qb][8 * s + 2 * p + 1]);
  a.P[qb][kb * 2 + s] = __builtin_bit_cast(b8, w);
  if constexpr (p == 3) asm volatile("" : "+v"(a.P[qb][kb * 2 + s]));
}
template <int X, bool FIRST, bool DK, bool DV, typename PF> DI void phaseA(AT& a, int t, PF&& pf) {
  constexpr int XP = X ^ 1;
  const unsigned kslot = X * 16384;
  b8 kf[16];
  auto kread = [&](auto ic) __attribute__((always_inline)) { constexpr int I = decltype(ic)::value, ks = I >> 1, kb = I & 1; kf[I] = ldA(a.kb_[ks & 1], kslot + 8192 * kb + 512 * (ks >> 1)); };
  kread(IC<0>{}); kread(IC<1>{}); kread(IC<2>{}); kread(IC<3>{});
  const char* gk = a.gk + (size_t)(t + 1) * 16384; const char* gv = a.gv + (size_t)t * 16384;
  auto gap = [&](auto gc) __attribute__((always_inline)) {
    constexpr int G = decltype(gc)::value, ks = G >> 2, kb = (G >> 1) & 1, qb = G & 1, I = G >> 1;
    qk<(ks == 0) ? (FIRST ? 0 : 1) : 2>(a.S[X][kb][qb], kf[I], a.q[qb][ks], a.negm[qb]);
    if constexpr ((G & 1) == 0 && I + 4 < 16) kread(IC<I + 4>{});
    if constexpr (!FIRST && G == 8) asm volatile("" :: "v"(a.negm[0]), "v"(a.negm[1]));
    if constexpr (DK) { if constexpr (G == 0) setm0(a.m0k + (X ^ 1) * 16384); if constexpr ((G & 1) && G < 16) dma<((G >> 1) - 4) * 1024>(a.voff[G >> 1], gk); }
    if constexpr (DV) { if constexpr (G == 16) setm0(a.m0v + X * 16384); if constexpr ((G & 1) && G > 16) dma<(((G - 16) >> 1) - 4) * 1024>(a.voff[(G - 16) >> 1], gv); }
    if constexpr (!FIRST) {
      if constexpr (G < 16) { sm_exp<XP, 1, 2 * G>(a); sm_exp<XP, 1, 2 * G + 1>(a); sm_pack<XP, G>(a); }
      else { sm_pack<XP, G>(a); sm_add<XP, 1, 2 * (G - 16)>(a); sm_add<XP, 1, 2 * (G - 16) + 1>(a); }
    }
    GAP();
  };
  UNROLL(32, gap);
  pf();
  if constexpr (!FIRST) {
#pragma unroll
    for (int qb = 0; qb < 2; ++qb) { a.l[qb] += a.rs0[qb] + a.rs1[qb]; asm volatile("" : "+v"(a.l[qb])); }
  }
}
DI float swap32_max(float x) { auto r = __builtin_amdgcn_permlane32_swap(__float_as_uint(x), __float_as_uint(x), false, false); return fmaxx(__uint_as_float(r[0]), __uint_as_float(r[1])); }
template <int X, bool NOPV, bool ZERO> DI void phaseB(AT& a) {
  const unsigned vslot = (X ^ 1) * 16384;
  b4 vlo[16], vhi[16];
  auto vread = [&](auto ic) __attribute__((always_inline)) {
    constexpr int I = decltype(ic)::value, k4 = I >> 2, db = I & 3;
    vlo[I] = ldT(a.vb_[0], vslot + 2048 * (2 * k4) + 512 * db);
    vhi[I] = ldT(a.vb_[1], vslot + 2048 * (2 * k4 + 1) + 512 * db);
  };
  if constexpr (!NOPV) { vread(IC<0>{}); vread(IC<1>{}); vread(IC<2>{}); }
  float rmax[2];
  auto pv = [&](auto gc) __attribute__((always_inline)) {
    constexpr int G = decltype(gc)::value, I = G >> 1, k4 = I >> 2, db = I & 3, qb = G & 1;
    if constexpr (!NOPV) {
      mfma32<true, ZERO && k4 == 0>(a.O[db][qb], cat(vlo[I], vhi[I]), a.P[qb][k4]);
      if constexpr ((G & 1) == 0 && I + 3 < 16) vread(IC<I + 3>{});
    }
  };
  auto gap1 = [&](auto gc) __attribute__((always_inline)) {
    constexpr int G = decltype(gc)::value;
    pv(gc);
    if constexpr (G >= 4 && G < 12) {
      constexpr int i = G - 4, q = i >> 2, r = 4 * (i & 3);
      if constexpr ((i & 3) == 0) { a.pm0[q] = fmax3(a.S[X][0][q][0], a.S[X][0][q][1], a.S[X][1][q][0]); a.pm1[q] = fmax3(a.S[X][0][q][2], a.S[X][0][q][3], a.S[X][1][q][1]);
                                    a.pm0[q] = fmax3(a.pm0[q], a.S[X][1][q][2], a.S[X][1][q][3]); }
      else { a.pm0[q] = fmax3(a.pm0[q], a.S[X][0][q][r], a.S[X][0][q][r + 1]); a.pm1[q] = fmax3(a.pm1[q], a.S[X][0][q][r + 2], a.S[X][0][q][r + 3]);
             a.pm0[q] = fmax3(a.pm0[q], a.S[X][1][q][r], a.S[X][1][q][r + 1]); a.pm1[q] = fmax3(a.pm1[q], a.S[X][1][q][r + 2], a.S[X][1][q][r + 3]); }
    }
    if constexpr (G == 12 || G == 13) { constexpr int q = G - 12; rmax[q] = swap32_max(fmaxx(a.pm0[q], a.pm1[q])); }
    GAP();
  };
  UNROLL(14, gap1);
  if constexpr (NOPV) {
#pragma unroll
    for (int q = 0; q < 2; ++q) {
      a.m[q] = rmax[q]; a.l[q] = 0.f; a.fct[q] = 1.f;
#pragma unroll
      for (int kb = 0; kb < 2; ++kb)
#pragma unroll
        for (int e = 0; e < 16; ++e) a.S[X][kb][q][e] -= rmax[q];
#pragma unroll
      for (int e = 0; e < 16; ++e) a.negm[q][e] = -rmax[q];
      asm volatile("" : "+v"(a.negm[q]));
    }
  } else {
    const bool grew = (rmax[0] > THR) | (rmax[1] > THR);
    if (__builtin_expect(__any((int)grew), 0)) {
#pragma unroll
      for (int q = 0; q < 2; ++q) {
        const float dl = fmaxx(rmax[q], 0.f); a.m[q] += dl; a.fct[q] = __builtin_amdgcn_exp2f(-dl);
#pragma unroll
        for (int kb = 0; kb < 2; ++kb)
#pragma unroll
          for (int e = 0; e < 16; ++e) a.S[X][kb][q][e] -= dl;
#pragma unroll
        for (int e = 0; e < 16; ++e) a.negm[q][e] = -a.m[q];
        asm volatile("" : "+v"(a.negm[q]));
      }
      a.pend = true;
    }
  }
  a.rs0[0] = a.rs0[1] = a.rs1[0] = a.rs1[1] = 0.f;
  GAP();
  auto gap2 = [&](auto gc) __attribute__((always_inline)) {
    constexpr int G = decltype(gc)::value + 14;
    pv(IC<G>{});
    if constexpr (G >= 15) {
      constexpr int e0 = 2 * (G - 15);
      if constexpr (e0 < 32) { sm_exp<X, 0, e0>(a); sm_exp<X, 0, e0 + 1>(a); }
      if constexpr (G >= 16) { sm_add<X, 0, e0 - 2>(a); sm_add<X, 0, e0 - 1>(a); }
    }
    GAP();
  };
  UNROLL(18, gap2);
  asm volatile("" : "+v"(a.S[X][0][0]), "+v"(a.S[X][0][1]), "+v"(a.rs0[0]), "+v"(a.rs0[1]), "+v"(a.rs1[0]), "+v"(a.rs1[1]));
  if constexpr (!NOPV) {
    if (__builtin_expect(a.pend, 0)) {
      asm volatile("s_nop 7\n\ts_nop 7" ::: "memory");
#pragma unroll
      for (int db = 0; db < 4; ++db)
#pragma unroll
        for (int q = 0; q < 2; ++q) {
          asm volatile("" : "+a"(a.O[db][q]));
          f16v o = a.O[db][q];
#pragma unroll
          for (int e = 0; e < 16; ++e) o[e] *= a.fct[q];
          a.O[db][q] = o;
          asm volatile("" : "+a"(a.O[db][q]));
        }
      a.l[0] *= a.fct[0]; a.l[1] *= a.fct[1]; a.pend = false;
      asm volatile("s_nop 1" ::: "memory");
    }
  }
}


template <int X, bool FIRST, bool DK, bool DV, typename PF> DI void fastA(AT& a, int t, PF&& pf) {
  constexpr int XP = X ^ 1;
  const unsigned kslot = X * 16384;
  b8 kf[16];
  auto kread = [&](auto ic) __attribute__((always_inline)) { constexpr int I = decltype(ic)::value, ks = I >> 1, kb = I & 1; kf[I] = ldA(a.kb_[ks & 1], kslot + 8192 * kb + 512 * (ks >> 1)); };
  kread(IC<0>{}); kread(IC<1>{}); kread(IC<2>{}); kread(IC<3>{});
  const char* gk = a.gk + (size_t)(t + 1) * 16384; const char* gv = a.gv + (size_t)t * 16384;
  auto gap = [&](auto gc) __attribute__((always_inline)) {
    constexpr int G = decltype(gc)::value, ks = G >> 2, kb = (G >> 1) & 1, qb = G & 1, I = G >> 1;
    qk<(ks == 0) ? (FIRST ? 0 : 1) : 2>(a.S[X][kb][qb], kf[I], a.q[qb][ks], a.negm[qb]);
    if constexpr ((G & 1) == 0 && I + 4 < 16) kread(IC<I + 4>{});
    if constexpr (!FIRST && G == 8) asm volatile("" :: "v"(a.negm[0]), "v"(a.negm[1]));
    if constexpr (DK) { if constexpr (G == 0) setm0(a.m0k + (X ^ 1) * 16384); if constexpr ((G & 1) && G < 16) dma<((G >> 1) - 4) * 1024>(a.voff[G >> 1], gk); }
    if constexpr (DV) { if constexpr (G == 16) setm0(a.m0v + X * 16384); if constexpr ((G & 1) && G > 16) dma<(((G - 16) >> 1) - 4) * 1024>(a.voff[(G - 16) >> 1], gv); }
    if constexpr (!FIRST) {
      sm_exp<XP, 1, G>(a);
      if constexpr (G >= 1) sm_add<XP, 1, G - 1>(a);
      if constexpr (G >= 3 && (G & 1)) sm_pack<XP, 16 + (G - 3) / 2>(a);
    }
    GAP();
  };
  UNROLL(32, gap);
  pf();
  if constexpr (!FIRST) {
    sm_add<XP, 1, 31>(a); sm_pack<XP, 31>(a);
#pragma unroll
    for (int qb = 0; qb < 2; ++qb) { a.l[qb] += a.rs0[qb] + a.rs1[qb]; asm volatile("" : "+v"(a.l[qb])); }
  }
}
constexpr int fb_e(int G) { return G < 2 ? 0 : G - 2 + (G > 5) + (G > 9); }
constexpr int dr_e(int n) { return n < 8 ? n : n < 16 ? n + 8 : n < 24 ? n - 8 : n; }
template <int X, int MODE> DI void fastB(AT& a) {
  constexpr bool ZERO = MODE == 1, DRAIN = MODE == 3;
  if constexpr (MODE == 2) {
#pragma unroll
    for (int q = 0; q < 2; ++q) {
      float p0 = fmax3(a.S[X][0][q][0], a.S[X][1][q][0], a.S[X][0][q][2]), p1 = fmax3(a.S[X][0][q][1], a.S[X][1][q][1], a.S[X][0][q][3]);
      p0 = fmaxx(p0, a.S[X][1][q][2]); p1 = fmaxx(p1, a.S[X][1][q][3]);
#pragma unroll
      for (int r = 4; r < 16; r += 2) { p0 = fmax3(p0, a.S[X][0][q][r], a.S[X][1][q][r]); p1 = fmax3(p1, a.S[X][0][q][r + 1], a.S[X][1][q][r + 1]); }
      const float mx = swap32_max(fmaxx(p0, p1));
      f2 m2 = {mx, mx}; asm volatile("" : "+v"(m2));
      a.l[q] = 0.f;
#pragma unroll
      for (int kb = 0; kb < 2; ++kb)
#pragma unroll
        for (int e = 0; e < 16; e += 2) { const f2 v = (f2){a.S[X][kb][q][e], a.S[X][kb][q][e + 1]} - m2; a.S[X][kb][q][e] = v[0]; a.S[X][kb][q][e + 1] = v[1]; }
#pragma unroll
      for (int e = 0; e < 16; ++e) a.negm[q][e] = -mx;
      asm volatile("" : "+v"(a.negm[q]));
    }
    a.rs0[0] = a.rs0[1] = a.rs1[0] = a.rs1[1] = 0.f;
    { auto f = [&](auto ic) __attribute__((always_inline)) { sm_exp<X, 0, decltype(ic)::value>(a); }; UNROLL(32, f); }
    { auto f = [&](auto ic) __attribute__((always_inline)) { sm_add<X, 0, decltype(ic)::value>(a); }; UNROLL(32, f); }
    { auto f = [&](auto ic) __attribute__((always_inline)) { sm_pack<X, decltype(ic)::value>(a); }; UNROLL(16, f); }
  } else {
    const unsigned vslot = (DRAIN ? X : X ^ 1) * 16384;
    b4 vlo[16], vhi[16];
    auto vread = [&](auto ic) __attribute__((always_inline)) {
      constexpr int I = decltype(ic)::value, k4 = I >> 2, db = I & 3;
      vlo[I] = ldT(a.vb_[0], vslot + 2048 * (2 * k4) + 512 * db);
      vhi[I] = ldT(a.vb_[1], vslot + 2048 * (2 * k4 + 1) + 512 * db);
    };
    vread(IC<0>{}); vread(IC<1>{}); vread(IC<2>{});
    if constexpr (!DRAIN) a.rs0[0] = a.rs0[1] = a.rs1[0] = a.rs1[1] = 0.f;
    auto gap = [&](auto gc) __attribute__((always_inline)) {
      constexpr int G = decltype(gc)::value, I = G >> 1, k4 = I >> 2, db = I & 3, qb = G & 1;
      mfma32<true, ZERO && k4 == 0>(a.O[db][qb], cat(vlo[I], vhi[I]), a.P[qb][k4]);
      if constexpr ((G & 1) == 0 && I + 3 < 16) vread(IC<I + 3>{});
      if constexpr (DRAIN) {
        if constexpr (G < 16) { sm_exp<X, 1, dr_e(2 * G)>(a); sm_exp<X, 1, dr_e(2 * G) + 1>(a); }
        if constexpr (G >= 2 && G < 18) sm_pack<X, 16 + dr_e(2 * (G - 2)) / 2>(a);
        if constexpr (G >= 16) { sm_add<X, 1, dr_e(2 * (G - 16))>(a); sm_add<X, 1, dr_e(2 * (G - 16)) + 1>(a); }
      } else {
        if constexpr (fb_e(G) < fb_e(G + 1)) sm_exp<X, 0, fb_e(G)>(a);
        if constexpr (fb_e(G) + 1 < fb_e(G + 1)) sm_exp<X, 0, fb_e(G) + 1>(a);
        if constexpr (G >= 3 && fb_e(G - 1) < fb_e(G)) sm_add<X, 0, G >= 3 ? fb_e(G - 1) : 0>(a);
        if constexpr (G >= 3 && fb_e(G - 1) + 1 < fb_e(G)) sm_add<X, 0, G >= 3 ? fb_e(G - 1) + 1 : 0>(a);
        if constexpr (G >= 16 && G <= 29) sm_pack<X, G >= 16 ? G - 16 : 0>(a);
        if constexpr (G == 31) sm_pack<X, 14>(a);
      }
      GAP();
    };
    UNROLL(32, gap);
    if constexpr (!DRAIN) {
      sm_add<X, 0, 31>(a);
      static_assert(fb_e(32) == 32 && fb_e(28) >= 28 && fb_e(30) >= 30, "schedule");
      sm_pack<X, 15>(a);
      asm volatile("" : "+v"(a.S[X][0][0]), "+v"(a.S[X][0][1]), "+v"(a.rs0[0]), "+v"(a.rs0[1]), "+v"(a.rs1[0]), "+v"(a.rs1[1]));
    } else {
#pragma unroll
      for (int qb = 0; qb < 2; ++qb) a.l[qb] += a.rs0[qb] + a.rs1[qb];
    }
  }
}

struct Params { const u16 *x, *c, *wmod, *wq, *kc, *vc, *wo, *wg, *wu, *wd; u16* out; char* ws; float one; unsigned gap; };

__global__ __launch_bounds__(256, 1) void k_main(Params p) {
  extern __shared__ __attribute__((aligned(16))) char lds[];
  const int tid = threadIdx.x, w = __builtin_amdgcn_readfirstlane(tid >> 6), lane = tid & 63;
  const int j = blockIdx.x & 7, c = blockIdx.x >> 3;
  const unsigned ldsb = (unsigned)(size_t)lds;
  const int hh = lane >> 5, r32 = lane & 31;
  u64* const flags = (u64*)(p.ws + WS_FLAG);
  unsigned* const tmo = (unsigned*)(p.ws + WS_TMO);
  QS qs;
  unsigned voffQ[8];
#pragma unroll
  for (int i = 0; i < 8; ++i) voffQ[i] = (4 * i + (lane >> 4)) * 8192 + 16 * ((lane & 15) ^ ((lane >> 4) << 2)) - (i - 4) * 1024 + 4096;
  const char* gq = (const char*)p.wq + (size_t)(4 * j + w) * 256 - 4096;
  qs.ring = __builtin_amdgcn_readfirstlane(ldsb + ABYTES + w * 2 * QSLOT);
  constexpr size_t KS_Q = (size_t)32 * 8192;
  auto issueq = [&](int slot, const char* gp) __attribute__((always_inline)) {
    setm0(qs.ring + slot * QSLOT + 4096); asm volatile("s_nop 0");
    dma<-4096>(voffQ[0], gp); dma<-3072>(voffQ[1], gp); dma<-2048>(voffQ[2], gp); dma<-1024>(voffQ[3], gp);
    dma<0>(voffQ[4], gp); dma<1024>(voffQ[5], gp); dma<2048>(voffQ[6], gp); dma<3072>(voffQ[7], gp);
  };
  const u64 e0 = __hip_atomic_load((gu64*)(p.ws + WS_EPOCH), __ATOMIC_RELAXED, __HIP_MEMORY_SCOPE_AGENT);
  AT a;
  u64 tag;
  {
    f16v Qt[4][2];
    QS& s = qs;
    {
      const int rr = lane >> 3, sub = lane & 7;
      if (c >= 16) {
        const u64 t0_ = __builtin_amdgcn_s_memrealtime();
        const int mb = blockIdx.x - 128, mg = mb % 54, ms = (mb / 54) & 1;
        u4 wm[9]; u16 cval[9];
#pragma unroll
        for (int i = 0; i < 9; ++i) {
          const int r = 288 * ms + (tid >> 3) + 32 * i;
          wm[i] = *(const u4*)(p.wmod + (size_t)r * (6 * D) + mg * 64 + (tid & 7) * 8); cval[i] = p.c[r];
        }
        GAP();
        const u64 tag = ((u64)(unsigned)__builtin_amdgcn_readfirstlane((unsigned)(e0 >> 32)) << 32 | (u64)(unsigned)__builtin_amdgcn_readfirstlane((unsigned)e0)) + 1;
        if (mb < 108) {
          float acc[8] = {};
#pragma unroll
          for (int i = 0; i < 9; ++i) {
            const float xc = bf2f(cval[i]);
            const float sc = rbf(xc * __builtin_amdgcn_rcpf(1.f + __expf(-xc)));
#pragma unroll
            for (int e = 0; e < 4; ++e) { acc[2 * e] += sc * bflo(wm[i][e]); acc[2 * e + 1] += sc * bfhi(wm[i][e]); }
          }
          LAS float* red = (LAS float*)(size_t)(ldsb + LDS_X1);
#pragma unroll
          for (int e = 0; e < 8; ++e) { float t = acc[e]; t += dpp<0x128>(t); t = swap32_add(swap16_add(t)); if (lane < 8) red[w * 64 + lane * 8 + e] = t; }
          asm volatile("s_waitcnt lgkmcnt(0)" ::: "memory");
          BAR();
          if (tid < 32) {
            f2 v = {0.f, 0.f};
#pragma unroll
            for (int k = 0; k < 4; ++k) v += *(LAS f2*)(red + k * 64 + 2 * tid);
            const __amdgpu_buffer_rsrc_t rP = __builtin_amdgcn_make_buffer_rsrc(p.ws + WS_MODP, 0, 54 * 1024, 0x00020000);
            __builtin_amdgcn_raw_buffer_store_b128((u4){__float_as_uint(v[0]), __float_as_uint(v[1]), (unsigned)tag, (unsigned)(tag >> 32)}, rP, mg * 1024 + ms * 512 + tid * 16, 0, 16);
          }
        }
        const unsigned dummy = __builtin_amdgcn_readfirstlane(ldsb + LDS_DUMMY);
        while (__builtin_amdgcn_s_memrealtime() - t0_ < 150) __builtin_amdgcn_s_sleep(2);
        const int li = tid < 144 ? tid : tid - 112;
        touch((const char*)p.wq + (size_t)512 * j * 2, (18 * (c - 16) + (li >> 3)) * 8192 + (li & 7) * 128, dummy);
        touch((const char*)p.wq + (size_t)512 * j * 2, (18 * c + (li >> 3)) * 8192 + (li & 7) * 128, dummy);
        touch((const char*)(w < 2 ? p.kc : p.vc) + ((size_t)(2 * j + (w & 1)) * SK + (SK - WIN)) * DH * 2, (32 * (c - 16 * (lane >> 5)) + (lane & 31)) * 128, dummy);
        while (__builtin_amdgcn_s_memrealtime() - t0_ < 560) __builtin_amdgcn_s_sleep(2);
      }
      u4 xv[2][9];
      GAP();
#pragma unroll
      for (int ps = 0; ps < 2; ++ps)
#pragma unroll
        for (int i = 0; i < 9; ++i) xv[ps][i] = *(const u4*)(p.x + (size_t)(c * 64 + 16 * w + 8 * ps + rr) * D + (sub + 8 * i) * 8);
      GAP();
      tag = ((u64)(unsigned)__builtin_amdgcn_readfirstlane((unsigned)(e0 >> 32)) << 32 | (u64)(unsigned)__builtin_amdgcn_readfirstlane((unsigned)e0)) + 1;
      f2 y[2][9][4];
      ModGather<1728, 256> mgat;
      {
#pragma unroll
        for (int ps = 0; ps < 2; ++ps) {
          f2 a1 = {0.f, 0.f}, a2 = {0.f, 0.f};
#pragma unroll
          for (int i = 0; i < 9; ++i)
#pragma unroll
            for (int e = 0; e < 4; ++e) { y[ps][i][e] = bf2(xv[ps][i][e]); a1 += y[ps][i][e]; a2 = __builtin_elementwise_fma(y[ps][i][e], y[ps][i][e], a2); }
          float s1 = a1[0] + a1[1], s2 = a2[0] + a2[1];
          s1 = sum8(s1); s2 = sum8(s2);
          const float mean = s1 * (1.f / D), var = __builtin_fmaxf(__builtin_fmaf(-mean, mean, s2 * (1.f / D)), 0.f);
          const float mr = rbf(mean), rs = __builtin_amdgcn_rcpf(rbf(__builtin_amdgcn_sqrtf(rbf(rbf(var) + 1e-5f))));
          f2 mr2 = {mr, mr}, rs2 = {rs, rs}; asm volatile("" : "+v"(mr2), "+v"(rs2));
          if (ps == 1) { GAP(); mgat.issue(p.ws, tid); GAP(); }
#pragma unroll
          for (int i = 0; i < 9; ++i) {
            f2 t[4];
#pragma unroll
            for (int e = 0; e < 4; ++e) t[e] = y[ps][i][e] - mr2;
#pragma unroll
            for (int e = 0; e < 4; ++e) t[e] = rb2(t[e]);
#pragma unroll
            for (int e = 0; e < 4; ++e) t[e] = t[e] * rs2;
#pragma unroll
            for (int e = 0; e < 4; ++e) y[ps][i][e] = rb2(t[e]);
            asm volatile("" : "+v"(y[ps][i][0]), "+v"(y[ps][i][1]), "+v"(y[ps][i][2]), "+v"(y[ps][i][3]));
          }
        }
      }
      mgat.finish(tag, ldsb, tmo, tid);
      issueq(0, gq); issueq(1, gq + KS_Q);
      asm volatile("s_waitcnt lgkmcnt(0)" ::: "memory");
      BAR();
      {
        const unsigned tb = ldsb + LDS_X1 + 1024 + sub * 32;
        f4 cst[2][4];
        auto ldc = [&](int i, f4 (&d)[4]) { d[0] = *(LAS f4*)(size_t)(tb + i * 256); d[1] = *(LAS f4*)(size_t)(tb + i * 256 + 16); d[2] = *(LAS f4*)(size_t)(tb + i * 256 + D * 4); d[3] = *(LAS f4*)(size_t)(tb + i * 256 + D * 4 + 16); };
        ldc(0, cst[0]);
        const unsigned ad0 = ldsb + (16 * w + rr) * ROWB + ((sub ^ ((rr >> 1) & 7)) * 16);
        const unsigned ad1 = ldsb + (16 * w + 8 + rr) * ROWB + ((sub ^ (((rr >> 1) + 4) & 7)) * 16);
#pragma unroll
        for (int i = 0; i < 9; ++i) {
          if (i + 1 < 9) ldc(i + 1, cst[(i + 1) & 1]);
          GAP();
          const f4 (&k)[4] = cst[i & 1];
          const f2 sh[4] = {{k[0][0], k[0][1]}, {k[0][2], k[0][3]}, {k[1][0], k[1][1]}, {k[1][2], k[1][3]}}, cc[4] = {{k[2][0], k[2][1]}, {k[2][2], k[2][3]}, {k[3][0], k[3][1]}, {k[3][2], k[3][3]}};
          f2 t[2][4]; u4 o[2];
#pragma unroll
          for (int ps = 0; ps < 2; ++ps)
#pragma unroll
            for (int e = 0; e < 4; ++e) t[ps][e] = y[ps][i][e] * cc[e];
#pragma unroll
          for (int ps = 0; ps < 2; ++ps)
#pragma unroll
            for (int e = 0; e < 4; ++e) t[ps][e] = rb2(t[ps][e]);
#pragma unroll
          for (int ps = 0; ps < 2; ++ps)
#pragma unroll
            for (int e = 0; e < 4; ++e) t[ps][e] = t[ps][e] + sh[e];
#pragma unroll
          for (int ps = 0; ps < 2; ++ps)
#pragma unroll
            for (int e = 0; e < 4; ++e) o[ps][e] = pk(t[ps][e]);
          *(LAS u4*)(size_t)(ad0 + i * 128) = o[0];
          *(LAS u4*)(size_t)(ad1 + i * 128) = o[1];
          GAP();
        }
      }
    }
    {
      const int blk = (lane >> 4) & 1, q = (lane & 15) >> 2, pp = lane & 3;
#pragma unroll
      for (int dt = 0; dt < 4; ++dt) s.wb[dt] = s.ring + (8 * hh + q) * 256 + 64 * (dt ^ q) + 32 * blk + 16 * (pp & 1) + 8 * (pp >> 1);
#pragma unroll
      for (int i = 0; i < 4; ++i) s.hb[i] = ldsb + r32 * ROWB + (((2 * i + hh) ^ ((r32 >> 1) & 7)) * 16);
    }
    asm volatile("s_waitcnt vmcnt(8) lgkmcnt(0)" ::: "memory");
    BAR();
#pragma unroll
    for (int f = 0; f < 8; ++f) { s.wlo[0][f] = ldT(s.wb[f & 3], 4096 * (f >> 2)); s.whi[0][f] = ldT(s.wb[f & 3], 4096 * (f >> 2) + 1024); }
#pragma unroll
    for (int i = 0; i < 4; ++i) s.hf[0][i] = ldA(s.hb[i >> 1], (i & 1) * 32 * ROWB);
    asm volatile("s_waitcnt lgkmcnt(0)" ::: "memory");
    issueq(0, gq + 2 * KS_Q);
    auto no = [&]() __attribute__((always_inline)) {};
    qstep<0, true, true, true, 8>(s, Qt, voffQ, gq + 3 * KS_Q, 0, no);
    qstep<1, false, true, true, 8>(s, Qt, voffQ, gq + 4 * KS_Q, 128, no);
    for (int n = 2; n < 14; n += 2) {
      qstep<0, false, true, true, 8>(s, Qt, voffQ, gq + (size_t)(n + 3) * KS_Q, 128 * ((n + 1) >> 1), no);
      qstep<1, false, true, true, 8>(s, Qt, voffQ, gq + (size_t)(n + 4) * KS_Q, 128 * ((n + 2) >> 1), no);
    }
    qstep<0, false, true, true, 8>(s, Qt, voffQ, gq + (size_t)17 * KS_Q, 128 * 7, no);
    qstep<1, false, true, false, 8>(s, Qt, voffQ, gq, 128 * 8, no);
    qstep<0, false, true, false, 0>(s, Qt, voffQ, gq, 128 * 8, no);
    qstep<1, false, false, false, 0>(s, Qt, voffQ, gq, 0, no);
    {
      const int kvl = w >> 1, half = w & 1;
      const unsigned kr = ldsb + kvl * 65536, vr = kr + 32768;
#pragma unroll
      for (int i = 0; i < 2; ++i) a.kb_[i] = kr + 2048 * (r32 >> 3) + 64 * (r32 & 7) + 16 * ((2 * i + hh) ^ ((r32 >> 2) & 3));
      const int blk = (lane >> 4) & 1, q = (lane & 15) >> 2, pp = lane & 3;
#pragma unroll
      for (int t = 0; t < 2; ++t) a.vb_[t] = vr + 64 * (4 * hh + q) + 16 * ((2 * blk + (pp >> 1)) ^ ((2 * t + hh) & 3)) + 8 * (pp & 1);
      const int st = lane >> 5, r7 = (lane >> 2) & 7, pos = lane & 3;
#pragma unroll
      for (int i = 0; i < 8; ++i) {
        const int rg = 4 * half + (i >> 1);
        a.voff[i] = rg * 2048 + r7 * 256 + (i & 1) * 128 + st * 64 + 16 * (pos ^ ((2 * (rg & 1) + (r7 >> 2)) & 3)) - (i - 4) * 1024 + 4096;
      }
      a.m0k = __builtin_amdgcn_readfirstlane(kr + 8192 * half + 4096); a.m0v = __builtin_amdgcn_readfirstlane(vr + 8192 * half + 4096);
      const size_t kvoff = ((size_t)(2 * j + kvl) * SK + (SK - WIN)) * DH * 2;
      a.gk = (const char*)p.kc + kvoff - 4096; a.gv = (const char*)p.vc + kvoff - 4096;
      a.pend = false; a.one = p.one;
    }
    asm volatile("s_waitcnt lgkmcnt(0)" ::: "memory");
    BAR();
    setm0(a.m0k); asm volatile("s_nop 0");
    dma<-4096>(a.voff[0], a.gk); dma<-3072>(a.voff[1], a.gk); dma<-2048>(a.voff[2], a.gk); dma<-1024>(a.voff[3], a.gk);
    dma<0>(a.voff[4], a.gk); dma<1024>(a.voff[5], a.gk); dma<2048>(a.voff[6], a.gk); dma<3072>(a.voff[7], a.gk);
    asm volatile("s_nop 7" ::: "memory");
#pragma unroll
    for (int dt = 0; dt < 4; ++dt)
#pragma unroll
      for (int qb = 0; qb < 2; ++qb) {
        asm volatile("" : "+v"(Qt[dt][qb]));
#pragma unroll
        for (int s2 = 0; s2 < 2; ++s2) {
          u4 w4;
#pragma unroll
          for (int e = 0; e < 4; ++e) w4[e] = pk((f2){Qt[dt][qb][8 * s2 + 2 * e], Qt[dt][qb][8 * s2 + 2 * e + 1]} * QSCALE);
          a.q[qb][2 * dt + s2] = __builtin_bit_cast(b8, w4);
          asm volatile("" : "+a"(a.q[qb][2 * dt + s2]));
        }
      }
  }
  {
    auto tile_bar = [&]() __attribute__((always_inline)) { asm volatile("s_waitcnt vmcnt(0) lgkmcnt(0)" ::: "memory"); BAR(); };
    const char* pfp = lane >= 32 ? p.ws + WS_X + (size_t)c * 64 * SLICE + (size_t)j * SLICE + (18 * w + lane - 32) * 128
                    : w < 2 ? (const char*)(w == 0 ? p.wg : p.wu) + (size_t)576 * j * 2 + (size_t)(18 * c + lane / 9 - 3) * (DF * 2) + (lane % 9) * 128
                            : (const char*)p.wo + (size_t)512 * j * ROWB + (16 * c) * ROWB + (12 * (w - 2) + lane - 24) * 128;
    const int pfs = lane >= 32 ? 8 * SLICE : w < 2 ? 3 * DF * 2 : 24 * 128, pfn = w < 2 ? 27 : 12;
    auto pft = [&](int t) __attribute__((always_inline)) {
      const bool on = lane < pfn && t >= 1 && t <= 6;
      touchp(on ? pfp : (const char*)p.wo, __builtin_amdgcn_readfirstlane(ldsb + LDS_DUMMY));
      pfp += pfs;
    };
    auto tile_bar1 = [&]() __attribute__((always_inline)) { asm volatile("s_waitcnt vmcnt(1) lgkmcnt(0)" ::: "memory"); BAR(); };
    LAS unsigned* flag = (LAS unsigned*)(size_t)(ldsb + LDS_X1);
    if (tid == 0) *flag = 0u;
    auto k0 = [&]() __attribute__((always_inline)) {
      setm0(a.m0k); asm volatile("s_nop 0");
      dma<-4096>(a.voff[0], a.gk); dma<-3072>(a.voff[1], a.gk); dma<-2048>(a.voff[2], a.gk); dma<-1024>(a.voff[3], a.gk);
      dma<0>(a.voff[4], a.gk); dma<1024>(a.voff[5], a.gk); dma<2048>(a.voff[6], a.gk); dma<3072>(a.voff[7], a.gk);
    };
    tile_bar(); fastA<0, true, true, true>(a, 0, [&]() __attribute__((always_inline)) { pft(0); }); asm volatile("s_nop 7\n\ts_nop 3" ::: "memory"); GAP(); fastB<0, 2>(a);
    tile_bar1(); fastA<1, false, true, true>(a, 1, [&]() __attribute__((always_inline)) { pft(1); }); fastB<1, 1>(a);
    for (int t = 2; t < 6; t += 2) {
      tile_bar1(); fastA<0, false, true, true>(a, t, [&]() __attribute__((always_inline)) { pft(t); }); fastB<0, 0>(a);
      tile_bar1(); fastA<1, false, true, true>(a, t + 1, [&]() __attribute__((always_inline)) { pft(t + 1); }); fastB<1, 0>(a);
    }
    tile_bar1(); fastA<0, false, true, true>(a, 6, [&]() __attribute__((always_inline)) { pft(6); }); fastB<0, 0>(a);
    tile_bar1(); fastA<1, false, false, true>(a, 7, [&]() __attribute__((always_inline)) { pft(7); }); fastB<1, 0>(a);
    tile_bar1();
    fastB<1, 3>(a);
    {
      const bool bad = !(a.l[0] < 0x1p40f) || !(a.l[1] < 0x1p40f);
      if (__any((int)bad) && lane == 0) *flag = 1u;
      asm volatile("s_waitcnt lgkmcnt(0)" ::: "memory"); BAR();
      unsigned redo = __builtin_amdgcn_readfirstlane(*(volatile LAS unsigned*)flag); asm volatile("" : "+s"(redo));
      if (__builtin_expect(redo != 0u, 0)) {
        auto nopf = [&]() __attribute__((always_inline)) {};
        a.pend = false;
        auto upt = [&](const char* q) __attribute__((always_inline)) { const u64 v = (u64)q; return (const char*)((u64)(unsigned)__builtin_amdgcn_readfirstlane((unsigned)(v >> 32)) << 32 | (u64)(unsigned)__builtin_amdgcn_readfirstlane((unsigned)v)); };
        a.gk = upt(a.gk); a.gv = upt(a.gv); a.m0k = __builtin_amdgcn_readfirstlane(a.m0k); a.m0v = __builtin_amdgcn_readfirstlane(a.m0v);
        k0();
        tile_bar(); phaseA<0, true, true, true>(a, 0, nopf); asm volatile("s_nop 7\n\ts_nop 3" ::: "memory"); GAP(); phaseB<0, true, false>(a);
        tile_bar(); phaseA<1, false, true, true>(a, 1, nopf); phaseB<1, false, true>(a);
        for (int t = 2; t < 6; t += 2) {
          tile_bar(); phaseA<0, false, true, true>(a, t, nopf); phaseB<0, false, false>(a);
          tile_bar(); phaseA<1, false, true, true>(a, t + 1, nopf); phaseB<1, false, false>(a);
        }
        tile_bar(); phaseA<0, false, true, true>(a, 6, nopf); phaseB<0, false, false>(a);
        tile_bar(); phaseA<1, false, false, true>(a, 7, nopf); phaseB<1, false, false>(a);
        { auto f1 = [&](auto ic) __attribute__((always_inline)) { sm_exp<1, 1, decltype(ic)::value>(a); }; UNROLL(32, f1);
          auto f2_ = [&](auto ic) __attribute__((always_inline)) { sm_pack<1, decltype(ic)::value>(a); }; UNROLL(32, f2_);
          auto f3 = [&](auto ic) __attribute__((always_inline)) { sm_add<1, 1, decltype(ic)::value>(a); }; UNROLL(32, f3); }
#pragma unroll
        for (int qb = 0; qb < 2; ++qb) a.l[qb] += a.rs0[qb] + a.rs1[qb];
        tile_bar();
        const unsigned vslot = 16384;
        b4 vlo[16], vhi[16];
#pragma unroll
        for (int I = 0; I < 16; ++I) { vlo[I] = ldT(a.vb_[0], vslot + 2048 * (2 * (I >> 2)) + 512 * (I & 3)); vhi[I] = ldT(a.vb_[1], vslot + 2048 * (2 * (I >> 2) + 1) + 512 * (I & 3)); }
        asm volatile("s_nop 1" ::: "memory"); GAP();
#pragma unroll
        for (int G = 0; G < 32; ++G) { const int I = G >> 1; mfma32<true, false>(a.O[I & 3][G & 1], cat(vlo[I], vhi[I]), a.P[G & 1][I >> 2]); }
        asm volatile("s_waitcnt vmcnt(0) lgkmcnt(0)" ::: "memory"); BAR();
      }
    }
  }
  int tid2 = threadIdx.x; asm volatile("" : "+v"(tid2));
  {
  const int tid = tid2, lane = tid2 & 63, hh = lane >> 5, r32 = lane & 31;
  GS s;
  unsigned voffB[9];
#pragma unroll
  for (int i = 0; i < 9; ++i) { const int idx = i * 64 + lane, imm = i < 8 ? (i - 4) * 1024 : 0; voffB[i] = (idx / 18) * ROWB + (idx % 18) * 16 - imm + 4096; }
  s.ring = __builtin_amdgcn_readfirstlane(ldsb + ABYTES + w * 2 * SLOT);
  const int g4 = lane >> 4, cl = lane & 15;
  s.wbase = s.ring + (4 * g4 + ((lane & 15) >> 2)) * 288 + 8 * (lane & 3);
  const unsigned sw = (cl >> 1) & 7;
  const unsigned ab_e = ldsb + cl * ROWB + ((g4 ^ sw) * 16), ab_o = ldsb + cl * ROWB + (((4 + g4) ^ sw) * 16);
  const __amdgpu_buffer_rsrc_t rX = __builtin_amdgcn_make_buffer_rsrc(p.ws + WS_X + (size_t)c * 64 * SLICE, 0, 64 * SLICE, 0x00020000);
  unsigned voffA[9];
#pragma unroll
  for (int i = 0; i < 9; ++i) { const int idx = i * 64 + lane, imm = i < 8 ? (i - 4) * 1024 : 0; voffA[i] = (idx / 18) * (DF * 2) + (idx % 18) * 16 - imm + 4096; }
  const char* gG = (const char*)p.wg + (size_t)(576 * j + 144 * w) * 2 - 4096;
  const char* gU = (const char*)p.wu + (size_t)(576 * j + 144 * w) * 2 - 4096;
  const char* gD = (const char*)p.wd + (size_t)(576 * j) * ROWB + 288 * w - 4096;
  f4 aG[9][4];
  {
    const char* gO = (const char*)p.wo + (size_t)(512 * j) * ROWB + 288 * w - 4096;
    issue_all(s, 0, voffB, gO); issue_all(s, 1, voffB, gO + (size_t)32 * ROWB);
    asm volatile("s_nop 7" ::: "memory");
#pragma unroll
    for (int qb = 0; qb < 2; ++qb) {
      const float il = __builtin_amdgcn_rcpf(swap32_add(a.l[qb]));
      const int row = 32 * qb + r32; const unsigned swr = (r32 >> 1) & 7;
#pragma unroll
      for (int db = 0; db < 4; ++db) {
        asm volatile("" : "+a"(a.O[db][qb]));
        const f16v o = a.O[db][qb];
#pragma unroll
        for (int u = 0; u < 2; ++u) {
          const u4 wv = {pk((f2){o[4 * u], o[4 * u + 1]} * il), pk((f2){o[4 * u + 2], o[4 * u + 3]} * il), pk((f2){o[4 * u + 8], o[4 * u + 9]} * il), pk((f2){o[4 * u + 10], o[4 * u + 11]} * il)};
          *(LAS u4*)(size_t)(ldsb + row * ROWB + (((4 * (4 * w + db) + 2 * u + hh) ^ swr) * 16)) = wv;
        }
      }
    }
    asm volatile("s_waitcnt vmcnt(9) lgkmcnt(0)" ::: "memory");
    BAR();
    read_w(s, 0); read_a(s, 0, ab_e);
    asm volatile("s_waitcnt lgkmcnt(0)" ::: "memory");
    issue_all(s, 0, voffB, gO + (size_t)2 * 32 * ROWB);
    const Epi e1 = {rX, (unsigned)(j * SLICE), 8u * SLICE, ldsb + LDS_X1, j, w, lane};
    gemm_rows<16, 2, 0>(s, aG, voffB, gO, ab_e, ab_o, voffA, gG, gU, e1, [&]() __attribute__((always_inline)) {});
  }
  {
    int zlr[3], zcc[3];
    const int t3 = tid < 192 ? tid : tid - 64;
    u4 xr[3], gv[3];
#pragma unroll
    for (int i = 0; i < 3; ++i) {
      zdecode(t3 + 192 * i, zlr[i], zcc[i]);
      xr[i] = *(const u4*)(p.x + (size_t)(c * 64 + j * 8 + zlr[i]) * D + zcc[i] * 8);
      gv[i] = *(LAS u4*)(size_t)(ldsb + LDS_MOD + (2 * D + zcc[i] * 8) * 2);
    }
    const int lrow = 2 * w + hh, grow = j * 8 + lrow;
    u2 cbv[3][2], sbv[3][2];
#pragma unroll
    for (int i = 0; i < 3; ++i) {
      const int pos = (i < 2 || r32 < 8) ? r32 + 32 * i : r32;
      const int q = pos ^ ((grow >> 1) & 7), k0 = 32 * (q >> 2) + 4 * (q & 3);
#pragma unroll
      for (int h = 0; h < 2; ++h) { cbv[i][h] = *(LAS u2*)(size_t)(ldsb + LDS_MOD + (4 * D + k0 + 16 * h) * 2); sbv[i][h] = *(LAS u2*)(size_t)(ldsb + LDS_MOD + (3 * D + k0 + 16 * h) * 2); }
    }
    asm volatile("s_waitcnt lgkmcnt(0)" ::: "memory");
    publish(flags + (0 * 256 + blockIdx.x) * FLAG_STRIDE, tag);
    await8(flags + (0 * 256 + c * 8) * FLAG_STRIDE, tag, tmo, w, lane);
    {
      u4 v[3][8];
#pragma unroll
      for (int i = 0; i < 3; ++i) {
#pragma unroll
        for (int jj = 0; jj < 7; ++jj) v[i][jj] = __builtin_amdgcn_raw_buffer_load_b128(rX, (j * 8 + jj + (jj >= j)) * SLICE + (t3 + 192 * i) * 16, 0, 16);
      }
#pragma unroll
      for (int i = 0; i < 3; ++i) v[i][7] = *(LAS u4*)(size_t)(ldsb + LDS_X1 + (t3 + 192 * i) * 16);
#pragma unroll
      for (int i = 0; i < 3; ++i) {
        const u4 x1 = gated_residual(v[i], xr[i], gv[i]);
        if (tid < 192) {
          *(LAS u4*)(size_t)(ldsb + LDS_X1 + (tid + 192 * i) * 16) = x1;
          LAS f4* dst = (LAS f4*)(size_t)(ldsb + (zlr[i] * D + zcc[i] * 8) * 4);
          const f2 a0 = bf2(x1[0]), a1 = bf2(x1[1]), a2 = bf2(x1[2]), a3 = bf2(x1[3]);
          dst[0] = (f4){a0[0], a0[1], a1[0], a1[1]}; dst[1] = (f4){a2[0], a2[1], a3[0], a3[1]};
        }
      }
    }
    asm volatile("s_waitcnt lgkmcnt(0)" ::: "memory");
    BAR();
    {
      f4 v[3][2];
      f2 sum = {0.f, 0.f};
#pragma unroll
      for (int i = 0; i < 3; ++i) {
        const bool on = (i < 2 || r32 < 8);
        const int pos = on ? r32 + 32 * i : r32;
        const int q = pos ^ ((grow >> 1) & 7), k0 = 32 * (q >> 2) + 4 * (q & 3);
#pragma unroll
        for (int h = 0; h < 2; ++h) {
          v[i][h] = *(LAS f4*)(size_t)(ldsb + (lrow * D + k0 + 16 * h) * 4);
          if (on) sum += (f2){v[i][h][0] + v[i][h][1], v[i][h][2] + v[i][h][3]};
        }
      }
      float sm = sum[0] + sum[1];
      sm = sum32(sm);
      const float mean = sm * (1.f / D);
      float q2 = 0.f;
#pragma unroll
      for (int i = 0; i < 3; ++i)
        if (i < 2 || r32 < 8) {
#pragma unroll
          for (int h = 0; h < 2; ++h)
#pragma unroll
            for (int e = 0; e < 4; ++e) { const float dl = v[i][h][e] - mean; q2 += dl * dl; }
        }
      q2 = sum32(q2);
      const float mr = rbf(mean), rs = __builtin_amdgcn_rcpf(rbf(__builtin_amdgcn_sqrtf(rbf(rbf(q2 * (1.f / D)) + 1e-5f))));
      const __amdgpu_buffer_rsrc_t rH = __builtin_amdgcn_make_buffer_rsrc(p.ws + WS_H2 + (size_t)c * ABYTES, 0, ABYTES, 0x00020000);
      u4 oh[3];
#pragma unroll
      for (int i = 0; i < 3; ++i) {
        u4& o = oh[i];
#pragma unroll
        for (int h = 0; h < 2; ++h)
#pragma unroll
          for (int e = 0; e < 2; ++e) {
            unsigned t = pk((f2){v[i][h][2 * e], v[i][h][2 * e + 1]} - mr);
            t = pk(bf2(t) * rs);
            t = pk(bf2(t) * bf2(pk(bf2(cbv[i][h][e]) + 1.f)));
            o[2 * h + e] = pk(bf2(t) + bf2(sbv[i][h][e]));
          }
        if (i < 2 || r32 < 8) __builtin_amdgcn_raw_buffer_store_b128(o, rH, grow * ROWB + (r32 + 32 * i) * 16, 0, 16);
      }
      publish(flags + (1 * 256 + blockIdx.x) * FLAG_STRIDE, tag);
      if (w != 0) {
        const unsigned dummy = __builtin_amdgcn_readfirstlane(ldsb + LDS_DUMMY);
        int li = tid - 64; li = li < 162 ? li : li - 94;
        const int r = 18 * c + li / 9, l = li % 9;
        touch((const char*)p.wg + (size_t)576 * j * 2, r * (DF * 2) + l * 128, dummy);
        touch((const char*)p.wu + (size_t)576 * j * 2, r * (DF * 2) + l * 128, dummy);
      }
      await8(flags + (1 * 256 + c * 8) * FLAG_STRIDE, tag, tmo, w, lane);
      u4 hv[16];
#pragma unroll
      for (int i = 0; i < 16; ++i) { const int y = i * 256 + tid < 4032 ? i * 256 + tid : tid; hv[i] = __builtin_amdgcn_raw_buffer_load_b128(rH, (y + (y >= j * 576 ? 576 : 0)) * 16, 0, 16); }
#pragma unroll
      for (int i = 0; i < 3; ++i) if (i < 2 || r32 < 8) *(LAS u4*)(size_t)(ldsb + grow * ROWB + (r32 + 32 * i) * 16) = oh[i];
#pragma unroll
      for (int i = 0; i < 16; ++i) { const int y = i * 256 + tid; if (i < 15 || y < 4032) *(LAS u4*)(size_t)(ldsb + (y + (y >= j * 576 ? 576 : 0)) * 16) = hv[i]; }
    }
    asm volatile("s_waitcnt vmcnt(0) lgkmcnt(0)" ::: "memory");
    BAR();
  }
  {
    constexpr size_t KS_GU = (size_t)32 * DF * 2, KS_D = (size_t)32 * ROWB;
    read_w(s, 0); read_a(s, 0, ab_e);
    asm volatile("s_waitcnt lgkmcnt(0)" ::: "memory");
    issue_all(s, 0, voffA, gG + KS_GU);
    f4 aU[9][4];
    auto no = [&]() __attribute__((always_inline)) {};
    auto pfd = [&](int h) __attribute__((always_inline)) {
      int li = 64 * h + 16 * w + lane; li = li < 162 ? li : 161;
      if (lane < 16) touch((const char*)p.wd + (size_t)576 * j * ROWB, (18 * c) * ROWB + li * 128, __builtin_amdgcn_readfirstlane(ldsb + LDS_DUMMY));
    };
    auto kpair = [&](auto zc, auto lastc, auto vmc, int k, auto&& pf) __attribute__((always_inline)) {
      constexpr bool Z = decltype(zc)::value, LAST = decltype(lastc)::value; constexpr int VM = decltype(vmc)::value;
      const char* u1 = gU + (size_t)(k + 1) * KS_GU; const char* g2 = gG + (size_t)(k + 2) * KS_GU;
      step<0, 0, Z, true, false, true, 9, 0>(s, aG, voffA, u1, 0, no);
      if constexpr (!LAST) {
        step<1, 0, Z, true, true, true, 9, 8>(s, aU, voffA, g2, ab_o + 128 * (k >> 1), pf);
        step<0, 1, false, true, false, true, VM, 0>(s, aG, voffA, u1 + KS_GU, 0, no);
        step<1, 1, false, true, true, true, VM, 8>(s, aU, voffA, g2 + KS_GU, ab_e + 128 * ((k + 2) >> 1), no);
      } else {
        step<1, 0, Z, true, true, true, 9, 8>(s, aU, voffB, gD, ab_o + 128 * (k >> 1), no);
        step<0, 1, false, true, false, true, 9, 0>(s, aG, voffB, gD + KS_D, 0, no);
        step<1, 1, false, true, false, true, 9, 8>(s, aU, voffB, gD + 2 * KS_D, 0, no);
      }
    };
    kpair(std::true_type{}, std::false_type{}, IC<9>{}, 0, no);
    for (int k = 2; k < 8; k += 2) kpair(std::false_type{}, std::false_type{}, IC<9>{}, k, no);
    for (int k = 8; k < 14; k += 2) kpair(std::false_type{}, std::false_type{}, IC<10>{}, k, [&]() __attribute__((always_inline)) { pfd((k - 8) >> 1); });
    kpair(std::false_type{}, std::false_type{}, IC<9>{}, 14, no);
    kpair(std::false_type{}, std::true_type{}, IC<9>{}, 16, no);
    settle<0>(aG); settle<8>(aU);
    u2 act[9][4];
#pragma unroll
    for (int t = 0; t < 9; ++t)
#pragma unroll
      for (int m = 0; m < 4; ++m)
#pragma unroll
        for (int e = 0; e < 2; ++e) {
          const f2 x = {aG[t][m][2 * e], aG[t][m][2 * e + 1]}, u = {aU[t][m][2 * e], aU[t][m][2 * e + 1]};
          const f2 z = x * (x * x * -0.10294324f + -2.3022082f);
          const f2 dn = (f2){__builtin_amdgcn_exp2f(z[0]), __builtin_amdgcn_exp2f(z[1])} + 1.f;
          act[t][m][e] = pk((x * u) * (f2){__builtin_amdgcn_rcpf(dn[0]), __builtin_amdgcn_rcpf(dn[1])});
        }
    asm volatile("s_waitcnt lgkmcnt(0)" ::: "memory");
    BAR();
#pragma unroll
    for (int t = 0; t < 9; ++t) {
      const int tt = 9 * w + t, ks = tt >> 1, ah = tt & 1;
      const unsigned ad = ldsb + cl * ROWB + (((4 * ks + g4) ^ sw) * 16) + 8 * ah;
#pragma unroll
      for (int m = 0; m < 4; ++m) *(LAS u2*)(size_t)(ad + m * 16 * ROWB) = act[t][m];
    }
    asm volatile("s_waitcnt lgkmcnt(0)" ::: "memory");
    BAR();
    read_a(s, 0, ab_e);
    const Epi e2 = {rX, (unsigned)(j * 8 * SLICE), (unsigned)SLICE, s.ring - w * 2304, j, w, lane};
    gemm_rows<18, 0, 0>(s, aG, voffB, gD, ab_e, ab_o, voffB, gD, gD, e2, [&]() __attribute__((always_inline)) {});
  }
  {
    int tid3 = threadIdx.x; asm volatile("" : "+v"(tid3));
    const int tid = tid3, lane = tid3 & 63;
    asm volatile("s_waitcnt lgkmcnt(0)" ::: "memory");
    publish(flags + (2 * 256 + blockIdx.x) * FLAG_STRIDE, tag);
    const int t3 = tid < 192 ? tid : tid - 64;
    int zlr[3], zcc[3];
    u4 gv[3], x1[3];
#pragma unroll
    for (int i = 0; i < 3; ++i) {
      zdecode(t3 + 192 * i, zlr[i], zcc[i]);
      gv[i] = *(LAS u4*)(size_t)(ldsb + LDS_MOD + (5 * D + zcc[i] * 8) * 2);
      x1[i] = *(LAS u4*)(size_t)(ldsb + LDS_X1 + (t3 + 192 * i) * 16);
    }
    if (blockIdx.x == 0 && w == 1) {
      for (unsigned spins = 0; spins <= SPIN_CAP; ++spins) {
        bool ok = true;
#pragma unroll
        for (int i = 0; i < 4; ++i) ok &= __hip_atomic_load((gu64*)(flags + (lane + 64 * i) * FLAG_STRIDE), __ATOMIC_RELAXED, __HIP_MEMORY_SCOPE_AGENT) == tag;
        if (__all(ok)) break;
        __builtin_amdgcn_s_sleep(4);
      }
      if (lane == 0) __hip_atomic_store((gu64*)(p.ws + WS_EPOCH), tag, __ATOMIC_RELAXED, __HIP_MEMORY_SCOPE_AGENT);
    }
    await8(flags + (2 * 256 + c * 8) * FLAG_STRIDE, tag, tmo, w, lane);
    const __amdgpu_buffer_rsrc_t rO = __builtin_amdgcn_make_buffer_rsrc(p.out + (size_t)(c * 64 + j * 8) * D, 0, SLICE, 0x00020000); (void)rO;
    u4 v[3][8];
#pragma unroll
    for (int i = 0; i < 3; ++i)
#pragma unroll
      for (int jj = 0; jj < 7; ++jj) v[i][jj] = __builtin_amdgcn_raw_buffer_load_b128(rX, ((jj + (jj >= j)) * 8 + j) * SLICE + (t3 + 192 * i) * 16, 0, 16);
#pragma unroll
    for (int i = 0; i < 3; ++i) { const int z = t3 + 192 * i, zw = z / 144; v[i][7] = *(LAS u4*)(size_t)(ldsb + ABYTES + zw * 2 * SLOT + (z - zw * 144) * 16); }
#pragma unroll
    for (int i = 0; i < 3; ++i) {
      const u4 o = gated_residual(v[i], x1[i], gv[i]);
      if (tid < 192) *(u4*)(p.out + (size_t)(c * 64 + j * 8 + zlr[i]) * D + zcc[i] * 8) = o;
    }
  }
  asm volatile("s_waitcnt lgkmcnt(0)" ::: "memory");
  }
}

extern "C" void kernel_launch(void* const* d_in, const int* in_sizes, int n_in, void* d_out, int out_size, void* d_ws, size_t ws_size, hipStream_t stream) {
  Params p;
  p.x = (const u16*)d_in[0]; p.kc = (const u16*)d_in[1]; p.vc = (const u16*)d_in[2]; p.wq = (const u16*)d_in[3]; p.wo = (const u16*)d_in[4];
  p.wg = (const u16*)d_in[6]; p.wu = (const u16*)d_in[7]; p.wd = (const u16*)d_in[8];
  p.c = (const u16*)d_in[9]; p.wmod = (const u16*)d_in[10];
  p.out = (u16*)d_out; p.ws = (char*)d_ws;
 p.one = 1.0f; p.gap = 0u;
  (void)hipFuncSetAttribute((const void*)k_main, hipFuncAttributeMaxDynamicSharedMemorySize, LDS_BYTES);
  k_main<<<256, 256, LDS_BYTES, stream>>>(p);
}
```
